```python
import torch

def _softmax(x, axis=-1):
    return torch.softmax(x, dim=axis)
def _gelu(x):
    return torch.nn.functional.gelu(x, approximate="tanh")
def _silu(x):
    return torch.nn.functional.silu(x)
def _sigmoid(x):
    return torch.sigmoid(x)
def _layernorm(x, axis=-1, eps=1e-5):
    m = x.mean(dim=axis, keepdim=True)
    v = x.var(dim=axis, keepdim=True, unbiased=False)
    return (x - m) / torch.sqrt(v + eps)
def _scatter_add(tgt, idx, src, axis=0):
    return tgt.clone().index_add_(axis, idx, src)
def _segment_sum(src, seg, n):
    out = torch.zeros((n,) + src.shape[1:], dtype=src.dtype)
    return out.index_add_(0, seg, src)
def _pad(x, pw):
    flat = tuple(v for pair in reversed(pw) for v in pair)
    return torch.nn.functional.pad(x, flat)

kernel_name = 'sequential_preLN__spectral_fnet__swiglu_ffn__prompt'

def setup_inputs():

    _w = (lambda a: (lambda t: t.to(torch.bfloat16) if t.is_floating_point() else t)(torch.as_tensor(a)))
    def _A_setup():
        B, S, D = 1, 2048, 1024
        ns = torch.arange(S)[:, None] * torch.arange(S)[None, :]
        nd = torch.arange(D)[:, None] * torch.arange(D)[None, :]
        Ws_re = torch.cos(-2.0 * torch.pi * ns / S).to(float)
        Ws_im = torch.sin(-2.0 * torch.pi * ns / S).to(float)
        Wd_re = torch.cos(-2.0 * torch.pi * nd / D).to(float)
        Wd_im = torch.sin(-2.0 * torch.pi * nd / D).to(float)
        return {
            "x": _w((lambda *s: torch.randn(*s, dtype=torch.bfloat16) * 0.05)(B, S, D)),
            "dft_seq_re": _w(Ws_re),
            "dft_seq_im": _w(Ws_im),
            "dft_hid_re": _w(Wd_re),
            "dft_hid_im": _w(Wd_im),
        }
    _A_in = _A_setup()

    def _B_setup():
        B, S, D, F = 1, 2048, 1024, 3584
        return {
            "x": _w((lambda *s: torch.randn(*s, dtype=torch.bfloat16) * 0.05)(B, S, D)),
            "w_gate": _w((lambda *s: torch.randn(*s, dtype=torch.bfloat16) * 0.2)(D, F)),
            "w_up": _w((lambda *s: torch.randn(*s, dtype=torch.bfloat16) * 0.2)(D, F)),
            "w_down": _w((lambda *s: torch.randn(*s, dtype=torch.bfloat16) * 0.2)(F, D)),
        }
    _B_in = _B_setup()

    return {
        "A_x": _A_in["x"],
        "A_dft_seq_re": _A_in["dft_seq_re"],
        "A_dft_seq_im": _A_in["dft_seq_im"],
        "A_dft_hid_re": _A_in["dft_hid_re"],
        "A_dft_hid_im": _A_in["dft_hid_im"],
        "B_x": _B_in["x"],
        "B_w_gate": _B_in["w_gate"],
        "B_w_up": _B_in["w_up"],
        "B_w_down": _B_in["w_down"],
    }

def reference(A_x, A_dft_seq_re, A_dft_seq_im, A_dft_hid_re, A_dft_hid_im, B_x, B_w_gate, B_w_up, B_w_down):
    def _A_ref(x, dft_seq_re, dft_seq_im, dft_hid_re, dft_hid_im):
        a = torch.einsum("bsd,ed->bse", x, dft_hid_re)
        b = torch.einsum("bsd,ed->bse", x, dft_hid_im)
        y = torch.einsum("ts,bse->bte", dft_seq_re, a) - torch.einsum("ts,bse->bte", dft_seq_im, b)
        return y

    def _B_ref(x, w_gate, w_up, w_down):
        g = _silu(x @ w_gate)
        u = x @ w_up
        return (g * u) @ w_down

    x = A_x
    h = x + _A_ref(_layernorm(x, axis=-1), A_dft_seq_re, A_dft_seq_im, A_dft_hid_re, A_dft_hid_im)
    h2 = h + _B_ref(_layernorm(h, axis=-1), B_w_gate, B_w_up, B_w_down)
    return h2
```

```cpp
#include <hip/hip_runtime.h>
#include <hip/hip_bf16.h>

namespace {
#define LAS __attribute__((address_space(3)))
typedef unsigned short u16;
typedef short s16x4 __attribute__((ext_vector_type(4)));
typedef short s16x8 __attribute__((ext_vector_type(8)));
typedef __bf16 bf16x8 __attribute__((ext_vector_type(8)));
typedef __bf16 bf16x2 __attribute__((ext_vector_type(2)));
typedef float f32x4 __attribute__((ext_vector_type(4)));
typedef float f32x2 __attribute__((ext_vector_type(2)));
typedef unsigned u32x4 __attribute__((ext_vector_type(4)));
typedef unsigned u32x2 __attribute__((ext_vector_type(2)));

constexpr int S_ = 2048, D_ = 1024, F_ = 3584;
constexpr float EPS = 1e-5f;

constexpr size_t WS_XN = 0;
constexpr size_t WS_AB = WS_XN + (size_t)S_ * D_ * 2;
constexpr size_t WS_H = WS_AB + (size_t)D_ * 2 * S_ * 2;
constexpr size_t WS_GU = WS_H + (size_t)S_ * D_ * 2;
constexpr size_t WS_ST = WS_GU + (size_t)S_ * F_ * 2;
constexpr size_t WS_CS = WS_ST + (size_t)S_ * 16 * 8;
constexpr int CS_SPLIT = 8;

__device__ __forceinline__ unsigned rfl(unsigned v) { return __builtin_amdgcn_readfirstlane(v); }
__device__ __forceinline__ unsigned pk_bf16(float lo, float hi) {
  bf16x2 c = {(__bf16)lo, (__bf16)hi};
  return __builtin_bit_cast(unsigned, c);
}
__device__ __forceinline__ float bf_lo(unsigned v) { return __uint_as_float(v << 16); }
__device__ __forceinline__ float bf_hi(unsigned v) { return __uint_as_float(v & 0xffff0000u); }

__device__ __forceinline__ u32x4 make_rsrc(const void* p, unsigned bytes) {
  unsigned long long a = (unsigned long long)p;
  u32x4 r;
  r.x = (unsigned)a;
  r.y = (unsigned)(a >> 32) & 0xffffu;
  r.z = bytes;
  r.w = 0x00020000u;
  return r;
}
__device__ __forceinline__ void dma16(unsigned ldsaddr, unsigned voff, u32x4 rsrc, unsigned soff) {
  asm volatile("s_mov_b32 m0, %0\n\ts_nop 0\n\tbuffer_load_dwordx4 %1, %2, %3 offen lds" ::"s"(ldsaddr), "v"(voff), "s"(rsrc), "s"(soff)
               : "memory");
}
template <int N>
__device__ __forceinline__ void wait_vm() {
  asm volatile("s_waitcnt vmcnt(%0)" ::"i"(N) : "memory");
}
__device__ __forceinline__ void wait_lgkm0() { __builtin_amdgcn_s_waitcnt(0xC07F); }
__device__ __forceinline__ void barrier() {
  asm volatile("" ::: "memory");
  __builtin_amdgcn_s_barrier();
  asm volatile("" ::: "memory");
}
__device__ __forceinline__ bf16x8 lds_b128(unsigned addr) { return *(const LAS bf16x8*)(addr); }
__device__ __forceinline__ bf16x8 lds_tr(unsigned a0, unsigned a1) {
  s16x4 lo = __builtin_amdgcn_ds_read_tr16_b64_v4i16((LAS s16x4*)(a0));
  s16x4 hi = __builtin_amdgcn_ds_read_tr16_b64_v4i16((LAS s16x4*)(a1));
  s16x8 f = __builtin_shufflevector(lo, hi, 0, 1, 2, 3, 4, 5, 6, 7);
  return __builtin_bit_cast(bf16x8, f);
}

__global__ __launch_bounds__(512) void k_prep(const u16* __restrict__ x, const u16* __restrict__ wg, const u16* __restrict__ wu,
                                              u16* __restrict__ xn, float* __restrict__ cs) {
  __shared__ float red[32 * 128];
  const int tid = threadIdx.x, lane = tid & 63, w = tid >> 6;
  if (blockIdx.x < 128) {
    const int r = lane & 15, g = lane >> 4;
    const int row = blockIdx.x * 16 + r;
    u32x4 v[4];
#pragma unroll
    for (int i = 0; i < 4; ++i) v[i] = *(const u32x4*)(x + (size_t)row * D_ + (w * 4 + i) * 32 + g * 8);
    float s = 0.f;
#pragma unroll
    for (int i = 0; i < 4; ++i)
#pragma unroll
      for (int j = 0; j < 4; ++j) s += bf_lo(v[i][j]) + bf_hi(v[i][j]);
    s += __shfl_xor(s, 16);
    s += __shfl_xor(s, 32);
    if (g == 0) red[w * 16 + r] = s;
    __syncthreads();
    float m = 0.f;
#pragma unroll
    for (int i = 0; i < 8; ++i) m += red[i * 16 + r];
    m *= (1.0f / D_);
    float q = 0.f;
#pragma unroll
    for (int i = 0; i < 4; ++i)
#pragma unroll
      for (int j = 0; j < 4; ++j) {
        float a = bf_lo(v[i][j]) - m, b = bf_hi(v[i][j]) - m;
        q += a * a + b * b;
      }
    q += __shfl_xor(q, 16);
    q += __shfl_xor(q, 32);
    if (g == 0) red[128 + w * 16 + r] = q;
    __syncthreads();
    float var = 0.f;
#pragma unroll
    for (int i = 0; i < 8; ++i) var += red[128 + i * 16 + r];
    const float rs = 1.0f / sqrtf(var * (1.0f / D_) + EPS);
#pragma unroll
    for (int i = 0; i < 4; ++i) {
      u32x4 o;
#pragma unroll
      for (int j = 0; j < 4; ++j) o[j] = pk_bf16((bf_lo(v[i][j]) - m) * rs, (bf_hi(v[i][j]) - m) * rs);
      *(u32x4*)(xn + ((size_t)blockIdx.x * (D_ / 32) + (w * 4 + i)) * 512 + lane * 8) = o;
    }
  } else {
    const int b = blockIdx.x - 128;
    const int cg = b % 56, ks = b / 56;
    const u16* src = (cg < 28) ? wg + cg * 128 : wu + (cg - 28) * 128;
    const int c = tid & 15, rg = tid >> 4;
    u32x4 v[4];
#pragma unroll
    for (int i = 0; i < 4; ++i) v[i] = *(const u32x4*)(src + (size_t)(ks * 128 + i * 32 + rg) * F_ + c * 8);
#pragma unroll
    for (int j = 0; j < 4; ++j) {
      red[rg * 128 + c * 8 + 2 * j] = (bf_lo(v[0][j]) + bf_lo(v[1][j])) + (bf_lo(v[2][j]) + bf_lo(v[3][j]));
      red[rg * 128 + c * 8 + 2 * j + 1] = (bf_hi(v[0][j]) + bf_hi(v[1][j])) + (bf_hi(v[2][j]) + bf_hi(v[3][j]));
    }
    __syncthreads();
    if (tid < 128) {
      float s = 0.f;
#pragma unroll
      for (int i = 0; i < 32; ++i) s += red[i * 128 + tid];
      cs[ks * (2 * F_) + cg * 128 + tid] = s;
    }
  }
}

enum { A_RM = 0, A_FT = 1 };
enum { B_FT = 0, B_NN = 1, B_GLU = 2 };

struct GArgs {
  const void* a0;
  const void* a1;
  const void* b0;
  const void* b1;
  void* out;
  const void* res;
  void* stats;
  const void* cs;
};

template <int ID> struct Cfg;
template <> struct Cfg<1> { static constexpr int BM = 128, BN = 128, BK = 64, K = 1024, NWM = 2, NWN = 2, NWK = 2, NST = 4, AM = A_RM, BMODE = B_FT, TM = 16, TN = 16, XM = 4; };
template <> struct Cfg<2> { static constexpr int BM = 128, BN = 64, BK = 64, K = 4096, NWM = 2, NWN = 2, NWK = 2, NST = 6, AM = A_RM, BMODE = B_FT, TM = 16, TN = 16, XM = 4; };
template <> struct Cfg<3> { static constexpr int BM = 256, BN = 224, BK = 32, K = 1024, NWM = 4, NWN = 2, NWK = 1, NST = 5, AM = A_FT, BMODE = B_GLU, TM = 8, TN = 32, XM = 8; };
template <> struct Cfg<4> { static constexpr int BM = 128, BN = 64, BK = 64, K = 3584, NWM = 2, NWN = 2, NWK = 2, NST = 6, AM = A_FT, BMODE = B_NN, TM = 16, TN = 16, XM = 4; };

template <int ID> constexpr int lds_bytes() {
  using C = Cfg<ID>;
  return C::NST * ((C::BM + C::BN) * C::BK * 2 + (C::BMODE == B_GLU ? 16 : 0)) + 4096;
}

template <int ID>
__global__ __launch_bounds__(512, 2) void k_gemm(GArgs p) {
  using C = Cfg<ID>;
  constexpr int BM = C::BM, BN = C::BN, BK = C::BK, K = C::K, NWM = C::NWM, NWN = C::NWN, NWK = C::NWK, NST = C::NST;
  constexpr int NW = NWM * NWN * NWK;
  static_assert(NW == 8, "8 waves");
  constexpr int WM = BM / NWM, WN = BN / NWN, MB = WM / 16, NB = WN / 16;
  static_assert(BK / 32 == NWK, "one k32 step per wave and stage");
  constexpr int A_BYTES = BM * BK * 2, B_BYTES = BN * BK * 2, ST_BYTES = A_BYTES + B_BYTES + (C::BMODE == B_GLU ? 16 : 0);
  constexpr int UP_OFF = 7168 + 16;
  constexpr int LA = A_BYTES / 1024 / NW, LB = (B_BYTES / 1024 + NW - 1) / NW, LPS = LA + LB;
  constexpr int NT = K / BK;
  constexpr int EXTRA = NST * ST_BYTES;
  extern __shared__ __attribute__((aligned(16))) char shm[];
  const unsigned lds0 = (unsigned)(size_t)(LAS char*)shm;

  const int tid = threadIdx.x, lane = tid & 63;
  const unsigned w = rfl(tid >> 6);
  const unsigned wk = w / (NWM * NWN), wmn = w % (NWM * NWN), wm = wmn / NWN, wn = wmn % NWN;
  const int fr = lane & 15, fq = lane >> 4;

  constexpr int XN = 32 / C::XM, PN = C::TN / XN;
  const unsigned xcd = blockIdx.x & 7, idx = blockIdx.x >> 3;
  const unsigned tm = (xcd / PN) * C::XM + idx % C::XM, tn = (xcd % PN) * XN + idx / C::XM;

  u32x4 ra, ra2, rb, rb2;
  unsigned voffA[LA], voffB[LB], ldsB[LB];
  unsigned kstepA, kstepB;
  if constexpr (C::AM == A_RM) {
    constexpr int LD = (ID == 1) ? D_ : S_;
    if constexpr (ID == 1) {
      const u16* base = (const u16*)(tm < 8 ? p.a0 : p.a1) + (size_t)(tm & 7) * BM * LD;
      ra = make_rsrc(base, BM * LD * 2);
      ra2 = ra;
    } else {
      ra = make_rsrc((const u16*)p.a0 + (size_t)tm * BM * LD, BM * LD * 2);
      ra2 = make_rsrc((const u16*)p.a1 + (size_t)tm * BM * LD, BM * LD * 2);
    }
#pragma unroll
    for (int i = 0; i < LA; ++i) {
      const int row = (w + i * NW) * 8 + (lane >> 3);
      voffA[i] = row * LD * 2 + (((lane & 7) ^ ((row >> 1) & 7)) << 4);
    }
    kstepA = BK * 2;
  } else {
    ra = make_rsrc((const u16*)p.a0 + (size_t)tm * BM * K, BM * K * 2);
    ra2 = ra;
#pragma unroll
    for (int i = 0; i < LA; ++i) {
      const int pc = w + i * NW;
      voffA[i] = ((pc / NWK) * (K / 32) + (pc % NWK)) * 1024 + lane * 16;
    }
    kstepA = (BK / 32) * 1024;
  }
  if constexpr (C::BMODE == B_FT) {
    rb = make_rsrc((const u16*)p.b0 + (size_t)tn * BN * K, BN * K * 2);
    rb2 = rb;
#pragma unroll
    for (int i = 0; i < LB; ++i) {
      const int pc = w + i * NW;
      voffB[i] = ((pc / NWK) * (K / 32) + (pc % NWK)) * 1024 + lane * 16;
      ldsB[i] = A_BYTES + pc * 1024;
    }
    kstepB = (BK / 32) * 1024;
  } else if constexpr (C::BMODE == B_NN) {
    rb = make_rsrc(p.b0, (unsigned)K * D_ * 2);
    rb2 = rb;
#pragma unroll
    for (int i = 0; i < LB; ++i) {
      const int k = (w + i * NW) * 8 + (lane >> 3);
      const int sw = ((k >> 1) & 1) | (((k >> 3) & 1) << 1);
      voffB[i] = k * D_ * 2 + tn * BN * 2 + (((lane & 7) ^ (sw << 1)) << 4);
      ldsB[i] = A_BYTES + (w + i * NW) * 1024;
    }
    kstepB = BK * D_ * 2;
  } else {
    rb = make_rsrc(p.b0, (unsigned)D_ * F_ * 2);
    rb2 = make_rsrc(p.b1, (unsigned)D_ * F_ * 2);
    {
      const int c = w * 64 + lane;
      const int r = c / 14, pos = c % 14;
      const int k = (r & 0x13) | ((r & 4) << 1) | ((r & 8) >> 1);
#pragma unroll
      for (int i = 0; i < LB; ++i) {
        voffB[i] = (w < 7) ? (unsigned)(k * F_ * 2 + tn * 224 + pos * 16) : 0x80000000u;
        ldsB[i] = A_BYTES + i * UP_OFF + w * 1024;
      }
    }
    kstepB = BK * F_ * 2;
  }

  auto issue = [&](int t) {
    const unsigned buf = lds0 + (unsigned)(t % NST) * ST_BYTES;
    u32x4 qa = ra, qb = rb, qb2 = rb2;
    unsigned sa = (unsigned)t * kstepA;
    if constexpr (ID == 2) {
      if (t >= NT / 2) { qa = ra2; sa = (unsigned)(t - NT / 2) * kstepA; }
    }
    if (t >= NT) { qa.z = 0; qb.z = 0; qb2.z = 0; }
    const unsigned sb = (unsigned)t * kstepB;
#pragma unroll
    for (int i = 0; i < LA; ++i) dma16(buf + (w + i * NW) * 1024, voffA[i], qa, sa);
#pragma unroll
    for (int i = 0; i < LB; ++i) {
      if constexpr (C::BMODE == B_GLU) {
        const unsigned dst = (w < 7) ? buf + ldsB[i] : lds0 + EXTRA;
        dma16(dst, voffB[i], i == 0 ? qb : qb2, sb);
      } else {
        dma16(buf + ldsB[i], voffB[i], qb, sb);
      }
    }
  };

  unsigned rdA, rdB[4];
  if constexpr (C::AM == A_RM) rdA = (wm * WM + fr) * 128 + (((wk * 4 + fq) ^ (fr >> 1)) << 4);
  else rdA = ((wm * MB) * NWK + wk) * 1024 + lane * 16;
  constexpr int A_MB_STRIDE = (C::AM == A_RM) ? 2048 : NWK * 1024;
  if constexpr (C::BMODE == B_FT) {
    rdB[0] = A_BYTES + ((wn * NB) * NWK + wk) * 1024 + lane * 16;
  } else if constexpr (C::BMODE == B_NN) {
    const int q = fr >> 2, pp = fr & 3;
    const int sw = ((q >> 1) & 1) | ((fq & 1) << 1);
#pragma unroll
    for (int nb = 0; nb < NB; ++nb)
      rdB[nb] = A_BYTES + (wk * 32 + 8 * fq + q) * 128 + ((((wn * NB + nb) ^ sw) * 2 + (pp >> 1)) << 4) + (pp & 1) * 8;
  } else {
    const int q = fr >> 2, pp = fr & 3;
    const int r = (fq >> 1) * 16 + (fq & 1) * 4 + q;
    rdB[0] = A_BYTES + r * 224 + wn * 112 + pp * 8;
    rdB[1] = A_BYTES + r * 224 + wn * 112 + 96 + (pp & 1) * 8 + (pp >> 1) * UP_OFF;
  }

  bf16x8 fa[2][MB], fb[2][NB];
  auto read_frags = [&](int t, bf16x8(&a)[MB], bf16x8(&b)[NB]) {
    const unsigned buf = lds0 + (unsigned)(t % NST) * ST_BYTES;
    if constexpr (C::BMODE == B_FT) {
#pragma unroll
      for (int nb = 0; nb < NB; ++nb) b[nb] = lds_b128(buf + rdB[0] + nb * NWK * 1024);
    } else if constexpr (C::BMODE == B_NN) {
#pragma unroll
      for (int nb = 0; nb < NB; ++nb) b[nb] = lds_tr(buf + rdB[nb], buf + rdB[nb] + 512);
    } else {
#pragma unroll
      for (int nb = 0; nb < 6; ++nb) {
        const unsigned o = buf + rdB[0] + (nb % 3) * 32 + (nb / 3) * UP_OFF;
        b[nb] = lds_tr(o, o + 1792);
      }
      b[6] = lds_tr(buf + rdB[1], buf + rdB[1] + 1792);
    }
#pragma unroll
    for (int mb = 0; mb < MB; ++mb) a[mb] = lds_b128(buf + rdA + mb * A_MB_STRIDE);
  };

  f32x4 acc[MB][NB];
#pragma unroll
  for (int mb = 0; mb < MB; ++mb)
#pragma unroll
    for (int nb = 0; nb < NB; ++nb) acc[mb][nb] = (f32x4){0.f, 0.f, 0.f, 0.f};
  auto mma = [&](const bf16x8(&a)[MB], const bf16x8(&b)[NB]) {
#pragma unroll
    for (int mb = 0; mb < MB; ++mb)
#pragma unroll
      for (int nb = 0; nb < NB; ++nb) acc[mb][nb] = __builtin_amdgcn_mfma_f32_16x16x32_bf16(b[nb], a[mb], acc[mb][nb], 0, 0, 0);
  };

#pragma unroll
  for (int t = 0; t < NST; ++t) issue(t);
  wait_vm<(NST - 1) * LPS>();
  barrier();
  read_frags(0, fa[0], fb[0]);
  for (int t = 0; t < NT; t += 2) {
    wait_vm<(NST - 2) * LPS>();
    wait_lgkm0();
    barrier();
    issue(t + NST);
    read_frags(t + 1, fa[1], fb[1]);
    mma(fa[0], fb[0]);
    wait_vm<(NST - 2) * LPS>();
    wait_lgkm0();
    barrier();
    issue(t + 1 + NST);
    read_frags(t + 2, fa[0], fb[0]);
    mma(fa[1], fb[1]);
  }
  wait_vm<0>();
  wait_lgkm0();
  barrier();

  if constexpr (NWK == 2) {
    const unsigned rbase = lds0 + wmn * (MB * NB * 1024) + lane * 16;
    if (wk == 1) {
#pragma unroll
      for (int mb = 0; mb < MB; ++mb)
#pragma unroll
        for (int nb = 0; nb < NB; ++nb) *(LAS f32x4*)(rbase + (mb * NB + nb) * 1024) = acc[mb][nb];
    }
    wait_lgkm0();
    barrier();
    if (wk == 1) return;
#pragma unroll
    for (int mb = 0; mb < MB; ++mb)
#pragma unroll
      for (int nb = 0; nb < NB; ++nb) acc[mb][nb] += *(const LAS f32x4*)(rbase + (mb * NB + nb) * 1024);
  }

  const int chunk = ((fq & 1) << 1) | (fq >> 1);

  if constexpr (ID == 1) {
    const float sg = tm < 8 ? 1.f : -1.f;
    u16* o = (u16*)p.out;
#pragma unroll
    for (int mb = 0; mb < MB; ++mb)
#pragma unroll
      for (int np = 0; np < NB / 2; ++np) {
        const f32x4 x0 = acc[mb][2 * np] * sg, x1 = acc[mb][2 * np + 1] * sg;
        auto s0 = __builtin_amdgcn_permlane16_swap(pk_bf16(x0[0], x0[1]), pk_bf16(x1[0], x1[1]), false, false);
        auto s1 = __builtin_amdgcn_permlane16_swap(pk_bf16(x0[2], x0[3]), pk_bf16(x1[2], x1[3]), false, false);
        const u32x4 v = {s0[0], s1[0], s0[1], s1[1]};
        const int rblk = (tm & 7) * 8 + wm * MB + mb;
        const int kblk = (tm >> 3) * 64 + tn * 4 + wn * (NB / 2) + np;
        *(u32x4*)(o + ((size_t)rblk * 128 + kblk) * 512 + (chunk * 16 + fr) * 8) = v;
      }
  } else if constexpr (ID == 2 || ID == 4) {
    static_assert(ID == 1 || ID == 3 || NB == 2, "epilogue assumes 32 columns per wave");
    LAS f32x2* tab = (LAS f32x2*)(lds0 + EXTRA + 1024);
#pragma unroll
    for (int mb = 0; mb < MB; ++mb) {
      const int row = tm * BM + wm * WM + mb * 16 + fr;
      const int col = tn * BN + wn * WN + chunk * 8;
      const size_t ft = ((size_t)(row >> 4) * (D_ / 32) + (col >> 5)) * 512 + (chunk * 16 + fr) * 8;
      float y[8];
#pragma unroll
      for (int j = 0; j < 4; ++j) {
        auto s = __builtin_amdgcn_permlane16_swap(__float_as_uint(acc[mb][0][j]), __float_as_uint(acc[mb][1][j]), false, false);
        y[j] = __uint_as_float(s[0]);
        y[4 + j] = __uint_as_float(s[1]);
      }
      u32x4 r;
      if constexpr (ID == 2) r = *(const u32x4*)((const u16*)p.res + (size_t)row * D_ + col);
      else r = *(const u32x4*)((const u16*)p.res + ft);
      u32x4 v;
#pragma unroll
      for (int j = 0; j < 4; ++j) v[j] = pk_bf16(bf_lo(r[j]) + y[2 * j], bf_hi(r[j]) + y[2 * j + 1]);
      if constexpr (ID == 2) {
        *(u32x4*)((u16*)p.out + ft) = v;
        float s = 0.f;
#pragma unroll
        for (int j = 0; j < 4; ++j) s += bf_lo(v[j]) + bf_hi(v[j]);
        s += __shfl_xor(s, 16);
        s += __shfl_xor(s, 32);
        const float m = s * (1.0f / 32.0f);
        float q = 0.f;
#pragma unroll
        for (int j = 0; j < 4; ++j) {
          const float a = bf_lo(v[j]) - m, b = bf_hi(v[j]) - m;
          q += a * a + b * b;
        }
        q += __shfl_xor(q, 16);
        q += __shfl_xor(q, 32);
        if (fq == 0) tab[(wm * WM + mb * 16 + fr) * 2 + wn] = (f32x2){m, q};
      } else {
        *(u32x4*)((u16*)p.out + (size_t)row * D_ + col) = v;
      }
    }
    if constexpr (ID == 2) {
      wait_lgkm0();
      barrier();
      if (tid < BM) {
        const f32x2 a = tab[tid * 2], b = tab[tid * 2 + 1];
        const float d = a.x - b.x;
        ((f32x2*)p.stats)[(size_t)(tm * BM + tid) * 16 + tn] = (f32x2){0.5f * (a.x + b.x), a.y + b.y + 16.0f * d * d};
      }
    }
  } else {
    LAS f32x2* tab = (LAS f32x2*)(lds0);
    LAS float* cst = (LAS float*)(lds0 + 2048);
    if (tid < 256) {
      const f32x4* sp = (const f32x4*)p.stats + (size_t)(tm * BM + tid) * 8;
      f32x4 sv[8];
#pragma unroll
      for (int i = 0; i < 8; ++i) sv[i] = sp[i];
      float m = 0.f;
#pragma unroll
      for (int i = 0; i < 8; ++i) m += sv[i][0] + sv[i][2];
      m *= (1.0f / 16.0f);
      float q = 0.f;
#pragma unroll
      for (int i = 0; i < 8; ++i) {
        const float d0 = sv[i][0] - m, d1 = sv[i][2] - m;
        q += sv[i][1] + sv[i][3] + 64.0f * (d0 * d0 + d1 * d1);
      }
      tab[tid] = (f32x2){m, 1.0f / sqrtf(q * (1.0f / D_) + EPS)};
    } else if (tid < 256 + 224) {
      const int c = tid - 256;
      const int col = (c < 112) ? tn * 112 + c : F_ + tn * 112 + (c - 112);
      float s = 0.f;
#pragma unroll
      for (int i = 0; i < CS_SPLIT; ++i) s += ((const float*)p.cs)[i * (2 * F_) + col];
      cst[c] = s;
    }
    wait_lgkm0();
    barrier();
    auto act = [](float g, float u) { return g * u * __builtin_amdgcn_rcpf(1.0f + __expf(-g)); };
    u16* o = (u16*)p.out;
    f32x2 st[MB];
#pragma unroll
    for (int mb = 0; mb < MB; ++mb) st[mb] = tab[wm * WM + mb * 16 + fr];
    const int gch0 = tn * 14 + wn * 7;
#pragma unroll
    for (int i = 0; i < 3; ++i) {
      const f32x4 cg = *(const LAS f32x4*)(cst + wn * 56 + i * 16 + fq * 4), cu = *(const LAS f32x4*)(cst + 112 + wn * 56 + i * 16 + fq * 4);
      u32x2 pkd[MB];
#pragma unroll
      for (int mb = 0; mb < MB; ++mb) {
        const f32x4 g = (acc[mb][i] - cg * st[mb].x) * st[mb].y, u = (acc[mb][3 + i] - cu * st[mb].x) * st[mb].y;
        pkd[mb] = (u32x2){pk_bf16(act(g[0], u[0]), act(g[1], u[1])), pk_bf16(act(g[2], u[2]), act(g[3], u[3]))};
      }
#pragma unroll
      for (int mp = 0; mp < MB / 2; ++mp) {
        auto s0 = __builtin_amdgcn_permlane16_swap(pkd[2 * mp].x, pkd[2 * mp + 1].x, false, false);
        auto s1 = __builtin_amdgcn_permlane16_swap(pkd[2 * mp].y, pkd[2 * mp + 1].y, false, false);
        const u32x4 v = {s0[0], s1[0], s0[1], s1[1]};
        const int rblk = tm * (BM / 16) + wm * MB + 2 * mp + (fq & 1);
        const int gch = gch0 + 2 * i + (fq >> 1);
        *(u32x4*)(o + ((size_t)rblk * (F_ / 32) + (gch >> 2)) * 512 + ((gch & 3) * 16 + fr) * 8) = v;
      }
    }
    {
      const f32x4 cm = *(const LAS f32x4*)(cst + (fq >> 1) * 112 + wn * 56 + 48 + (fq & 1) * 4);
      u32x2 pkd[MB / 2];
#pragma unroll
      for (int mp = 0; mp < MB / 2; ++mp) {
        const f32x4 v0 = (acc[2 * mp][6] - cm * st[2 * mp].x) * st[2 * mp].y, v1 = (acc[2 * mp + 1][6] - cm * st[2 * mp + 1].x) * st[2 * mp + 1].y;
        float r[4];
#pragma unroll
        for (int j = 0; j < 4; ++j) {
          auto s = __builtin_amdgcn_permlane32_swap(__float_as_uint(v0[j]), __float_as_uint(v1[j]), false, false);
          r[j] = act(__uint_as_float(s[0]), __uint_as_float(s[1]));
        }
        pkd[mp] = (u32x2){pk_bf16(r[0], r[1]), pk_bf16(r[2], r[3])};
      }
      auto s0 = __builtin_amdgcn_permlane16_swap(pkd[0].x, pkd[1].x, false, false);
      auto s1 = __builtin_amdgcn_permlane16_swap(pkd[0].y, pkd[1].y, false, false);
      const u32x4 v = {s0[0], s1[0], s0[1], s1[1]};
      const int rblk = tm * (BM / 16) + wm * MB + (fq & 1) * 2 + (fq >> 1);
      const int gch = gch0 + 6;
      *(u32x4*)(o + ((size_t)rblk * (F_ / 32) + (gch >> 2)) * 512 + ((gch & 3) * 16 + fr) * 8) = v;
    }
  }
}

template <int ID>
void launch_gemm(const GArgs& a, hipStream_t stream) {
  (void)hipFuncSetAttribute((const void*)k_gemm<ID>, hipFuncAttributeMaxDynamicSharedMemorySize, lds_bytes<ID>());
  hipLaunchKernelGGL(k_gemm<ID>, dim3(256), dim3(512), lds_bytes<ID>(), stream, a);
}
}

extern "C" void kernel_launch(void* const* d_in, const int* in_sizes, int n_in, void* d_out, int out_size, void* d_ws, size_t ws_size,
                              hipStream_t stream) {
  const u16* x = (const u16*)d_in[0];
  char* ws = (char*)d_ws;
  hipLaunchKernelGGL(k_prep, dim3(128 + 448), dim3(512), 0, stream, x, (const u16*)d_in[6], (const u16*)d_in[7], (u16*)(ws + WS_XN),
                     (float*)(ws + WS_CS));
  GArgs g1 = {d_in[3], d_in[4], ws + WS_XN, nullptr, ws + WS_AB, nullptr, nullptr, nullptr};
  launch_gemm<1>(g1, stream);
  GArgs g2 = {d_in[1], d_in[2], ws + WS_AB, nullptr, ws + WS_H, x, ws + WS_ST, nullptr};
  launch_gemm<2>(g2, stream);
  GArgs g3 = {ws + WS_H, nullptr, d_in[6], d_in[7], ws + WS_GU, nullptr, ws + WS_ST, ws + WS_CS};
  launch_gemm<3>(g3, stream);
  GArgs g4 = {ws + WS_GU, nullptr, d_in[8], nullptr, d_out, ws + WS_H, nullptr, nullptr};
  launch_gemm<4>(g4, stream);
}
```

```cpp
#include <hip/hip_runtime.h>
#include <hip/hip_bf16.h>

namespace {
#define LAS __attribute__((address_space(3)))
typedef unsigned short u16;
typedef short s16x4 __attribute__((ext_vector_type(4)));
typedef short s16x8 __attribute__((ext_vector_type(8)));
typedef __bf16 bf16x8 __attribute__((ext_vector_type(8)));
typedef __bf16 bf16x2 __attribute__((ext_vector_type(2)));
typedef float f32x4 __attribute__((ext_vector_type(4)));
typedef float f32x2 __attribute__((ext_vector_type(2)));
typedef unsigned u32x4 __attribute__((ext_vector_type(4)));
typedef unsigned u32x2 __attribute__((ext_vector_type(2)));

constexpr int S_ = 2048, D_ = 1024, F_ = 3584;
constexpr float EPS = 1e-5f;

constexpr size_t WS_XN = 0;
constexpr size_t WS_AB = WS_XN + (size_t)S_ * D_ * 2;
constexpr size_t WS_H = WS_AB + (size_t)D_ * 2 * S_ * 2;
constexpr size_t WS_GU = WS_H + (size_t)S_ * D_ * 2;
constexpr size_t WS_ST = WS_GU + (size_t)S_ * F_ * 2;
constexpr size_t WS_CS = WS_ST + (size_t)S_ * 16 * 8;
constexpr int CS_SPLIT = 8;

__device__ __forceinline__ unsigned rfl(unsigned v) { return __builtin_amdgcn_readfirstlane(v); }
__device__ __forceinline__ unsigned pk_bf16(float lo, float hi) {
  bf16x2 c = {(__bf16)lo, (__bf16)hi};
  return __builtin_bit_cast(unsigned, c);
}
__device__ __forceinline__ float bf_lo(unsigned v) { return __uint_as_float(v << 16); }
__device__ __forceinline__ float bf_hi(unsigned v) { return __uint_as_float(v & 0xffff0000u); }

__device__ __forceinline__ u32x4 make_rsrc(const void* p, unsigned bytes) {
  unsigned long long a = (unsigned long long)p;
  u32x4 r;
  r.x = (unsigned)a;
  r.y = (unsigned)(a >> 32) & 0xffffu;
  r.z = bytes;
  r.w = 0x00020000u;
  return r;
}
__device__ __forceinline__ void dma16(unsigned ldsaddr, unsigned voff, u32x4 rsrc, unsigned soff) {
  asm volatile("s_mov_b32 m0, %0\n\ts_nop 0\n\tbuffer_load_dwordx4 %1, %2, %3 offen lds" ::"s"(ldsaddr), "v"(voff), "s"(rsrc), "s"(soff)
               : "memory");
}
template <int N>
__device__ __forceinline__ void wait_vm() {
  asm volatile("s_waitcnt vmcnt(%0)" ::"i"(N) : "memory");
}
__device__ __forceinline__ void wait_lgkm0() { __builtin_amdgcn_s_waitcnt(0xC07F); }
__device__ __forceinline__ void barrier() {
  asm volatile("" ::: "memory");
  __builtin_amdgcn_s_barrier();
  asm volatile("" ::: "memory");
}
__device__ __forceinline__ bf16x8 lds_b128(unsigned addr) { return *(const LAS bf16x8*)(addr); }
__device__ __forceinline__ bf16x8 lds_tr(unsigned a0, unsigned a1) {
  s16x4 lo = __builtin_amdgcn_ds_read_tr16_b64_v4i16((LAS s16x4*)(a0));
  s16x4 hi = __builtin_amdgcn_ds_read_tr16_b64_v4i16((LAS s16x4*)(a1));
  s16x8 f = __builtin_shufflevector(lo, hi, 0, 1, 2, 3, 4, 5, 6, 7);
  return __builtin_bit_cast(bf16x8, f);
}

__global__ __launch_bounds__(512) void k_prep(const u16* __restrict__ x, const u16* __restrict__ wg, const u16* __restrict__ wu,
                                              u16* __restrict__ xn, float* __restrict__ cs) {
  __shared__ float red[32 * 128];
  const int tid = threadIdx.x, lane = tid & 63, w = tid >> 6;
  if (blockIdx.x < 128) {
    const int r = lane & 15, g = lane >> 4;
    const int row = blockIdx.x * 16 + r;
    u32x4 v[4];
#pragma unroll
    for (int i = 0; i < 4; ++i) v[i] = *(const u32x4*)(x + (size_t)row * D_ + (w * 4 + i) * 32 + g * 8);
    float s = 0.f;
#pragma unroll
    for (int i = 0; i < 4; ++i)
#pragma unroll
      for (int j = 0; j < 4; ++j) s += bf_lo(v[i][j]) + bf_hi(v[i][j]);
    s += __shfl_xor(s, 16);
    s += __shfl_xor(s, 32);
    if (g == 0) red[w * 16 + r] = s;
    __syncthreads();
    float m = 0.f;
#pragma unroll
    for (int i = 0; i < 8; ++i) m += red[i * 16 + r];
    m *= (1.0f / D_);
    float q = 0.f;
#pragma unroll
    for (int i = 0; i < 4; ++i)
#pragma unroll
      for (int j = 0; j < 4; ++j) {
        float a = bf_lo(v[i][j]) - m, b = bf_hi(v[i][j]) - m;
        q += a * a + b * b;
      }
    q += __shfl_xor(q, 16);
    q += __shfl_xor(q, 32);
    if (g == 0) red[128 + w * 16 + r] = q;
    __syncthreads();
    float var = 0.f;
#pragma unroll
    for (int i = 0; i < 8; ++i) var += red[128 + i * 16 + r];
    const float rs = 1.0f / sqrtf(var * (1.0f / D_) + EPS);
#pragma unroll
    for (int i = 0; i < 4; ++i) {
      u32x4 o;
#pragma unroll
      for (int j = 0; j < 4; ++j) o[j] = pk_bf16((bf_lo(v[i][j]) - m) * rs, (bf_hi(v[i][j]) - m) * rs);
      *(u32x4*)(xn + ((size_t)blockIdx.x * (D_ / 32) + (w * 4 + i)) * 512 + lane * 8) = o;
    }
  } else {
    const int b = blockIdx.x - 128;
    const int cg = b % 56, ks = b / 56;
    const u16* src = (cg < 28) ? wg + cg * 128 : wu + (cg - 28) * 128;
    const int c = tid & 15, rg = tid >> 4;
    u32x4 v[4];
#pragma unroll
    for (int i = 0; i < 4; ++i) v[i] = *(const u32x4*)(src + (size_t)(ks * 128 + i * 32 + rg) * F_ + c * 8);
#pragma unroll
    for (int j = 0; j < 4; ++j) {
      red[rg * 128 + c * 8 + 2 * j] = (bf_lo(v[0][j]) + bf_lo(v[1][j])) + (bf_lo(v[2][j]) + bf_lo(v[3][j]));
      red[rg * 128 + c * 8 + 2 * j + 1] = (bf_hi(v[0][j]) + bf_hi(v[1][j])) + (bf_hi(v[2][j]) + bf_hi(v[3][j]));
    }
    __syncthreads();
    if (tid < 128) {
      float s = 0.f;
#pragma unroll
      for (int i = 0; i < 32; ++i) s += red[i * 128 + tid];
      cs[ks * (2 * F_) + cg * 128 + tid] = s;
    }
  }
}

enum { A_RM = 0, A_FT = 1 };
enum { B_FT = 0, B_NN = 1, B_GLU = 2 };

#define STAMP(k, i)
struct GArgs {
  const void* a0;
  const void* a1;
  const void* b0;
  const void* b1;
  void* out;
  const void* res;
  void* stats;
  const void* cs;
};

template <int ID> struct Cfg;
template <> struct Cfg<1> { static constexpr int BM = 128, BN = 128, BK = 64, K = 1024, NWM = 2, NWN = 2, NWK = 2, NST = 4, AM = A_RM, BMODE = B_FT, TM = 16, TN = 16, XM = 4; };
template <> struct Cfg<2> { static constexpr int BM = 128, BN = 64, BK = 64, K = 4096, NWM = 2, NWN = 2, NWK = 2, NST = 6, AM = A_RM, BMODE = B_FT, TM = 16, TN = 16, XM = 4; };
template <> struct Cfg<3> { static constexpr int BM = 256, BN = 224, BK = 32, K = 1024, NWM = 4, NWN = 2, NWK = 1, NST = 5, AM = A_FT, BMODE = B_GLU, TM = 8, TN = 32, XM = 8; };
template <> struct Cfg<4> { static constexpr int BM = 128, BN = 64, BK = 64, K = 3584, NWM = 2, NWN = 2, NWK = 2, NST = 6, AM = A_FT, BMODE = B_NN, TM = 16, TN = 16, XM = 4; };

template <int ID> constexpr int lds_bytes() {
  using C = Cfg<ID>;
  return C::NST * ((C::BM + C::BN) * C::BK * 2 + (C::BMODE == B_GLU ? 16 : 0)) + 4096;
}

template <int ID>
__global__ __launch_bounds__(512, 2) void k_gemm(GArgs p) {
  using C = Cfg<ID>;
  constexpr int BM = C::BM, BN = C::BN, BK = C::BK, K = C::K, NWM = C::NWM, NWN = C::NWN, NWK = C::NWK, NST = C::NST;
  constexpr int NW = NWM * NWN * NWK;
  static_assert(NW == 8, "8 waves");
  constexpr int WM = BM / NWM, WN = BN / NWN, MB = WM / 16, NB = WN / 16;
  static_assert(BK / 32 == NWK, "one k32 step per wave and stage");
  constexpr int A_BYTES = BM * BK * 2, B_BYTES = BN * BK * 2, ST_BYTES = A_BYTES + B_BYTES + (C::BMODE == B_GLU ? 16 : 0);
  constexpr int UP_OFF = 7168 + 16;
  constexpr int LA = A_BYTES / 1024 / NW, LB = (B_BYTES / 1024 + NW - 1) / NW, LPS = LA + LB;
  constexpr int NT = K / BK;
  constexpr int EXTRA = NST * ST_BYTES;
  extern __shared__ __attribute__((aligned(16))) char shm[];
  const unsigned lds0 = (unsigned)(size_t)(LAS char*)shm;

  STAMP(ID, 0);
  const int tid = threadIdx.x, lane = tid & 63;
  const unsigned w = rfl(tid >> 6);
  const unsigned wk = w / (NWM * NWN), wmn = w % (NWM * NWN), wm = wmn / NWN, wn = wmn % NWN;
  const int fr = lane & 15, fq = lane >> 4;

  constexpr int XN = 32 / C::XM, PN = C::TN / XN;
  const unsigned xcd = blockIdx.x & 7, idx = blockIdx.x >> 3;
  const unsigned tm = (xcd / PN) * C::XM + idx % C::XM, tn = (xcd % PN) * XN + idx / C::XM;

  u32x4 ra, ra2, rb, rb2;
  unsigned voffA[LA], voffB[LB], ldsB[LB];
  unsigned kstepA, kstepB;
  if constexpr (C::AM == A_RM) {
    constexpr int LD = (ID == 1) ? D_ : S_;
    if constexpr (ID == 1) {
      const u16* base = (const u16*)(tm < 8 ? p.a0 : p.a1) + (size_t)(tm & 7) * BM * LD;
      ra = make_rsrc(base, BM * LD * 2);
      ra2 = ra;
    } else {
      ra = make_rsrc((const u16*)p.a0 + (size_t)tm * BM * LD, BM * LD * 2);
      ra2 = make_rsrc((const u16*)p.a1 + (size_t)tm * BM * LD, BM * LD * 2);
    }
#pragma unroll
    for (int i = 0; i < LA; ++i) {
      const int row = (w + i * NW) * 8 + (lane >> 3);
      voffA[i] = row * LD * 2 + (((lane & 7) ^ ((row >> 1) & 7)) << 4);
    }
    kstepA = BK * 2;
  } else {
    ra = make_rsrc((const u16*)p.a0 + (size_t)tm * BM * K, BM * K * 2);
    ra2 = ra;
#pragma unroll
    for (int i = 0; i < LA; ++i) {
      const int pc = w + i * NW;
      voffA[i] = ((pc / NWK) * (K / 32) + (pc % NWK)) * 1024 + lane * 16;
    }
    kstepA = (BK / 32) * 1024;
  }
  if constexpr (C::BMODE == B_FT) {
    rb = make_rsrc((const u16*)p.b0 + (size_t)tn * BN * K, BN * K * 2);
    rb2 = rb;
#pragma unroll
    for (int i = 0; i < LB; ++i) {
      const int pc = w + i * NW;
      voffB[i] = ((pc / NWK) * (K / 32) + (pc % NWK)) * 1024 + lane * 16;
      ldsB[i] = A_BYTES + pc * 1024;
    }
    kstepB = (BK / 32) * 1024;
  } else if constexpr (C::BMODE == B_NN) {
    rb = make_rsrc(p.b0, (unsigned)K * D_ * 2);
    rb2 = rb;
#pragma unroll
    for (int i = 0; i < LB; ++i) {
      const int k = (w + i * NW) * 8 + (lane >> 3);
      const int sw = ((k >> 1) & 1) | (((k >> 3) & 1) << 1);
      voffB[i] = k * D_ * 2 + tn * BN * 2 + (((lane & 7) ^ (sw << 1)) << 4);
      ldsB[i] = A_BYTES + (w + i * NW) * 1024;
    }
    kstepB = BK * D_ * 2;
  } else {
    rb = make_rsrc(p.b0, (unsigned)D_ * F_ * 2);
    rb2 = make_rsrc(p.b1, (unsigned)D_ * F_ * 2);
    {
      const int c = w * 64 + lane;
      const int r = c / 14, pos = c % 14;
      const int k = (r & 0x13) | ((r & 4) << 1) | ((r & 8) >> 1);
#pragma unroll
      for (int i = 0; i < LB; ++i) {
        voffB[i] = (w < 7) ? (unsigned)(k * F_ * 2 + tn * 224 + pos * 16) : 0x80000000u;
        ldsB[i] = A_BYTES + i * UP_OFF + w * 1024;
      }
    }
    kstepB = BK * F_ * 2;
  }

  unsigned wbuf = lds0, rbuf = lds0, sa = 0, sb = 0;
  u32x4 qa = ra, qb = rb, qb2 = rb2;
  auto piece = [&](int i) {
    if (i < LA) dma16(wbuf + (w + i * NW) * 1024, voffA[i], qa, sa);
    else {
      const int j = i - LA;
      if constexpr (C::BMODE == B_GLU) dma16((w < 7) ? wbuf + ldsB[j] : lds0 + EXTRA, voffB[j], j == 0 ? qb : qb2, sb);
      else dma16(wbuf + ldsB[j], voffB[j], qb, sb);
    }
  };
  auto next_stage = [&](int t) {
    wbuf = (wbuf + ST_BYTES == lds0 + NST * ST_BYTES) ? lds0 : wbuf + ST_BYTES;
    sa += kstepA;
    sb += kstepB;
    if constexpr (ID == 2) {
      if (t == NT / 2) { qa = ra2; sa = 0; }
    }
    if (t == NT) { qa.z = 0; qb.z = 0; qb2.z = 0; }
  };

  unsigned rdA, rdB[4];
  if constexpr (C::AM == A_RM) rdA = (wm * WM + fr) * 128 + (((wk * 4 + fq) ^ (fr >> 1)) << 4);
  else rdA = ((wm * MB) * NWK + wk) * 1024 + lane * 16;
  constexpr int A_MB_STRIDE = (C::AM == A_RM) ? 2048 : NWK * 1024;
  if constexpr (C::BMODE == B_FT) {
    rdB[0] = A_BYTES + ((wn * NB) * NWK + wk) * 1024 + lane * 16;
  } else if constexpr (C::BMODE == B_NN) {
    const int q = fr >> 2, pp = fr & 3;
    const int sw = ((q >> 1) & 1) | ((fq & 1) << 1);
#pragma unroll
    for (int nb = 0; nb < NB; ++nb)
      rdB[nb] = A_BYTES + (wk * 32 + 8 * fq + q) * 128 + ((((wn * NB + nb) ^ sw) * 2 + (pp >> 1)) << 4) + (pp & 1) * 8;
  } else {
    const int q = fr >> 2, pp = fr & 3;
    const int r = (fq >> 1) * 16 + (fq & 1) * 4 + q;
    rdB[0] = A_BYTES + r * 224 + wn * 112 + pp * 8;
    rdB[1] = A_BYTES + r * 224 + wn * 112 + 96 + (pp & 1) * 8 + (pp >> 1) * UP_OFF;
  }

  bf16x8 fa[2][MB], fb[2][NB];
  auto read_frag = [&](int j, bf16x8(&a)[MB], bf16x8(&b)[NB], const unsigned(&vb)[4], unsigned va) {
    if (j < NB) {
      if constexpr (C::BMODE == B_FT) b[j] = lds_b128(vb[0] + j * NWK * 1024);
      else if constexpr (C::BMODE == B_NN) b[j] = lds_tr(vb[j], vb[j] + 512);
      else {
        if (j < 6) { const unsigned o = vb[0] + (j % 3) * 32 + (j / 3) * UP_OFF; b[j] = lds_tr(o, o + 1792); }
        else b[j] = lds_tr(vb[1], vb[1] + 1792);
      }
    } else {
      a[j - NB] = lds_b128(va + (j - NB) * A_MB_STRIDE);
    }
  };
  constexpr int NVB = (C::BMODE == B_FT) ? 1 : (C::BMODE == B_NN ? NB : 2);

  f32x4 acc[MB][NB];
#pragma unroll
  for (int mb = 0; mb < MB; ++mb)
#pragma unroll
    for (int nb = 0; nb < NB; ++nb) acc[mb][nb] = (f32x4){0.f, 0.f, 0.f, 0.f};

  auto step = [&](const bf16x8(&ca)[MB], const bf16x8(&cb)[NB], bf16x8(&na)[MB], bf16x8(&nb_)[NB], int tnext) {
    wait_vm<(NST - 2) * LPS>();
    wait_lgkm0();
    barrier();
    rbuf = (rbuf + ST_BYTES == lds0 + NST * ST_BYTES) ? lds0 : rbuf + ST_BYTES;
    unsigned vb[4], va = rbuf + rdA;
#pragma unroll
    for (int i = 0; i < NVB; ++i) vb[i] = rbuf + rdB[i];
    __builtin_amdgcn_sched_barrier(0);
    constexpr int NM = MB * NB, NR = MB + NB, NSL = NR + LPS;
#pragma unroll
    for (int i = 0; i < NM; ++i) {
      acc[i / NB][i % NB] = __builtin_amdgcn_mfma_f32_16x16x32_bf16(cb[i % NB], ca[i / NB], acc[i / NB][i % NB], 0, 0, 0);
      constexpr int SPAN = (NM > NSL) ? NSL : NM;
#pragma unroll
      for (int sl = 0; sl < NSL; ++sl) {
        if (sl * SPAN / NSL == i) {
          const int np = (sl * LPS) / NSL, np1 = ((sl + 1) * LPS) / NSL;
          if (np1 != np) piece(np);
          else read_frag(sl - np1, na, nb_, vb, va);
        }
      }
      __builtin_amdgcn_sched_barrier(0);
    }
    next_stage(tnext + 1);
  };

#pragma unroll
  for (int t = 0; t < NST; ++t) {
#pragma unroll
    for (int i = 0; i < LPS; ++i) piece(i);
    next_stage(t + 1);
  }
  wait_vm<(NST - 1) * LPS>();
  barrier();
  {
    unsigned vb[4], va = rbuf + rdA;
#pragma unroll
    for (int i = 0; i < NVB; ++i) vb[i] = rbuf + rdB[i];
#pragma unroll
    for (int j = 0; j < MB + NB; ++j) read_frag(j, fa[0], fb[0], vb, va);
  }
#pragma unroll 1
  for (int t = 0; t < NT; t += 2) {
    step(fa[0], fb[0], fa[1], fb[1], t + NST);
    step(fa[1], fb[1], fa[0], fb[0], t + 1 + NST);
  }
  wait_vm<0>();
  wait_lgkm0();
  barrier();
  STAMP(ID, 2);

  if constexpr (NWK == 2) {
    const unsigned rbase = lds0 + wmn * (MB * NB * 1024) + lane * 16;
    if (wk == 1) {
#pragma unroll
      for (int mb = 0; mb < MB; ++mb)
#pragma unroll
        for (int nb = 0; nb < NB; ++nb) *(LAS f32x4*)(rbase + (mb * NB + nb) * 1024) = acc[mb][nb];
    }
    wait_lgkm0();
    barrier();
    if (wk == 1) return;
#pragma unroll
    for (int mb = 0; mb < MB; ++mb)
#pragma unroll
      for (int nb = 0; nb < NB; ++nb) acc[mb][nb] += *(const LAS f32x4*)(rbase + (mb * NB + nb) * 1024);
  }

  const int chunk = ((fq & 1) << 1) | (fq >> 1);

  if constexpr (ID == 1) {
    const float sg = tm < 8 ? 1.f : -1.f;
    u16* o = (u16*)p.out;
#pragma unroll
    for (int mb = 0; mb < MB; ++mb)
#pragma unroll
      for (int np = 0; np < NB / 2; ++np) {
        const f32x4 x0 = acc[mb][2 * np] * sg, x1 = acc[mb][2 * np + 1] * sg;
        auto s0 = __builtin_amdgcn_permlane16_swap(pk_bf16(x0[0], x0[1]), pk_bf16(x1[0], x1[1]), false, false);
        auto s1 = __builtin_amdgcn_permlane16_swap(pk_bf16(x0[2], x0[3]), pk_bf16(x1[2], x1[3]), false, false);
        const u32x4 v = {s0[0], s1[0], s0[1], s1[1]};
        const int rblk = (tm & 7) * 8 + wm * MB + mb;
        const int kblk = (tm >> 3) * 64 + tn * 4 + wn * (NB / 2) + np;
        *(u32x4*)(o + ((size_t)rblk * 128 + kblk) * 512 + (chunk * 16 + fr) * 8) = v;
      }
  } else if constexpr (ID == 2 || ID == 4) {
    static_assert(ID == 1 || ID == 3 || NB == 2, "epilogue assumes 32 columns per wave");
    LAS f32x2* tab = (LAS f32x2*)(lds0 + EXTRA + 1024);
#pragma unroll
    for (int mb = 0; mb < MB; ++mb) {
      const int row = tm * BM + wm * WM + mb * 16 + fr;
      const int col = tn * BN + wn * WN + chunk * 8;
      const size_t ft = ((size_t)(row >> 4) * (D_ / 32) + (col >> 5)) * 512 + (chunk * 16 + fr) * 8;
      float y[8];
#pragma unroll
      for (int j = 0; j < 4; ++j) {
        auto s = __builtin_amdgcn_permlane16_swap(__float_as_uint(acc[mb][0][j]), __float_as_uint(acc[mb][1][j]), false, false);
        y[j] = __uint_as_float(s[0]);
        y[4 + j] = __uint_as_float(s[1]);
      }
      u32x4 r;
      if constexpr (ID == 2) r = *(const u32x4*)((const u16*)p.res + (size_t)row * D_ + col);
      else r = *(const u32x4*)((const u16*)p.res + ft);
      u32x4 v;
#pragma unroll
      for (int j = 0; j < 4; ++j) v[j] = pk_bf16(bf_lo(r[j]) + y[2 * j], bf_hi(r[j]) + y[2 * j + 1]);
      if constexpr (ID == 2) {
        *(u32x4*)((u16*)p.out + ft) = v;
        float s = 0.f;
#pragma unroll
        for (int j = 0; j < 4; ++j) s += bf_lo(v[j]) + bf_hi(v[j]);
        s += __shfl_xor(s, 16);
        s += __shfl_xor(s, 32);
        const float m = s * (1.0f / 32.0f);
        float q = 0.f;
#pragma unroll
        for (int j = 0; j < 4; ++j) {
          const float a = bf_lo(v[j]) - m, b = bf_hi(v[j]) - m;
          q += a * a + b * b;
        }
        q += __shfl_xor(q, 16);
        q += __shfl_xor(q, 32);
        if (fq == 0) tab[(wm * WM + mb * 16 + fr) * 2 + wn] = (f32x2){m, q};
      } else {
        *(u32x4*)((u16*)p.out + (size_t)row * D_ + col) = v;
      }
    }
    if constexpr (ID == 2) {
      wait_lgkm0();
      barrier();
      if (tid < BM) {
        const f32x2 a = tab[tid * 2], b = tab[tid * 2 + 1];
        const float d = a.x - b.x;
        ((f32x2*)p.stats)[(size_t)(tm * BM + tid) * 16 + tn] = (f32x2){0.5f * (a.x + b.x), a.y + b.y + 16.0f * d * d};
      }
    }
  } else {
    LAS f32x2* tab = (LAS f32x2*)(lds0);
    LAS float* cst = (LAS float*)(lds0 + 2048);
    if (tid < 256) {
      const f32x4* sp = (const f32x4*)p.stats + (size_t)(tm * BM + tid) * 8;
      f32x4 sv[8];
#pragma unroll
      for (int i = 0; i < 8; ++i) sv[i] = sp[i];
      float m = 0.f;
#pragma unroll
      for (int i = 0; i < 8; ++i) m += sv[i][0] + sv[i][2];
      m *= (1.0f / 16.0f);
      float q = 0.f;
#pragma unroll
      for (int i = 0; i < 8; ++i) {
        const float d0 = sv[i][0] - m, d1 = sv[i][2] - m;
        q += sv[i][1] + sv[i][3] + 64.0f * (d0 * d0 + d1 * d1);
      }
      tab[tid] = (f32x2){m, 1.0f / sqrtf(q * (1.0f / D_) + EPS)};
    } else if (tid < 256 + 224) {
      const int c = tid - 256;
      const int col = (c < 112) ? tn * 112 + c : F_ + tn * 112 + (c - 112);
      float s = 0.f;
#pragma unroll
      for (int i = 0; i < CS_SPLIT; ++i) s += ((const float*)p.cs)[i * (2 * F_) + col];
      cst[c] = s;
    }
    wait_lgkm0();
    barrier();
    auto act = [](float g, float u) { return g * u * __builtin_amdgcn_rcpf(1.0f + __expf(-g)); };
    u16* o = (u16*)p.out;
    f32x2 st[MB];
#pragma unroll
    for (int mb = 0; mb < MB; ++mb) st[mb] = tab[wm * WM + mb * 16 + fr];
    const int gch0 = tn * 14 + wn * 7;
#pragma unroll
    for (int i = 0; i < 3; ++i) {
      const f32x4 cg = *(const LAS f32x4*)(cst + wn * 56 + i * 16 + fq * 4), cu = *(const LAS f32x4*)(cst + 112 + wn * 56 + i * 16 + fq * 4);
      u32x2 pkd[MB];
#pragma unroll
      for (int mb = 0; mb < MB; ++mb) {
        const f32x4 g = (acc[mb][i] - cg * st[mb].x) * st[mb].y, u = (acc[mb][3 + i] - cu * st[mb].x) * st[mb].y;
        pkd[mb] = (u32x2){pk_bf16(act(g[0], u[0]), act(g[1], u[1])), pk_bf16(act(g[2], u[2]), act(g[3], u[3]))};
      }
#pragma unroll
      for (int mp = 0; mp < MB / 2; ++mp) {
        auto s0 = __builtin_amdgcn_permlane16_swap(pkd[2 * mp].x, pkd[2 * mp + 1].x, false, false);
        auto s1 = __builtin_amdgcn_permlane16_swap(pkd[2 * mp].y, pkd[2 * mp + 1].y, false, false);
        const u32x4 v = {s0[0], s1[0], s0[1], s1[1]};
        const int rblk = tm * (BM / 16) + wm * MB + 2 * mp + (fq & 1);
        const int gch = gch0 + 2 * i + (fq >> 1);
        *(u32x4*)(o + ((size_t)rblk * (F_ / 32) + (gch >> 2)) * 512 + ((gch & 3) * 16 + fr) * 8) = v;
      }
    }
    {
      const f32x4 cm = *(const LAS f32x4*)(cst + (fq >> 1) * 112 + wn * 56 + 48 + (fq & 1) * 4);
      u32x2 pkd[MB / 2];
#pragma unroll
      for (int mp = 0; mp < MB / 2; ++mp) {
        const f32x4 v0 = (acc[2 * mp][6] - cm * st[2 * mp].x) * st[2 * mp].y, v1 = (acc[2 * mp + 1][6] - cm * st[2 * mp + 1].x) * st[2 * mp + 1].y;
        float r[4];
#pragma unroll
        for (int j = 0; j < 4; ++j) {
          auto s = __builtin_amdgcn_permlane32_swap(__float_as_uint(v0[j]), __float_as_uint(v1[j]), false, false);
          r[j] = act(__uint_as_float(s[0]), __uint_as_float(s[1]));
        }
        pkd[mp] = (u32x2){pk_bf16(r[0], r[1]), pk_bf16(r[2], r[3])};
      }
      auto s0 = __builtin_amdgcn_permlane16_swap(pkd[0].x, pkd[1].x, false, false);
      auto s1 = __builtin_amdgcn_permlane16_swap(pkd[0].y, pkd[1].y, false, false);
      const u32x4 v = {s0[0], s1[0], s0[1], s1[1]};
      const int rblk = tm * (BM / 16) + wm * MB + (fq & 1) * 2 + (fq >> 1);
      const int gch = gch0 + 6;
      *(u32x4*)(o + ((size_t)rblk * (F_ / 32) + (gch >> 2)) * 512 + ((gch & 3) * 16 + fr) * 8) = v;
    }
  }
  STAMP(ID, 3);
}

template <int ID>
void launch_gemm(const GArgs& a, hipStream_t stream) {
  (void)hipFuncSetAttribute((const void*)k_gemm<ID>, hipFuncAttributeMaxDynamicSharedMemorySize, lds_bytes<ID>());
  hipLaunchKernelGGL(k_gemm<ID>, dim3(256), dim3(512), lds_bytes<ID>(), stream, a);
}
}

extern "C" void kernel_launch(void* const* d_in, const int* in_sizes, int n_in, void* d_out, int out_size, void* d_ws, size_t ws_size,
                              hipStream_t stream) {
  const u16* x = (const u16*)d_in[0];
  char* ws = (char*)d_ws;
  hipLaunchKernelGGL(k_prep, dim3(128 + 448), dim3(512), 0, stream, x, (const u16*)d_in[6], (const u16*)d_in[7], (u16*)(ws + WS_XN),
                     (float*)(ws + WS_CS));
  GArgs g1 = {d_in[3], d_in[4], ws + WS_XN, nullptr, ws + WS_AB, nullptr, nullptr, nullptr};
  launch_gemm<1>(g1, stream);
  GArgs g2 = {d_in[1], d_in[2], ws + WS_AB, nullptr, ws + WS_H, x, ws + WS_ST, nullptr};
  launch_gemm<2>(g2, stream);
  GArgs g3 = {ws + WS_H, nullptr, d_in[6], d_in[7], ws + WS_GU, nullptr, ws + WS_ST, ws + WS_CS};
  launch_gemm<3>(g3, stream);
  GArgs g4 = {ws + WS_GU, nullptr, d_in[8], nullptr, d_out, ws + WS_H, nullptr, nullptr};
  launch_gemm<4>(g4, stream);
}
```

```cpp
#include <hip/hip_runtime.h>
#include <hip/hip_bf16.h>

namespace {
#define LAS __attribute__((address_space(3)))
typedef unsigned short u16;
typedef short s16x4 __attribute__((ext_vector_type(4)));
typedef short s16x8 __attribute__((ext_vector_type(8)));
typedef __bf16 bf16x8 __attribute__((ext_vector_type(8)));
typedef __bf16 bf16x2 __attribute__((ext_vector_type(2)));
typedef float f32x4 __attribute__((ext_vector_type(4)));
typedef float f32x2 __attribute__((ext_vector_type(2)));
typedef unsigned u32x4 __attribute__((ext_vector_type(4)));
typedef unsigned u32x2 __attribute__((ext_vector_type(2)));

constexpr int S_ = 2048, D_ = 1024, F_ = 3584;
constexpr float EPS = 1e-5f;

constexpr size_t WS_AB = 0;
constexpr size_t WS_H = WS_AB + (size_t)D_ * 2 * S_ * 2;
constexpr size_t WS_GU = WS_H + (size_t)S_ * D_ * 2;
constexpr size_t WS_ST = WS_GU + (size_t)S_ * F_ * 2;
constexpr size_t WS_XC = WS_ST + (size_t)S_ * 32 * 8;
constexpr size_t WS_FL = WS_XC + (size_t)2 * 256 * 36864;
constexpr int FL_CS = 2 * 256 * 128;

__device__ __forceinline__ unsigned rfl(unsigned v) { return __builtin_amdgcn_readfirstlane(v); }
__device__ __forceinline__ unsigned pk_bf16(float lo, float hi) {
  bf16x2 c = {(__bf16)lo, (__bf16)hi};
  return __builtin_bit_cast(unsigned, c);
}
__device__ __forceinline__ float bf_lo(unsigned v) { return __uint_as_float(v << 16); }
__device__ __forceinline__ float bf_hi(unsigned v) { return __uint_as_float(v & 0xffff0000u); }

__device__ __forceinline__ u32x4 make_rsrc(const void* p, unsigned bytes) {
  unsigned long long a = (unsigned long long)p;
  u32x4 r;
  r.x = (unsigned)a;
  r.y = (unsigned)(a >> 32) & 0xffffu;
  r.z = bytes;
  r.w = 0x00020000u;
  return r;
}
__device__ __forceinline__ void dma16(unsigned ldsaddr, unsigned voff, u32x4 rsrc, unsigned soff) {
  asm volatile("s_mov_b32 m0, %0\n\ts_nop 4\n\tbuffer_load_dwordx4 %1, %2, %3 offen lds" ::"s"(ldsaddr), "v"(voff), "s"(rsrc), "s"(soff)
               : "memory");
}
template <int N>
__device__ __forceinline__ void wait_vm() {
  asm volatile("s_waitcnt vmcnt(%0)" ::"i"(N) : "memory");
}
__device__ __forceinline__ void wait_lgkm0() { __builtin_amdgcn_s_waitcnt(0xC07F); }
__device__ __forceinline__ void barrier() {
  asm volatile("" ::: "memory");
  __builtin_amdgcn_s_barrier();
  asm volatile("" ::: "memory");
}
__device__ __forceinline__ bf16x8 lds_b128(unsigned addr) { return *(const LAS bf16x8*)(addr); }
__device__ __forceinline__ bf16x8 lds_tr(unsigned a0, unsigned a1) {
  s16x4 lo = __builtin_amdgcn_ds_read_tr16_b64_v4i16((LAS s16x4*)(a0));
  s16x4 hi = __builtin_amdgcn_ds_read_tr16_b64_v4i16((LAS s16x4*)(a1));
  s16x8 f = __builtin_shufflevector(lo, hi, 0, 1, 2, 3, 4, 5, 6, 7);
  return __builtin_bit_cast(bf16x8, f);
}

#define ST_AUX 16
#define OUT_RSRC(ptr, bytes) __builtin_amdgcn_make_buffer_rsrc((void*)(ptr), 0, (bytes), 0x00020000)
#define STORE16(rs, byteoff, v) __builtin_amdgcn_raw_buffer_store_b128(v, rs, byteoff, 0, ST_AUX)
#define STAMP(k, i)
#define STAMPX(k, i)
#define STAMPL(k, i, first)

struct GArgs {
  const void* a0;
  const void* a1;
  const void* b0;
  const void* b1;
  void* out;
  const void* res;
  void* stats;
  void* xch;
  void* flags;
};

template <int N>
__device__ __forceinline__ void dma_grp(unsigned ldsaddr, const unsigned (&v)[4], u32x4 rsrc, unsigned soff) {
  if constexpr (N == 4)
    asm volatile("s_mov_b32 m0, %0\n\ts_nop 4\n\tbuffer_load_dwordx4 %1, %5, %6 offen lds\n\tbuffer_load_dwordx4 %2, %5, %6 offen offset:1024 lds\n\t"
                 "buffer_load_dwordx4 %3, %5, %6 offen offset:2048 lds\n\tbuffer_load_dwordx4 %4, %5, %6 offen offset:3072 lds" ::"s"(ldsaddr),
                 "v"(v[0]), "v"(v[1]), "v"(v[2]), "v"(v[3]), "s"(rsrc), "s"(soff)
                 : "memory");
  else
    asm volatile("s_mov_b32 m0, %0\n\ts_nop 4\n\tbuffer_load_dwordx4 %1, %4, %5 offen lds\n\tbuffer_load_dwordx4 %2, %4, %5 offen offset:1024 lds\n\t"
                 "buffer_load_dwordx4 %3, %4, %5 offen offset:2048 lds" ::"s"(ldsaddr),
                 "v"(v[0]), "v"(v[1]), "v"(v[2]), "s"(rsrc), "s"(soff)
                 : "memory");
}
__device__ __forceinline__ void lds_inc(unsigned addr) { asm volatile("ds_add_u32 %0, %1" ::"v"(addr), "v"(1u) : "memory"); }
__device__ __forceinline__ unsigned inc_base(unsigned sync, int lane) { return sync + lane * 68; }
__device__ __forceinline__ unsigned lds_peek(unsigned addr) { return *(volatile LAS unsigned*)(addr); }
__device__ __forceinline__ void lds_wait(unsigned addr, unsigned pv, unsigned need) {
  if (__builtin_expect(rfl(pv) < need, 0)) {
    do {
      __builtin_amdgcn_s_sleep(1);
    } while (rfl(lds_peek(addr)) < need);
  }
  asm volatile("" ::: "memory");
}
__device__ __forceinline__ float sum_rows(float x) {
  auto a = __builtin_amdgcn_permlane16_swap(__float_as_uint(x), __float_as_uint(x), false, false);
  x = __uint_as_float(a[0]) + __uint_as_float(a[1]);
  auto b = __builtin_amdgcn_permlane32_swap(__float_as_uint(x), __float_as_uint(x), false, false);
  return __uint_as_float(b[0]) + __uint_as_float(b[1]);
}
__device__ __forceinline__ float sum_pair(float x) { return x + __uint_as_float(__builtin_amdgcn_mov_dpp(__float_as_uint(x), 0xB1, 0xF, 0xF, true)); }
__device__ __forceinline__ int lane_id() {
  int l;
  asm volatile("v_mbcnt_lo_u32_b32 %0, -1, 0\n\tv_mbcnt_hi_u32_b32 %0, -1, %0" : "=v"(l));
  return l;
}
__device__ __forceinline__ void mfma_drain() { asm volatile("s_nop 15" ::: "memory"); }
__device__ __forceinline__ void fadd(float& a, float b) { asm("v_add_f32 %0, %1, %0" : "+v"(a) : "v"(b)); }
template <int NC, int NT, int WRAP>
__device__ __forceinline__ unsigned stage_of(unsigned n, unsigned c) {
  if (NC == 1) return n;
  if (WRAP) {
    const unsigned t = ((n & (NC - 1)) == c) ? n + NC : n;
    return rfl(t >= NT ? t - NT : t);
  }
  unsigned t = (n > NT - NC + c) ? n : n - 1;
  t = ((t & (NC - 1)) == c) ? t + NC : t;
  return rfl(n == 0 ? c : t);
}
#define NC1 4
#define NC2 8
#define NC3 8
#define NC4 4
#define TOUCH_C 0
#define TOUCH_W 0
#define TOUCH_AT 6
#define XSTR 16384
#define WRAPM 0x18
#define SQ_PD 2
#define BAR_AT 0
#define CPRIO 0
template <int V> struct IC { static constexpr int value = V; };
#define LPRIO 0
#define SB() __builtin_amdgcn_sched_barrier(0)
#define MFMA(b, a, c) asm volatile("v_mfma_f32_16x16x32_bf16 %0, %1, %2, %0" : "+v"(c) : "v"(b), "v"(a))
#define STAMP1_TAKE()
#define STAMP1_PUT(k)

enum { OP_RM = 0, OP_FT = 1, OP_NN = 2 };
template <int ID> struct Cfg;
template <> struct Cfg<1> { static constexpr int NT = 16, AM = OP_RM, BMODE = OP_RM, NC = NC1; };
template <> struct Cfg<2> { static constexpr int NT = 32, AM = OP_RM, BMODE = OP_FT, NC = NC2; };
template <> struct Cfg<4> { static constexpr int NT = 28, AM = OP_FT, BMODE = OP_NN, NC = NC4; };
constexpr int SQ_NST = 4, SQ_ST = 32768, SQ_SYNC = SQ_NST * SQ_ST, SQ_TAB = SQ_SYNC + 4608, SQ_LDS = SQ_TAB + 2048;

template <int ID>
__global__ __launch_bounds__(512, 2) void k_sq(GArgs p) {
  using C = Cfg<ID>;
  constexpr int NT = C::NT, NST = SQ_NST, ST = SQ_ST, A_BYTES = 16384;
  extern __shared__ __attribute__((aligned(16))) char shm[];
  const unsigned lds0 = (unsigned)(size_t)(LAS char*)shm;
  const unsigned vfull = lds0 + SQ_SYNC, vfree = lds0 + SQ_SYNC + 32;
  const int tid = threadIdx.x, lane = tid & 63;
  const unsigned w = rfl(tid >> 6);
  STAMP(ID, 0);
  const int fr = lane & 15, fq = lane >> 4;
  const unsigned ifull = inc_base(vfull, lane), ifree = ifull + 32;
  const unsigned xcd = blockIdx.x & 7, idx = blockIdx.x >> 3, li = idx & 3, lj = idx >> 2;
  const unsigned kh = (ID == 1) ? 0u : (xcd & 1);
  const unsigned tm = (xcd >> 1) * 4 + li, tn = (ID == 1) ? (xcd & 1) * 8 + lj : lj;

  if (w >= 4) {
    if (LPRIO) __builtin_amdgcn_s_setprio(LPRIO);
    const unsigned lw = w - 4;
    u32x4 qa, qb;
    unsigned va[4], vb[4], kstepA, kstepB;
    if constexpr (ID == 1) {
      qa = make_rsrc((const u16*)(tm < 8 ? p.a0 : p.a1) + (size_t)(tm & 7) * 128 * D_, 128 * D_ * 2);
      qb = make_rsrc((const u16*)p.b0 + (size_t)tn * 128 * D_, 128 * D_ * 2);
    } else if constexpr (ID == 2) {
      qa = make_rsrc((const u16*)(kh ? p.a1 : p.a0) + (size_t)tm * 128 * S_, 128 * S_ * 2);
      qb = make_rsrc((const u16*)p.b0 + (size_t)tn * 128 * 4096 + kh * 64 * 512, 128 * 4096 * 2 - kh * 64 * 1024);
    } else {
      qa = make_rsrc((const u16*)p.a0 + (size_t)tm * 128 * F_ + kh * 56 * 512, 128 * F_ * 2 - kh * 56 * 1024);
      qb = make_rsrc((const u16*)p.b0 + (size_t)kh * 1792 * D_, 1792 * D_ * 2);
    }
#pragma unroll
    for (int i = 0; i < 4; ++i) {
      const int pc = lw * 4 + i;
      const int row = pc * 8 + (lane >> 3), rsw = ((lane & 7) ^ ((row >> 1) & 7)) << 4;
      if constexpr (C::AM == OP_RM) va[i] = row * (ID == 1 ? D_ : S_) * 2 + rsw - i * 1024;
      else va[i] = ((pc >> 1) * (F_ / 32) + (pc & 1)) * 1024 + lane * 16 - i * 1024;
      if constexpr (C::BMODE == OP_RM) vb[i] = row * D_ * 2 + rsw - i * 1024;
      else if constexpr (C::BMODE == OP_FT) vb[i] = ((pc >> 1) * 128 + (pc & 1)) * 1024 + lane * 16 - i * 1024;
      else {
        const int k = pc * 4 + (lane >> 4), f = (k & 3) | (((k >> 3) & 1) << 2);
        vb[i] = k * D_ * 2 + tn * 256 + (((lane & 15) ^ (f << 1)) << 4) - i * 1024;
      }
    }
    kstepA = (C::AM == OP_RM) ? 128 : 2048;
    kstepB = (C::BMODE == OP_RM) ? 128 : (C::BMODE == OP_FT ? 2048 : 64 * D_ * 2);
    const unsigned cls = (C::NC == 8) ? ((lj + 2 * li) & 7) : ((lj + li) & (C::NC - 1));
    auto issue = [&](unsigned n) {
      unsigned st = stage_of<C::NC, NT, (WRAPM >> ID) & 1>(n, cls);
      const unsigned wbuf = lds0 + (n & (NST - 1)) * ST + lw * 4096;
      dma_grp<4>(wbuf, va, qa, st * kstepA);
      dma_grp<4>(wbuf + A_BYTES, vb, qb, st * kstepB);
    };
    unsigned touch = 0;
#pragma unroll
    for (int n = 0; n < SQ_PD; ++n) issue(n);
#pragma unroll 1
    for (unsigned b = 0; b + SQ_PD < NT; ++b) {
      wait_vm<8 * (SQ_PD - 1)>();
      barrier();
      if constexpr (ID == 1) {
        if (TOUCH_C && b >= TOUCH_AT && b < TOUCH_AT + TOUCH_C) {
          const char* tp = (const char*)((xcd & 1) ? p.stats : p.res) + (size_t)((xcd >> 1) * 512 + idx * 16 + lw * 4 + (lane >> 4)) * (S_ * 2) + (b - TOUCH_AT) * 2048 + (lane & 15) * 128;
          asm volatile("global_load_dword %0, %1, off" : "+v"(touch) : "v"(tp) : "memory");
        }
        if (TOUCH_W && b >= TOUCH_AT + TOUCH_C && b < TOUCH_AT + TOUCH_C + TOUCH_W) {
          const unsigned ln = ((b - TOUCH_AT - TOUCH_C) * 512 + (blockIdx.x >> 1) * 4 + lw) * 56 + (lane < 56 ? lane : 55);
          const char* tp = (const char*)((blockIdx.x & 1) ? p.xch : p.b1) + (size_t)ln * 128;
          asm volatile("global_load_dword %0, %1, off" : "+v"(touch) : "v"(tp) : "memory");
        }
      }
      issue(b + SQ_PD);
    }
    if (SQ_PD == 3) { wait_vm<16>(); barrier(); }
    wait_vm<8>();
    barrier();
    wait_vm<0>();
    asm volatile("" ::"v"(touch));
    barrier();
    return;
  }

  const unsigned wm = w >> 1, wx = w & 1;
  if constexpr (ID == 1) {
    if (tid < 2) *(unsigned*)((char*)p.flags + (tid * 256 + blockIdx.x) * 128) = 0u;
    if (tid < 224) *(unsigned long long*)((char*)p.flags + FL_CS + (blockIdx.x * 224 + tid) * 8) = 0ull;
  }
  const int chunk = ((fq & 1) << 1) | (fq >> 1);
  u32x4 res[4];
  if constexpr (ID != 1) {
#pragma unroll
    for (int mb = 0; mb < 4; ++mb) {
      const int row = tm * 128 + wm * 64 + mb * 16 + fr, col = tn * 128 + wx * 64 + kh * 32 + chunk * 8;
      if constexpr (ID == 2) res[mb] = *(const u32x4*)((const u16*)p.res + (size_t)row * D_ + col);
      else res[mb] = *(const u32x4*)((const u16*)p.res + ((size_t)(row >> 4) * (D_ / 32) + (col >> 5)) * 512 + (chunk * 16 + fr) * 8);
    }
  }
  const unsigned rotA = (ID == 1) ? 2 * wx : 0u, rotB = (ID == 1) ? 2 * wm : 2 * kh;
  unsigned rdA[4], rdB[4];
#pragma unroll
  for (int i = 0; i < 4; ++i) {
    const unsigned kk = i >> 1, hf = (i & 1) * 2, sw = ((kk * 4 + fq) ^ (fr >> 1)) << 4;
    if constexpr (C::AM == OP_RM) rdA[i] = (wm * 64 + (hf ^ rotA) * 16 + fr) * 128 + sw;
    else rdA[i] = wm * 8192 + lane * 16;
    if constexpr (C::BMODE == OP_RM) rdB[i] = A_BYTES + (wx * 64 + (hf ^ rotB) * 16 + fr) * 128 + sw;
    else if constexpr (C::BMODE == OP_FT) rdB[i] = A_BYTES + wx * 8192 + (hf ^ rotB) * 2048 + lane * 16;
    else {
      const int q = fr >> 2, pp = fr & 3, f = q | ((fq & 1) << 2);
      rdB[i] = A_BYTES + (8 * fq + q) * 256 + (((wx * 4 + (i ^ rotB)) ^ f) << 5) + pp * 8;
    }
  }
  unsigned rdA2[4], rdB2[4];
#pragma unroll
  for (int i = 0; i < 4; ++i) {
    rdA[i] += lds0; rdB[i] += lds0;
    rdA2[i] = rdA[i] + 2 * ST; rdB2[i] = rdB[i] + 2 * ST;
    asm volatile("" : "+v"(rdA2[i]), "+v"(rdB2[i]), "+v"(rdA[i]), "+v"(rdB[i]));
  }
  bf16x8 fa[2][2][4], fb[2][2][4];
  auto read_frag = [&](int j, bf16x8(&a)[2][4], bf16x8(&b)[2][4], const unsigned(&xa)[4], const unsigned(&xb)[4], int so) {
    const int s = j >> 3, i = j & 3;
    if (j & 4) {
      if constexpr (C::AM == OP_RM) a[s][i] = lds_b128(xa[s * 2 + (i >> 1)] + (i & 1) * 2048 + so);
      else a[s][i] = lds_b128(xa[0] + i * 2048 + s * 1024 + so);
    } else {
      if constexpr (C::BMODE == OP_RM) b[s][i] = lds_b128(xb[s * 2 + (i >> 1)] + (i & 1) * 2048 + so);
      else if constexpr (C::BMODE == OP_FT) b[s][i] = lds_b128(xb[i >> 1] + (i & 1) * 2048 + s * 1024 + so);
      else b[s][i] = lds_tr(xb[i] + s * 8192 + so, xb[i] + s * 8192 + 1024 + so);
    }
  };
  f32x4 acc[4][4], axs[2], axq[2], aws[2];
#pragma unroll
  for (int i = 0; i < 16; ++i) { acc[i >> 2][i & 3] = (f32x4){0.f, 0.f, 0.f, 0.f}; asm volatile("" : "+v"(acc[i >> 2][i & 3])); }
#pragma unroll
  for (int i = 0; i < 2; ++i) {
    axs[i] = axq[i] = aws[i] = (f32x4){0.f, 0.f, 0.f, 0.f};
    if (ID == 1) asm volatile("" : "+v"(axs[i]), "+v"(axq[i]), "+v"(aws[i]));
  }
  bf16x8 ones = __builtin_bit_cast(bf16x8, (u32x4){0x3f803f80u, 0x3f803f80u, 0x3f803f80u, 0x3f803f80u});
  asm volatile("" : "+v"(ones));
  auto step = [&](const bf16x8(&ca)[2][4], const bf16x8(&cb)[2][4], bf16x8(&na)[2][4], bf16x8(&nb)[2][4], auto slc) {
    constexpr int SL = decltype(slc)::value & 3;
    constexpr bool RD = decltype(slc)::value < 4;
#pragma unroll
    for (int i = 0; i < 32; ++i) {
      if (RD && i == BAR_AT) { barrier(); SB(); }
      if constexpr (ID == 1) {
        if ((i & 15) == 15) {
#pragma unroll
          for (int j = 0; j < 2; ++j) {
            MFMA(cb[i >> 4][j], ones, axs[j]);
            MFMA(cb[i >> 4][j], cb[i >> 4][j], axq[j]);
            MFMA(ones, ca[i >> 4][j], aws[j]);
          }
          SB();
        }
      }
      MFMA(cb[i >> 4][i & 3], ca[i >> 4][(i >> 2) & 3], acc[(i >> 2) & 3][i & 3]);
      SB();
      if (RD && i >= BAR_AT && i < BAR_AT + 16) {
        if (SL < 2) read_frag(i - BAR_AT, na, nb, rdA, rdB, SL * ST);
        else read_frag(i - BAR_AT, na, nb, rdA2, rdB2, (SL - 2) * ST);
      }
      SB();
    }
    wait_lgkm0();
  };
  barrier();
  STAMP1_TAKE();
#pragma unroll
  for (int j = 0; j < 16; ++j) read_frag(j, fa[0], fb[0], rdA, rdB, 0);
  wait_lgkm0();
#pragma unroll 1
  for (unsigned t = 0; t < NT - 4; t += 4) {
    step(fa[0], fb[0], fa[1], fb[1], IC<1>{});
    step(fa[1], fb[1], fa[0], fb[0], IC<2>{});
    step(fa[0], fb[0], fa[1], fb[1], IC<3>{});
    step(fa[1], fb[1], fa[0], fb[0], IC<0>{});
  }
  step(fa[0], fb[0], fa[1], fb[1], IC<1>{});
  step(fa[1], fb[1], fa[0], fb[0], IC<2>{});
  step(fa[0], fb[0], fa[1], fb[1], IC<3>{});
  step(fa[1], fb[1], fa[0], fb[0], IC<4>{});
  mfma_drain();
  SB();
  STAMP1_PUT(ID);
  STAMP(ID, 2);

  if constexpr (ID == 1) {
    LAS float* xs = (LAS float*)(lds0 + SQ_TAB);
    LAS float* xq = xs + 128;
    LAS float* wsum = xs + 256;
#pragma unroll
    for (int j = 0; j < 2; ++j) {
      const unsigned nb = wx * 64 + (j ^ rotB) * 16, mb = wm * 64 + (j ^ rotA) * 16;
      if (fr == 0) *(LAS f32x4*)(xs + nb + fq * 4) = axs[j];
      const float dg = (fr & 2) ? ((fr & 1) ? axq[j][3] : axq[j][2]) : ((fr & 1) ? axq[j][1] : axq[j][0]);
      if (fq == (fr >> 2)) xq[nb + fr] = dg;
      if (fq == 0) wsum[mb + fr] = aws[j][0];
    }
    wait_lgkm0();
    barrier();
    STAMPX(ID, 0);
    const float sg = tm < 8 ? 1.f : -1.f;
    f32x4 mean[4], rstd[4];
    float wsm[4];
#pragma unroll
    for (int j = 0; j < 4; ++j) {
      const unsigned nb = wx * 64 + (j ^ rotB) * 16 + fq * 4;
      mean[j] = *(const LAS f32x4*)(xs + nb) * (1.0f / D_);
      const f32x4 var = *(const LAS f32x4*)(xq + nb) * (1.0f / D_) - mean[j] * mean[j];
#pragma unroll
      for (int r = 0; r < 4; ++r) rstd[j][r] = sg * __builtin_amdgcn_rsqf(fmaxf(var[r], 0.f) + EPS);
      wsm[j] = wsum[wm * 64 + (j ^ rotA) * 16 + fr];
    }
    const auto ro = OUT_RSRC(p.out, D_ * 4096 * 2);
#pragma unroll
    for (int mb = 0; mb < 4; ++mb)
#pragma unroll
      for (int np = 0; np < 2; ++np) {
        const f32x4 x0 = (acc[mb][2 * np] - mean[2 * np] * wsm[mb]) * rstd[2 * np], x1 = (acc[mb][2 * np + 1] - mean[2 * np + 1] * wsm[mb]) * rstd[2 * np + 1];
        auto s0 = __builtin_amdgcn_permlane16_swap(pk_bf16(x0[0], x0[1]), pk_bf16(x1[0], x1[1]), false, false);
        auto s1 = __builtin_amdgcn_permlane16_swap(pk_bf16(x0[2], x0[3]), pk_bf16(x1[2], x1[3]), false, false);
        const u32x4 v = {s0[0], s1[0], s0[1], s1[1]};
        const int rblk = (tm & 7) * 8 + wm * 4 + (mb ^ rotA);
        const int kblk = (tm >> 3) * 64 + tn * 4 + wx * 2 + (np ^ (rotB >> 1));
        STORE16(ro, (rblk * 128 + kblk) * 1024 + (chunk * 16 + fr) * 16, v);
      }
  } else {
    constexpr int XI = (ID == 2) ? 0 : 1;
    const auto rs = __builtin_amdgcn_make_buffer_rsrc((char*)p.xch + (size_t)XI * 256 * 36864, 0, 256 * 36864, 0x00020000);
#pragma unroll
    for (int mb = 0; mb < 4; ++mb) {
      auto s0 = __builtin_amdgcn_permlane16_swap(pk_bf16(acc[mb][2][0], acc[mb][2][1]), pk_bf16(acc[mb][3][0], acc[mb][3][1]), false, false);
      auto s1 = __builtin_amdgcn_permlane16_swap(pk_bf16(acc[mb][2][2], acc[mb][2][3]), pk_bf16(acc[mb][3][2], acc[mb][3][3]), false, false);
      __builtin_amdgcn_raw_buffer_store_b128((u32x4){s0[0], s1[0], s0[1], s1[1]}, rs, w * 4096 + lane * 16 + mb * 1024, blockIdx.x * XSTR, 16);
    }
    f32x4 y[4][2];
#pragma unroll
    for (int mb = 0; mb < 4; ++mb)
#pragma unroll
      for (int j = 0; j < 4; ++j) {
        auto s = __builtin_amdgcn_permlane16_swap(__float_as_uint(acc[mb][0][j]), __float_as_uint(acc[mb][1][j]), false, false);
        y[mb][0][j] = __uint_as_float(s[0]) + ((j & 1) ? bf_hi(res[mb][j >> 1]) : bf_lo(res[mb][j >> 1]));
        y[mb][1][j] = __uint_as_float(s[1]) + ((j & 1) ? bf_hi(res[mb][2 + (j >> 1)]) : bf_lo(res[mb][2 + (j >> 1)]));
      }
    const int row0 = tm * 128 + wm * 64 + fr, col = tn * 128 + wx * 64 + kh * 32 + chunk * 8;
    unsigned optr = (ID == 2) ? ((row0 >> 4) * (D_ / 32) + (col >> 5)) * 1024 + (chunk * 16 + fr) * 16 : (row0 * D_ + col) * 2;
    const auto ro = OUT_RSRC(p.out, S_ * D_ * 2);
    asm volatile("" : "+v"(y[0][0]), "+v"(y[0][1]), "+v"(y[1][0]), "+v"(y[1][1]), "+v"(y[2][0]), "+v"(y[2][1]), "+v"(y[3][0]), "+v"(y[3][1]), "+v"(optr));
    wait_vm<0>();
    barrier();
    if (w == 0) {
      unsigned* fl = (unsigned*)((char*)p.flags + XI * 256 * 128);
      if (lane == 0) __hip_atomic_store(fl + blockIdx.x * 32, 1u, __ATOMIC_RELAXED, __HIP_MEMORY_SCOPE_AGENT);
      unsigned spins = 0;
      while (rfl(__hip_atomic_load(fl + (blockIdx.x ^ 1) * 32, __ATOMIC_RELAXED, __HIP_MEMORY_SCOPE_AGENT)) != 1u && ++spins < (1u << 22)) __builtin_amdgcn_s_sleep(1);
    }
    barrier();
    u32x4 in[4];
#pragma unroll
    for (int i = 0; i < 4; ++i) in[i] = __builtin_amdgcn_raw_buffer_load_b128(rs, w * 4096 + lane * 16 + i * 1024, (blockIdx.x ^ 1) * XSTR, 16);
    u32x4 v[4];
#pragma unroll
    for (int mb = 0; mb < 4; ++mb) {
      const f32x4 a = y[mb][0] + (f32x4){bf_lo(in[mb][0]), bf_hi(in[mb][0]), bf_lo(in[mb][1]), bf_hi(in[mb][1])};
      const f32x4 b = y[mb][1] + (f32x4){bf_lo(in[mb][2]), bf_hi(in[mb][2]), bf_lo(in[mb][3]), bf_hi(in[mb][3])};
      v[mb] = (u32x4){pk_bf16(a[0], a[1]), pk_bf16(a[2], a[3]), pk_bf16(b[0], b[1]), pk_bf16(b[2], b[3])};
      STORE16(ro, optr + mb * 16 * D_ * 2, v[mb]);
    }
    SB();
    if constexpr (ID == 2) {
      float m[4], q[4];
#pragma unroll
      for (int mb = 0; mb < 4; ++mb) {
        float s = 0.f;
#pragma unroll
        for (int j = 0; j < 4; ++j) s += bf_lo(v[mb][j]) + bf_hi(v[mb][j]);
        m[mb] = sum_rows(s) * (1.0f / 32.0f);
      }
#pragma unroll
      for (int mb = 0; mb < 4; ++mb) {
        float s = 0.f;
#pragma unroll
        for (int j = 0; j < 4; ++j) {
          const float a = bf_lo(v[mb][j]) - m[mb], b = bf_hi(v[mb][j]) - m[mb];
          s += a * a + b * b;
        }
        q[mb] = sum_rows(s);
      }
      const float ms = (fq & 2) ? ((fq & 1) ? m[3] : m[2]) : ((fq & 1) ? m[1] : m[0]);
      const float qs = (fq & 2) ? ((fq & 1) ? q[3] : q[2]) : ((fq & 1) ? q[1] : q[0]);
      LAS f32x2* tb = (LAS f32x2*)(lds0 + SQ_TAB);
      if (wx) tb[wm * 64 + lane] = (f32x2){ms, qs};
      wait_lgkm0();
      barrier();
      if (!wx) {
        const f32x2 o = tb[wm * 64 + lane];
        const float d = ms - o.x;
        ((f32x2*)p.stats)[((size_t)tn * S_ + tm * 128 + wm * 64 + lane) * 2 + kh] = (f32x2){0.5f * (ms + o.x), qs + o.y + 16.0f * d * d};
      }
    }
  }
  STAMPX(ID, 4);
  STAMP(ID, 3);
}

constexpr int G3_NST = 4, G3_UP = 7168 + 16, G3_ST = 16384 + 2 * 7168 + 16, G3_SYNC = G3_NST * G3_ST, G3_TAB = G3_SYNC + 4608, G3_LDS = G3_TAB + 3072;
__global__ __launch_bounds__(768, 3) void k_glu(GArgs p) {
  constexpr int ID = 3, BM = 256, K = D_, NT = K / 32, NST = G3_NST, ST = G3_ST, A_BYTES = 16384, MB = 4, NB = 7;
  extern __shared__ __attribute__((aligned(16))) char shm[];
  const unsigned lds0 = (unsigned)(size_t)(LAS char*)shm;
  const unsigned vfull = lds0 + G3_SYNC, vfree = vfull + 32, vaux = vfull + 24;
  LAS f32x2* tab = (LAS f32x2*)(lds0 + G3_TAB);
  LAS float* cst = (LAS float*)(lds0 + G3_TAB + 2048);
  const unsigned w = rfl(threadIdx.x >> 6);
#define threadIdx_x_w w
  STAMP(ID, 0);
  const unsigned xcd = blockIdx.x & 7, idx = blockIdx.x >> 3;
  const unsigned tm = idx & 7, tn = xcd * 4 + (idx >> 3);
  const unsigned cls = (tm + 2 * (idx >> 3)) & 7;
  unsigned long long* gcs = (unsigned long long*)((char*)p.flags + FL_CS) + tn * (8 * 224);

  if (w >= 8) {
    if (LPRIO) __builtin_amdgcn_s_setprio(LPRIO);
    const unsigned lw = w - 8;
    const int lane = lane_id();
    const u32x4 qa = make_rsrc((const u16*)p.a0 + (size_t)tm * BM * K, BM * K * 2);
    const u32x4 qb = make_rsrc(lw < 2 ? p.b0 : p.b1, (unsigned)D_ * F_ * 2);
    unsigned va[4], vb[4];
#pragma unroll
    for (int i = 0; i < 4; ++i) {
      va[i] = (lw * 4 + i) * (K / 32) * 1024 + lane * 16 - i * 1024;
      const int c = ((lw & 1) * 4 + i) * 64 + lane, r = c / 14, pos = c % 14;
      const int k = (r & 0x13) | ((r & 4) << 1) | ((r & 8) >> 1);
      vb[i] = k * F_ * 2 + tn * 224 + pos * 16 - i * 1024;
    }
    const unsigned boff = A_BYTES + (lw >> 1) * G3_UP + (lw & 1) * 4096;
    auto stage = [&](unsigned n) {
      unsigned st = stage_of<NC3, NT, (WRAPM >> 3) & 1>(n, cls);
      return st;
    };
    auto issue_all = [&](int n) {
      const unsigned st = stage(n);
      dma_grp<4>(lds0 + n * ST + lw * 4096, va, qa, st * 1024);
      if (lw & 1) dma_grp<3>(lds0 + n * ST + boff, vb, qb, st * (32 * F_ * 2));
      else dma_grp<4>(lds0 + n * ST + boff, vb, qb, st * (32 * F_ * 2));
    };
    issue_all(0);
    issue_all(1);
    wait_vm<7>();
    barrier();
    issue_all(2);
    unsigned touch = 0;
#pragma unroll 1
    for (unsigned t = 0; t + NST - 1 < NT; ++t) {
      const unsigned st = stage(t + NST - 1), wbuf = lds0 + ((t + NST - 1) & (NST - 1)) * ST;
      if (t == 12) {
        const char* tp = (const char*)p.res + ((size_t)blockIdx.x * 224 + (lw * 56 + (lane < 56 ? lane : 55))) * 128;
        asm volatile("global_load_dword %0, %1, off" : "+v"(touch) : "v"(tp) : "memory");
      }
      dma_grp<4>(wbuf + lw * 4096, va, qa, st * 1024);
      barrier();
      if (lw & 1) dma_grp<3>(wbuf + boff, vb, qb, st * (32 * F_ * 2));
      else dma_grp<4>(wbuf + boff, vb, qb, st * (32 * F_ * 2));
      wait_vm<7 * (NST - 2)>();
      barrier();
    }
    barrier();
    wait_vm<7>();
    barrier();
    barrier();
    wait_vm<0>();
    {
      const unsigned u = lw * 64 + lane < 224 ? lw * 64 + lane : 223u;
      unsigned long long g[8];
#pragma unroll
      for (int i = 0; i < 8; ++i) g[i] = __hip_atomic_load(gcs + i * 224 + u, __ATOMIC_RELAXED, __HIP_MEMORY_SCOPE_AGENT);
      SB();
      barrier();
      barrier();
      barrier();
      SB();
#pragma unroll 1
      for (unsigned spins = 0; spins < (1u << 20); ++spins) {
        unsigned tg = 1u;
#pragma unroll
        for (int i = 0; i < 8; ++i) tg &= (unsigned)(g[i] >> 32);
        if (__all(tg == 1u)) break;
        __builtin_amdgcn_s_sleep(2);
#pragma unroll
        for (int i = 0; i < 8; ++i) g[i] = __hip_atomic_load(gcs + i * 224 + u, __ATOMIC_RELAXED, __HIP_MEMORY_SCOPE_AGENT);
      }
      float s = 0.f;
#pragma unroll
      for (int i = 0; i < 8; ++i) s += __uint_as_float((unsigned)g[i]);
      cst[u] = s;
      wait_lgkm0();
    }
    asm volatile("" ::"v"(touch));
    return;
  }

  const unsigned wm = w & 3, wn = w >> 2;
  const int lane = lane_id(), fr = lane & 15, fq = lane >> 4;
  STAMPX(ID, 7);
  const unsigned rot = wm == 3 ? 0u : wm;
  unsigned xa = lds0 + wm * 4096 + lane * 16, xb[3], xb1;
  {
    const int q = fr >> 2, pp = fr & 3, r = (fq >> 1) * 16 + (fq & 1) * 4 + q;
#pragma unroll
    for (int i = 0; i < 3; ++i) xb[i] = lds0 + A_BYTES + r * 224 + wn * 112 + pp * 8 + ((i + rot) % 3) * 32;
    xb1 = lds0 + A_BYTES + r * 224 + wn * 112 + 96 + (pp & 1) * 8 + (pp >> 1) * G3_UP;
  }
  float cs[3] = {0.f, 0.f, 0.f};
  if (wn == 1) {
    const f32x4* sp = (const f32x4*)p.stats + tm * BM + wm * 64 + lane;
    f32x4 sv[8];
#pragma unroll
    for (int i = 0; i < 8; ++i) sv[i] = sp[(size_t)i * S_];
    SB();
    barrier();
    SB();
    float m = 0.f;
#pragma unroll
    for (int i = 0; i < 8; ++i) m += sv[i][0] + sv[i][2];
    m *= (1.0f / 16.0f);
    float q = 0.f;
#pragma unroll
    for (int i = 0; i < 8; ++i) {
      const float d0 = sv[i][0] - m, d1 = sv[i][2] - m;
      q += sv[i][1] + sv[i][3] + 64.0f * (d0 * d0 + d1 * d1);
    }
    tab[wm * 64 + lane] = (f32x2){m, __builtin_amdgcn_rsqf(q * (1.0f / D_) + EPS)};
  }
  bf16x8 fa[MB], fb[NB];
  f32x4 acc[MB][NB];
#pragma unroll
  for (int mb = 0; mb < MB; ++mb)
#pragma unroll
    for (int nb = 0; nb < NB; ++nb) { acc[mb][nb] = (f32x4){0.f, 0.f, 0.f, 0.f}; asm volatile("" : "+v"(acc[mb][nb])); }
  asm volatile("" : "+v"(xa), "+v"(xb[0]), "+v"(xb[1]), "+v"(xb[2]), "+v"(xb1));
  wait_lgkm0();
  barrier();
  STAMP1_TAKE();
  unsigned slot = 0;
  const unsigned csmask = (0x010101u << cls) | (1u << ((cls + 1) & 7));
#pragma unroll 1
  for (unsigned t = 0; t < NT; ++t) {
    if (t == 24) {
      const int l2 = lane_id();
      unsigned tag = 1u;
      asm volatile("" : "+v"(tag));
      const float c0 = sum_rows(cs[0]), c1 = sum_rows(cs[1]), c2 = sum_rows(cs[2]);
      const int r = l2 & 15, q = l2 >> 4;
      const float val = wm == 3 ? c2 : (q ? c1 : c0);
      const int col = wm == 3 ? (r >> 3) * 112 + wn * 56 + 48 + (r & 7) : q * 112 + wn * 56 + rot * 16 + r;
      if (q < (wm == 3 ? 1 : 2))
        __hip_atomic_store(gcs + cls * 224 + col, ((unsigned long long)tag << 32) | __float_as_uint(val), __ATOMIC_RELAXED, __HIP_MEMORY_SCOPE_AGENT);
    }
    SB();
    fa[0] = lds_b128(xa);
#pragma unroll
    for (int j = 0; j < 7; ++j) {
      const unsigned o = j < 6 ? xb[j % 3] + (j / 3) * G3_UP : xb1;
      fb[j] = lds_tr(o, o + 1792);
    }
#pragma unroll
    for (int mb = 1; mb < MB; ++mb) fa[mb] = lds_b128(xa + mb * 1024);
    SB();
    slot = (slot + 1 == NST) ? 0u : slot + 1;
    const unsigned delta = (slot == 0) ? (unsigned)(-(NST - 1) * ST) : (unsigned)ST;
    xa += delta; xb[0] += delta; xb[1] += delta; xb[2] += delta; xb1 += delta;
    asm volatile("" : "+v"(xa), "+v"(xb[0]), "+v"(xb[1]), "+v"(xb[2]), "+v"(xb1));
    wait_lgkm0();
    barrier();
    SB();
    if (CPRIO == 2) __builtin_amdgcn_s_setprio(1);
#pragma unroll
    for (int mb = 0; mb < MB; ++mb)
#pragma unroll
      for (int nb = 0; nb < NB; ++nb) MFMA(fb[nb], fa[mb], acc[mb][nb]);
    if (CPRIO == 2) __builtin_amdgcn_s_setprio(0);
    SB();
    barrier();
    SB();
    if ((csmask >> t) & 1u) {
#pragma unroll
      for (int j = 0; j < 4; ++j)
#pragma unroll
        for (int i = 0; i < 3; ++i)
          asm volatile("v_dot2c_f32_bf16 %0, %1, %2" : "+v"(cs[i]) : "s"(0x3f803f80u), "v"(__builtin_bit_cast(u32x4, fb[3 * i])[j]));
      asm volatile("s_nop 2");
    }
  }
  if (wn == 0) barrier();
  mfma_drain();
  SB();
  const int lane2 = lane_id();
  const int gr = lane2 & 15, gq = lane2 >> 4;
  STAMP1_PUT(ID);
  STAMP(ID, 2);
  STAMPX(ID, 0);
  auto act = [](float g, float u) { return g * u * __builtin_amdgcn_rcpf(1.0f + __expf(-g)); };
  const auto ro = OUT_RSRC(p.out, S_ * F_ * 2);
  f32x2 st[MB];
#pragma unroll
  for (int mb = 0; mb < MB; ++mb) st[mb] = tab[wm * 64 + mb * 16 + gr];
  const int gch0 = tn * 14 + wn * 7;
#pragma unroll
  for (int i = 0; i < 3; ++i) {
    const unsigned ib = (i + rot) % 3;
    const f32x4 cg = *(const LAS f32x4*)(cst + wn * 56 + ib * 16 + gq * 4), cu = *(const LAS f32x4*)(cst + 112 + wn * 56 + ib * 16 + gq * 4);
    u32x2 pkd[MB];
#pragma unroll
    for (int mb = 0; mb < MB; ++mb) {
      const f32x4 g = (acc[mb][i] - cg * st[mb].x) * st[mb].y, u = (acc[mb][3 + i] - cu * st[mb].x) * st[mb].y;
      pkd[mb] = (u32x2){pk_bf16(act(g[0], u[0]), act(g[1], u[1])), pk_bf16(act(g[2], u[2]), act(g[3], u[3]))};
    }
#pragma unroll
    for (int mp = 0; mp < MB / 2; ++mp) {
      auto s0 = __builtin_amdgcn_permlane16_swap(pkd[2 * mp].x, pkd[2 * mp + 1].x, false, false);
      auto s1 = __builtin_amdgcn_permlane16_swap(pkd[2 * mp].y, pkd[2 * mp + 1].y, false, false);
      const u32x4 v = {s0[0], s1[0], s0[1], s1[1]};
      const int rblk = tm * (BM / 16) + wm * MB + 2 * mp + (gq & 1);
      const int gch = gch0 + 2 * ib + (gq >> 1);
      STORE16(ro, (rblk * (F_ / 32) + (gch >> 2)) * 1024 + ((gch & 3) * 16 + gr) * 16, v);
    }
  }
  {
    const f32x4 cm = *(const LAS f32x4*)(cst + (gq >> 1) * 112 + wn * 56 + 48 + (gq & 1) * 4);
    u32x2 pkd[MB / 2];
#pragma unroll
    for (int mp = 0; mp < MB / 2; ++mp) {
      const f32x4 v0 = (acc[2 * mp][6] - cm * st[2 * mp].x) * st[2 * mp].y, v1 = (acc[2 * mp + 1][6] - cm * st[2 * mp + 1].x) * st[2 * mp + 1].y;
      float r[4];
#pragma unroll
      for (int j = 0; j < 4; ++j) {
        auto s = __builtin_amdgcn_permlane32_swap(__float_as_uint(v0[j]), __float_as_uint(v1[j]), false, false);
        r[j] = act(__uint_as_float(s[0]), __uint_as_float(s[1]));
      }
      pkd[mp] = (u32x2){pk_bf16(r[0], r[1]), pk_bf16(r[2], r[3])};
    }
    auto s0 = __builtin_amdgcn_permlane16_swap(pkd[0].x, pkd[1].x, false, false);
    auto s1 = __builtin_amdgcn_permlane16_swap(pkd[0].y, pkd[1].y, false, false);
    const u32x4 v = {s0[0], s1[0], s0[1], s1[1]};
    const int rblk = tm * (BM / 16) + wm * MB + (gq & 1) * 2 + (gq >> 1);
    const int gch = gch0 + 6;
    STORE16(ro, (rblk * (F_ / 32) + (gch >> 2)) * 1024 + ((gch & 3) * 16 + gr) * 16, v);
  }
  STAMPX(ID, 4);
  STAMP(ID, 3);
}

template <int ID>
void launch_sq(const GArgs& a, hipStream_t stream) {
  (void)hipFuncSetAttribute((const void*)k_sq<ID>, hipFuncAttributeMaxDynamicSharedMemorySize, SQ_LDS);
  hipLaunchKernelGGL(k_sq<ID>, dim3(256), dim3(512), SQ_LDS, stream, a);
}
}

extern "C" void kernel_launch(void* const* d_in, const int* in_sizes, int n_in, void* d_out, int out_size, void* d_ws, size_t ws_size,
                              hipStream_t stream) {
  char* ws = (char*)d_ws;
  GArgs g1 = {d_in[3], d_in[4], d_in[0], d_in[6], ws + WS_AB, d_in[1], d_in[2], d_in[7], ws + WS_FL};
  launch_sq<1>(g1, stream);
  GArgs g2 = {d_in[1], d_in[2], ws + WS_AB, nullptr, ws + WS_H, d_in[0], ws + WS_ST, ws + WS_XC, ws + WS_FL};
  launch_sq<2>(g2, stream);
  GArgs g3 = {ws + WS_H, nullptr, d_in[6], d_in[7], ws + WS_GU, d_in[8], ws + WS_ST, nullptr, ws + WS_FL};
  (void)hipFuncSetAttribute((const void*)k_glu, hipFuncAttributeMaxDynamicSharedMemorySize, G3_LDS);
  hipLaunchKernelGGL(k_glu, dim3(256), dim3(768), G3_LDS, stream, g3);
  GArgs g4 = {ws + WS_GU, nullptr, d_in[8], nullptr, d_out, ws + WS_H, nullptr, ws + WS_XC, ws + WS_FL};
  launch_sq<4>(g4, stream);
}
```

```cpp
#include <hip/hip_runtime.h>
#include <hip/hip_bf16.h>

namespace {
#define LAS __attribute__((address_space(3)))
typedef unsigned short u16;
typedef short s16x4 __attribute__((ext_vector_type(4)));
typedef short s16x8 __attribute__((ext_vector_type(8)));
typedef __bf16 bf16x8 __attribute__((ext_vector_type(8)));
typedef __bf16 bf16x2 __attribute__((ext_vector_type(2)));
typedef float f32x4 __attribute__((ext_vector_type(4)));
typedef float f32x2 __attribute__((ext_vector_type(2)));
typedef unsigned u32x4 __attribute__((ext_vector_type(4)));
typedef unsigned u32x2 __attribute__((ext_vector_type(2)));

constexpr int S_ = 2048, D_ = 1024, F_ = 3584;
constexpr float EPS = 1e-5f;

constexpr size_t WS_AB = 0;
constexpr size_t WS_H = WS_AB + (size_t)D_ * 2 * S_ * 2;
constexpr size_t WS_GU = WS_H + (size_t)S_ * D_ * 2;
constexpr size_t WS_ST = WS_GU + (size_t)S_ * F_ * 2;
constexpr size_t WS_XC = WS_ST + (size_t)S_ * 32 * 8;
constexpr size_t WS_FL = WS_XC + (size_t)2 * 256 * 36864;
constexpr int FL_CS = 2 * 256 * 128;

__device__ __forceinline__ unsigned rfl(unsigned v) { return __builtin_amdgcn_readfirstlane(v); }
__device__ __forceinline__ unsigned pk_bf16(float lo, float hi) {
  bf16x2 c = {(__bf16)lo, (__bf16)hi};
  return __builtin_bit_cast(unsigned, c);
}
__device__ __forceinline__ float bf_lo(unsigned v) { return __uint_as_float(v << 16); }
__device__ __forceinline__ float bf_hi(unsigned v) { return __uint_as_float(v & 0xffff0000u); }

__device__ __forceinline__ u32x4 make_rsrc(const void* p, unsigned bytes) {
  unsigned long long a = (unsigned long long)p;
  u32x4 r;
  r.x = (unsigned)a;
  r.y = (unsigned)(a >> 32) & 0xffffu;
  r.z = bytes;
  r.w = 0x00020000u;
  return r;
}
__device__ __forceinline__ void dma16(unsigned ldsaddr, unsigned voff, u32x4 rsrc, unsigned soff) {
  asm volatile("s_mov_b32 m0, %0\n\ts_nop 4\n\tbuffer_load_dwordx4 %1, %2, %3 offen lds" ::"s"(ldsaddr), "v"(voff), "s"(rsrc), "s"(soff)
               : "memory");
}
template <int N>
__device__ __forceinline__ void wait_vm() {
  asm volatile("s_waitcnt vmcnt(%0)" ::"i"(N) : "memory");
}
__device__ __forceinline__ void wait_lgkm0() { __builtin_amdgcn_s_waitcnt(0xC07F); }
__device__ __forceinline__ void barrier() {
  asm volatile("" ::: "memory");
  __builtin_amdgcn_s_barrier();
  asm volatile("" ::: "memory");
}
__device__ __forceinline__ bf16x8 lds_b128(unsigned addr) { return *(const LAS bf16x8*)(addr); }
__device__ __forceinline__ bf16x8 lds_tr(unsigned a0, unsigned a1) {
  s16x4 lo = __builtin_amdgcn_ds_read_tr16_b64_v4i16((LAS s16x4*)(a0));
  s16x4 hi = __builtin_amdgcn_ds_read_tr16_b64_v4i16((LAS s16x4*)(a1));
  s16x8 f = __builtin_shufflevector(lo, hi, 0, 1, 2, 3, 4, 5, 6, 7);
  return __builtin_bit_cast(bf16x8, f);
}

#define ST_AUX 16
#define OUT_RSRC(ptr, bytes) __builtin_amdgcn_make_buffer_rsrc((void*)(ptr), 0, (bytes), 0x00020000)
#define STORE16(rs, byteoff, v) __builtin_amdgcn_raw_buffer_store_b128(v, rs, byteoff, 0, ST_AUX)
#define STAMP(k, i)
#define STAMPX(k, i)
#define STAMPL(k, i, first)

struct GArgs {
  const void* a0;
  const void* a1;
  const void* b0;
  const void* b1;
  void* out;
  const void* res;
  void* stats;
  void* xch;
  void* flags;
};

template <int N>
__device__ __forceinline__ void dma_grp(unsigned ldsaddr, const unsigned (&v)[4], u32x4 rsrc, unsigned soff) {
  if constexpr (N == 4)
    asm volatile("s_mov_b32 m0, %0\n\ts_nop 4\n\tbuffer_load_dwordx4 %1, %5, %6 offen lds\n\tbuffer_load_dwordx4 %2, %5, %6 offen offset:1024 lds\n\t"
                 "buffer_load_dwordx4 %3, %5, %6 offen offset:2048 lds\n\tbuffer_load_dwordx4 %4, %5, %6 offen offset:3072 lds" ::"s"(ldsaddr),
                 "v"(v[0]), "v"(v[1]), "v"(v[2]), "v"(v[3]), "s"(rsrc), "s"(soff)
                 : "memory");
  else
    asm volatile("s_mov_b32 m0, %0\n\ts_nop 4\n\tbuffer_load_dwordx4 %1, %4, %5 offen lds\n\tbuffer_load_dwordx4 %2, %4, %5 offen offset:1024 lds\n\t"
                 "buffer_load_dwordx4 %3, %4, %5 offen offset:2048 lds" ::"s"(ldsaddr),
                 "v"(v[0]), "v"(v[1]), "v"(v[2]), "s"(rsrc), "s"(soff)
                 : "memory");
}
__device__ __forceinline__ void lds_inc(unsigned addr) { asm volatile("ds_add_u32 %0, %1" ::"v"(addr), "v"(1u) : "memory"); }
__device__ __forceinline__ unsigned inc_base(unsigned sync, int lane) { return sync + lane * 68; }
__device__ __forceinline__ unsigned lds_peek(unsigned addr) { return *(volatile LAS unsigned*)(addr); }
__device__ __forceinline__ void lds_wait(unsigned addr, unsigned pv, unsigned need) {
  if (__builtin_expect(rfl(pv) < need, 0)) {
    do {
      __builtin_amdgcn_s_sleep(1);
    } while (rfl(lds_peek(addr)) < need);
  }
  asm volatile("" ::: "memory");
}
__device__ __forceinline__ float sum_rows(float x) {
  auto a = __builtin_amdgcn_permlane16_swap(__float_as_uint(x), __float_as_uint(x), false, false);
  x = __uint_as_float(a[0]) + __uint_as_float(a[1]);
  auto b = __builtin_amdgcn_permlane32_swap(__float_as_uint(x), __float_as_uint(x), false, false);
  return __uint_as_float(b[0]) + __uint_as_float(b[1]);
}
__device__ __forceinline__ float sum_pair(float x) { return x + __uint_as_float(__builtin_amdgcn_mov_dpp(__float_as_uint(x), 0xB1, 0xF, 0xF, true)); }
__device__ __forceinline__ int lane_id() {
  int l;
  asm volatile("v_mbcnt_lo_u32_b32 %0, -1, 0\n\tv_mbcnt_hi_u32_b32 %0, -1, %0" : "=v"(l));
  return l;
}
__device__ __forceinline__ void mfma_drain() { asm volatile("s_nop 15" ::: "memory"); }
__device__ __forceinline__ void fadd(float& a, float b) { asm("v_add_f32 %0, %1, %0" : "+v"(a) : "v"(b)); }
template <int NC, int NT, int WRAP>
__device__ __forceinline__ unsigned stage_of(unsigned n, unsigned c) {
  if (NC == 1) return n;
  if (WRAP) {
    const unsigned t = ((n & (NC - 1)) == c) ? n + NC : n;
    return rfl(t >= NT ? t - NT : t);
  }
  unsigned t = (n > NT - NC + c) ? n : n - 1;
  t = ((t & (NC - 1)) == c) ? t + NC : t;
  return rfl(n == 0 ? c : t);
}
#define NC1 4
#define NC2 8
#define NC3 8
#define NC4 4
#define TOUCH_C 0
#define TOUCH_W 0
#define TOUCH_AT 6
#define XSTR 16384
#define WRAPM 0x18
#define SQ_PD 2
#define BAR_AT 0
#define CPRIO 0
template <int V> struct IC { static constexpr int value = V; };
#define LPRIO 0
#define SB() __builtin_amdgcn_sched_barrier(0)
#define MFMA(b, a, c) asm volatile("v_mfma_f32_16x16x32_bf16 %0, %1, %2, %0" : "+v"(c) : "v"(b), "v"(a))
#define STAMP1_TAKE()
#define STAMP1_PUT(k)

#define CODE_LABEL(name) asm volatile(#name ":")
#define CODE_ADDR(dst, name) do { int o_; asm volatile("s_getpc_b64 %0\n\ts_mov_b32 %1, " #name "@rel32@lo+4" : "=s"(dst), "=s"(o_)); dst += (long long)o_; } while (0)
#define CODE_PF 3
__device__ __forceinline__ void touch_code(unsigned long long base, int lane, int last, unsigned& sink) {
  const unsigned off = (lane < last ? lane : last) * 128;
  asm volatile("global_load_dword %0, %1, %2" : "+v"(sink) : "v"(off), "s"(base) : "memory");
}
enum { OP_RM = 0, OP_FT = 1, OP_NN = 2 };
template <int ID> struct Cfg;
template <> struct Cfg<1> { static constexpr int NT = 16, AM = OP_RM, BMODE = OP_RM, NC = NC1; };
template <> struct Cfg<2> { static constexpr int NT = 32, AM = OP_RM, BMODE = OP_FT, NC = NC2; };
template <> struct Cfg<4> { static constexpr int NT = 28, AM = OP_FT, BMODE = OP_NN, NC = NC4; };
constexpr int SQ_NST = 4, SQ_ST = 32768, SQ_SYNC = SQ_NST * SQ_ST, SQ_TAB = SQ_SYNC + 4608, SQ_LDS = SQ_TAB + 2048;

template <int ID>
__global__ __launch_bounds__(512, 2) void k_sq(GArgs p) {
  if constexpr (ID == 1) CODE_LABEL(fz_code_g1);
  if constexpr (ID == 2) CODE_LABEL(fz_code_g2);
  if constexpr (ID == 4) CODE_LABEL(fz_code_g4);
  using C = Cfg<ID>;
  constexpr int NT = C::NT, NST = SQ_NST, ST = SQ_ST, A_BYTES = 16384;
  extern __shared__ __attribute__((aligned(16))) char shm[];
  const unsigned lds0 = (unsigned)(size_t)(LAS char*)shm;
  const unsigned vfull = lds0 + SQ_SYNC, vfree = lds0 + SQ_SYNC + 32;
  const int tid = threadIdx.x, lane = tid & 63;
  const unsigned w = rfl(tid >> 6);
  STAMP(ID, 0);
  const int fr = lane & 15, fq = lane >> 4;
  const unsigned ifull = inc_base(vfull, lane), ifree = ifull + 32;
  const unsigned xcd = blockIdx.x & 7, idx = blockIdx.x >> 3, li = idx & 3, lj = idx >> 2;
  const unsigned kh = (ID == 1) ? 0u : (xcd & 1);
  const unsigned tm = (xcd >> 1) * 4 + li, tn = (ID == 1) ? (xcd & 1) * 8 + lj : lj;

  if (w >= 4) {
    if (LPRIO) __builtin_amdgcn_s_setprio(LPRIO);
    const unsigned lw = w - 4;
    u32x4 qa, qb;
    unsigned va[4], vb[4], kstepA, kstepB;
    if constexpr (ID == 1) {
      qa = make_rsrc((const u16*)(tm < 8 ? p.a0 : p.a1) + (size_t)(tm & 7) * 128 * D_, 128 * D_ * 2);
      qb = make_rsrc((const u16*)p.b0 + (size_t)tn * 128 * D_, 128 * D_ * 2);
    } else if constexpr (ID == 2) {
      qa = make_rsrc((const u16*)(kh ? p.a1 : p.a0) + (size_t)tm * 128 * S_, 128 * S_ * 2);
      qb = make_rsrc((const u16*)p.b0 + (size_t)tn * 128 * 4096 + kh * 64 * 512, 128 * 4096 * 2 - kh * 64 * 1024);
    } else {
      qa = make_rsrc((const u16*)p.a0 + (size_t)tm * 128 * F_ + kh * 56 * 512, 128 * F_ * 2 - kh * 56 * 1024);
      qb = make_rsrc((const u16*)p.b0 + (size_t)kh * 1792 * D_, 1792 * D_ * 2);
    }
#pragma unroll
    for (int i = 0; i < 4; ++i) {
      const int pc = lw * 4 + i;
      const int row = pc * 8 + (lane >> 3), rsw = ((lane & 7) ^ ((row >> 1) & 7)) << 4;
      if constexpr (C::AM == OP_RM) va[i] = row * (ID == 1 ? D_ : S_) * 2 + rsw - i * 1024;
      else va[i] = ((pc >> 1) * (F_ / 32) + (pc & 1)) * 1024 + lane * 16 - i * 1024;
      if constexpr (C::BMODE == OP_RM) vb[i] = row * D_ * 2 + rsw - i * 1024;
      else if constexpr (C::BMODE == OP_FT) vb[i] = ((pc >> 1) * 128 + (pc & 1)) * 1024 + lane * 16 - i * 1024;
      else {
        const int k = pc * 4 + (lane >> 4), f = (k & 3) | (((k >> 3) & 1) << 2);
        vb[i] = k * D_ * 2 + tn * 256 + (((lane & 15) ^ (f << 1)) << 4) - i * 1024;
      }
    }
    kstepA = (C::AM == OP_RM) ? 128 : 2048;
    kstepB = (C::BMODE == OP_RM) ? 128 : (C::BMODE == OP_FT ? 2048 : 64 * D_ * 2);
    const unsigned cls = (C::NC == 8) ? ((lj + 2 * li) & 7) : ((lj + li) & (C::NC - 1));
    auto issue = [&](unsigned n) {
      unsigned st = stage_of<C::NC, NT, (WRAPM >> ID) & 1>(n, cls);
      const unsigned wbuf = lds0 + (n & (NST - 1)) * ST + lw * 4096;
      dma_grp<4>(wbuf, va, qa, st * kstepA);
      dma_grp<4>(wbuf + A_BYTES, vb, qb, st * kstepB);
    };
    unsigned touch = 0;
#pragma unroll
    for (int n = 0; n < SQ_PD; ++n) issue(n);
#pragma unroll 1
    for (unsigned b = 0; b + SQ_PD < NT; ++b) {
      wait_vm<8 * (SQ_PD - 1)>();
      barrier();
      if constexpr (ID == 1) {
        if (TOUCH_C && b >= TOUCH_AT && b < TOUCH_AT + TOUCH_C) {
          const char* tp = (const char*)((xcd & 1) ? p.stats : p.res) + (size_t)((xcd >> 1) * 512 + idx * 16 + lw * 4 + (lane >> 4)) * (S_ * 2) + (b - TOUCH_AT) * 2048 + (lane & 15) * 128;
          asm volatile("global_load_dword %0, %1, off" : "+v"(touch) : "v"(tp) : "memory");
        }
        if (TOUCH_W && b >= TOUCH_AT + TOUCH_C && b < TOUCH_AT + TOUCH_C + TOUCH_W) {
          const unsigned ln = ((b - TOUCH_AT - TOUCH_C) * 512 + (blockIdx.x >> 1) * 4 + lw) * 56 + (lane < 56 ? lane : 55);
          const char* tp = (const char*)((blockIdx.x & 1) ? p.xch : p.b1) + (size_t)ln * 128;
          asm volatile("global_load_dword %0, %1, off" : "+v"(touch) : "v"(tp) : "memory");
        }
      }
      issue(b + SQ_PD);
    }
    if (SQ_PD == 3) { wait_vm<16>(); barrier(); }
    wait_vm<8>();
    barrier();
    if constexpr (ID != 4) {
      if ((CODE_PF & 2) && lw == 0 && (idx & 7) == 0) {
        unsigned long long a;
        if constexpr (ID == 1) CODE_ADDR(a, fz_code_g2); else CODE_ADDR(a, fz_code_g3);
        touch_code(a, lane, 61, touch);
      }
    }
    wait_vm<0>();
    asm volatile("" ::"v"(touch));
    barrier();
    return;
  }

  const unsigned wm = w >> 1, wx = w & 1;
  unsigned csink = 0;
  if constexpr (ID == 1) {
    if ((CODE_PF & 1) && w == 0 && idx < 4) {
      unsigned long long a1, a2, a3, a4;
      CODE_ADDR(a1, fz_code_g1); CODE_ADDR(a2, fz_code_g2); CODE_ADDR(a3, fz_code_g3); CODE_ADDR(a4, fz_code_g4);
      touch_code(idx == 0 ? a1 : (idx == 1 ? a2 : (idx == 2 ? a3 : a4)), lane, idx == 3 ? 50 : 61, csink);
    }
    if (tid < 2) *(unsigned*)((char*)p.flags + (tid * 256 + blockIdx.x) * 128) = 0u;
    if (tid < 224) *(unsigned long long*)((char*)p.flags + FL_CS + (blockIdx.x * 224 + tid) * 8) = 0ull;
  }
  const int chunk = ((fq & 1) << 1) | (fq >> 1);
  u32x4 res[4];
  if constexpr (ID != 1) {
#pragma unroll
    for (int mb = 0; mb < 4; ++mb) {
      const int row = tm * 128 + wm * 64 + mb * 16 + fr, col = tn * 128 + wx * 64 + kh * 32 + chunk * 8;
      if constexpr (ID == 2) res[mb] = *(const u32x4*)((const u16*)p.res + (size_t)row * D_ + col);
      else res[mb] = *(const u32x4*)((const u16*)p.res + ((size_t)(row >> 4) * (D_ / 32) + (col >> 5)) * 512 + (chunk * 16 + fr) * 8);
    }
  }
  const unsigned rotA = (ID == 1) ? 2 * wx : 0u, rotB = (ID == 1) ? 2 * wm : 2 * kh;
  unsigned rdA[4], rdB[4];
#pragma unroll
  for (int i = 0; i < 4; ++i) {
    const unsigned kk = i >> 1, hf = (i & 1) * 2, sw = ((kk * 4 + fq) ^ (fr >> 1)) << 4;
    if constexpr (C::AM == OP_RM) rdA[i] = (wm * 64 + (hf ^ rotA) * 16 + fr) * 128 + sw;
    else rdA[i] = wm * 8192 + lane * 16;
    if constexpr (C::BMODE == OP_RM) rdB[i] = A_BYTES + (wx * 64 + (hf ^ rotB) * 16 + fr) * 128 + sw;
    else if constexpr (C::BMODE == OP_FT) rdB[i] = A_BYTES + wx * 8192 + (hf ^ rotB) * 2048 + lane * 16;
    else {
      const int q = fr >> 2, pp = fr & 3, f = q | ((fq & 1) << 2);
      rdB[i] = A_BYTES + (8 * fq + q) * 256 + (((wx * 4 + (i ^ rotB)) ^ f) << 5) + pp * 8;
    }
  }
  unsigned rdA2[4], rdB2[4];
#pragma unroll
  for (int i = 0; i < 4; ++i) {
    rdA[i] += lds0; rdB[i] += lds0;
    rdA2[i] = rdA[i] + 2 * ST; rdB2[i] = rdB[i] + 2 * ST;
    asm volatile("" : "+v"(rdA2[i]), "+v"(rdB2[i]), "+v"(rdA[i]), "+v"(rdB[i]));
  }
  bf16x8 fa[2][2][4], fb[2][2][4];
  auto read_frag = [&](int j, bf16x8(&a)[2][4], bf16x8(&b)[2][4], const unsigned(&xa)[4], const unsigned(&xb)[4], int so) {
    const int s = j >> 3, i = j & 3;
    if (j & 4) {
      if constexpr (C::AM == OP_RM) a[s][i] = lds_b128(xa[s * 2 + (i >> 1)] + (i & 1) * 2048 + so);
      else a[s][i] = lds_b128(xa[0] + i * 2048 + s * 1024 + so);
    } else {
      if constexpr (C::BMODE == OP_RM) b[s][i] = lds_b128(xb[s * 2 + (i >> 1)] + (i & 1) * 2048 + so);
      else if constexpr (C::BMODE == OP_FT) b[s][i] = lds_b128(xb[i >> 1] + (i & 1) * 2048 + s * 1024 + so);
      else b[s][i] = lds_tr(xb[i] + s * 8192 + so, xb[i] + s * 8192 + 1024 + so);
    }
  };
  f32x4 acc[4][4], axs[2], axq[2], aws[2];
#pragma unroll
  for (int i = 0; i < 16; ++i) { acc[i >> 2][i & 3] = (f32x4){0.f, 0.f, 0.f, 0.f}; asm volatile("" : "+v"(acc[i >> 2][i & 3])); }
#pragma unroll
  for (int i = 0; i < 2; ++i) {
    axs[i] = axq[i] = aws[i] = (f32x4){0.f, 0.f, 0.f, 0.f};
    if (ID == 1) asm volatile("" : "+v"(axs[i]), "+v"(axq[i]), "+v"(aws[i]));
  }
  bf16x8 ones = __builtin_bit_cast(bf16x8, (u32x4){0x3f803f80u, 0x3f803f80u, 0x3f803f80u, 0x3f803f80u});
  asm volatile("" : "+v"(ones));
  auto step = [&](const bf16x8(&ca)[2][4], const bf16x8(&cb)[2][4], bf16x8(&na)[2][4], bf16x8(&nb)[2][4], auto slc) {
    constexpr int SL = decltype(slc)::value & 3;
    constexpr bool RD = decltype(slc)::value < 4;
#pragma unroll
    for (int i = 0; i < 32; ++i) {
      if (RD && i == BAR_AT) { barrier(); SB(); }
      if constexpr (ID == 1) {
        if ((i & 15) == 15) {
#pragma unroll
          for (int j = 0; j < 2; ++j) {
            MFMA(cb[i >> 4][j], ones, axs[j]);
            MFMA(cb[i >> 4][j], cb[i >> 4][j], axq[j]);
            MFMA(ones, ca[i >> 4][j], aws[j]);
          }
          SB();
        }
      }
      MFMA(cb[i >> 4][i & 3], ca[i >> 4][(i >> 2) & 3], acc[(i >> 2) & 3][i & 3]);
      SB();
      if (RD && i >= BAR_AT && i < BAR_AT + 16) {
        if (SL < 2) read_frag(i - BAR_AT, na, nb, rdA, rdB, SL * ST);
        else read_frag(i - BAR_AT, na, nb, rdA2, rdB2, (SL - 2) * ST);
      }
      SB();
    }
    wait_lgkm0();
  };
  if constexpr (ID == 1) asm volatile("s_waitcnt vmcnt(0)" : "+v"(csink));
  barrier();
  STAMP1_TAKE();
#pragma unroll
  for (int j = 0; j < 16; ++j) read_frag(j, fa[0], fb[0], rdA, rdB, 0);
  wait_lgkm0();
#pragma unroll 1
  for (unsigned t = 0; t < NT - 4; t += 4) {
    step(fa[0], fb[0], fa[1], fb[1], IC<1>{});
    step(fa[1], fb[1], fa[0], fb[0], IC<2>{});
    step(fa[0], fb[0], fa[1], fb[1], IC<3>{});
    step(fa[1], fb[1], fa[0], fb[0], IC<0>{});
  }
  step(fa[0], fb[0], fa[1], fb[1], IC<1>{});
  step(fa[1], fb[1], fa[0], fb[0], IC<2>{});
  step(fa[0], fb[0], fa[1], fb[1], IC<3>{});
  step(fa[1], fb[1], fa[0], fb[0], IC<4>{});
  mfma_drain();
  SB();
  STAMP1_PUT(ID);
  STAMP(ID, 2);

  if constexpr (ID == 1) {
    LAS float* xs = (LAS float*)(lds0 + SQ_TAB);
    LAS float* xq = xs + 128;
    LAS float* wsum = xs + 256;
#pragma unroll
    for (int j = 0; j < 2; ++j) {
      const unsigned nb = wx * 64 + (j ^ rotB) * 16, mb = wm * 64 + (j ^ rotA) * 16;
      if (fr == 0) *(LAS f32x4*)(xs + nb + fq * 4) = axs[j];
      const float dg = (fr & 2) ? ((fr & 1) ? axq[j][3] : axq[j][2]) : ((fr & 1) ? axq[j][1] : axq[j][0]);
      if (fq == (fr >> 2)) xq[nb + fr] = dg;
      if (fq == 0) wsum[mb + fr] = aws[j][0];
    }
    wait_lgkm0();
    barrier();
    STAMPX(ID, 0);
    const float sg = tm < 8 ? 1.f : -1.f;
    f32x4 mean[4], rstd[4];
    float wsm[4];
#pragma unroll
    for (int j = 0; j < 4; ++j) {
      const unsigned nb = wx * 64 + (j ^ rotB) * 16 + fq * 4;
      mean[j] = *(const LAS f32x4*)(xs + nb) * (1.0f / D_);
      const f32x4 var = *(const LAS f32x4*)(xq + nb) * (1.0f / D_) - mean[j] * mean[j];
#pragma unroll
      for (int r = 0; r < 4; ++r) rstd[j][r] = sg * __builtin_amdgcn_rsqf(fmaxf(var[r], 0.f) + EPS);
      wsm[j] = wsum[wm * 64 + (j ^ rotA) * 16 + fr];
    }
    const auto ro = OUT_RSRC(p.out, D_ * 4096 * 2);
#pragma unroll
    for (int mb = 0; mb < 4; ++mb)
#pragma unroll
      for (int np = 0; np < 2; ++np) {
        const f32x4 x0 = (acc[mb][2 * np] - mean[2 * np] * wsm[mb]) * rstd[2 * np], x1 = (acc[mb][2 * np + 1] - mean[2 * np + 1] * wsm[mb]) * rstd[2 * np + 1];
        auto s0 = __builtin_amdgcn_permlane16_swap(pk_bf16(x0[0], x0[1]), pk_bf16(x1[0], x1[1]), false, false);
        auto s1 = __builtin_amdgcn_permlane16_swap(pk_bf16(x0[2], x0[3]), pk_bf16(x1[2], x1[3]), false, false);
        const u32x4 v = {s0[0], s1[0], s0[1], s1[1]};
        const int rblk = (tm & 7) * 8 + wm * 4 + (mb ^ rotA);
        const int kblk = (tm >> 3) * 64 + tn * 4 + wx * 2 + (np ^ (rotB >> 1));
        STORE16(ro, (rblk * 128 + kblk) * 1024 + (chunk * 16 + fr) * 16, v);
      }
  } else {
    constexpr int XI = (ID == 2) ? 0 : 1;
    const auto rs = __builtin_amdgcn_make_buffer_rsrc((char*)p.xch + (size_t)XI * 256 * 36864, 0, 256 * 36864, 0x00020000);
#pragma unroll
    for (int mb = 0; mb < 4; ++mb) {
      auto s0 = __builtin_amdgcn_permlane16_swap(pk_bf16(acc[mb][2][0], acc[mb][2][1]), pk_bf16(acc[mb][3][0], acc[mb][3][1]), false, false);
      auto s1 = __builtin_amdgcn_permlane16_swap(pk_bf16(acc[mb][2][2], acc[mb][2][3]), pk_bf16(acc[mb][3][2], acc[mb][3][3]), false, false);
      __builtin_amdgcn_raw_buffer_store_b128((u32x4){s0[0], s1[0], s0[1], s1[1]}, rs, w * 4096 + lane * 16 + mb * 1024, blockIdx.x * XSTR, 16);
    }
    f32x4 y[4][2];
#pragma unroll
    for (int mb = 0; mb < 4; ++mb)
#pragma unroll
      for (int j = 0; j < 4; ++j) {
        auto s = __builtin_amdgcn_permlane16_swap(__float_as_uint(acc[mb][0][j]), __float_as_uint(acc[mb][1][j]), false, false);
        y[mb][0][j] = __uint_as_float(s[0]) + ((j & 1) ? bf_hi(res[mb][j >> 1]) : bf_lo(res[mb][j >> 1]));
        y[mb][1][j] = __uint_as_float(s[1]) + ((j & 1) ? bf_hi(res[mb][2 + (j >> 1)]) : bf_lo(res[mb][2 + (j >> 1)]));
      }
    const int row0 = tm * 128 + wm * 64 + fr, col = tn * 128 + wx * 64 + kh * 32 + chunk * 8;
    unsigned optr = (ID == 2) ? ((row0 >> 4) * (D_ / 32) + (col >> 5)) * 1024 + (chunk * 16 + fr) * 16 : (row0 * D_ + col) * 2;
    const auto ro = OUT_RSRC(p.out, S_ * D_ * 2);
    asm volatile("" : "+v"(y[0][0]), "+v"(y[0][1]), "+v"(y[1][0]), "+v"(y[1][1]), "+v"(y[2][0]), "+v"(y[2][1]), "+v"(y[3][0]), "+v"(y[3][1]), "+v"(optr));
    wait_vm<0>();
    barrier();
    if (w == 0) {
      unsigned* fl = (unsigned*)((char*)p.flags + XI * 256 * 128);
      if (lane == 0) __hip_atomic_store(fl + blockIdx.x * 32, 1u, __ATOMIC_RELAXED, __HIP_MEMORY_SCOPE_AGENT);
      unsigned spins = 0;
      while (rfl(__hip_atomic_load(fl + (blockIdx.x ^ 1) * 32, __ATOMIC_RELAXED, __HIP_MEMORY_SCOPE_AGENT)) != 1u && ++spins < (1u << 22)) __builtin_amdgcn_s_sleep(1);
    }
    barrier();
    u32x4 in[4];
#pragma unroll
    for (int i = 0; i < 4; ++i) in[i] = __builtin_amdgcn_raw_buffer_load_b128(rs, w * 4096 + lane * 16 + i * 1024, (blockIdx.x ^ 1) * XSTR, 16);
    u32x4 v[4];
#pragma unroll
    for (int mb = 0; mb < 4; ++mb) {
      const f32x4 a = y[mb][0] + (f32x4){bf_lo(in[mb][0]), bf_hi(in[mb][0]), bf_lo(in[mb][1]), bf_hi(in[mb][1])};
      const f32x4 b = y[mb][1] + (f32x4){bf_lo(in[mb][2]), bf_hi(in[mb][2]), bf_lo(in[mb][3]), bf_hi(in[mb][3])};
      v[mb] = (u32x4){pk_bf16(a[0], a[1]), pk_bf16(a[2], a[3]), pk_bf16(b[0], b[1]), pk_bf16(b[2], b[3])};
      STORE16(ro, optr + mb * 16 * D_ * 2, v[mb]);
    }
    SB();
    if constexpr (ID == 2) {
      float m[4], q[4];
#pragma unroll
      for (int mb = 0; mb < 4; ++mb) {
        float s = 0.f;
#pragma unroll
        for (int j = 0; j < 4; ++j) s += bf_lo(v[mb][j]) + bf_hi(v[mb][j]);
        m[mb] = sum_rows(s) * (1.0f / 32.0f);
      }
#pragma unroll
      for (int mb = 0; mb < 4; ++mb) {
        float s = 0.f;
#pragma unroll
        for (int j = 0; j < 4; ++j) {
          const float a = bf_lo(v[mb][j]) - m[mb], b = bf_hi(v[mb][j]) - m[mb];
          s += a * a + b * b;
        }
        q[mb] = sum_rows(s);
      }
      const float ms = (fq & 2) ? ((fq & 1) ? m[3] : m[2]) : ((fq & 1) ? m[1] : m[0]);
      const float qs = (fq & 2) ? ((fq & 1) ? q[3] : q[2]) : ((fq & 1) ? q[1] : q[0]);
      LAS f32x2* tb = (LAS f32x2*)(lds0 + SQ_TAB);
      if (wx) tb[wm * 64 + lane] = (f32x2){ms, qs};
      wait_lgkm0();
      barrier();
      if (!wx) {
        const f32x2 o = tb[wm * 64 + lane];
        const float d = ms - o.x;
        ((f32x2*)p.stats)[((size_t)tn * S_ + tm * 128 + wm * 64 + lane) * 2 + kh] = (f32x2){0.5f * (ms + o.x), qs + o.y + 16.0f * d * d};
      }
    }
  }
  STAMPX(ID, 4);
  STAMP(ID, 3);
}

constexpr int G3_NST = 4, G3_UP = 7168 + 16, G3_ST = 16384 + 2 * 7168 + 16, G3_RING = 6144, G3_TAB = G3_RING + G3_NST * G3_ST, G3_LDS = G3_TAB + 3072;
__global__ __launch_bounds__(768, 3) void k_glu(GArgs p) {
  CODE_LABEL(fz_code_g3);
  constexpr int ID = 3, BM = 256, K = D_, NT = K / 32, NST = G3_NST, ST = G3_ST, A_BYTES = 16384, MB = 4, NB = 7;
  extern __shared__ __attribute__((aligned(16))) char shm[];
  const unsigned lds0 = (unsigned)(size_t)(LAS char*)shm;
  const unsigned ring = lds0 + G3_RING;
  LAS f32x2* tab = (LAS f32x2*)(lds0 + G3_TAB);
  LAS float* cst = (LAS float*)(lds0 + G3_TAB + 2048);
  const unsigned w = rfl(threadIdx.x >> 6);
#define threadIdx_x_w w
  STAMP(ID, 0);
  const unsigned xcd = blockIdx.x & 7, idx = blockIdx.x >> 3;
  const unsigned tm = idx & 7, tn = xcd * 4 + (idx >> 3);
  const unsigned cls = (tm + 2 * (idx >> 3)) & 7;
  unsigned long long* gcs = (unsigned long long*)((char*)p.flags + FL_CS) + tn * (8 * 224);

  if (w >= 8) {
    if (LPRIO) __builtin_amdgcn_s_setprio(LPRIO);
    const unsigned lw = w - 8;
    const int lane = lane_id();
    const u32x4 qa = make_rsrc((const u16*)p.a0 + (size_t)tm * BM * K, BM * K * 2);
    const u32x4 qb = make_rsrc(lw < 2 ? p.b0 : p.b1, (unsigned)D_ * F_ * 2);
    unsigned va[4], vb[4];
#pragma unroll
    for (int i = 0; i < 4; ++i) {
      va[i] = (lw * 4 + i) * (K / 32) * 1024 + lane * 16 - i * 1024;
      const int c = ((lw & 1) * 4 + i) * 64 + lane, r = c / 14, pos = c % 14;
      const int k = (r & 0x13) | ((r & 4) << 1) | ((r & 8) >> 1);
      vb[i] = k * F_ * 2 + tn * 224 + pos * 16 - i * 1024;
    }
    const unsigned boff = A_BYTES + (lw >> 1) * G3_UP + (lw & 1) * 4096;
    auto stage = [&](unsigned n) {
      unsigned st = stage_of<NC3, NT, (WRAPM >> 3) & 1>(n, cls);
      return st;
    };
    auto issue_all = [&](int n) {
      const unsigned st = stage(n);
      dma_grp<4>(ring + n * ST + lw * 4096, va, qa, st * 1024);
      if (lw & 1) dma_grp<3>(ring + n * ST + boff, vb, qb, st * (32 * F_ * 2));
      else dma_grp<4>(ring + n * ST + boff, vb, qb, st * (32 * F_ * 2));
    };
    issue_all(0);
    issue_all(1);
    wait_vm<7>();
    barrier();
    issue_all(2);
    unsigned touch = 0;
#pragma unroll 1
    for (unsigned t = 0; t + NST - 1 < NT; ++t) {
      const unsigned st = stage(t + NST - 1), wbuf = ring + ((t + NST - 1) & (NST - 1)) * ST;
      if (t == 12) {
        const char* tp = (const char*)p.res + ((size_t)blockIdx.x * 224 + (lw * 56 + (lane < 56 ? lane : 55))) * 128;
        asm volatile("global_load_dword %0, %1, off" : "+v"(touch) : "v"(tp) : "memory");
      }
      if (t == 25) {
        const unsigned u = lw * 64 + lane < 224 ? lw * 64 + lane : 223u;
        const unsigned hf = u >= 112, c = u - hf * 112, wn_ = c >= 56, cc = c - wn_ * 56, blk = cc >> 4;
        const unsigned src = lds0 + (wn_ * 4 + blk) * 768 + (blk == 3 ? 512 + (hf * 8 + (cc & 7)) * 4 : hf * 256 + (cc & 15) * 4);
        const float v = (*(const LAS float*)(src) + *(const LAS float*)(src + 64)) + (*(const LAS float*)(src + 128) + *(const LAS float*)(src + 192));
        asm volatile("global_store_dwordx2 %0, %1, off sc1" ::"v"(gcs + cls * 224 + u), "v"((1ull << 32) | __float_as_uint(v)) : "memory");
      }
      dma_grp<4>(wbuf + lw * 4096, va, qa, st * 1024);
      barrier();
      if (lw & 1) dma_grp<3>(wbuf + boff, vb, qb, st * (32 * F_ * 2));
      else dma_grp<4>(wbuf + boff, vb, qb, st * (32 * F_ * 2));
      wait_vm<7 * (NST - 2)>();
      barrier();
    }
    barrier();
    wait_vm<7>();
    barrier();
    if ((CODE_PF & 2) && lw == 0 && (idx & 7) == 0) {
      unsigned long long a;
      CODE_ADDR(a, fz_code_g4);
      touch_code(a, lane, 50, touch);
    }
    barrier();
    wait_vm<0>();
    {
      const unsigned u = lw * 64 + lane < 224 ? lw * 64 + lane : 223u;
      unsigned long long g[8];
#pragma unroll
      for (int i = 0; i < 8; ++i) g[i] = __hip_atomic_load(gcs + i * 224 + u, __ATOMIC_RELAXED, __HIP_MEMORY_SCOPE_AGENT);
      SB();
      barrier();
      barrier();
      barrier();
      SB();
#pragma unroll 1
      for (unsigned spins = 0; spins < (1u << 20); ++spins) {
        unsigned tg = 1u;
#pragma unroll
        for (int i = 0; i < 8; ++i) tg &= (unsigned)(g[i] >> 32);
        if (__all(tg == 1u)) break;
        __builtin_amdgcn_s_sleep(2);
#pragma unroll
        for (int i = 0; i < 8; ++i) g[i] = __hip_atomic_load(gcs + i * 224 + u, __ATOMIC_RELAXED, __HIP_MEMORY_SCOPE_AGENT);
      }
      float s = 0.f;
#pragma unroll
      for (int i = 0; i < 8; ++i) s += __uint_as_float((unsigned)g[i]);
      cst[u] = s;
      wait_lgkm0();
    }
    asm volatile("" ::"v"(touch));
    return;
  }

  const unsigned wm = w & 3, wn = w >> 2;
  const int lane = lane_id(), fr = lane & 15, fq = lane >> 4;
  STAMPX(ID, 7);
  const unsigned rot = wm == 3 ? 0u : wm;
  unsigned xa = ring + wm * 4096 + lane * 16, xb[3], xb1;
  {
    const int q = fr >> 2, pp = fr & 3, r = (fq >> 1) * 16 + (fq & 1) * 4 + q;
#pragma unroll
    for (int i = 0; i < 3; ++i) xb[i] = ring + A_BYTES + r * 224 + wn * 112 + pp * 8 + ((i + rot) % 3) * 32;
    xb1 = ring + A_BYTES + r * 224 + wn * 112 + 96 + (pp & 1) * 8 + (pp >> 1) * G3_UP;
  }
  float cs[3] = {0.f, 0.f, 0.f};
  if (wn == 1) {
    const f32x4* sp = (const f32x4*)p.stats + tm * BM + wm * 64 + lane;
    f32x4 sv[8];
#pragma unroll
    for (int i = 0; i < 8; ++i) sv[i] = sp[(size_t)i * S_];
    SB();
    barrier();
    SB();
    float m = 0.f;
#pragma unroll
    for (int i = 0; i < 8; ++i) m += sv[i][0] + sv[i][2];
    m *= (1.0f / 16.0f);
    float q = 0.f;
#pragma unroll
    for (int i = 0; i < 8; ++i) {
      const float d0 = sv[i][0] - m, d1 = sv[i][2] - m;
      q += sv[i][1] + sv[i][3] + 64.0f * (d0 * d0 + d1 * d1);
    }
    tab[wm * 64 + lane] = (f32x2){m, __builtin_amdgcn_rsqf(q * (1.0f / D_) + EPS)};
  }
  bf16x8 fa[MB], fb[NB];
  f32x4 acc[MB][NB];
#pragma unroll
  for (int mb = 0; mb < MB; ++mb)
#pragma unroll
    for (int nb = 0; nb < NB; ++nb) { acc[mb][nb] = (f32x4){0.f, 0.f, 0.f, 0.f}; asm volatile("" : "+v"(acc[mb][nb])); }
  asm volatile("" : "+v"(xa), "+v"(xb[0]), "+v"(xb[1]), "+v"(xb[2]), "+v"(xb1));
  wait_lgkm0();
  barrier();
  STAMP1_TAKE();
  const unsigned csmask = (0x010101u << cls) | (1u << ((cls + 1) & 7));
  auto reads = [&]() {
    SB();
    fa[0] = lds_b128(xa);
#pragma unroll
    for (int j = 0; j < 7; ++j) {
      const unsigned o = j < 6 ? xb[j % 3] + (j / 3) * G3_UP : xb1;
      fb[j] = lds_tr(o, o + 1792);
    }
#pragma unroll
    for (int mb = 1; mb < MB; ++mb) fa[mb] = lds_b128(xa + mb * 1024);
    SB();
  };
  reads();
#pragma unroll 1
  for (unsigned t = 0; t < NT; ++t) {
    const unsigned delta = ((t & (NST - 1)) == NST - 1) ? (unsigned)(-(NST - 1) * ST) : (unsigned)ST;
    xa += delta; xb[0] += delta; xb[1] += delta; xb[2] += delta; xb1 += delta;
    asm volatile("" : "+v"(xa), "+v"(xb[0]), "+v"(xb[1]), "+v"(xb[2]), "+v"(xb1));
    if (__builtin_expect((csmask >> t) & 1u, 0)) {
#pragma unroll
      for (int j = 0; j < 4; ++j)
#pragma unroll
        for (int i = 0; i < 3; ++i)
          asm volatile("v_dot2c_f32_bf16 %0, %1, %2" : "+v"(cs[i]) : "s"(0x3f803f80u), "v"(__builtin_bit_cast(u32x4, fb[3 * i])[j]));
      asm volatile("s_mov_b32 m0, %3\n\ts_nop 2\n\tds_write_addtid_b32 %0\n\tds_write_addtid_b32 %1 offset:256\n\tds_write_addtid_b32 %2 offset:512" ::"v"(cs[0]),
                   "v"(cs[1]), "v"(cs[2]), "s"(lds0 + w * 768)
                   : "memory");
    }
    wait_lgkm0();
    barrier();
    SB();
    if (CPRIO == 2) __builtin_amdgcn_s_setprio(1);
#pragma unroll
    for (int mb = 0; mb < MB; ++mb)
#pragma unroll
      for (int nb = 0; nb < NB; ++nb) MFMA(fb[nb], fa[mb], acc[mb][nb]);
    if (CPRIO == 2) __builtin_amdgcn_s_setprio(0);
    SB();
    barrier();
    reads();
  }
  if (wn == 0) barrier();
  mfma_drain();
  SB();
  const int lane2 = lane_id();
  const int gr = lane2 & 15, gq = lane2 >> 4;
  STAMP1_PUT(ID);
  STAMP(ID, 2);
  STAMPX(ID, 0);
  auto act = [](float g, float u) { return g * u * __builtin_amdgcn_rcpf(1.0f + __expf(-g)); };
  const auto ro = OUT_RSRC(p.out, S_ * F_ * 2);
  f32x2 st[MB];
#pragma unroll
  for (int mb = 0; mb < MB; ++mb) st[mb] = tab[wm * 64 + mb * 16 + gr];
  const int gch0 = tn * 14 + wn * 7;
#pragma unroll
  for (int i = 0; i < 3; ++i) {
    const unsigned ib = (i + rot) % 3;
    const f32x4 cg = *(const LAS f32x4*)(cst + wn * 56 + ib * 16 + gq * 4), cu = *(const LAS f32x4*)(cst + 112 + wn * 56 + ib * 16 + gq * 4);
    u32x2 pkd[MB];
#pragma unroll
    for (int mb = 0; mb < MB; ++mb) {
      const f32x4 g = (acc[mb][i] - cg * st[mb].x) * st[mb].y, u = (acc[mb][3 + i] - cu * st[mb].x) * st[mb].y;
      pkd[mb] = (u32x2){pk_bf16(act(g[0], u[0]), act(g[1], u[1])), pk_bf16(act(g[2], u[2]), act(g[3], u[3]))};
    }
#pragma unroll
    for (int mp = 0; mp < MB / 2; ++mp) {
      auto s0 = __builtin_amdgcn_permlane16_swap(pkd[2 * mp].x, pkd[2 * mp + 1].x, false, false);
      auto s1 = __builtin_amdgcn_permlane16_swap(pkd[2 * mp].y, pkd[2 * mp + 1].y, false, false);
      const u32x4 v = {s0[0], s1[0], s0[1], s1[1]};
      const int rblk = tm * (BM / 16) + wm * MB + 2 * mp + (gq & 1);
      const int gch = gch0 + 2 * ib + (gq >> 1);
      STORE16(ro, (rblk * (F_ / 32) + (gch >> 2)) * 1024 + ((gch & 3) * 16 + gr) * 16, v);
    }
  }
  {
    const f32x4 cm = *(const LAS f32x4*)(cst + (gq >> 1) * 112 + wn * 56 + 48 + (gq & 1) * 4);
    u32x2 pkd[MB / 2];
#pragma unroll
    for (int mp = 0; mp < MB / 2; ++mp) {
      const f32x4 v0 = (acc[2 * mp][6] - cm * st[2 * mp].x) * st[2 * mp].y, v1 = (acc[2 * mp + 1][6] - cm * st[2 * mp + 1].x) * st[2 * mp + 1].y;
      float r[4];
#pragma unroll
      for (int j = 0; j < 4; ++j) {
        auto s = __builtin_amdgcn_permlane32_swap(__float_as_uint(v0[j]), __float_as_uint(v1[j]), false, false);
        r[j] = act(__uint_as_float(s[0]), __uint_as_float(s[1]));
      }
      pkd[mp] = (u32x2){pk_bf16(r[0], r[1]), pk_bf16(r[2], r[3])};
    }
    auto s0 = __builtin_amdgcn_permlane16_swap(pkd[0].x, pkd[1].x, false, false);
    auto s1 = __builtin_amdgcn_permlane16_swap(pkd[0].y, pkd[1].y, false, false);
    const u32x4 v = {s0[0], s1[0], s0[1], s1[1]};
    const int rblk = tm * (BM / 16) + wm * MB + (gq & 1) * 2 + (gq >> 1);
    const int gch = gch0 + 6;
    STORE16(ro, (rblk * (F_ / 32) + (gch >> 2)) * 1024 + ((gch & 3) * 16 + gr) * 16, v);
  }
  STAMPX(ID, 4);
  STAMP(ID, 3);
}

template <int ID>
void launch_sq(const GArgs& a, hipStream_t stream) {
  (void)hipFuncSetAttribute((const void*)k_sq<ID>, hipFuncAttributeMaxDynamicSharedMemorySize, SQ_LDS);
  hipLaunchKernelGGL(k_sq<ID>, dim3(256), dim3(512), SQ_LDS, stream, a);
}
}

extern "C" void kernel_launch(void* const* d_in, const int* in_sizes, int n_in, void* d_out, int out_size, void* d_ws, size_t ws_size,
                              hipStream_t stream) {
  char* ws = (char*)d_ws;
  GArgs g1 = {d_in[3], d_in[4], d_in[0], d_in[6], ws + WS_AB, d_in[1], d_in[2], d_in[7], ws + WS_FL};
  launch_sq<1>(g1, stream);
  GArgs g2 = {d_in[1], d_in[2], ws + WS_AB, nullptr, ws + WS_H, d_in[0], ws + WS_ST, ws + WS_XC, ws + WS_FL};
  launch_sq<2>(g2, stream);
  GArgs g3 = {ws + WS_H, nullptr, d_in[6], d_in[7], ws + WS_GU, d_in[8], ws + WS_ST, nullptr, ws + WS_FL};
  (void)hipFuncSetAttribute((const void*)k_glu, hipFuncAttributeMaxDynamicSharedMemorySize, G3_LDS);
  hipLaunchKernelGGL(k_glu, dim3(256), dim3(768), G3_LDS, stream, g3);
  GArgs g4 = {ws + WS_GU, nullptr, d_in[8], nullptr, d_out, ws + WS_H, nullptr, ws + WS_XC, ws + WS_FL};
  launch_sq<4>(g4, stream);
}
```

```cpp
#include <hip/hip_runtime.h>
#include <hip/hip_bf16.h>

namespace {
#define LAS __attribute__((address_space(3)))
typedef unsigned short u16;
typedef short s16x4 __attribute__((ext_vector_type(4)));
typedef short s16x8 __attribute__((ext_vector_type(8)));
typedef __bf16 bf16x8 __attribute__((ext_vector_type(8)));
typedef __bf16 bf16x2 __attribute__((ext_vector_type(2)));
typedef float f32x4 __attribute__((ext_vector_type(4)));
typedef float f32x2 __attribute__((ext_vector_type(2)));
typedef unsigned u32x4 __attribute__((ext_vector_type(4)));
typedef unsigned u32x2 __attribute__((ext_vector_type(2)));

constexpr int S_ = 2048, D_ = 1024, F_ = 3584;
constexpr float EPS = 1e-5f;

constexpr size_t WS_AB = 0;
constexpr size_t WS_H = WS_AB + (size_t)D_ * 2 * S_ * 2;
constexpr size_t WS_GU = WS_H + (size_t)S_ * D_ * 2;
constexpr size_t WS_ST = WS_GU + (size_t)S_ * F_ * 2;
constexpr size_t WS_XC = WS_ST + (size_t)S_ * 32 * 8;
constexpr size_t WS_FL = WS_XC + (size_t)2 * 256 * 36864;
constexpr int FL_CS = 2 * 256 * 128, FL_P2 = FL_CS + 32 * 8 * 224 * 8, FL_P3 = FL_P2 + 64 * 128;

__device__ __forceinline__ unsigned rfl(unsigned v) { return __builtin_amdgcn_readfirstlane(v); }
__device__ __forceinline__ unsigned pk_bf16(float lo, float hi) {
  bf16x2 c = {(__bf16)lo, (__bf16)hi};
  return __builtin_bit_cast(unsigned, c);
}
__device__ __forceinline__ float bf_lo(unsigned v) { return __uint_as_float(v << 16); }
__device__ __forceinline__ float bf_hi(unsigned v) { return __uint_as_float(v & 0xffff0000u); }

__device__ __forceinline__ u32x4 make_rsrc(const void* p, unsigned bytes) {
  unsigned long long a = (unsigned long long)p;
  u32x4 r;
  r.x = (unsigned)a;
  r.y = (unsigned)(a >> 32) & 0xffffu;
  r.z = bytes;
  r.w = 0x00020000u;
  return r;
}
__device__ __forceinline__ void dma16(unsigned ldsaddr, unsigned voff, u32x4 rsrc, unsigned soff) {
  asm volatile("s_mov_b32 m0, %0\n\ts_nop 4\n\tbuffer_load_dwordx4 %1, %2, %3 offen lds" ::"s"(ldsaddr), "v"(voff), "s"(rsrc), "s"(soff)
               : "memory");
}
template <int N>
__device__ __forceinline__ void wait_vm() {
  asm volatile("s_waitcnt vmcnt(%0)" ::"i"(N) : "memory");
}
__device__ __forceinline__ void wait_lgkm0() { __builtin_amdgcn_s_waitcnt(0xC07F); }
__device__ __forceinline__ void barrier() {
  asm volatile("" ::: "memory");
  __builtin_amdgcn_s_barrier();
  asm volatile("" ::: "memory");
}
__device__ __forceinline__ bf16x8 lds_b128(unsigned addr) { return *(const LAS bf16x8*)(addr); }
__device__ __forceinline__ bf16x8 lds_tr(unsigned a0, unsigned a1) {
  s16x4 lo = __builtin_amdgcn_ds_read_tr16_b64_v4i16((LAS s16x4*)(a0));
  s16x4 hi = __builtin_amdgcn_ds_read_tr16_b64_v4i16((LAS s16x4*)(a1));
  s16x8 f = __builtin_shufflevector(lo, hi, 0, 1, 2, 3, 4, 5, 6, 7);
  return __builtin_bit_cast(bf16x8, f);
}

#define ST_AUX 16
#define OUT_RSRC(ptr, bytes) __builtin_amdgcn_make_buffer_rsrc((void*)(ptr), 0, (bytes), 0x00020000)
#define STORE16(rs, byteoff, v) __builtin_amdgcn_raw_buffer_store_b128(v, rs, byteoff, 0, ST_AUX)
#define STAMP(k, i)
#define STAMPX(k, i)
#define STAMPL(k, i, first)

struct GArgs {
  const void* a0;
  const void* a1;
  const void* b0;
  const void* b1;
  void* out;
  const void* res;
  void* stats;
  void* xch;
  void* flags;
  const void* aux0;
  const void* aux1;
};

template <int N, bool SC1 = false>
__device__ __forceinline__ void dma_grp(unsigned ldsaddr, const unsigned (&v)[4], u32x4 rsrc, unsigned soff) {
  if constexpr (N == 4 && SC1)
    asm volatile("s_mov_b32 m0, %0\n\ts_nop 4\n\tbuffer_load_dwordx4 %1, %5, %6 offen sc1 lds\n\tbuffer_load_dwordx4 %2, %5, %6 offen offset:1024 sc1 lds\n\t"
                 "buffer_load_dwordx4 %3, %5, %6 offen offset:2048 sc1 lds\n\tbuffer_load_dwordx4 %4, %5, %6 offen offset:3072 sc1 lds" ::"s"(ldsaddr),
                 "v"(v[0]), "v"(v[1]), "v"(v[2]), "v"(v[3]), "s"(rsrc), "s"(soff)
                 : "memory");
  else if constexpr (N == 4)
    asm volatile("s_mov_b32 m0, %0\n\ts_nop 4\n\tbuffer_load_dwordx4 %1, %5, %6 offen lds\n\tbuffer_load_dwordx4 %2, %5, %6 offen offset:1024 lds\n\t"
                 "buffer_load_dwordx4 %3, %5, %6 offen offset:2048 lds\n\tbuffer_load_dwordx4 %4, %5, %6 offen offset:3072 lds" ::"s"(ldsaddr),
                 "v"(v[0]), "v"(v[1]), "v"(v[2]), "v"(v[3]), "s"(rsrc), "s"(soff)
                 : "memory");
  else
    asm volatile("s_mov_b32 m0, %0\n\ts_nop 4\n\tbuffer_load_dwordx4 %1, %4, %5 offen lds\n\tbuffer_load_dwordx4 %2, %4, %5 offen offset:1024 lds\n\t"
                 "buffer_load_dwordx4 %3, %4, %5 offen offset:2048 lds" ::"s"(ldsaddr),
                 "v"(v[0]), "v"(v[1]), "v"(v[2]), "s"(rsrc), "s"(soff)
                 : "memory");
}
__device__ __forceinline__ void lds_inc(unsigned addr) { asm volatile("ds_add_u32 %0, %1" ::"v"(addr), "v"(1u) : "memory"); }
__device__ __forceinline__ unsigned inc_base(unsigned sync, int lane) { return sync + lane * 68; }
__device__ __forceinline__ unsigned lds_peek(unsigned addr) { return *(volatile LAS unsigned*)(addr); }
__device__ __forceinline__ void lds_wait(unsigned addr, unsigned pv, unsigned need) {
  if (__builtin_expect(rfl(pv) < need, 0)) {
    do {
      __builtin_amdgcn_s_sleep(1);
    } while (rfl(lds_peek(addr)) < need);
  }
  asm volatile("" ::: "memory");
}
__device__ __forceinline__ float sum_rows(float x) {
  auto a = __builtin_amdgcn_permlane16_swap(__float_as_uint(x), __float_as_uint(x), false, false);
  x = __uint_as_float(a[0]) + __uint_as_float(a[1]);
  auto b = __builtin_amdgcn_permlane32_swap(__float_as_uint(x), __float_as_uint(x), false, false);
  return __uint_as_float(b[0]) + __uint_as_float(b[1]);
}
__device__ __forceinline__ float sum_pair(float x) { return x + __uint_as_float(__builtin_amdgcn_mov_dpp(__float_as_uint(x), 0xB1, 0xF, 0xF, true)); }
__device__ __forceinline__ int lane_id() {
  int l;
  asm volatile("v_mbcnt_lo_u32_b32 %0, -1, 0\n\tv_mbcnt_hi_u32_b32 %0, -1, %0" : "=v"(l));
  return l;
}
__device__ __forceinline__ void mfma_drain() { asm volatile("s_nop 15" ::: "memory"); }
__device__ __forceinline__ void fadd(float& a, float b) { asm("v_add_f32 %0, %1, %0" : "+v"(a) : "v"(b)); }
template <int NC, int NT, int WRAP>
__device__ __forceinline__ unsigned stage_of(unsigned n, unsigned c) {
  if (NC == 1) return n;
  if (WRAP) {
    const unsigned t = ((n & (NC - 1)) == c) ? n + NC : n;
    return rfl(t >= NT ? t - NT : t);
  }
  unsigned t = (n > NT - NC + c) ? n : n - 1;
  t = ((t & (NC - 1)) == c) ? t + NC : t;
  return rfl(n == 0 ? c : t);
}
#define NC1 4
#define NC2 8
#define NC3 8
#define NC4 4
#define TOUCH_C 1
#define TOUCH_W 1
#define TOUCH_D 2
#define TOUCH_AT 6
#define FT_W 1
#define FT_D 1
#define POLL_SLEEP 1
#define XSTR 16384
#define WRAPM 0x1c
#define SQ_PD 2
#define BAR_AT 0
#define CPRIO 0
template <int V> struct IC { static constexpr int value = V; };
#define LPRIO 0
#define SB() __builtin_amdgcn_sched_barrier(0)
#define MFMA(b, a, c) asm volatile("v_mfma_f32_16x16x32_bf16 %0, %1, %2, %0" : "+v"(c) : "v"(b), "v"(a))
#define STAMP_END(k)
#define STAMP1_TAKE()
#define STAMP1_PUT(k)

#define CODE_LABEL(name) asm volatile(#name ":")
#define CODE_ADDR(dst, name) do { int o_; asm volatile("s_getpc_b64 %0\n\ts_mov_b32 %1, " #name "@rel32@lo+4" : "=s"(dst), "=s"(o_)); dst += (long long)o_; } while (0)
#define CODE_LAST 54
#define CODE_PF 3
__device__ __forceinline__ void touch_code(unsigned long long base, int lane, int last, unsigned& sink) {
  const unsigned off = (lane < last ? lane : last) * 128;
  asm volatile("global_load_dword %0, %1, %2" : "+v"(sink) : "v"(off), "s"(base) : "memory");
}
enum { OP_RM = 0, OP_FT = 1, OP_NN = 2 };
template <int ID> struct Cfg;
template <> struct Cfg<1> { static constexpr int NT = 16, AM = OP_RM, BMODE = OP_RM, NC = NC1; };
template <> struct Cfg<2> { static constexpr int NT = 32, AM = OP_RM, BMODE = OP_FT, NC = NC2; };
template <> struct Cfg<4> { static constexpr int NT = 28, AM = OP_FT, BMODE = OP_NN, NC = NC4; };
constexpr int SQ_NST = 4, SQ_ST = 32768, SQ_SYNC = SQ_NST * SQ_ST, SQ_TAB = SQ_SYNC + 4608, SQ_LDS = SQ_TAB + 2048;

template <int ID>
__global__ __launch_bounds__(512, 2) void k_sq(GArgs p) {
  if constexpr (ID == 1) CODE_LABEL(fz_code_g1);
  if constexpr (ID == 2) CODE_LABEL(fz_code_g2);
  if constexpr (ID == 4) CODE_LABEL(fz_code_g4);
  using C = Cfg<ID>;
  constexpr int NT = C::NT, NST = SQ_NST, ST = SQ_ST, A_BYTES = 16384;
  extern __shared__ __attribute__((aligned(16))) char shm[];
  const unsigned lds0 = (unsigned)(size_t)(LAS char*)shm;
  const unsigned vfull = lds0 + SQ_SYNC, vfree = lds0 + SQ_SYNC + 32;
  const int tid = threadIdx.x, lane = tid & 63;
  const unsigned w = rfl(tid >> 6);
  STAMP(ID, 0);
  const int fr = lane & 15, fq = lane >> 4;
  const unsigned ifull = inc_base(vfull, lane), ifree = ifull + 32;
  const unsigned xcd = blockIdx.x & 7, idx = blockIdx.x >> 3, li = idx & 3, lj = idx >> 2;
  const unsigned kh = (ID == 1) ? 0u : (xcd & 1);
  const unsigned tm = (xcd >> 1) * 4 + li, tn = (ID == 1) ? (xcd & 1) * 8 + lj : lj;

  if (w >= 4) {
    if (LPRIO) __builtin_amdgcn_s_setprio(LPRIO);
    const unsigned lw = w - 4;
    u32x4 qa, qb;
    unsigned va[4], vb[4], kstepA, kstepB;
    if constexpr (ID == 1) {
      qa = make_rsrc((const u16*)(tm < 8 ? p.a0 : p.a1) + (size_t)(tm & 7) * 128 * D_, 128 * D_ * 2);
      qb = make_rsrc((const u16*)p.b0 + (size_t)tn * 128 * D_, 128 * D_ * 2);
    } else if constexpr (ID == 2) {
      qa = make_rsrc((const u16*)(kh ? p.a1 : p.a0) + (size_t)tm * 128 * S_, 128 * S_ * 2);
      qb = make_rsrc((const u16*)p.b0 + (size_t)tn * 128 * 4096 + kh * 64 * 512, 128 * 4096 * 2 - kh * 64 * 1024);
    } else {
      qa = make_rsrc((const u16*)p.a0 + (size_t)tm * 128 * F_ + kh * 56 * 512, 128 * F_ * 2 - kh * 56 * 1024);
      qb = make_rsrc((const u16*)p.b0 + (size_t)kh * 1792 * D_, 1792 * D_ * 2);
    }
#pragma unroll
    for (int i = 0; i < 4; ++i) {
      const int pc = lw * 4 + i;
      const int row = pc * 8 + (lane >> 3), rsw = ((lane & 7) ^ ((row >> 1) & 7)) << 4;
      if constexpr (C::AM == OP_RM) va[i] = row * (ID == 1 ? D_ : S_) * 2 + rsw - i * 1024;
      else va[i] = ((pc >> 1) * (F_ / 32) + (pc & 1)) * 1024 + lane * 16 - i * 1024;
      if constexpr (C::BMODE == OP_RM) vb[i] = row * D_ * 2 + rsw - i * 1024;
      else if constexpr (C::BMODE == OP_FT) vb[i] = ((pc >> 1) * 128 + (pc & 1)) * 1024 + lane * 16 - i * 1024;
      else {
        const int k = pc * 4 + (lane >> 4), f = (k & 3) | (((k >> 3) & 1) << 2);
        vb[i] = k * D_ * 2 + tn * 256 + (((lane & 15) ^ (f << 1)) << 4) - i * 1024;
      }
    }
    kstepA = (C::AM == OP_RM) ? 128 : 2048;
    kstepB = (C::BMODE == OP_RM) ? 128 : (C::BMODE == OP_FT ? 2048 : 64 * D_ * 2);
    const unsigned cls = (C::NC == 8) ? ((lj + 2 * li) & 7) : ((lj + li) & (C::NC - 1));
    auto issue = [&](unsigned n) {
      unsigned st = stage_of<C::NC, NT, (WRAPM >> ID) & 1>(n, cls);
      const unsigned wbuf = lds0 + (n & (NST - 1)) * ST + lw * 4096;
      dma_grp<4>(wbuf, va, qa, st * kstepA);
      dma_grp<4>(wbuf + A_BYTES, vb, qb, st * kstepB);
    };
    unsigned touch = 0;
#pragma unroll
    for (int n = 0; n < SQ_PD; ++n) issue(n);
#pragma unroll 1
    for (unsigned b = 0; b + SQ_PD < NT; ++b) {
      wait_vm<8 * (SQ_PD - 1)>();
      barrier();
      issue(b + SQ_PD);
    }
    if (SQ_PD == 3) { wait_vm<16>(); barrier(); }
    wait_vm<8>();
    barrier();
    wait_vm<0>();
    barrier();
    if constexpr (ID == 2) {
      if (TOUCH_W) { barrier(); barrier(); barrier(); }
    }
    if constexpr (ID != 4) {
      if ((CODE_PF & 2) && lw == 0 && (idx & 7) == 0) {
        unsigned long long a;
        CODE_ADDR(a, fz_code_f);
        touch_code(a + ((idx >> 3) & 1) * 8192, lane, ((idx >> 3) & 1) ? CODE_LAST : 63, touch);
      }
    }
    if constexpr (ID == 1) {
      if (TOUCH_C == 1 && lw < 2) {
        const char* tp = (const char*)((xcd & 1) ? p.stats : p.res) + (size_t)((xcd >> 1) * 512 + idx * 16 + lw * 8 + (lane >> 3)) * (S_ * 2) + (lane & 7) * 128;
        asm volatile("global_load_dword %0, %1, off" : "+v"(touch) : "v"(tp) : "memory");
      }
      if (TOUCH_C == 2) {
        const char* tp = (const char*)((xcd & 1) ? p.stats : p.res) + (size_t)((xcd >> 1) * 512 + idx * 16 + lw * 4 + (lane >> 4)) * (S_ * 2) + (lane & 15) * 128;
        asm volatile("global_load_dword %0, %1, off" : "+v"(touch) : "v"(tp) : "memory");
      }
    }
    if constexpr (ID == 2) {
      if (TOUCH_W && lw < 2 * TOUCH_W) {
        const char* tp = (const char*)((lw & 1) ? p.aux1 : p.aux0) + (size_t)((lw >> 1) * 256 + idx * 8 + (lane >> 3)) * (F_ * 2) + xcd * 896 + ((lane & 7) < 7 ? (lane & 7) : 6) * 128;
        asm volatile("global_load_dword %0, %1, off" : "+v"(touch) : "v"(tp) : "memory");
      }
    }
    if constexpr (ID == 1) barrier();
    asm volatile("s_waitcnt vmcnt(0)" : "+v"(touch));
    return;
  }

  const unsigned wm = w >> 1, wx = w & 1;
  unsigned csink = 0;
  if constexpr (ID == 1) {
    if ((CODE_PF & 1) && w == 0 && idx < 3) {
      unsigned long long a1, a2;
      CODE_ADDR(a1, fz_code_g1); CODE_ADDR(a2, fz_code_f);
      touch_code(idx == 0 ? a1 : a2 + (idx - 1) * 8192, lane, idx == 0 ? 55 : (idx == 2 ? CODE_LAST : 63), csink);
    }
    if (tid < 2) *(unsigned*)((char*)p.flags + (tid * 256 + blockIdx.x) * 128) = 0u;
    if (tid == 2 && blockIdx.x < 80) *(unsigned*)((char*)p.flags + FL_P2 + blockIdx.x * 128) = 0u;
    if (tid < 224) *(unsigned long long*)((char*)p.flags + FL_CS + (blockIdx.x * 224 + tid) * 8) = 0ull;
  }
  const int chunk = ((fq & 1) << 1) | (fq >> 1);
  u32x4 res[4];
  if constexpr (ID != 1) {
#pragma unroll
    for (int mb = 0; mb < 4; ++mb) {
      const int row = tm * 128 + wm * 64 + mb * 16 + fr, col = tn * 128 + wx * 64 + kh * 32 + chunk * 8;
      if constexpr (ID == 2) res[mb] = *(const u32x4*)((const u16*)p.res + (size_t)row * D_ + col);
      else res[mb] = *(const u32x4*)((const u16*)p.res + ((size_t)(row >> 4) * (D_ / 32) + (col >> 5)) * 512 + (chunk * 16 + fr) * 8);
    }
  }
  const unsigned rotA = (ID == 1) ? 2 * wx : 0u, rotB = (ID == 1) ? 2 * wm : 2 * kh;
  unsigned rdA[4], rdB[4];
#pragma unroll
  for (int i = 0; i < 4; ++i) {
    const unsigned kk = i >> 1, hf = (i & 1) * 2, sw = ((kk * 4 + fq) ^ (fr >> 1)) << 4;
    if constexpr (C::AM == OP_RM) rdA[i] = (wm * 64 + (hf ^ rotA) * 16 + fr) * 128 + sw;
    else rdA[i] = wm * 8192 + lane * 16;
    if constexpr (C::BMODE == OP_RM) rdB[i] = A_BYTES + (wx * 64 + (hf ^ rotB) * 16 + fr) * 128 + sw;
    else if constexpr (C::BMODE == OP_FT) rdB[i] = A_BYTES + wx * 8192 + (hf ^ rotB) * 2048 + lane * 16;
    else {
      const int q = fr >> 2, pp = fr & 3, f = q | ((fq & 1) << 2);
      rdB[i] = A_BYTES + (8 * fq + q) * 256 + (((wx * 4 + (i ^ rotB)) ^ f) << 5) + pp * 8;
    }
  }
  unsigned rdA2[4], rdB2[4];
#pragma unroll
  for (int i = 0; i < 4; ++i) {
    rdA[i] += lds0; rdB[i] += lds0;
    rdA2[i] = rdA[i] + 2 * ST; rdB2[i] = rdB[i] + 2 * ST;
    asm volatile("" : "+v"(rdA2[i]), "+v"(rdB2[i]), "+v"(rdA[i]), "+v"(rdB[i]));
  }
  bf16x8 fa[2][2][4], fb[2][2][4];
  auto read_frag = [&](int j, bf16x8(&a)[2][4], bf16x8(&b)[2][4], const unsigned(&xa)[4], const unsigned(&xb)[4], int so) {
    const int s = j >> 3, i = j & 3;
    if (j & 4) {
      if constexpr (C::AM == OP_RM) a[s][i] = lds_b128(xa[s * 2 + (i >> 1)] + (i & 1) * 2048 + so);
      else a[s][i] = lds_b128(xa[0] + i * 2048 + s * 1024 + so);
    } else {
      if constexpr (C::BMODE == OP_RM) b[s][i] = lds_b128(xb[s * 2 + (i >> 1)] + (i & 1) * 2048 + so);
      else if constexpr (C::BMODE == OP_FT) b[s][i] = lds_b128(xb[i >> 1] + (i & 1) * 2048 + s * 1024 + so);
      else b[s][i] = lds_tr(xb[i] + s * 8192 + so, xb[i] + s * 8192 + 1024 + so);
    }
  };
  f32x4 acc[4][4], axs[2], axq[2], aws[2];
#pragma unroll
  for (int i = 0; i < 16; ++i) { acc[i >> 2][i & 3] = (f32x4){0.f, 0.f, 0.f, 0.f}; asm volatile("" : "+v"(acc[i >> 2][i & 3])); }
#pragma unroll
  for (int i = 0; i < 2; ++i) {
    axs[i] = axq[i] = aws[i] = (f32x4){0.f, 0.f, 0.f, 0.f};
    if (ID == 1) asm volatile("" : "+v"(axs[i]), "+v"(axq[i]), "+v"(aws[i]));
  }
  bf16x8 ones = __builtin_bit_cast(bf16x8, (u32x4){0x3f803f80u, 0x3f803f80u, 0x3f803f80u, 0x3f803f80u});
  asm volatile("" : "+v"(ones));
  auto step = [&](const bf16x8(&ca)[2][4], const bf16x8(&cb)[2][4], bf16x8(&na)[2][4], bf16x8(&nb)[2][4], auto slc) {
    constexpr int SL = decltype(slc)::value & 3;
    constexpr bool RD = decltype(slc)::value < 4;
#pragma unroll
    for (int i = 0; i < 32; ++i) {
      if (RD && i == BAR_AT) { barrier(); SB(); }
      if constexpr (ID == 1) {
        if ((i & 15) == 15) {
#pragma unroll
          for (int j = 0; j < 2; ++j) {
            MFMA(cb[i >> 4][j], ones, axs[j]);
            MFMA(cb[i >> 4][j], cb[i >> 4][j], axq[j]);
            MFMA(ones, ca[i >> 4][j], aws[j]);
          }
          SB();
        }
      }
      MFMA(cb[i >> 4][i & 3], ca[i >> 4][(i >> 2) & 3], acc[(i >> 2) & 3][i & 3]);
      SB();
      if (RD && i >= BAR_AT && i < BAR_AT + 16) {
        if (SL < 2) read_frag(i - BAR_AT, na, nb, rdA, rdB, SL * ST);
        else read_frag(i - BAR_AT, na, nb, rdA2, rdB2, (SL - 2) * ST);
      }
      SB();
    }
    wait_lgkm0();
  };
  if constexpr (ID == 1) asm volatile("s_waitcnt vmcnt(0)" : "+v"(csink));
  barrier();
  STAMP1_TAKE();
#pragma unroll
  for (int j = 0; j < 16; ++j) read_frag(j, fa[0], fb[0], rdA, rdB, 0);
  wait_lgkm0();
#pragma unroll 1
  for (unsigned t = 0; t < NT - 4; t += 4) {
    step(fa[0], fb[0], fa[1], fb[1], IC<1>{});
    step(fa[1], fb[1], fa[0], fb[0], IC<2>{});
    step(fa[0], fb[0], fa[1], fb[1], IC<3>{});
    step(fa[1], fb[1], fa[0], fb[0], IC<0>{});
  }
  step(fa[0], fb[0], fa[1], fb[1], IC<1>{});
  step(fa[1], fb[1], fa[0], fb[0], IC<2>{});
  step(fa[0], fb[0], fa[1], fb[1], IC<3>{});
  step(fa[1], fb[1], fa[0], fb[0], IC<4>{});
  mfma_drain();
  SB();
  STAMP1_PUT(ID);
  STAMP(ID, 2);

  if constexpr (ID == 1) {
    LAS float* xs = (LAS float*)(lds0 + SQ_TAB);
    LAS float* xq = xs + 128;
    LAS float* wsum = xs + 256;
#pragma unroll
    for (int j = 0; j < 2; ++j) {
      const unsigned nb = wx * 64 + (j ^ rotB) * 16, mb = wm * 64 + (j ^ rotA) * 16;
      if (fr == 0) *(LAS f32x4*)(xs + nb + fq * 4) = axs[j];
      const float dg = (fr & 2) ? ((fr & 1) ? axq[j][3] : axq[j][2]) : ((fr & 1) ? axq[j][1] : axq[j][0]);
      if (fq == (fr >> 2)) xq[nb + fr] = dg;
      if (fq == 0) wsum[mb + fr] = aws[j][0];
    }
    wait_lgkm0();
    barrier();
    STAMPX(ID, 0);
    const float sg = tm < 8 ? 1.f : -1.f;
    f32x4 mean[4], rstd[4];
    float wsm[4];
#pragma unroll
    for (int j = 0; j < 4; ++j) {
      const unsigned nb = wx * 64 + (j ^ rotB) * 16 + fq * 4;
      mean[j] = *(const LAS f32x4*)(xs + nb) * (1.0f / D_);
      const f32x4 var = *(const LAS f32x4*)(xq + nb) * (1.0f / D_) - mean[j] * mean[j];
#pragma unroll
      for (int r = 0; r < 4; ++r) rstd[j][r] = sg * __builtin_amdgcn_rsqf(fmaxf(var[r], 0.f) + EPS);
      wsm[j] = wsum[wm * 64 + (j ^ rotA) * 16 + fr];
    }
    const auto ro = OUT_RSRC(p.out, D_ * 4096 * 2);
#pragma unroll
    for (int mb = 0; mb < 4; ++mb)
#pragma unroll
      for (int np = 0; np < 2; ++np) {
        const f32x4 x0 = (acc[mb][2 * np] - mean[2 * np] * wsm[mb]) * rstd[2 * np], x1 = (acc[mb][2 * np + 1] - mean[2 * np + 1] * wsm[mb]) * rstd[2 * np + 1];
        auto s0 = __builtin_amdgcn_permlane16_swap(pk_bf16(x0[0], x0[1]), pk_bf16(x1[0], x1[1]), false, false);
        auto s1 = __builtin_amdgcn_permlane16_swap(pk_bf16(x0[2], x0[3]), pk_bf16(x1[2], x1[3]), false, false);
        const u32x4 v = {s0[0], s1[0], s0[1], s1[1]};
        const int rblk = (tm & 7) * 8 + wm * 4 + (mb ^ rotA);
        const int kblk = (tm >> 3) * 64 + tn * 4 + wx * 2 + (np ^ (rotB >> 1));
        STORE16(ro, (rblk * 128 + kblk) * 1024 + (chunk * 16 + fr) * 16, v);
      }
  } else {
    constexpr int XI = (ID == 2) ? 0 : 1;
    const auto rs = __builtin_amdgcn_make_buffer_rsrc((char*)p.xch + (size_t)XI * 256 * 36864, 0, 256 * 36864, 0x00020000);
#pragma unroll
    for (int mb = 0; mb < 4; ++mb) {
      auto s0 = __builtin_amdgcn_permlane16_swap(pk_bf16(acc[mb][2][0], acc[mb][2][1]), pk_bf16(acc[mb][3][0], acc[mb][3][1]), false, false);
      auto s1 = __builtin_amdgcn_permlane16_swap(pk_bf16(acc[mb][2][2], acc[mb][2][3]), pk_bf16(acc[mb][3][2], acc[mb][3][3]), false, false);
      __builtin_amdgcn_raw_buffer_store_b128((u32x4){s0[0], s1[0], s0[1], s1[1]}, rs, w * 4096 + lane * 16 + mb * 1024, blockIdx.x * XSTR, 16);
    }
    f32x4 y[4][2];
#pragma unroll
    for (int mb = 0; mb < 4; ++mb)
#pragma unroll
      for (int j = 0; j < 4; ++j) {
        auto s = __builtin_amdgcn_permlane16_swap(__float_as_uint(acc[mb][0][j]), __float_as_uint(acc[mb][1][j]), false, false);
        y[mb][0][j] = __uint_as_float(s[0]) + ((j & 1) ? bf_hi(res[mb][j >> 1]) : bf_lo(res[mb][j >> 1]));
        y[mb][1][j] = __uint_as_float(s[1]) + ((j & 1) ? bf_hi(res[mb][2 + (j >> 1)]) : bf_lo(res[mb][2 + (j >> 1)]));
      }
    const int row0 = tm * 128 + wm * 64 + fr, col = tn * 128 + wx * 64 + kh * 32 + chunk * 8;
    unsigned optr = (ID == 2) ? ((row0 >> 4) * (D_ / 32) + (col >> 5)) * 1024 + (chunk * 16 + fr) * 16 : (row0 * D_ + col) * 2;
    const auto ro = OUT_RSRC(p.out, S_ * D_ * 2);
    asm volatile("" : "+v"(y[0][0]), "+v"(y[0][1]), "+v"(y[1][0]), "+v"(y[1][1]), "+v"(y[2][0]), "+v"(y[2][1]), "+v"(y[3][0]), "+v"(y[3][1]), "+v"(optr));
    wait_vm<0>();
    barrier();
    if (w == 0) {
      unsigned* fl = (unsigned*)((char*)p.flags + XI * 256 * 128);
      if (lane == 0) __hip_atomic_store(fl + blockIdx.x * 32, 1u, __ATOMIC_RELAXED, __HIP_MEMORY_SCOPE_AGENT);
      unsigned spins = 0;
      while (rfl(__hip_atomic_load(fl + (blockIdx.x ^ 1) * 32, __ATOMIC_RELAXED, __HIP_MEMORY_SCOPE_AGENT)) != 1u && ++spins < (1u << 22)) __builtin_amdgcn_s_sleep(1);
    }
    barrier();
    u32x4 in[4];
#pragma unroll
    for (int i = 0; i < 4; ++i) in[i] = __builtin_amdgcn_raw_buffer_load_b128(rs, w * 4096 + lane * 16 + i * 1024, (blockIdx.x ^ 1) * XSTR, 16);
    u32x4 v[4];
#pragma unroll
    for (int mb = 0; mb < 4; ++mb) {
      const f32x4 a = y[mb][0] + (f32x4){bf_lo(in[mb][0]), bf_hi(in[mb][0]), bf_lo(in[mb][1]), bf_hi(in[mb][1])};
      const f32x4 b = y[mb][1] + (f32x4){bf_lo(in[mb][2]), bf_hi(in[mb][2]), bf_lo(in[mb][3]), bf_hi(in[mb][3])};
      v[mb] = (u32x4){pk_bf16(a[0], a[1]), pk_bf16(a[2], a[3]), pk_bf16(b[0], b[1]), pk_bf16(b[2], b[3])};
      STORE16(ro, optr + mb * 16 * D_ * 2, v[mb]);
    }
    SB();
    if constexpr (ID == 2) {
      float m[4], q[4];
#pragma unroll
      for (int mb = 0; mb < 4; ++mb) {
        float s = 0.f;
#pragma unroll
        for (int j = 0; j < 4; ++j) s += bf_lo(v[mb][j]) + bf_hi(v[mb][j]);
        m[mb] = sum_rows(s) * (1.0f / 32.0f);
      }
#pragma unroll
      for (int mb = 0; mb < 4; ++mb) {
        float s = 0.f;
#pragma unroll
        for (int j = 0; j < 4; ++j) {
          const float a = bf_lo(v[mb][j]) - m[mb], b = bf_hi(v[mb][j]) - m[mb];
          s += a * a + b * b;
        }
        q[mb] = sum_rows(s);
      }
      const float ms = (fq & 2) ? ((fq & 1) ? m[3] : m[2]) : ((fq & 1) ? m[1] : m[0]);
      const float qs = (fq & 2) ? ((fq & 1) ? q[3] : q[2]) : ((fq & 1) ? q[1] : q[0]);
      LAS f32x2* tb = (LAS f32x2*)(lds0 + SQ_TAB);
      if (wx) tb[wm * 64 + lane] = (f32x2){ms, qs};
      wait_lgkm0();
      barrier();
      if (!wx) {
        const f32x2 o = tb[wm * 64 + lane];
        const float d = ms - o.x;
        ((f32x2*)p.stats)[((size_t)tn * S_ + tm * 128 + wm * 64 + lane) * 2 + kh] = (f32x2){0.5f * (ms + o.x), qs + o.y + 16.0f * d * d};
      }
    }
  }
  STAMPX(ID, 4);
  STAMP(ID, 3);
}

struct FArgs {
  const void *cre, *cim, *ab, *x, *wg, *wu, *wd;
  void *h, *stats, *gu, *out, *xch, *flags;
};
constexpr int F_RING = 6144, F_ST = 32768, F_TAB = F_RING + 4 * F_ST, F_LDS = F_TAB + 5120, F_A = 16384, G3_UP = 7168 + 16;

__device__ __forceinline__ void poll_ge(const void* ptr, unsigned need) {
  unsigned spins = 0;
  while (rfl(__hip_atomic_load((const unsigned*)ptr, __ATOMIC_RELAXED, __HIP_MEMORY_SCOPE_AGENT)) < need && ++spins < (1u << 22)) __builtin_amdgcn_s_sleep(1);
}
struct Ld {
  u32x4 qa, qb;
  unsigned va[4], vb[4], cls;
};
template <int ID>
__device__ __forceinline__ Ld ld_setup(const FArgs& p, unsigned lw, unsigned xcd, unsigned idx) {
  const int lane = lane_id();
  Ld L;
  if constexpr (ID == 3) {
    const unsigned tm = idx & 7, tn = xcd * 4 + (idx >> 3);
    L.qa = make_rsrc((const u16*)p.h + (size_t)tm * 256 * D_, 256 * D_ * 2);
    L.qb = make_rsrc(lw < 2 ? p.wg : p.wu, (unsigned)D_ * F_ * 2);
#pragma unroll
    for (int i = 0; i < 4; ++i) {
      L.va[i] = (lw * 4 + i) * (D_ / 32) * 1024 + lane * 16 - i * 1024;
      const int c = ((lw & 1) * 4 + i) * 64 + lane, r = c / 14, pos = c % 14;
      const int k = (r & 0x13) | ((r & 4) << 1) | ((r & 8) >> 1);
      L.vb[i] = k * F_ * 2 + tn * 224 + pos * 16 - i * 1024;
    }
    L.cls = (tm + 2 * (idx >> 3)) & 7;
  } else {
    const unsigned li = idx & 3, lj = idx >> 2, kh = xcd & 1, tm = (xcd >> 1) * 4 + li, tn = lj;
    if constexpr (ID == 2) {
      L.qa = make_rsrc((const u16*)(kh ? p.cim : p.cre) + (size_t)tm * 128 * S_, 128 * S_ * 2);
      L.qb = make_rsrc((const u16*)p.ab + (size_t)tn * 128 * 4096 + kh * 64 * 512, 128 * 4096 * 2 - kh * 64 * 1024);
    } else {
      L.qa = make_rsrc((const u16*)p.gu + (size_t)tm * 128 * F_ + kh * 56 * 512, 128 * F_ * 2 - kh * 56 * 1024);
      L.qb = make_rsrc((const u16*)p.wd + (size_t)kh * 1792 * D_, 1792 * D_ * 2);
    }
#pragma unroll
    for (int i = 0; i < 4; ++i) {
      const int pc = lw * 4 + i;
      if constexpr (ID == 2) {
        const int row = pc * 8 + (lane >> 3);
        L.va[i] = row * S_ * 2 + (((lane & 7) ^ ((row >> 1) & 7)) << 4) - i * 1024;
        L.vb[i] = ((pc >> 1) * 128 + (pc & 1)) * 1024 + lane * 16 - i * 1024;
      } else {
        const int k = pc * 4 + (lane >> 4), f = (k & 3) | (((k >> 3) & 1) << 2);
        L.va[i] = ((pc >> 1) * (F_ / 32) + (pc & 1)) * 1024 + lane * 16 - i * 1024;
        L.vb[i] = k * D_ * 2 + tn * 256 + (((lane & 15) ^ (f << 1)) << 4) - i * 1024;
      }
    }
    L.cls = (ID == 2) ? ((lj + 2 * li) & 7) : ((lj + li) & 3);
  }
  return L;
}
__device__ __forceinline__ void ld_pin(Ld& L) {
  asm volatile("" : "+v"(L.va[0]), "+v"(L.va[1]), "+v"(L.va[2]), "+v"(L.va[3]), "+v"(L.vb[0]), "+v"(L.vb[1]), "+v"(L.vb[2]), "+v"(L.vb[3]));
}
template <int ID> constexpr int nt_of() { return ID == 4 ? 28 : 32; }
template <int ID>
__device__ __forceinline__ void ld_a(const Ld& L, unsigned ring, unsigned lw, unsigned n) {
  const unsigned st = stage_of<(ID == 4 ? 4 : 8), nt_of<ID>(), 1>(n, L.cls);
  dma_grp<4, ID != 2>(ring + (n & 3) * F_ST + lw * 4096, L.va, L.qa, st * (ID == 2 ? 128 : (ID == 3 ? 1024 : 2048)));
}
template <int ID>
__device__ __forceinline__ void ld_b(const Ld& L, unsigned ring, unsigned lw, unsigned n) {
  const unsigned st = stage_of<(ID == 4 ? 4 : 8), nt_of<ID>(), 1>(n, L.cls);
  const unsigned dst = ring + (n & 3) * F_ST + F_A;
  if constexpr (ID == 3) {
    const unsigned d = dst + (lw >> 1) * G3_UP + (lw & 1) * 4096;
    if (lw & 1) dma_grp<3>(d, L.vb, L.qb, st * (32 * F_ * 2));
    else dma_grp<4>(d, L.vb, L.qb, st * (32 * F_ * 2));
  } else {
    dma_grp<4>(dst + lw * 4096, L.vb, L.qb, st * (ID == 2 ? 2048 : 64 * D_ * 2));
  }
}

template <int ID, bool PRE, int NEXT>
__device__ __forceinline__ void sq_loader(const FArgs& p, const Ld& L, Ld& N, unsigned ring, unsigned lw, unsigned xcd, unsigned idx) {
  constexpr int NT = nt_of<ID>();
  if (PRE) {
    ld_a<ID>(L, ring, lw, 0); ld_a<ID>(L, ring, lw, 1);
  } else {
    ld_a<ID>(L, ring, lw, 0); ld_b<ID>(L, ring, lw, 0);
    if constexpr (NEXT != 0) { N = ld_setup<NEXT>(p, lw, xcd, idx); ld_pin(N); }
    ld_a<ID>(L, ring, lw, 1); ld_b<ID>(L, ring, lw, 1);
  }
  wait_vm<(PRE ? 4 : 8)>();
  unsigned touch = 0;
  barrier();
#pragma unroll 1
  for (unsigned n = 0; n + 2 < NT; ++n) {
    ld_a<ID>(L, ring, lw, n + 2);
    ld_b<ID>(L, ring, lw, n + 2);
    wait_vm<8>();
    barrier();
  }
  if constexpr (NEXT != 0) { ld_b<NEXT>(N, ring, lw, 0); wait_vm<3>(); } else wait_vm<0>();
  barrier();
  if constexpr (NEXT != 0) {
    ld_b<NEXT>(N, ring, lw, 1);
    if (FT_W && lw < 2 * FT_W) {
      const int lane = lane_id();
      const char* tp = (const char*)((lw & 1) ? p.wu : p.wg) + (size_t)((lw >> 1) * 256 + idx * 8 + (lane >> 3)) * (F_ * 2) + xcd * 896 + ((lane & 7) < 7 ? (lane & 7) : 6) * 128;
      asm volatile("global_load_dword %0, %1, off" : "+v"(touch) : "v"(tp) : "memory");
    }
  }
  barrier();
  barrier();
  barrier();
  barrier();
  {
    unsigned* fl = (unsigned*)((char*)p.flags + (ID == 2 ? 0 : 256 * 128));
    if (lw == 1) poll_ge(fl + (blockIdx.x ^ 1) * 32, 1u);
  }
  barrier();
  if constexpr (ID == 2) barrier();
  asm volatile("s_waitcnt vmcnt(0)" : "+v"(touch));
}

template <int ID>
__device__ __forceinline__ void sq_compute(const FArgs& p, unsigned lds0, unsigned w, unsigned xcd, unsigned idx) {
  constexpr int NT = nt_of<ID>();
  const unsigned ring = lds0 + F_RING;
  const unsigned g = w >> 2, wm = (w >> 1) & 1, wx = w & 1;
  const unsigned kh = xcd & 1, tm = (xcd >> 1) * 4 + (idx & 3), tn = idx >> 2;
  u32x4 res[2];
  unsigned rdA[4], rdB[4];
  {
    const int lane = lane_id(), fr = lane & 15, fq = lane >> 4, chunk = ((fq & 1) << 1) | (fq >> 1);
    const int row = tm * 128 + wm * 64 + g * 32 + fr, col = tn * 128 + wx * 64 + kh * 32 + chunk * 8;
    if constexpr (ID == 2) {
#pragma unroll
      for (int mb = 0; mb < 2; ++mb) res[mb] = *(const u32x4*)((const u16*)p.x + (size_t)(row + mb * 16) * D_ + col);
    } else {
      const auto rh = OUT_RSRC(p.h, S_ * D_ * 2);
#pragma unroll
      for (int mb = 0; mb < 2; ++mb)
        res[mb] = __builtin_amdgcn_raw_buffer_load_b128(rh, (((row >> 4) + mb) * (D_ / 32) + (col >> 5)) * 1024 + (chunk * 16 + fr) * 16, 0, 16);
    }
    const unsigned rotA = 2 * g, rotB = 2 * kh, base = ring + g * F_ST;
#pragma unroll
    for (int i = 0; i < 4; ++i) {
      const unsigned kk = i >> 1, hf = (i & 1) * 2;
      if constexpr (ID == 2) {
        rdA[i] = base + (wm * 64 + (hf ^ rotA) * 16 + fr) * 128 + (((kk * 4 + fq) ^ (fr >> 1)) << 4);
        rdB[i] = base + F_A + wx * 8192 + (hf ^ rotB) * 2048 + lane * 16;
      } else {
        const int q = fr >> 2, pp = fr & 3, f = q | ((fq & 1) << 2);
        rdA[i] = base + wm * 8192 + (hf ^ rotA) * 2048 + lane * 16;
        rdB[i] = base + F_A + (8 * fq + q) * 256 + (((wx * 4 + (i ^ rotB)) ^ f) << 5) + pp * 8;
      }
    }
  }
  constexpr int NVA = (ID == 2) ? 4 : 2, NVB = (ID == 2) ? 2 : 4;
  bf16x8 fa[2][4], fb[2][4];
  f32x4 acc[4][4];
#pragma unroll
  for (int i = 0; i < 16; ++i) { acc[i >> 2][i & 3] = (f32x4){0.f, 0.f, 0.f, 0.f}; asm volatile("" : "+v"(acc[i >> 2][i & 3])); }
  auto reads = [&]() {
    SB();
#pragma unroll
    for (int s = 0; s < 2; ++s)
#pragma unroll
      for (int i = 0; i < 4; ++i) {
        if constexpr (ID == 2) {
          fb[s][i] = lds_b128(rdB[i >> 1] + (i & 1) * 2048 + s * 1024);
          fa[s][i] = lds_b128(rdA[s * 2 + (i >> 1)] + (i & 1) * 2048);
        } else {
          fb[s][i] = lds_tr(rdB[i] + s * 8192, rdB[i] + s * 8192 + 1024);
          fa[s][i] = lds_b128(rdA[i >> 1] + (i & 1) * 2048 + s * 1024);
        }
      }
    SB();
  };
  barrier();
  STAMP1_TAKE();
  if (g == 1) barrier();
  reads();
#pragma unroll 1
  for (unsigned i = 0; i < NT / 2; ++i) {
    const unsigned delta = (i & 1) ? (unsigned)(-2 * F_ST) : (unsigned)(2 * F_ST);
#pragma unroll
    for (int j = 0; j < NVA; ++j) { rdA[j] += delta; asm volatile("" : "+v"(rdA[j])); }
#pragma unroll
    for (int j = 0; j < NVB; ++j) { rdB[j] += delta; asm volatile("" : "+v"(rdB[j])); }
    wait_lgkm0();
    barrier();
    SB();
#pragma unroll
    for (int m = 0; m < 32; ++m) MFMA(fb[m >> 4][m & 3], fa[m >> 4][(m >> 2) & 3], acc[(m >> 2) & 3][m & 3]);
    SB();
    barrier();
    if (i + 1 < NT / 2) reads();
  }
  mfma_drain();
  SB();
  const int lane = lane_id(), fr = lane & 15, fq = lane >> 4, chunk = ((fq & 1) << 1) | (fq >> 1);
  {
    const unsigned wb = ring + 2 * F_ST + (w ^ 4) * 8192 + lane * 16, rb = ring + 2 * F_ST + w * 8192 + lane * 16;
#pragma unroll
    for (int i = 0; i < 8; ++i) *(LAS f32x4*)(wb + i * 1024) = acc[2 + (i >> 2)][i & 3];
    if (g == 0) barrier();
    STAMP1_PUT(ID);
    STAMP(ID, 2);
    wait_lgkm0();
    barrier();
#pragma unroll
    for (int i = 0; i < 8; ++i) acc[i >> 2][i & 3] += *(const LAS f32x4*)(rb + i * 1024);
  }
  const auto rs = OUT_RSRC((char*)p.xch + (ID == 2 ? 0 : 256 * 16384), 256 * 16384);
#pragma unroll
  for (int mb = 0; mb < 2; ++mb) {
    auto s0 = __builtin_amdgcn_permlane16_swap(pk_bf16(acc[mb][2][0], acc[mb][2][1]), pk_bf16(acc[mb][3][0], acc[mb][3][1]), false, false);
    auto s1 = __builtin_amdgcn_permlane16_swap(pk_bf16(acc[mb][2][2], acc[mb][2][3]), pk_bf16(acc[mb][3][2], acc[mb][3][3]), false, false);
    __builtin_amdgcn_raw_buffer_store_b128((u32x4){s0[0], s1[0], s0[1], s1[1]}, rs, w * 2048 + lane * 16 + mb * 1024, blockIdx.x * 16384, 16);
  }
  f32x4 y[2][2];
#pragma unroll
  for (int mb = 0; mb < 2; ++mb)
#pragma unroll
    for (int j = 0; j < 4; ++j) {
      auto s = __builtin_amdgcn_permlane16_swap(__float_as_uint(acc[mb][0][j]), __float_as_uint(acc[mb][1][j]), false, false);
      y[mb][0][j] = __uint_as_float(s[0]) + ((j & 1) ? bf_hi(res[mb][j >> 1]) : bf_lo(res[mb][j >> 1]));
      y[mb][1][j] = __uint_as_float(s[1]) + ((j & 1) ? bf_hi(res[mb][2 + (j >> 1)]) : bf_lo(res[mb][2 + (j >> 1)]));
    }
  const int row0 = tm * 128 + wm * 64 + g * 32 + fr, col = tn * 128 + wx * 64 + kh * 32 + chunk * 8;
  unsigned optr = (ID == 2) ? ((row0 >> 4) * (D_ / 32) + (col >> 5)) * 1024 + (chunk * 16 + fr) * 16 : (row0 * D_ + col) * 2;
  const auto ro = OUT_RSRC(ID == 2 ? p.h : p.out, S_ * D_ * 2);
  asm volatile("" : "+v"(y[0][0]), "+v"(y[0][1]), "+v"(y[1][0]), "+v"(y[1][1]), "+v"(optr));
  wait_vm<0>();
  barrier();
  if (w == 0 && lane == 0) __hip_atomic_store((unsigned*)((char*)p.flags + ((ID == 2 ? 0 : 256) + blockIdx.x) * 128), 1u, __ATOMIC_RELAXED, __HIP_MEMORY_SCOPE_AGENT);
  barrier();
  u32x4 in[2], v[2];
#pragma unroll
  for (int i = 0; i < 2; ++i) in[i] = __builtin_amdgcn_raw_buffer_load_b128(rs, w * 2048 + lane * 16 + i * 1024, (blockIdx.x ^ 1) * 16384, 16);
#pragma unroll
  for (int mb = 0; mb < 2; ++mb) {
    const f32x4 a = y[mb][0] + (f32x4){bf_lo(in[mb][0]), bf_hi(in[mb][0]), bf_lo(in[mb][1]), bf_hi(in[mb][1])};
    const f32x4 b = y[mb][1] + (f32x4){bf_lo(in[mb][2]), bf_hi(in[mb][2]), bf_lo(in[mb][3]), bf_hi(in[mb][3])};
    v[mb] = (u32x4){pk_bf16(a[0], a[1]), pk_bf16(a[2], a[3]), pk_bf16(b[0], b[1]), pk_bf16(b[2], b[3])};
    STORE16(ro, optr + mb * 16 * D_ * 2, v[mb]);
  }
  SB();
  if constexpr (ID == 2) {
    float m[2], q[2];
#pragma unroll
    for (int mb = 0; mb < 2; ++mb) {
      float s = 0.f;
#pragma unroll
      for (int j = 0; j < 4; ++j) s += bf_lo(v[mb][j]) + bf_hi(v[mb][j]);
      m[mb] = sum_rows(s) * (1.0f / 32.0f);
    }
#pragma unroll
    for (int mb = 0; mb < 2; ++mb) {
      float s = 0.f;
#pragma unroll
      for (int j = 0; j < 4; ++j) {
        const float a = bf_lo(v[mb][j]) - m[mb], b = bf_hi(v[mb][j]) - m[mb];
        s += a * a + b * b;
      }
      q[mb] = sum_rows(s);
    }
    const float ms = (fq & 1) ? m[1] : m[0], qs = (fq & 1) ? q[1] : q[0];
    LAS f32x2* tb = (LAS f32x2*)(lds0 + F_TAB + 3072);
    const int r = wm * 64 + g * 32 + (lane & 31);
    if (wx) tb[r] = (f32x2){ms, qs};
    wait_lgkm0();
    barrier();
    if (!wx) {
      const f32x2 o = tb[r];
      const float d = ms - o.x;
      const f32x2 st = {0.5f * (ms + o.x), qs + o.y + 16.0f * d * d};
      __hip_atomic_store((unsigned long long*)p.stats + ((size_t)tn * S_ + tm * 128 + r) * 2 + kh, __builtin_bit_cast(unsigned long long, st), __ATOMIC_RELAXED,
                         __HIP_MEMORY_SCOPE_AGENT);
    }
  }
}

template <bool PRE, int NEXT>
__device__ __forceinline__ void glu_loader(const FArgs& p, const Ld& L, Ld& N, unsigned lds0, unsigned lw, unsigned xcd, unsigned idx) {
  constexpr int NT = 32;
  const unsigned ring = lds0 + F_RING;
  LAS float* cst = (LAS float*)(lds0 + F_TAB + 2048);
  unsigned long long* gcs = (unsigned long long*)((char*)p.flags + FL_CS) + (xcd * 4 + (idx >> 3)) * (8 * 224);
  if (PRE) {
    ld_a<3>(L, ring, lw, 0); ld_a<3>(L, ring, lw, 1);
  } else {
    ld_a<3>(L, ring, lw, 0); ld_b<3>(L, ring, lw, 0); ld_a<3>(L, ring, lw, 1); ld_b<3>(L, ring, lw, 1);
  }
  if constexpr (NEXT != 0) { N = ld_setup<NEXT>(p, lw, xcd, idx); ld_pin(N); }
  const int lane = lane_id();
  const unsigned u = lw * 64 + lane < 224 ? lw * 64 + lane : 223u;
  wait_vm<(PRE ? 4 : 7)>();
  barrier();
  ld_a<3>(L, ring, lw, 2);
  ld_b<3>(L, ring, lw, 2);
#pragma unroll 1
  for (unsigned t = 0; t + 3 < NT; ++t) {
    if (t == 25) {
      const unsigned hf = u >= 112, c = u - hf * 112, wn_ = c >= 56, cc = c - wn_ * 56, blk = cc >> 4;
      const unsigned src = lds0 + (wn_ * 4 + blk) * 768 + (blk == 3 ? 512 + (hf * 8 + (cc & 7)) * 4 : hf * 256 + (cc & 15) * 4);
      const float v = (*(const LAS float*)(src) + *(const LAS float*)(src + 64)) + (*(const LAS float*)(src + 128) + *(const LAS float*)(src + 192));
      asm volatile("global_store_dwordx2 %0, %1, off sc1" ::"v"(gcs + L.cls * 224 + u), "v"((1ull << 32) | __float_as_uint(v)) : "memory");
    }
    ld_a<3>(L, ring, lw, t + 3);
    barrier();
    ld_b<3>(L, ring, lw, t + 3);
    wait_vm<14>();
    barrier();
  }
  unsigned long long g[8];
#pragma unroll
  for (int i = 0; i < 8; ++i) asm volatile("global_load_dwordx2 %0, %1, off sc1" : "=v"(g[i]) : "v"(gcs + i * 224 + u) : "memory");
  barrier();
  if constexpr (NEXT != 0) { ld_b<NEXT>(N, ring, lw, 0); wait_vm<19>(); } else wait_vm<15>();
  barrier();
  barrier();
  if constexpr (NEXT != 0) { ld_b<NEXT>(N, ring, lw, 1); wait_vm<16>(); } else wait_vm<8>();
  barrier();
  barrier();
  barrier();
  if constexpr (NEXT != 0) asm volatile("s_waitcnt vmcnt(8)" : "+v"(g[0]), "+v"(g[1]), "+v"(g[2]), "+v"(g[3]), "+v"(g[4]), "+v"(g[5]), "+v"(g[6]), "+v"(g[7]));
  else asm volatile("s_waitcnt vmcnt(0)" : "+v"(g[0]), "+v"(g[1]), "+v"(g[2]), "+v"(g[3]), "+v"(g[4]), "+v"(g[5]), "+v"(g[6]), "+v"(g[7]));
#pragma unroll 1
  for (unsigned spins = 0; spins < (1u << 15); ++spins) {
    unsigned tg = 1u;
#pragma unroll
    for (int i = 0; i < 8; ++i) tg &= (unsigned)(g[i] >> 32);
    if (__all(tg == 1u)) break;
    __builtin_amdgcn_s_sleep(2);
#pragma unroll
    for (int i = 0; i < 8; ++i) asm volatile("global_load_dwordx2 %0, %1, off sc1\n\ts_waitcnt vmcnt(0)" : "=&v"(g[i]) : "v"(gcs + i * 224 + u) : "memory");
  }
  float s = 0.f;
#pragma unroll
  for (int i = 0; i < 8; ++i) s += __uint_as_float((unsigned)g[i]);
  cst[u] = s;
  wait_lgkm0();
  unsigned touch = 0;
  if (NEXT != 0 && FT_D == 1) {
    const char* tp = (const char*)p.wd + ((size_t)blockIdx.x * 224 + (lw * 56 + (lane < 56 ? lane : 55))) * 128;
    asm volatile("global_load_dword %0, %1, off" : "+v"(touch) : "v"(tp) : "memory");
  }
  if (NEXT != 0 && FT_D == 2) {
    const char* tp = (const char*)p.wd + (size_t)((xcd & 1) * 1792 + idx * 16 + lw * 4 + (lane >> 4)) * (D_ * 2) + (lane & 15) * 128;
    asm volatile("global_load_dword %0, %1, off" : "+v"(touch) : "v"(tp) : "memory");
  }
  barrier();
  asm volatile("s_waitcnt vmcnt(0)" : "+v"(touch));
}

__device__ __forceinline__ void glu_compute(const FArgs& p, unsigned lds0, unsigned w, unsigned xcd, unsigned idx) {
  constexpr int ID = 3, BM = 256, NT = 32, MB = 4, NB = 7;
  const unsigned ring = lds0 + F_RING;
  LAS f32x2* tab = (LAS f32x2*)(lds0 + F_TAB);
  LAS float* cst = (LAS float*)(lds0 + F_TAB + 2048);
  const unsigned tm = idx & 7, tn = xcd * 4 + (idx >> 3), cls = (tm + 2 * (idx >> 3)) & 7;
  const unsigned wm = w & 3, wn = w >> 2;
  const int lane = lane_id(), fr = lane & 15, fq = lane >> 4;
  const unsigned rot = wm == 3 ? 0u : wm;
  unsigned xa = ring + wm * 4096 + lane * 16, xb[3], xb1;
  {
    const int q = fr >> 2, pp = fr & 3, r = (fq >> 1) * 16 + (fq & 1) * 4 + q;
#pragma unroll
    for (int i = 0; i < 3; ++i) xb[i] = ring + F_A + r * 224 + wn * 112 + pp * 8 + ((i + rot) % 3) * 32;
    xb1 = ring + F_A + r * 224 + wn * 112 + 96 + (pp & 1) * 8 + (pp >> 1) * G3_UP;
  }
  float cs[3] = {0.f, 0.f, 0.f};
  if (wn == 1) {
    const auto rst = OUT_RSRC(p.stats, 8 * S_ * 16);
    f32x4 sv[8];
#pragma unroll
    for (int i = 0; i < 8; ++i) sv[i] = __builtin_bit_cast(f32x4, __builtin_amdgcn_raw_buffer_load_b128(rst, (tm * BM + wm * 64 + lane) * 16, i * S_ * 16, 16));
    SB();
    barrier();
    SB();
    float m = 0.f;
#pragma unroll
    for (int i = 0; i < 8; ++i) m += sv[i][0] + sv[i][2];
    m *= (1.0f / 16.0f);
    float q = 0.f;
#pragma unroll
    for (int i = 0; i < 8; ++i) {
      const float d0 = sv[i][0] - m, d1 = sv[i][2] - m;
      q += sv[i][1] + sv[i][3] + 64.0f * (d0 * d0 + d1 * d1);
    }
    tab[wm * 64 + lane] = (f32x2){m, __builtin_amdgcn_rsqf(q * (1.0f / D_) + EPS)};
  }
  bf16x8 fa[MB], fb[NB];
  f32x4 acc[MB][NB];
#pragma unroll
  for (int mb = 0; mb < MB; ++mb)
#pragma unroll
    for (int nb = 0; nb < NB; ++nb) { acc[mb][nb] = (f32x4){0.f, 0.f, 0.f, 0.f}; asm volatile("" : "+v"(acc[mb][nb])); }
  asm volatile("" : "+v"(xa), "+v"(xb[0]), "+v"(xb[1]), "+v"(xb[2]), "+v"(xb1));
  wait_lgkm0();
  barrier();
  STAMP1_TAKE();
  const unsigned csmask = (0x010101u << cls) | (1u << ((cls + 1) & 7));
  auto reads = [&]() {
    SB();
    fa[0] = lds_b128(xa);
#pragma unroll
    for (int j = 0; j < 7; ++j) {
      const unsigned o = j < 6 ? xb[j % 3] + (j / 3) * G3_UP : xb1;
      fb[j] = lds_tr(o, o + 1792);
    }
#pragma unroll
    for (int mb = 1; mb < MB; ++mb) fa[mb] = lds_b128(xa + mb * 1024);
    SB();
  };
  reads();
#pragma unroll 1
  for (unsigned t = 0; t < NT; ++t) {
    const unsigned delta = ((t & 3) == 3) ? (unsigned)(-3 * F_ST) : (unsigned)F_ST;
    xa += delta; xb[0] += delta; xb[1] += delta; xb[2] += delta; xb1 += delta;
    asm volatile("" : "+v"(xa), "+v"(xb[0]), "+v"(xb[1]), "+v"(xb[2]), "+v"(xb1));
    if (__builtin_expect((csmask >> t) & 1u, 0)) {
#pragma unroll
      for (int j = 0; j < 4; ++j)
#pragma unroll
        for (int i = 0; i < 3; ++i)
          asm volatile("v_dot2c_f32_bf16 %0, %1, %2" : "+v"(cs[i]) : "s"(0x3f803f80u), "v"(__builtin_bit_cast(u32x4, fb[3 * i])[j]));
      asm volatile("s_mov_b32 m0, %3\n\ts_nop 2\n\tds_write_addtid_b32 %0\n\tds_write_addtid_b32 %1 offset:256\n\tds_write_addtid_b32 %2 offset:512" ::"v"(cs[0]),
                   "v"(cs[1]), "v"(cs[2]), "s"(lds0 + w * 768)
                   : "memory");
    }
    wait_lgkm0();
    barrier();
    SB();
#pragma unroll
    for (int mb = 0; mb < MB; ++mb)
#pragma unroll
      for (int nb = 0; nb < NB; ++nb) MFMA(fb[nb], fa[mb], acc[mb][nb]);
    SB();
    barrier();
    reads();
  }
  if (wn == 0) barrier();
  mfma_drain();
  SB();
  const int lane2 = lane_id();
  const int gr = lane2 & 15, gq = lane2 >> 4;
  STAMP1_PUT(ID);
  STAMP(ID, 2);
  auto act = [](float g, float u) { return g * u * __builtin_amdgcn_rcpf(1.0f + __expf(-g)); };
  const auto ro = OUT_RSRC(p.gu, S_ * F_ * 2);
  f32x2 st[MB];
#pragma unroll
  for (int mb = 0; mb < MB; ++mb) st[mb] = tab[wm * 64 + mb * 16 + gr];
  const int gch0 = tn * 14 + wn * 7;
#pragma unroll
  for (int i = 0; i < 3; ++i) {
    const unsigned ib = (i + rot) % 3;
    const f32x4 cg = *(const LAS f32x4*)(cst + wn * 56 + ib * 16 + gq * 4), cu = *(const LAS f32x4*)(cst + 112 + wn * 56 + ib * 16 + gq * 4);
    u32x2 pkd[MB];
#pragma unroll
    for (int mb = 0; mb < MB; ++mb) {
      const f32x4 g = (acc[mb][i] - cg * st[mb].x) * st[mb].y, u = (acc[mb][3 + i] - cu * st[mb].x) * st[mb].y;
      pkd[mb] = (u32x2){pk_bf16(act(g[0], u[0]), act(g[1], u[1])), pk_bf16(act(g[2], u[2]), act(g[3], u[3]))};
    }
#pragma unroll
    for (int mp = 0; mp < MB / 2; ++mp) {
      auto s0 = __builtin_amdgcn_permlane16_swap(pkd[2 * mp].x, pkd[2 * mp + 1].x, false, false);
      auto s1 = __builtin_amdgcn_permlane16_swap(pkd[2 * mp].y, pkd[2 * mp + 1].y, false, false);
      const u32x4 v = {s0[0], s1[0], s0[1], s1[1]};
      const int rblk = tm * (BM / 16) + wm * MB + 2 * mp + (gq & 1);
      const int gch = gch0 + 2 * ib + (gq >> 1);
      STORE16(ro, (rblk * (F_ / 32) + (gch >> 2)) * 1024 + ((gch & 3) * 16 + gr) * 16, v);
    }
  }
  {
    const f32x4 cm = *(const LAS f32x4*)(cst + (gq >> 1) * 112 + wn * 56 + 48 + (gq & 1) * 4);
    u32x2 pkd[MB / 2];
#pragma unroll
    for (int mp = 0; mp < MB / 2; ++mp) {
      const f32x4 v0 = (acc[2 * mp][6] - cm * st[2 * mp].x) * st[2 * mp].y, v1 = (acc[2 * mp + 1][6] - cm * st[2 * mp + 1].x) * st[2 * mp + 1].y;
      float r[4];
#pragma unroll
      for (int j = 0; j < 4; ++j) {
        auto s = __builtin_amdgcn_permlane32_swap(__float_as_uint(v0[j]), __float_as_uint(v1[j]), false, false);
        r[j] = act(__uint_as_float(s[0]), __uint_as_float(s[1]));
      }
      pkd[mp] = (u32x2){pk_bf16(r[0], r[1]), pk_bf16(r[2], r[3])};
    }
    auto s0 = __builtin_amdgcn_permlane16_swap(pkd[0].x, pkd[1].x, false, false);
    auto s1 = __builtin_amdgcn_permlane16_swap(pkd[0].y, pkd[1].y, false, false);
    const u32x4 v = {s0[0], s1[0], s0[1], s1[1]};
    const int rblk = tm * (BM / 16) + wm * MB + (gq & 1) * 2 + (gq >> 1);
    const int gch = gch0 + 6;
    STORE16(ro, (rblk * (F_ / 32) + (gch >> 2)) * 1024 + ((gch & 3) * 16 + gr) * 16, v);
  }
}

template <int P0, int P1>
__global__ __launch_bounds__(768, 3) void k_fused(FArgs p) {
  if constexpr (P0 == 2) CODE_LABEL(fz_code_f);
  extern __shared__ __attribute__((aligned(16))) char shm[];
  const unsigned lds0 = (unsigned)(size_t)(LAS char*)shm;
  const unsigned w = rfl(threadIdx.x >> 6);
  const unsigned xcd = blockIdx.x & 7, idx = blockIdx.x >> 3;
  char* fl = (char*)p.flags;
  if (w >= 8) {
    const unsigned lw = w - 8, ring = lds0 + F_RING;
    Ld L2, L3, L4;
    if constexpr (P0 == 2) {
      L2 = ld_setup<2>(p, lw, xcd, idx);
      sq_loader<2, false, (P1 >= 3 ? 3 : 0)>(p, L2, L3, ring, lw, xcd, idx);
      if constexpr (P1 >= 3) {
        barrier();
        if (lw == 1) poll_ge(fl + FL_P2 + ((idx & 7) * 8 + xcd) * 128, 32u);
        barrier();
      }
    }
    if constexpr (P0 <= 3 && P1 >= 3) {
      if constexpr (P0 == 3) L3 = ld_setup<3>(p, lw, xcd, idx);
      glu_loader<(P0 < 3), (P1 >= 4 ? 4 : 0)>(p, L3, L4, lds0, lw, xcd, idx);
      if constexpr (P1 >= 4) {
        barrier();
        if (lw == 1) poll_ge(fl + FL_P3 + (((xcd >> 1) * 2 + ((idx & 3) >> 1)) * 2 + (xcd & 1)) * 128, 16u);
        barrier();
      }
    }
    if constexpr (P1 == 4) {
      if constexpr (P0 == 4) L4 = ld_setup<4>(p, lw, xcd, idx);
      sq_loader<4, (P0 < 4), 0>(p, L4, L2, ring, lw, xcd, idx);
    }
    return;
  }
  if constexpr (P0 == 2) {
    STAMP(2, 0);
    sq_compute<2>(p, lds0, w, xcd, idx);
    if constexpr (P1 >= 3) {
      wait_vm<0>();
      barrier();
      if (w == 0) {
        const int l = lane_id();
        if (l < 8) __hip_atomic_fetch_add((unsigned*)(fl + FL_P2 + (((xcd >> 1) * 2 + ((idx & 3) >> 1)) * 8 + l) * 128), 1u, __ATOMIC_RELAXED, __HIP_MEMORY_SCOPE_AGENT);
      }
      STAMP(2, 3);
      barrier();
    }
    else { STAMP_END(2); }
  }
  if constexpr (P0 <= 3 && P1 >= 3) {
    STAMP(3, 0);
    glu_compute(p, lds0, w, xcd, idx);
    if constexpr (P1 >= 4) {
      wait_vm<0>();
      barrier();
      if (w == 0 && lane_id() == 0) __hip_atomic_fetch_add((unsigned*)(fl + FL_P3 + ((idx & 7) * 2 + (xcd >> 2)) * 128), 1u, __ATOMIC_RELAXED, __HIP_MEMORY_SCOPE_AGENT);
      STAMP(3, 3);
      barrier();
    }
    else { STAMP_END(3); }
  }
  if constexpr (P1 == 4) {
    STAMP(4, 0);
    sq_compute<4>(p, lds0, w, xcd, idx);
    STAMP_END(4);
  }
}

template <int P0, int P1>
void launch_f(const FArgs& a, hipStream_t stream) {
  (void)hipFuncSetAttribute((const void*)k_fused<P0, P1>, hipFuncAttributeMaxDynamicSharedMemorySize, F_LDS);
  hipLaunchKernelGGL((k_fused<P0, P1>), dim3(256), dim3(768), F_LDS, stream, a);
}
}

#define FUSE 2
extern "C" void kernel_launch(void* const* d_in, const int* in_sizes, int n_in, void* d_out, int out_size, void* d_ws, size_t ws_size,
                              hipStream_t stream) {
  char* ws = (char*)d_ws;
  GArgs g1 = {d_in[3], d_in[4], d_in[0], d_in[6], ws + WS_AB, d_in[1], d_in[2], d_in[7], ws + WS_FL, nullptr, nullptr};
  (void)hipFuncSetAttribute((const void*)k_sq<1>, hipFuncAttributeMaxDynamicSharedMemorySize, SQ_LDS);
  hipLaunchKernelGGL(k_sq<1>, dim3(256), dim3(512), SQ_LDS, stream, g1);
  FArgs f = {d_in[1], d_in[2], ws + WS_AB, d_in[0], d_in[6], d_in[7], d_in[8], ws + WS_H, ws + WS_ST, ws + WS_GU, d_out, ws + WS_XC, ws + WS_FL};
  launch_f<2, 4>(f, stream);
}
```

```cpp
#include <hip/hip_runtime.h>
#include <hip/hip_bf16.h>

namespace {
#define LAS __attribute__((address_space(3)))
typedef unsigned short u16;
typedef short s16x4 __attribute__((ext_vector_type(4)));
typedef short s16x8 __attribute__((ext_vector_type(8)));
typedef __bf16 bf16x8 __attribute__((ext_vector_type(8)));
typedef __bf16 bf16x2 __attribute__((ext_vector_type(2)));
typedef float f32x4 __attribute__((ext_vector_type(4)));
typedef float f32x2 __attribute__((ext_vector_type(2)));
typedef unsigned u32x4 __attribute__((ext_vector_type(4)));
typedef unsigned u32x2 __attribute__((ext_vector_type(2)));

constexpr int S_ = 2048, D_ = 1024, F_ = 3584;
constexpr float EPS = 1e-5f;

constexpr size_t WS_AB = 0;
constexpr size_t WS_H = WS_AB + (size_t)D_ * 2 * S_ * 2;
constexpr size_t WS_GU = WS_H + (size_t)S_ * D_ * 2;
constexpr size_t WS_ST = WS_GU + (size_t)S_ * F_ * 2;
constexpr size_t WS_XC = WS_ST + (size_t)S_ * 32 * 8;
constexpr size_t WS_FL = WS_XC + (size_t)2 * 256 * 36864;
constexpr int FL_CS = 2 * 256 * 128;

__device__ __forceinline__ unsigned rfl(unsigned v) { return __builtin_amdgcn_readfirstlane(v); }
__device__ __forceinline__ unsigned pk_bf16(float lo, float hi) {
  bf16x2 c = {(__bf16)lo, (__bf16)hi};
  return __builtin_bit_cast(unsigned, c);
}
__device__ __forceinline__ float bf_lo(unsigned v) { return __uint_as_float(v << 16); }
__device__ __forceinline__ float bf_hi(unsigned v) { return __uint_as_float(v & 0xffff0000u); }

__device__ __forceinline__ u32x4 make_rsrc(const void* p, unsigned bytes) {
  unsigned long long a = (unsigned long long)p;
  u32x4 r;
  r.x = (unsigned)a;
  r.y = (unsigned)(a >> 32) & 0xffffu;
  r.z = bytes;
  r.w = 0x00020000u;
  return r;
}
__device__ __forceinline__ void dma16(unsigned ldsaddr, unsigned voff, u32x4 rsrc, unsigned soff) {
  asm volatile("s_mov_b32 m0, %0\n\ts_nop 4\n\tbuffer_load_dwordx4 %1, %2, %3 offen lds" ::"s"(ldsaddr), "v"(voff), "s"(rsrc), "s"(soff)
               : "memory");
}
template <int N>
__device__ __forceinline__ void wait_vm() {
  asm volatile("s_waitcnt vmcnt(%0)" ::"i"(N) : "memory");
}
__device__ __forceinline__ void wait_lgkm0() { __builtin_amdgcn_s_waitcnt(0xC07F); }
__device__ __forceinline__ void barrier() {
  asm volatile("" ::: "memory");
  __builtin_amdgcn_s_barrier();
  asm volatile("" ::: "memory");
}
__device__ __forceinline__ bf16x8 lds_b128(unsigned addr) { return *(const LAS bf16x8*)(addr); }
__device__ __forceinline__ bf16x8 lds_tr(unsigned a0, unsigned a1) {
  s16x4 lo = __builtin_amdgcn_ds_read_tr16_b64_v4i16((LAS s16x4*)(a0));
  s16x4 hi = __builtin_amdgcn_ds_read_tr16_b64_v4i16((LAS s16x4*)(a1));
  s16x8 f = __builtin_shufflevector(lo, hi, 0, 1, 2, 3, 4, 5, 6, 7);
  return __builtin_bit_cast(bf16x8, f);
}

#define ST_AUX 16
#define OUT_RSRC(ptr, bytes) __builtin_amdgcn_make_buffer_rsrc((void*)(ptr), 0, (bytes), 0x00020000)
#define STORE16(rs, byteoff, v) __builtin_amdgcn_raw_buffer_store_b128(v, rs, byteoff, 0, ST_AUX)
#define STAMP(k, i)
#define STAMPX(k, i)
#define STAMPL(k, i, first)

struct GArgs {
  const void* a0;
  const void* a1;
  const void* b0;
  const void* b1;
  void* out;
  const void* res;
  void* stats;
  void* xch;
  void* flags;
  const void* aux0;
  const void* aux1;
};

template <int N>
__device__ __forceinline__ void dma_grp(unsigned ldsaddr, const unsigned (&v)[4], u32x4 rsrc, unsigned soff) {
  if constexpr (N == 4)
    asm volatile("s_mov_b32 m0, %0\n\ts_nop 4\n\tbuffer_load_dwordx4 %1, %5, %6 offen lds\n\tbuffer_load_dwordx4 %2, %5, %6 offen offset:1024 lds\n\t"
                 "buffer_load_dwordx4 %3, %5, %6 offen offset:2048 lds\n\tbuffer_load_dwordx4 %4, %5, %6 offen offset:3072 lds" ::"s"(ldsaddr),
                 "v"(v[0]), "v"(v[1]), "v"(v[2]), "v"(v[3]), "s"(rsrc), "s"(soff)
                 : "memory");
  else
    asm volatile("s_mov_b32 m0, %0\n\ts_nop 4\n\tbuffer_load_dwordx4 %1, %4, %5 offen lds\n\tbuffer_load_dwordx4 %2, %4, %5 offen offset:1024 lds\n\t"
                 "buffer_load_dwordx4 %3, %4, %5 offen offset:2048 lds" ::"s"(ldsaddr),
                 "v"(v[0]), "v"(v[1]), "v"(v[2]), "s"(rsrc), "s"(soff)
                 : "memory");
}
__device__ __forceinline__ void lds_inc(unsigned addr) { asm volatile("ds_add_u32 %0, %1" ::"v"(addr), "v"(1u) : "memory"); }
__device__ __forceinline__ unsigned inc_base(unsigned sync, int lane) { return sync + lane * 68; }
__device__ __forceinline__ unsigned lds_peek(unsigned addr) { return *(volatile LAS unsigned*)(addr); }
__device__ __forceinline__ void lds_wait(unsigned addr, unsigned pv, unsigned need) {
  if (__builtin_expect(rfl(pv) < need, 0)) {
    do {
      __builtin_amdgcn_s_sleep(1);
    } while (rfl(lds_peek(addr)) < need);
  }
  asm volatile("" ::: "memory");
}
__device__ __forceinline__ float sum_rows(float x) {
  auto a = __builtin_amdgcn_permlane16_swap(__float_as_uint(x), __float_as_uint(x), false, false);
  x = __uint_as_float(a[0]) + __uint_as_float(a[1]);
  auto b = __builtin_amdgcn_permlane32_swap(__float_as_uint(x), __float_as_uint(x), false, false);
  return __uint_as_float(b[0]) + __uint_as_float(b[1]);
}
__device__ __forceinline__ float sum_pair(float x) { return x + __uint_as_float(__builtin_amdgcn_mov_dpp(__float_as_uint(x), 0xB1, 0xF, 0xF, true)); }
__device__ __forceinline__ int lane_id() {
  int l;
  asm volatile("v_mbcnt_lo_u32_b32 %0, -1, 0\n\tv_mbcnt_hi_u32_b32 %0, -1, %0" : "=v"(l));
  return l;
}
__device__ __forceinline__ void mfma_drain() { asm volatile("s_nop 15" ::: "memory"); }
__device__ __forceinline__ void fadd(float& a, float b) { asm("v_add_f32 %0, %1, %0" : "+v"(a) : "v"(b)); }
template <int NC, int NT, int WRAP>
__device__ __forceinline__ unsigned stage_of(unsigned n, unsigned c) {
  if (NC == 1) return n;
  if (WRAP) {
    const unsigned t = ((n & (NC - 1)) == c) ? n + NC : n;
    return rfl(t >= NT ? t - NT : t);
  }
  unsigned t = (n > NT - NC + c) ? n : n - 1;
  t = ((t & (NC - 1)) == c) ? t + NC : t;
  return rfl(n == 0 ? c : t);
}
#define NC1 4
#define NC2 8
#define NC3 8
#define NC4 4
#define TOUCH_C 1
#define TOUCH_W 1
#define TOUCH_D 2
#define TOUCH_AT 6
#define XSTR 16384
#define WRAPM 0x1c
#define SQ_PD 2
#define BAR_AT 0
#define CPRIO 0
template <int V> struct IC { static constexpr int value = V; };
#define LPRIO 0
#define SB() __builtin_amdgcn_sched_barrier(0)
#define MFMA(b, a, c) asm volatile("v_mfma_f32_16x16x32_bf16 %0, %1, %2, %0" : "+v"(c) : "v"(b), "v"(a))
#define STAMP1_TAKE()
#define STAMP1_PUT(k)

#define CODE_LABEL(name) asm volatile(#name ":")
#define CODE_ADDR(dst, name) do { int o_; asm volatile("s_getpc_b64 %0\n\ts_mov_b32 %1, " #name "@rel32@lo+4" : "=s"(dst), "=s"(o_)); dst += (long long)o_; } while (0)
#define CODE_PF 3
__device__ __forceinline__ void touch_code(unsigned long long base, int lane, int last, unsigned& sink) {
  const unsigned off = (lane < last ? lane : last) * 128;
  asm volatile("global_load_dword %0, %1, %2" : "+v"(sink) : "v"(off), "s"(base) : "memory");
}
enum { OP_RM = 0, OP_FT = 1, OP_NN = 2 };
template <int ID> struct Cfg;
template <> struct Cfg<1> { static constexpr int NT = 16, AM = OP_RM, BMODE = OP_RM, NC = NC1; };
template <> struct Cfg<2> { static constexpr int NT = 32, AM = OP_RM, BMODE = OP_FT, NC = NC2; };
template <> struct Cfg<4> { static constexpr int NT = 28, AM = OP_FT, BMODE = OP_NN, NC = NC4; };
constexpr int SQ_NST = 4, SQ_ST = 32768, SQ_SYNC = SQ_NST * SQ_ST, SQ_TAB = SQ_SYNC + 4608, SQ_LDS = SQ_TAB + 2048;

template <int ID>
__global__ __launch_bounds__(512, 2) void k_sq(GArgs p) {
  if constexpr (ID == 1) CODE_LABEL(fz_code_g1);
  if constexpr (ID == 2) CODE_LABEL(fz_code_g2);
  if constexpr (ID == 4) CODE_LABEL(fz_code_g4);
  using C = Cfg<ID>;
  constexpr int NT = C::NT, NST = SQ_NST, ST = SQ_ST, A_BYTES = 16384;
  extern __shared__ __attribute__((aligned(16))) char shm[];
  const unsigned lds0 = (unsigned)(size_t)(LAS char*)shm;
  const unsigned vfull = lds0 + SQ_SYNC, vfree = lds0 + SQ_SYNC + 32;
  const int tid = threadIdx.x, lane = tid & 63;
  const unsigned w = rfl(tid >> 6);
  STAMP(ID, 0);
  const int fr = lane & 15, fq = lane >> 4;
  const unsigned ifull = inc_base(vfull, lane), ifree = ifull + 32;
  const unsigned xcd = blockIdx.x & 7, idx = blockIdx.x >> 3, li = idx & 3, lj = idx >> 2;
  const unsigned kh = (ID == 1) ? 0u : (xcd & 1);
  const unsigned tm = (xcd >> 1) * 4 + li, tn = (ID == 1) ? (xcd & 1) * 8 + lj : lj;

  if (w >= 4) {
    if (LPRIO) __builtin_amdgcn_s_setprio(LPRIO);
    const unsigned lw = w - 4;
    u32x4 qa, qb;
    unsigned va[4], vb[4], kstepA, kstepB;
    if constexpr (ID == 1) {
      qa = make_rsrc((const u16*)(tm < 8 ? p.a0 : p.a1) + (size_t)(tm & 7) * 128 * D_, 128 * D_ * 2);
      qb = make_rsrc((const u16*)p.b0 + (size_t)tn * 128 * D_, 128 * D_ * 2);
    } else if constexpr (ID == 2) {
      qa = make_rsrc((const u16*)(kh ? p.a1 : p.a0) + (size_t)tm * 128 * S_, 128 * S_ * 2);
      qb = make_rsrc((const u16*)p.b0 + (size_t)tn * 128 * 4096 + kh * 64 * 512, 128 * 4096 * 2 - kh * 64 * 1024);
    } else {
      qa = make_rsrc((const u16*)p.a0 + (size_t)tm * 128 * F_ + kh * 56 * 512, 128 * F_ * 2 - kh * 56 * 1024);
      qb = make_rsrc((const u16*)p.b0 + (size_t)kh * 1792 * D_, 1792 * D_ * 2);
    }
#pragma unroll
    for (int i = 0; i < 4; ++i) {
      const int pc = lw * 4 + i;
      const int row = pc * 8 + (lane >> 3), rsw = ((lane & 7) ^ ((row >> 1) & 7)) << 4;
      if constexpr (C::AM == OP_RM) va[i] = row * (ID == 1 ? D_ : S_) * 2 + rsw - i * 1024;
      else va[i] = ((pc >> 1) * (F_ / 32) + (pc & 1)) * 1024 + lane * 16 - i * 1024;
      if constexpr (C::BMODE == OP_RM) vb[i] = row * D_ * 2 + rsw - i * 1024;
      else if constexpr (C::BMODE == OP_FT) vb[i] = ((pc >> 1) * 128 + (pc & 1)) * 1024 + lane * 16 - i * 1024;
      else {
        const int k = pc * 4 + (lane >> 4), f = (k & 3) | (((k >> 3) & 1) << 2);
        vb[i] = k * D_ * 2 + tn * 256 + (((lane & 15) ^ (f << 1)) << 4) - i * 1024;
      }
    }
    kstepA = (C::AM == OP_RM) ? 128 : 2048;
    kstepB = (C::BMODE == OP_RM) ? 128 : (C::BMODE == OP_FT ? 2048 : 64 * D_ * 2);
    const unsigned cls = (C::NC == 8) ? ((lj + 2 * li) & 7) : ((lj + li) & (C::NC - 1));
    auto issue = [&](unsigned n) {
      unsigned st = stage_of<C::NC, NT, (WRAPM >> ID) & 1>(n, cls);
      const unsigned wbuf = lds0 + (n & (NST - 1)) * ST + lw * 4096;
      dma_grp<4>(wbuf, va, qa, st * kstepA);
      dma_grp<4>(wbuf + A_BYTES, vb, qb, st * kstepB);
    };
    unsigned touch = 0;
#pragma unroll
    for (int n = 0; n < SQ_PD; ++n) issue(n);
#pragma unroll 1
    for (unsigned b = 0; b + SQ_PD < NT; ++b) {
      wait_vm<8 * (SQ_PD - 1)>();
      barrier();
      issue(b + SQ_PD);
    }
    if (SQ_PD == 3) { wait_vm<16>(); barrier(); }
    wait_vm<8>();
    barrier();
    wait_vm<0>();
    barrier();
    if constexpr (ID == 2) {
      if (TOUCH_W) { barrier(); barrier(); barrier(); }
    }
    if constexpr (ID != 4) {
      if ((CODE_PF & 2) && lw == 0 && (idx & 7) == 0) {
        unsigned long long a;
        if constexpr (ID == 1) CODE_ADDR(a, fz_code_g2); else CODE_ADDR(a, fz_code_g3);
        touch_code(a, lane, 61, touch);
      }
    }
    if constexpr (ID == 1) {
      if (TOUCH_C == 1 && lw < 2) {
        const char* tp = (const char*)((xcd & 1) ? p.stats : p.res) + (size_t)((xcd >> 1) * 512 + idx * 16 + lw * 8 + (lane >> 3)) * (S_ * 2) + (lane & 7) * 128;
        asm volatile("global_load_dword %0, %1, off" : "+v"(touch) : "v"(tp) : "memory");
      }
      if (TOUCH_C == 2) {
        const char* tp = (const char*)((xcd & 1) ? p.stats : p.res) + (size_t)((xcd >> 1) * 512 + idx * 16 + lw * 4 + (lane >> 4)) * (S_ * 2) + (lane & 15) * 128;
        asm volatile("global_load_dword %0, %1, off" : "+v"(touch) : "v"(tp) : "memory");
      }
    }
    if constexpr (ID == 2) {
      if (TOUCH_W && lw < 2 * TOUCH_W) {
        const char* tp = (const char*)((lw & 1) ? p.aux1 : p.aux0) + (size_t)((lw >> 1) * 256 + idx * 8 + (lane >> 3)) * (F_ * 2) + xcd * 896 + ((lane & 7) < 7 ? (lane & 7) : 6) * 128;
        asm volatile("global_load_dword %0, %1, off" : "+v"(touch) : "v"(tp) : "memory");
      }
    }
    if constexpr (ID == 1) barrier();
    asm volatile("s_waitcnt vmcnt(0)" : "+v"(touch));
    return;
  }

  const unsigned wm = w >> 1, wx = w & 1;
  unsigned csink = 0;
  if constexpr (ID == 1) {
    if ((CODE_PF & 1) && w == 0 && idx < 4) {
      unsigned long long a1, a2, a3, a4;
      CODE_ADDR(a1, fz_code_g1); CODE_ADDR(a2, fz_code_g2); CODE_ADDR(a3, fz_code_g3); CODE_ADDR(a4, fz_code_g4);
      touch_code(idx == 0 ? a1 : (idx == 1 ? a2 : (idx == 2 ? a3 : a4)), lane, idx == 3 ? 50 : 61, csink);
    }
    if (tid < 2) *(unsigned*)((char*)p.flags + (tid * 256 + blockIdx.x) * 128) = 0u;
    if (tid < 224) *(unsigned long long*)((char*)p.flags + FL_CS + (blockIdx.x * 224 + tid) * 8) = 0ull;
  }
  const int chunk = ((fq & 1) << 1) | (fq >> 1);
  u32x4 res[4];
  if constexpr (ID != 1) {
#pragma unroll
    for (int mb = 0; mb < 4; ++mb) {
      const int row = tm * 128 + wm * 64 + mb * 16 + fr, col = tn * 128 + wx * 64 + kh * 32 + chunk * 8;
      if constexpr (ID == 2) res[mb] = *(const u32x4*)((const u16*)p.res + (size_t)row * D_ + col);
      else res[mb] = *(const u32x4*)((const u16*)p.res + ((size_t)(row >> 4) * (D_ / 32) + (col >> 5)) * 512 + (chunk * 16 + fr) * 8);
    }
  }
  const unsigned rotA = (ID == 1) ? 2 * wx : 0u, rotB = (ID == 1) ? 2 * wm : 2 * kh;
  unsigned rdA[4], rdB[4];
#pragma unroll
  for (int i = 0; i < 4; ++i) {
    const unsigned kk = i >> 1, hf = (i & 1) * 2, sw = ((kk * 4 + fq) ^ (fr >> 1)) << 4;
    if constexpr (C::AM == OP_RM) rdA[i] = (wm * 64 + (hf ^ rotA) * 16 + fr) * 128 + sw;
    else rdA[i] = wm * 8192 + lane * 16;
    if constexpr (C::BMODE == OP_RM) rdB[i] = A_BYTES + (wx * 64 + (hf ^ rotB) * 16 + fr) * 128 + sw;
    else if constexpr (C::BMODE == OP_FT) rdB[i] = A_BYTES + wx * 8192 + (hf ^ rotB) * 2048 + lane * 16;
    else {
      const int q = fr >> 2, pp = fr & 3, f = q | ((fq & 1) << 2);
      rdB[i] = A_BYTES + (8 * fq + q) * 256 + (((wx * 4 + (i ^ rotB)) ^ f) << 5) + pp * 8;
    }
  }
  unsigned rdA2[4], rdB2[4];
#pragma unroll
  for (int i = 0; i < 4; ++i) {
    rdA[i] += lds0; rdB[i] += lds0;
    rdA2[i] = rdA[i] + 2 * ST; rdB2[i] = rdB[i] + 2 * ST;
    asm volatile("" : "+v"(rdA2[i]), "+v"(rdB2[i]), "+v"(rdA[i]), "+v"(rdB[i]));
  }
  bf16x8 fa[2][2][4], fb[2][2][4];
  auto read_frag = [&](int j, bf16x8(&a)[2][4], bf16x8(&b)[2][4], const unsigned(&xa)[4], const unsigned(&xb)[4], int so) {
    const int s = j >> 3, i = j & 3;
    if (j & 4) {
      if constexpr (C::AM == OP_RM) a[s][i] = lds_b128(xa[s * 2 + (i >> 1)] + (i & 1) * 2048 + so);
      else a[s][i] = lds_b128(xa[0] + i * 2048 + s * 1024 + so);
    } else {
      if constexpr (C::BMODE == OP_RM) b[s][i] = lds_b128(xb[s * 2 + (i >> 1)] + (i & 1) * 2048 + so);
      else if constexpr (C::BMODE == OP_FT) b[s][i] = lds_b128(xb[i >> 1] + (i & 1) * 2048 + s * 1024 + so);
      else b[s][i] = lds_tr(xb[i] + s * 8192 + so, xb[i] + s * 8192 + 1024 + so);
    }
  };
  f32x4 acc[4][4], axs[2], axq[2], aws[2];
#pragma unroll
  for (int i = 0; i < 16; ++i) { acc[i >> 2][i & 3] = (f32x4){0.f, 0.f, 0.f, 0.f}; asm volatile("" : "+v"(acc[i >> 2][i & 3])); }
#pragma unroll
  for (int i = 0; i < 2; ++i) {
    axs[i] = axq[i] = aws[i] = (f32x4){0.f, 0.f, 0.f, 0.f};
    if (ID == 1) asm volatile("" : "+v"(axs[i]), "+v"(axq[i]), "+v"(aws[i]));
  }
  bf16x8 ones = __builtin_bit_cast(bf16x8, (u32x4){0x3f803f80u, 0x3f803f80u, 0x3f803f80u, 0x3f803f80u});
  asm volatile("" : "+v"(ones));
  auto step = [&](const bf16x8(&ca)[2][4], const bf16x8(&cb)[2][4], bf16x8(&na)[2][4], bf16x8(&nb)[2][4], auto slc) {
    constexpr int SL = decltype(slc)::value & 3;
    constexpr bool RD = decltype(slc)::value < 4;
#pragma unroll
    for (int i = 0; i < 32; ++i) {
      if (RD && i == BAR_AT) { barrier(); SB(); }
      if constexpr (ID == 1) {
        if ((i & 15) == 15) {
#pragma unroll
          for (int j = 0; j < 2; ++j) {
            MFMA(cb[i >> 4][j], ones, axs[j]);
            MFMA(cb[i >> 4][j], cb[i >> 4][j], axq[j]);
            MFMA(ones, ca[i >> 4][j], aws[j]);
          }
          SB();
        }
      }
      MFMA(cb[i >> 4][i & 3], ca[i >> 4][(i >> 2) & 3], acc[(i >> 2) & 3][i & 3]);
      SB();
      if (RD && i >= BAR_AT && i < BAR_AT + 16) {
        if (SL < 2) read_frag(i - BAR_AT, na, nb, rdA, rdB, SL * ST);
        else read_frag(i - BAR_AT, na, nb, rdA2, rdB2, (SL - 2) * ST);
      }
      SB();
    }
    wait_lgkm0();
  };
  if constexpr (ID == 1) asm volatile("s_waitcnt vmcnt(0)" : "+v"(csink));
  barrier();
  STAMP1_TAKE();
#pragma unroll
  for (int j = 0; j < 16; ++j) read_frag(j, fa[0], fb[0], rdA, rdB, 0);
  wait_lgkm0();
#pragma unroll 1
  for (unsigned t = 0; t < NT - 4; t += 4) {
    step(fa[0], fb[0], fa[1], fb[1], IC<1>{});
    step(fa[1], fb[1], fa[0], fb[0], IC<2>{});
    step(fa[0], fb[0], fa[1], fb[1], IC<3>{});
    step(fa[1], fb[1], fa[0], fb[0], IC<0>{});
  }
  step(fa[0], fb[0], fa[1], fb[1], IC<1>{});
  step(fa[1], fb[1], fa[0], fb[0], IC<2>{});
  step(fa[0], fb[0], fa[1], fb[1], IC<3>{});
  step(fa[1], fb[1], fa[0], fb[0], IC<4>{});
  mfma_drain();
  SB();
  STAMP1_PUT(ID);
  STAMP(ID, 2);

  if constexpr (ID == 1) {
    LAS float* xs = (LAS float*)(lds0 + SQ_TAB);
    LAS float* xq = xs + 128;
    LAS float* wsum = xs + 256;
#pragma unroll
    for (int j = 0; j < 2; ++j) {
      const unsigned nb = wx * 64 + (j ^ rotB) * 16, mb = wm * 64 + (j ^ rotA) * 16;
      if (fr == 0) *(LAS f32x4*)(xs + nb + fq * 4) = axs[j];
      const float dg = (fr & 2) ? ((fr & 1) ? axq[j][3] : axq[j][2]) : ((fr & 1) ? axq[j][1] : axq[j][0]);
      if (fq == (fr >> 2)) xq[nb + fr] = dg;
      if (fq == 0) wsum[mb + fr] = aws[j][0];
    }
    wait_lgkm0();
    barrier();
    STAMPX(ID, 0);
    const float sg = tm < 8 ? 1.f : -1.f;
    f32x4 mean[4], rstd[4];
    float wsm[4];
#pragma unroll
    for (int j = 0; j < 4; ++j) {
      const unsigned nb = wx * 64 + (j ^ rotB) * 16 + fq * 4;
      mean[j] = *(const LAS f32x4*)(xs + nb) * (1.0f / D_);
      const f32x4 var = *(const LAS f32x4*)(xq + nb) * (1.0f / D_) - mean[j] * mean[j];
#pragma unroll
      for (int r = 0; r < 4; ++r) rstd[j][r] = sg * __builtin_amdgcn_rsqf(fmaxf(var[r], 0.f) + EPS);
      wsm[j] = wsum[wm * 64 + (j ^ rotA) * 16 + fr];
    }
    const auto ro = OUT_RSRC(p.out, D_ * 4096 * 2);
#pragma unroll
    for (int mb = 0; mb < 4; ++mb)
#pragma unroll
      for (int np = 0; np < 2; ++np) {
        const f32x4 x0 = (acc[mb][2 * np] - mean[2 * np] * wsm[mb]) * rstd[2 * np], x1 = (acc[mb][2 * np + 1] - mean[2 * np + 1] * wsm[mb]) * rstd[2 * np + 1];
        auto s0 = __builtin_amdgcn_permlane16_swap(pk_bf16(x0[0], x0[1]), pk_bf16(x1[0], x1[1]), false, false);
        auto s1 = __builtin_amdgcn_permlane16_swap(pk_bf16(x0[2], x0[3]), pk_bf16(x1[2], x1[3]), false, false);
        const u32x4 v = {s0[0], s1[0], s0[1], s1[1]};
        const int rblk = (tm & 7) * 8 + wm * 4 + (mb ^ rotA);
        const int kblk = (tm >> 3) * 64 + tn * 4 + wx * 2 + (np ^ (rotB >> 1));
        STORE16(ro, (rblk * 128 + kblk) * 1024 + (chunk * 16 + fr) * 16, v);
      }
  } else {
    constexpr int XI = (ID == 2) ? 0 : 1;
    const auto rs = __builtin_amdgcn_make_buffer_rsrc((char*)p.xch + (size_t)XI * 256 * 36864, 0, 256 * 36864, 0x00020000);
#pragma unroll
    for (int mb = 0; mb < 4; ++mb) {
      auto s0 = __builtin_amdgcn_permlane16_swap(pk_bf16(acc[mb][2][0], acc[mb][2][1]), pk_bf16(acc[mb][3][0], acc[mb][3][1]), false, false);
      auto s1 = __builtin_amdgcn_permlane16_swap(pk_bf16(acc[mb][2][2], acc[mb][2][3]), pk_bf16(acc[mb][3][2], acc[mb][3][3]), false, false);
      __builtin_amdgcn_raw_buffer_store_b128((u32x4){s0[0], s1[0], s0[1], s1[1]}, rs, w * 4096 + lane * 16 + mb * 1024, blockIdx.x * XSTR, 16);
    }
    f32x4 y[4][2];
#pragma unroll
    for (int mb = 0; mb < 4; ++mb)
#pragma unroll
      for (int j = 0; j < 4; ++j) {
        auto s = __builtin_amdgcn_permlane16_swap(__float_as_uint(acc[mb][0][j]), __float_as_uint(acc[mb][1][j]), false, false);
        y[mb][0][j] = __uint_as_float(s[0]) + ((j & 1) ? bf_hi(res[mb][j >> 1]) : bf_lo(res[mb][j >> 1]));
        y[mb][1][j] = __uint_as_float(s[1]) + ((j & 1) ? bf_hi(res[mb][2 + (j >> 1)]) : bf_lo(res[mb][2 + (j >> 1)]));
      }
    const int row0 = tm * 128 + wm * 64 + fr, col = tn * 128 + wx * 64 + kh * 32 + chunk * 8;
    unsigned optr = (ID == 2) ? ((row0 >> 4) * (D_ / 32) + (col >> 5)) * 1024 + (chunk * 16 + fr) * 16 : (row0 * D_ + col) * 2;
    const auto ro = OUT_RSRC(p.out, S_ * D_ * 2);
    asm volatile("" : "+v"(y[0][0]), "+v"(y[0][1]), "+v"(y[1][0]), "+v"(y[1][1]), "+v"(y[2][0]), "+v"(y[2][1]), "+v"(y[3][0]), "+v"(y[3][1]), "+v"(optr));
    wait_vm<0>();
    barrier();
    if (w == 0) {
      unsigned* fl = (unsigned*)((char*)p.flags + XI * 256 * 128);
      if (lane == 0) __hip_atomic_store(fl + blockIdx.x * 32, 1u, __ATOMIC_RELAXED, __HIP_MEMORY_SCOPE_AGENT);
      unsigned spins = 0;
      while (rfl(__hip_atomic_load(fl + (blockIdx.x ^ 1) * 32, __ATOMIC_RELAXED, __HIP_MEMORY_SCOPE_AGENT)) != 1u && ++spins < (1u << 22)) __builtin_amdgcn_s_sleep(1);
    }
    barrier();
    u32x4 in[4];
#pragma unroll
    for (int i = 0; i < 4; ++i) in[i] = __builtin_amdgcn_raw_buffer_load_b128(rs, w * 4096 + lane * 16 + i * 1024, (blockIdx.x ^ 1) * XSTR, 16);
    u32x4 v[4];
#pragma unroll
    for (int mb = 0; mb < 4; ++mb) {
      const f32x4 a = y[mb][0] + (f32x4){bf_lo(in[mb][0]), bf_hi(in[mb][0]), bf_lo(in[mb][1]), bf_hi(in[mb][1])};
      const f32x4 b = y[mb][1] + (f32x4){bf_lo(in[mb][2]), bf_hi(in[mb][2]), bf_lo(in[mb][3]), bf_hi(in[mb][3])};
      v[mb] = (u32x4){pk_bf16(a[0], a[1]), pk_bf16(a[2], a[3]), pk_bf16(b[0], b[1]), pk_bf16(b[2], b[3])};
      STORE16(ro, optr + mb * 16 * D_ * 2, v[mb]);
    }
    SB();
    if constexpr (ID == 2) {
      float m[4], q[4];
#pragma unroll
      for (int mb = 0; mb < 4; ++mb) {
        float s = 0.f;
#pragma unroll
        for (int j = 0; j < 4; ++j) s += bf_lo(v[mb][j]) + bf_hi(v[mb][j]);
        m[mb] = sum_rows(s) * (1.0f / 32.0f);
      }
#pragma unroll
      for (int mb = 0; mb < 4; ++mb) {
        float s = 0.f;
#pragma unroll
        for (int j = 0; j < 4; ++j) {
          const float a = bf_lo(v[mb][j]) - m[mb], b = bf_hi(v[mb][j]) - m[mb];
          s += a * a + b * b;
        }
        q[mb] = sum_rows(s);
      }
      const float ms = (fq & 2) ? ((fq & 1) ? m[3] : m[2]) : ((fq & 1) ? m[1] : m[0]);
      const float qs = (fq & 2) ? ((fq & 1) ? q[3] : q[2]) : ((fq & 1) ? q[1] : q[0]);
      LAS f32x2* tb = (LAS f32x2*)(lds0 + SQ_TAB);
      if (wx) tb[wm * 64 + lane] = (f32x2){ms, qs};
      wait_lgkm0();
      barrier();
      if (!wx) {
        const f32x2 o = tb[wm * 64 + lane];
        const float d = ms - o.x;
        ((f32x2*)p.stats)[((size_t)tn * S_ + tm * 128 + wm * 64 + lane) * 2 + kh] = (f32x2){0.5f * (ms + o.x), qs + o.y + 16.0f * d * d};
      }
    }
  }
  STAMPX(ID, 4);
  STAMP(ID, 3);
}

constexpr int G3_NST = 4, G3_UP = 7168 + 16, G3_ST = 16384 + 2 * 7168 + 16, G3_RING = 6144, G3_TAB = G3_RING + G3_NST * G3_ST, G3_LDS = G3_TAB + 3072;
__global__ __launch_bounds__(768, 3) void k_glu(GArgs p) {
  CODE_LABEL(fz_code_g3);
  constexpr int ID = 3, BM = 256, K = D_, NT = K / 32, NST = G3_NST, ST = G3_ST, A_BYTES = 16384, MB = 4, NB = 7;
  extern __shared__ __attribute__((aligned(16))) char shm[];
  const unsigned lds0 = (unsigned)(size_t)(LAS char*)shm;
  const unsigned ring = lds0 + G3_RING;
  LAS f32x2* tab = (LAS f32x2*)(lds0 + G3_TAB);
  LAS float* cst = (LAS float*)(lds0 + G3_TAB + 2048);
  const unsigned w = rfl(threadIdx.x >> 6);
#define threadIdx_x_w w
  STAMP(ID, 0);
  const unsigned xcd = blockIdx.x & 7, idx = blockIdx.x >> 3;
  const unsigned tm = idx & 7, tn = xcd * 4 + (idx >> 3);
  const unsigned cls = (tm + 2 * (idx >> 3)) & 7;
  unsigned long long* gcs = (unsigned long long*)((char*)p.flags + FL_CS) + tn * (8 * 224);

  if (w >= 8) {
    if (LPRIO) __builtin_amdgcn_s_setprio(LPRIO);
    const unsigned lw = w - 8;
    const int lane = lane_id();
    const u32x4 qa = make_rsrc((const u16*)p.a0 + (size_t)tm * BM * K, BM * K * 2);
    const u32x4 qb = make_rsrc(lw < 2 ? p.b0 : p.b1, (unsigned)D_ * F_ * 2);
    unsigned va[4], vb[4];
#pragma unroll
    for (int i = 0; i < 4; ++i) {
      va[i] = (lw * 4 + i) * (K / 32) * 1024 + lane * 16 - i * 1024;
      const int c = ((lw & 1) * 4 + i) * 64 + lane, r = c / 14, pos = c % 14;
      const int k = (r & 0x13) | ((r & 4) << 1) | ((r & 8) >> 1);
      vb[i] = k * F_ * 2 + tn * 224 + pos * 16 - i * 1024;
    }
    const unsigned boff = A_BYTES + (lw >> 1) * G3_UP + (lw & 1) * 4096;
    auto stage = [&](unsigned n) {
      unsigned st = stage_of<NC3, NT, (WRAPM >> 3) & 1>(n, cls);
      return st;
    };
    auto issue_all = [&](int n) {
      const unsigned st = stage(n);
      dma_grp<4>(ring + n * ST + lw * 4096, va, qa, st * 1024);
      if (lw & 1) dma_grp<3>(ring + n * ST + boff, vb, qb, st * (32 * F_ * 2));
      else dma_grp<4>(ring + n * ST + boff, vb, qb, st * (32 * F_ * 2));
    };
    issue_all(0);
    issue_all(1);
    wait_vm<7>();
    barrier();
    issue_all(2);
    unsigned touch = 0;
#pragma unroll 1
    for (unsigned t = 0; t + NST - 1 < NT; ++t) {
      const unsigned st = stage(t + NST - 1), wbuf = ring + ((t + NST - 1) & (NST - 1)) * ST;
      if (t == 25) {
        const unsigned u = lw * 64 + lane < 224 ? lw * 64 + lane : 223u;
        const unsigned hf = u >= 112, c = u - hf * 112, wn_ = c >= 56, cc = c - wn_ * 56, blk = cc >> 4;
        const unsigned src = lds0 + (wn_ * 4 + blk) * 768 + (blk == 3 ? 512 + (hf * 8 + (cc & 7)) * 4 : hf * 256 + (cc & 15) * 4);
        const float v = (*(const LAS float*)(src) + *(const LAS float*)(src + 64)) + (*(const LAS float*)(src + 128) + *(const LAS float*)(src + 192));
        asm volatile("global_store_dwordx2 %0, %1, off sc1" ::"v"(gcs + cls * 224 + u), "v"((1ull << 32) | __float_as_uint(v)) : "memory");
      }
      dma_grp<4>(wbuf + lw * 4096, va, qa, st * 1024);
      barrier();
      if (lw & 1) dma_grp<3>(wbuf + boff, vb, qb, st * (32 * F_ * 2));
      else dma_grp<4>(wbuf + boff, vb, qb, st * (32 * F_ * 2));
      wait_vm<7 * (NST - 2)>();
      barrier();
    }
    barrier();
    wait_vm<7>();
    barrier();
    if ((CODE_PF & 2) && lw == 0 && (idx & 7) == 0) {
      unsigned long long a;
      CODE_ADDR(a, fz_code_g4);
      touch_code(a, lane, 50, touch);
    }
    barrier();
    wait_vm<0>();
    {
      const unsigned u = lw * 64 + lane < 224 ? lw * 64 + lane : 223u;
      unsigned long long g[8];
#pragma unroll
      for (int i = 0; i < 8; ++i) asm volatile("global_load_dwordx2 %0, %1, off sc1" : "=v"(g[i]) : "v"(gcs + i * 224 + u) : "memory");
      if (TOUCH_D != 1 && (TOUCH_D != 2 || lw >= 2)) asm volatile("global_load_dword %0, %1, off" : "+v"(touch) : "v"(gcs) : "memory");
      if (TOUCH_D == 1) {
        const char* tp = (const char*)p.res + ((size_t)blockIdx.x * 224 + (lw * 56 + (lane < 56 ? lane : 55))) * 128;
        asm volatile("global_load_dword %0, %1, off" : "+v"(touch) : "v"(tp) : "memory");
      }
      if (TOUCH_D == 2) {
        if (lw < 2) {
          const char* tp = (const char*)p.res + (size_t)((xcd & 1) * 1792 + idx * 8 + lw * 4 + (lane >> 4)) * (D_ * 2) + (lane & 15) * 128;
          asm volatile("global_load_dword %0, %1, off" : "+v"(touch) : "v"(tp) : "memory");
        }
      }
      SB();
      barrier();
      barrier();
      barrier();
      SB();
      asm volatile("s_waitcnt vmcnt(1)" : "+v"(g[0]), "+v"(g[1]), "+v"(g[2]), "+v"(g[3]), "+v"(g[4]), "+v"(g[5]), "+v"(g[6]), "+v"(g[7]));
#pragma unroll 1
      for (unsigned spins = 0; spins < (1u << 20); ++spins) {
        unsigned tg = 1u;
#pragma unroll
        for (int i = 0; i < 8; ++i) tg &= (unsigned)(g[i] >> 32);
        if (__all(tg == 1u)) break;
        __builtin_amdgcn_s_sleep(2);
#pragma unroll
        for (int i = 0; i < 8; ++i) asm volatile("global_load_dwordx2 %0, %1, off sc1\n\ts_waitcnt vmcnt(0)" : "=&v"(g[i]) : "v"(gcs + i * 224 + u) : "memory");
      }
      float s = 0.f;
#pragma unroll
      for (int i = 0; i < 8; ++i) s += __uint_as_float((unsigned)g[i]);
      cst[u] = s;
      wait_lgkm0();
      barrier();
    }
    asm volatile("s_waitcnt vmcnt(0)" : "+v"(touch));
    return;
  }

  const unsigned wm = w & 3, wn = w >> 2;
  const int lane = lane_id(), fr = lane & 15, fq = lane >> 4;
  STAMPX(ID, 7);
  const unsigned rot = wm == 3 ? 0u : wm;
  unsigned xa = ring + wm * 4096 + lane * 16, xb[3], xb1;
  {
    const int q = fr >> 2, pp = fr & 3, r = (fq >> 1) * 16 + (fq & 1) * 4 + q;
#pragma unroll
    for (int i = 0; i < 3; ++i) xb[i] = ring + A_BYTES + r * 224 + wn * 112 + pp * 8 + ((i + rot) % 3) * 32;
    xb1 = ring + A_BYTES + r * 224 + wn * 112 + 96 + (pp & 1) * 8 + (pp >> 1) * G3_UP;
  }
  float cs[3] = {0.f, 0.f, 0.f};
  if (wn == 1) {
    const f32x4* sp = (const f32x4*)p.stats + tm * BM + wm * 64 + lane;
    f32x4 sv[8];
#pragma unroll
    for (int i = 0; i < 8; ++i) sv[i] = sp[(size_t)i * S_];
    SB();
    barrier();
    SB();
    float m = 0.f;
#pragma unroll
    for (int i = 0; i < 8; ++i) m += sv[i][0] + sv[i][2];
    m *= (1.0f / 16.0f);
    float q = 0.f;
#pragma unroll
    for (int i = 0; i < 8; ++i) {
      const float d0 = sv[i][0] - m, d1 = sv[i][2] - m;
      q += sv[i][1] + sv[i][3] + 64.0f * (d0 * d0 + d1 * d1);
    }
    tab[wm * 64 + lane] = (f32x2){m, __builtin_amdgcn_rsqf(q * (1.0f / D_) + EPS)};
  }
  bf16x8 fa[MB], fb[NB];
  f32x4 acc[MB][NB];
#pragma unroll
  for (int mb = 0; mb < MB; ++mb)
#pragma unroll
    for (int nb = 0; nb < NB; ++nb) { acc[mb][nb] = (f32x4){0.f, 0.f, 0.f, 0.f}; asm volatile("" : "+v"(acc[mb][nb])); }
  asm volatile("" : "+v"(xa), "+v"(xb[0]), "+v"(xb[1]), "+v"(xb[2]), "+v"(xb1));
  wait_lgkm0();
  barrier();
  STAMP1_TAKE();
  const unsigned csmask = (0x010101u << cls) | (1u << ((cls + 1) & 7));
  auto reads = [&]() {
    SB();
    fa[0] = lds_b128(xa);
#pragma unroll
    for (int j = 0; j < 7; ++j) {
      const unsigned o = j < 6 ? xb[j % 3] + (j / 3) * G3_UP : xb1;
      fb[j] = lds_tr(o, o + 1792);
    }
#pragma unroll
    for (int mb = 1; mb < MB; ++mb) fa[mb] = lds_b128(xa + mb * 1024);
    SB();
  };
  reads();
#pragma unroll 1
  for (unsigned t = 0; t < NT; ++t) {
    const unsigned delta = ((t & (NST - 1)) == NST - 1) ? (unsigned)(-(NST - 1) * ST) : (unsigned)ST;
    xa += delta; xb[0] += delta; xb[1] += delta; xb[2] += delta; xb1 += delta;
    asm volatile("" : "+v"(xa), "+v"(xb[0]), "+v"(xb[1]), "+v"(xb[2]), "+v"(xb1));
    if (__builtin_expect((csmask >> t) & 1u, 0)) {
#pragma unroll
      for (int j = 0; j < 4; ++j)
#pragma unroll
        for (int i = 0; i < 3; ++i)
          asm volatile("v_dot2c_f32_bf16 %0, %1, %2" : "+v"(cs[i]) : "s"(0x3f803f80u), "v"(__builtin_bit_cast(u32x4, fb[3 * i])[j]));
      asm volatile("s_mov_b32 m0, %3\n\ts_nop 2\n\tds_write_addtid_b32 %0\n\tds_write_addtid_b32 %1 offset:256\n\tds_write_addtid_b32 %2 offset:512" ::"v"(cs[0]),
                   "v"(cs[1]), "v"(cs[2]), "s"(lds0 + w * 768)
                   : "memory");
    }
    wait_lgkm0();
    barrier();
    SB();
    if (CPRIO == 2) __builtin_amdgcn_s_setprio(1);
#pragma unroll
    for (int mb = 0; mb < MB; ++mb)
#pragma unroll
      for (int nb = 0; nb < NB; ++nb) MFMA(fb[nb], fa[mb], acc[mb][nb]);
    if (CPRIO == 2) __builtin_amdgcn_s_setprio(0);
    SB();
    barrier();
    reads();
  }
  if (wn == 0) barrier();
  mfma_drain();
  SB();
  const int lane2 = lane_id();
  const int gr = lane2 & 15, gq = lane2 >> 4;
  STAMP1_PUT(ID);
  STAMP(ID, 2);
  STAMPX(ID, 0);
  auto act = [](float g, float u) { return g * u * __builtin_amdgcn_rcpf(1.0f + __expf(-g)); };
  const auto ro = OUT_RSRC(p.out, S_ * F_ * 2);
  f32x2 st[MB];
#pragma unroll
  for (int mb = 0; mb < MB; ++mb) st[mb] = tab[wm * 64 + mb * 16 + gr];
  const int gch0 = tn * 14 + wn * 7;
#pragma unroll
  for (int i = 0; i < 3; ++i) {
    const unsigned ib = (i + rot) % 3;
    const f32x4 cg = *(const LAS f32x4*)(cst + wn * 56 + ib * 16 + gq * 4), cu = *(const LAS f32x4*)(cst + 112 + wn * 56 + ib * 16 + gq * 4);
    u32x2 pkd[MB];
#pragma unroll
    for (int mb = 0; mb < MB; ++mb) {
      const f32x4 g = (acc[mb][i] - cg * st[mb].x) * st[mb].y, u = (acc[mb][3 + i] - cu * st[mb].x) * st[mb].y;
      pkd[mb] = (u32x2){pk_bf16(act(g[0], u[0]), act(g[1], u[1])), pk_bf16(act(g[2], u[2]), act(g[3], u[3]))};
    }
#pragma unroll
    for (int mp = 0; mp < MB / 2; ++mp) {
      auto s0 = __builtin_amdgcn_permlane16_swap(pkd[2 * mp].x, pkd[2 * mp + 1].x, false, false);
      auto s1 = __builtin_amdgcn_permlane16_swap(pkd[2 * mp].y, pkd[2 * mp + 1].y, false, false);
      const u32x4 v = {s0[0], s1[0], s0[1], s1[1]};
      const int rblk = tm * (BM / 16) + wm * MB + 2 * mp + (gq & 1);
      const int gch = gch0 + 2 * ib + (gq >> 1);
      STORE16(ro, (rblk * (F_ / 32) + (gch >> 2)) * 1024 + ((gch & 3) * 16 + gr) * 16, v);
    }
  }
  {
    const f32x4 cm = *(const LAS f32x4*)(cst + (gq >> 1) * 112 + wn * 56 + 48 + (gq & 1) * 4);
    u32x2 pkd[MB / 2];
#pragma unroll
    for (int mp = 0; mp < MB / 2; ++mp) {
      const f32x4 v0 = (acc[2 * mp][6] - cm * st[2 * mp].x) * st[2 * mp].y, v1 = (acc[2 * mp + 1][6] - cm * st[2 * mp + 1].x) * st[2 * mp + 1].y;
      float r[4];
#pragma unroll
      for (int j = 0; j < 4; ++j) {
        auto s = __builtin_amdgcn_permlane32_swap(__float_as_uint(v0[j]), __float_as_uint(v1[j]), false, false);
        r[j] = act(__uint_as_float(s[0]), __uint_as_float(s[1]));
      }
      pkd[mp] = (u32x2){pk_bf16(r[0], r[1]), pk_bf16(r[2], r[3])};
    }
    auto s0 = __builtin_amdgcn_permlane16_swap(pkd[0].x, pkd[1].x, false, false);
    auto s1 = __builtin_amdgcn_permlane16_swap(pkd[0].y, pkd[1].y, false, false);
    const u32x4 v = {s0[0], s1[0], s0[1], s1[1]};
    const int rblk = tm * (BM / 16) + wm * MB + (gq & 1) * 2 + (gq >> 1);
    const int gch = gch0 + 6;
    STORE16(ro, (rblk * (F_ / 32) + (gch >> 2)) * 1024 + ((gch & 3) * 16 + gr) * 16, v);
  }
  STAMPX(ID, 4);
  STAMP(ID, 3);
}

template <int ID>
void launch_sq(const GArgs& a, hipStream_t stream) {
  (void)hipFuncSetAttribute((const void*)k_sq<ID>, hipFuncAttributeMaxDynamicSharedMemorySize, SQ_LDS);
  hipLaunchKernelGGL(k_sq<ID>, dim3(256), dim3(512), SQ_LDS, stream, a);
}
}

extern "C" void kernel_launch(void* const* d_in, const int* in_sizes, int n_in, void* d_out, int out_size, void* d_ws, size_t ws_size,
                              hipStream_t stream) {
  char* ws = (char*)d_ws;
  GArgs g1 = {d_in[3], d_in[4], d_in[0], d_in[6], ws + WS_AB, d_in[1], d_in[2], d_in[7], ws + WS_FL, nullptr, nullptr};
  launch_sq<1>(g1, stream);
  GArgs g2 = {d_in[1], d_in[2], ws + WS_AB, nullptr, ws + WS_H, d_in[0], ws + WS_ST, ws + WS_XC, ws + WS_FL, d_in[6], d_in[7]};
  launch_sq<2>(g2, stream);
  GArgs g3 = {ws + WS_H, nullptr, d_in[6], d_in[7], ws + WS_GU, d_in[8], ws + WS_ST, nullptr, ws + WS_FL, nullptr, nullptr};
  (void)hipFuncSetAttribute((const void*)k_glu, hipFuncAttributeMaxDynamicSharedMemorySize, G3_LDS);
  hipLaunchKernelGGL(k_glu, dim3(256), dim3(768), G3_LDS, stream, g3);
  GArgs g4 = {ws + WS_GU, nullptr, d_in[8], nullptr, d_out, ws + WS_H, nullptr, ws + WS_XC, ws + WS_FL, nullptr, nullptr};
  launch_sq<4>(g4, stream);
}
```

```cpp
#include <hip/hip_runtime.h>
#include <hip/hip_bf16.h>

namespace {
#define LAS __attribute__((address_space(3)))
typedef unsigned short u16;
typedef short s16x4 __attribute__((ext_vector_type(4)));
typedef short s16x8 __attribute__((ext_vector_type(8)));
typedef __bf16 bf16x8 __attribute__((ext_vector_type(8)));
typedef __bf16 bf16x2 __attribute__((ext_vector_type(2)));
typedef float f32x4 __attribute__((ext_vector_type(4)));
typedef float f32x2 __attribute__((ext_vector_type(2)));
typedef unsigned u32x4 __attribute__((ext_vector_type(4)));
typedef unsigned u32x2 __attribute__((ext_vector_type(2)));

constexpr int S_ = 2048, D_ = 1024, F_ = 3584;
constexpr float EPS = 1e-5f;

constexpr size_t WS_AB = 0;
constexpr size_t WS_H = WS_AB + (size_t)D_ * 2 * S_ * 2;
constexpr size_t WS_GU = WS_H + (size_t)S_ * D_ * 2;
constexpr size_t WS_ST = WS_GU + (size_t)S_ * F_ * 2;
constexpr size_t WS_XC = WS_ST + (size_t)S_ * 16 * 8;
constexpr size_t WS_FL = WS_XC + (size_t)2 * 256 * 16384;
constexpr int FL_EPOCH = 0;
constexpr int FL_PAIR = 128;
constexpr int FL_P1 = FL_PAIR + 512 * 128;
constexpr int FL_P2 = FL_P1 + 64 * 128;
constexpr int FL_P3 = FL_P2 + 64 * 128;
constexpr int FL_LN = FL_P3 + 16 * 128;
constexpr int FL_CS = FL_LN + 4096 * 16;

__device__ __forceinline__ unsigned rfl(unsigned v) { return __builtin_amdgcn_readfirstlane(v); }
__device__ __forceinline__ unsigned pk_bf16(float lo, float hi) {
  bf16x2 c = {(__bf16)lo, (__bf16)hi};
  return __builtin_bit_cast(unsigned, c);
}
__device__ __forceinline__ float bf_lo(unsigned v) { return __uint_as_float(v << 16); }
__device__ __forceinline__ float bf_hi(unsigned v) { return __uint_as_float(v & 0xffff0000u); }

__device__ __forceinline__ u32x4 make_rsrc(const void* p, unsigned bytes) {
  unsigned long long a = (unsigned long long)p;
  u32x4 r;
  r.x = (unsigned)a;
  r.y = (unsigned)(a >> 32) & 0xffffu;
  r.z = bytes;
  r.w = 0x00020000u;
  return r;
}
__device__ __forceinline__ void dma16(unsigned ldsaddr, unsigned voff, u32x4 rsrc, unsigned soff) {
  asm volatile("s_mov_b32 m0, %0\n\ts_nop 4\n\tbuffer_load_dwordx4 %1, %2, %3 offen lds" ::"s"(ldsaddr), "v"(voff), "s"(rsrc), "s"(soff)
               : "memory");
}
template <int N>
__device__ __forceinline__ void wait_vm() {
  asm volatile("s_waitcnt vmcnt(%0)" ::"i"(N) : "memory");
}
__device__ __forceinline__ void wait_lgkm0() { __builtin_amdgcn_s_waitcnt(0xC07F); }
__device__ __forceinline__ void barrier() {
  asm volatile("" ::: "memory");
  __builtin_amdgcn_s_barrier();
  asm volatile("" ::: "memory");
}
__device__ __forceinline__ bf16x8 lds_b128(unsigned addr) { return *(const LAS bf16x8*)(addr); }
__device__ __forceinline__ bf16x8 lds_tr(unsigned a0, unsigned a1) {
  s16x4 lo = __builtin_amdgcn_ds_read_tr16_b64_v4i16((LAS s16x4*)(a0));
  s16x4 hi = __builtin_amdgcn_ds_read_tr16_b64_v4i16((LAS s16x4*)(a1));
  s16x8 f = __builtin_shufflevector(lo, hi, 0, 1, 2, 3, 4, 5, 6, 7);
  return __builtin_bit_cast(bf16x8, f);
}

#define ST_AUX 16
#define OUT_RSRC(ptr, bytes) __builtin_amdgcn_make_buffer_rsrc((void*)(ptr), 0, (bytes), 0x00020000)
#define STORE16(rs, byteoff, v) __builtin_amdgcn_raw_buffer_store_b128(v, rs, byteoff, 0, ST_AUX)
#define STAMP(k, i)
#define STAMPX(k, i)
#define STAMPL(k, i, first)

template <int N, bool SC1 = false>
__device__ __forceinline__ void dma_grp(unsigned ldsaddr, const unsigned (&v)[4], u32x4 rsrc, unsigned soff) {
  if constexpr (N == 4 && SC1)
    asm volatile("s_mov_b32 m0, %0\n\ts_nop 4\n\tbuffer_load_dwordx4 %1, %5, %6 offen sc1 lds\n\tbuffer_load_dwordx4 %2, %5, %6 offen offset:1024 sc1 lds\n\t"
                 "buffer_load_dwordx4 %3, %5, %6 offen offset:2048 sc1 lds\n\tbuffer_load_dwordx4 %4, %5, %6 offen offset:3072 sc1 lds" ::"s"(ldsaddr),
                 "v"(v[0]), "v"(v[1]), "v"(v[2]), "v"(v[3]), "s"(rsrc), "s"(soff)
                 : "memory");
  else if constexpr (N == 4)
    asm volatile("s_mov_b32 m0, %0\n\ts_nop 4\n\tbuffer_load_dwordx4 %1, %5, %6 offen lds\n\tbuffer_load_dwordx4 %2, %5, %6 offen offset:1024 lds\n\t"
                 "buffer_load_dwordx4 %3, %5, %6 offen offset:2048 lds\n\tbuffer_load_dwordx4 %4, %5, %6 offen offset:3072 lds" ::"s"(ldsaddr),
                 "v"(v[0]), "v"(v[1]), "v"(v[2]), "v"(v[3]), "s"(rsrc), "s"(soff)
                 : "memory");
  else
    asm volatile("s_mov_b32 m0, %0\n\ts_nop 4\n\tbuffer_load_dwordx4 %1, %4, %5 offen lds\n\tbuffer_load_dwordx4 %2, %4, %5 offen offset:1024 lds\n\t"
                 "buffer_load_dwordx4 %3, %4, %5 offen offset:2048 lds" ::"s"(ldsaddr),
                 "v"(v[0]), "v"(v[1]), "v"(v[2]), "s"(rsrc), "s"(soff)
                 : "memory");
}
__device__ __forceinline__ float sum_rows(float x) {
  auto a = __builtin_amdgcn_permlane16_swap(__float_as_uint(x), __float_as_uint(x), false, false);
  x = __uint_as_float(a[0]) + __uint_as_float(a[1]);
  auto b = __builtin_amdgcn_permlane32_swap(__float_as_uint(x), __float_as_uint(x), false, false);
  return __uint_as_float(b[0]) + __uint_as_float(b[1]);
}
__device__ __forceinline__ float sum_pair(float x) { return x + __uint_as_float(__builtin_amdgcn_mov_dpp(__float_as_uint(x), 0xB1, 0xF, 0xF, true)); }
template <int CTRL> __device__ __forceinline__ float dpp_add(float x) { return x + __uint_as_float(__builtin_amdgcn_mov_dpp(__float_as_uint(x), CTRL, 0xF, 0xF, true)); }
__device__ __forceinline__ float wave_sum(float x) { return sum_rows(dpp_add<0x140>(dpp_add<0x141>(dpp_add<0x4E>(dpp_add<0xB1>(x))))); }
__device__ __forceinline__ int lane_id() {
  int l;
  asm volatile("v_mbcnt_lo_u32_b32 %0, -1, 0\n\tv_mbcnt_hi_u32_b32 %0, -1, %0" : "=v"(l));
  return l;
}
__device__ __forceinline__ void mfma_drain() { asm volatile("s_nop 15" ::: "memory"); }
__device__ __forceinline__ void fadd(float& a, float b) { asm("v_add_f32 %0, %1, %0" : "+v"(a) : "v"(b)); }
template <int NC, int NT, int WRAP>
__device__ __forceinline__ unsigned stage_of(unsigned n, unsigned c) {
  if (NC == 1) return n;
  if (WRAP) {
    const unsigned t = ((n & (NC - 1)) == c) ? n + NC : n;
    return rfl(t >= NT ? t - NT : t);
  }
  unsigned t = (n > NT - NC + c) ? n : n - 1;
  t = ((t & (NC - 1)) == c) ? t + NC : t;
  return rfl(n == 0 ? c : t);
}
#define NC1 4
#define NC2 8
#define NC3 8
#define NC4 4
#define TOUCH_C 1
#define TOUCH_W 1
#define TOUCH_D 2
#define TOUCH_AT 6
#define LN_NT 0
#define FT_W 1
#define FT_D 1
#define POLL_SLEEP 1
#define XSTR 16384
#define WRAPM 0x1e
#define SQ_PD 2
#define BAR_AT 0
#define CPRIO 0
template <int V> struct IC { static constexpr int value = V; };
#define LPRIO 0
#define SB() __builtin_amdgcn_sched_barrier(0)
#define MFMA(b, a, c) asm volatile("v_mfma_f32_16x16x32_bf16 %0, %1, %2, %0" : "+v"(c) : "v"(b), "v"(a))
#define STAMP_END(k)
#define STAMP1_TAKE()
#define STAMP1_PUT(k)

#define CODE_LABEL(name) asm volatile(#name ":")
#define CODE_ADDR(dst, name) do { int o_; asm volatile("s_getpc_b64 %0\n\ts_mov_b32 %1, " #name "@rel32@lo+4" : "=s"(dst), "=s"(o_)); dst += (long long)o_; } while (0)
#define CODE_LAST 30
#define CODE_PF 3
__device__ __forceinline__ void touch_code(unsigned long long base, int lane, int last, unsigned& sink) {
  const unsigned off = (lane < last ? lane : last) * 128;
  asm volatile("global_load_dword %0, %1, %2" : "+v"(sink) : "v"(off), "s"(base) : "memory");
}
struct FArgs {
  const void *dre, *dim, *cre, *cim, *x, *wg, *wu, *wd;
  void *ab, *h, *stats, *gu, *out, *xch, *flags;
};
constexpr int F_RING = 6144, F_ST = 32768, F_TAB = F_RING + 4 * F_ST, F_TAG = F_TAB + 4096, F_LDS = F_TAB + 4160, F_A = 16384, G3_UP = 7168 + 16;

__device__ __forceinline__ void poll_tags(const void* line, int n, unsigned tag) {
  const int l = lane_id();
  const unsigned* q = (const unsigned*)line + (l < n ? l : 0);
  unsigned spins = 0;
  while (!__all(__hip_atomic_load(q, __ATOMIC_RELAXED, __HIP_MEMORY_SCOPE_AGENT) == tag) && ++spins < (1u << 17)) __builtin_amdgcn_s_sleep(POLL_SLEEP);
}
__device__ __forceinline__ void put_tag(void* word, unsigned tag) { __hip_atomic_store((unsigned*)word, tag, __ATOMIC_RELAXED, __HIP_MEMORY_SCOPE_AGENT); }
__device__ __forceinline__ unsigned lds_tag(unsigned lds0) { return rfl(*(volatile LAS unsigned*)(lds0 + F_TAG)); }
template <int ID> constexpr int nt_of() { return ID == 1 ? 16 : (ID == 4 ? 28 : 32); }
template <int ID> constexpr int nc_of() { return ID == 1 ? NC1 : (ID == 2 ? NC2 : (ID == 3 ? 8 : NC4)); }
struct Ld {
  u32x4 qa, qb;
  unsigned va[4], vb[4], cls;
};
template <int ID>
__device__ __forceinline__ Ld ld_setup(const FArgs& p, unsigned lw, unsigned xcd, unsigned idx) {
  const int lane = lane_id();
  Ld L;
  if constexpr (ID == 3) {
    const unsigned tm = idx & 7, tn = xcd * 4 + (idx >> 3);
    L.qa = make_rsrc((const u16*)p.h + (size_t)tm * 256 * D_, 256 * D_ * 2);
    L.qb = make_rsrc(lw < 2 ? p.wg : p.wu, (unsigned)D_ * F_ * 2);
#pragma unroll
    for (int i = 0; i < 4; ++i) {
      L.va[i] = (lw * 4 + i) * (D_ / 32) * 1024 + lane * 16 - i * 1024;
      const int c = ((lw & 1) * 4 + i) * 64 + lane, r = c / 14, pos = c % 14;
      const int k = (r & 0x13) | ((r & 4) << 1) | ((r & 8) >> 1);
      L.vb[i] = k * F_ * 2 + tn * 224 + pos * 16 - i * 1024;
    }
    L.cls = (tm + 2 * (idx >> 3)) & 7;
  } else {
    const unsigned li = idx & 3, lj = idx >> 2, kh = xcd & 1, tm = (xcd >> 1) * 4 + li, tn = lj;
    if constexpr (ID == 1) {
      L.qa = make_rsrc((const u16*)(tm < 8 ? p.dre : p.dim) + (size_t)(tm & 7) * 128 * D_, 128 * D_ * 2);
      L.qb = make_rsrc((const u16*)p.x + (size_t)(kh * 8 + lj) * 128 * D_, 128 * D_ * 2);
    } else if constexpr (ID == 2) {
      L.qa = make_rsrc((const u16*)(kh ? p.cim : p.cre) + (size_t)tm * 128 * S_, 128 * S_ * 2);
      L.qb = make_rsrc((const u16*)p.ab + (size_t)tn * 128 * 4096 + kh * 64 * 512, 128 * 4096 * 2 - kh * 64 * 1024);
    } else {
      L.qa = make_rsrc((const u16*)p.gu + (size_t)tm * 128 * F_ + kh * 56 * 512, 128 * F_ * 2 - kh * 56 * 1024);
      L.qb = make_rsrc((const u16*)p.wd + (size_t)kh * 1792 * D_, 1792 * D_ * 2);
    }
#pragma unroll
    for (int i = 0; i < 4; ++i) {
      const int pc = lw * 4 + i;
      if constexpr (ID == 1) {
        const int row = pc * 8 + (lane >> 3);
        L.va[i] = L.vb[i] = row * D_ * 2 + (((lane & 7) ^ ((row >> 1) & 7)) << 4) - i * 1024;
      } else if constexpr (ID == 2) {
        const int row = pc * 8 + (lane >> 3);
        L.va[i] = row * S_ * 2 + (((lane & 7) ^ ((row >> 1) & 7)) << 4) - i * 1024;
        L.vb[i] = ((pc >> 1) * 128 + (pc & 1)) * 1024 + lane * 16 - i * 1024;
      } else {
        const int k = pc * 4 + (lane >> 4), f = (k & 3) | (((k >> 3) & 1) << 2);
        L.va[i] = ((pc >> 1) * (F_ / 32) + (pc & 1)) * 1024 + lane * 16 - i * 1024;
        L.vb[i] = k * D_ * 2 + tn * 256 + (((lane & 15) ^ (f << 1)) << 4) - i * 1024;
      }
    }
    L.cls = (nc_of<ID>() == 8) ? ((lj + 2 * li) & 7) : ((lj + li) & 3);
  }
  return L;
}
__device__ __forceinline__ void ld_pin(Ld& L) {
  asm volatile("" : "+v"(L.va[0]), "+v"(L.va[1]), "+v"(L.va[2]), "+v"(L.va[3]), "+v"(L.vb[0]), "+v"(L.vb[1]), "+v"(L.vb[2]), "+v"(L.vb[3]));
}
template <int ID>
__device__ __forceinline__ void ld_a(const Ld& L, unsigned ring, unsigned lw, unsigned n) {
  const unsigned st = stage_of<nc_of<ID>(), nt_of<ID>(), (WRAPM >> ID) & 1>(n, L.cls);
  dma_grp<4, (ID > 2)>(ring + (n & 3) * F_ST + lw * 4096, L.va, L.qa, st * (ID <= 2 ? 128 : (ID == 3 ? 1024 : 2048)));
}
template <int ID>
__device__ __forceinline__ void ld_b(const Ld& L, unsigned ring, unsigned lw, unsigned n) {
  const unsigned st = stage_of<nc_of<ID>(), nt_of<ID>(), (WRAPM >> ID) & 1>(n, L.cls);
  const unsigned dst = ring + (n & 3) * F_ST + F_A;
  if constexpr (ID == 3) {
    const unsigned d = dst + (lw >> 1) * G3_UP + (lw & 1) * 4096;
    if (lw & 1) dma_grp<3>(d, L.vb, L.qb, st * (32 * F_ * 2));
    else dma_grp<4>(d, L.vb, L.qb, st * (32 * F_ * 2));
  } else {
    dma_grp<4, ID == 2>(dst + lw * 4096, L.vb, L.qb, st * (ID == 1 ? 128 : (ID == 2 ? 2048 : 64 * D_ * 2)));
  }
}

template <int ID> __device__ __forceinline__ void ld_const(const Ld& L, unsigned ring, unsigned lw, unsigned n) { if constexpr (ID == 2) ld_a<ID>(L, ring, lw, n); else ld_b<ID>(L, ring, lw, n); }
template <int ID> __device__ __forceinline__ void ld_dep(const Ld& L, unsigned ring, unsigned lw, unsigned n) { if constexpr (ID == 2) ld_b<ID>(L, ring, lw, n); else ld_a<ID>(L, ring, lw, n); }

template <int ID>
__device__ __forceinline__ void sq_loader(const FArgs& p, const Ld& L, Ld& N, unsigned lds0, unsigned lw, unsigned xcd, unsigned idx) {
  constexpr int NT = nt_of<ID>(), NEXT = ID == 4 ? 0 : ID + 1;
  const unsigned ring = lds0 + F_RING;
  if constexpr (ID == 1) {
    ld_a<ID>(L, ring, lw, 0); ld_b<ID>(L, ring, lw, 0);
    N = ld_setup<NEXT>(p, lw, xcd, idx); ld_pin(N);
    ld_a<ID>(L, ring, lw, 1); ld_b<ID>(L, ring, lw, 1);
  } else {
    ld_dep<ID>(L, ring, lw, 0); ld_dep<ID>(L, ring, lw, 1);
    if constexpr (NEXT != 0) { N = ld_setup<NEXT>(p, lw, xcd, idx); ld_pin(N); }
  }
  wait_vm<(ID == 1 ? 8 : 4)>();
  unsigned touch = 0;
  barrier();
#pragma unroll 1
  for (unsigned n = 0; n + 2 < NT; ++n) {
    ld_a<ID>(L, ring, lw, n + 2);
    ld_b<ID>(L, ring, lw, n + 2);
    wait_vm<8>();
    barrier();
  }
  const int lane = lane_id();
  u32x4 ln;
  const char* lp = (const char*)p.flags + FL_LN + (lw < 2 ? (((xcd & 1) * 8 + (idx >> 2)) * 128 + lw * 64 + lane) : (2048 + ((xcd >> 1) * 4 + (idx & 3)) * 128 + (lw - 2) * 64 + lane)) * 16;
  if constexpr (ID == 1) asm volatile("global_load_dwordx4 %0, %1, off sc1" : "=v"(ln) : "v"(lp) : "memory");
  if constexpr (NEXT != 0) { ld_const<NEXT>(N, ring, lw, 0); wait_vm<(ID == 1 ? 5 : 3)>(); } else wait_vm<0>();
  barrier();
  if constexpr (NEXT != 0) ld_const<NEXT>(N, ring, lw, 1);
  if constexpr (ID == 1) {
    if (TOUCH_C && lw < 2) {
      const char* tp = (const char*)((xcd & 1) ? p.cim : p.cre) + (size_t)((xcd >> 1) * 512 + idx * 16 + lw * 8 + (lane >> 3)) * (S_ * 2) + (lane & 7) * 128;
      asm volatile("global_load_dword %0, %1, off" : "+v"(touch) : "v"(tp) : "memory");
    }
  }
  if constexpr (ID == 2) {
    if (FT_W && lw < 2 * FT_W) {
      const char* tp = (const char*)((lw & 1) ? p.wu : p.wg) + (size_t)((lw >> 1) * 256 + idx * 8 + (lane >> 3)) * (F_ * 2) + xcd * 896 + ((lane & 7) < 7 ? (lane & 7) : 6) * 128;
      asm volatile("global_load_dword %0, %1, off" : "+v"(touch) : "v"(tp) : "memory");
    }
  }
  barrier();
  const unsigned tag = lds_tag(lds0);
  if constexpr (ID == 1) {
    asm volatile("s_waitcnt vmcnt(8)" : "+v"(ln));
#pragma unroll 1
    for (unsigned spins = 0; spins < (1u << 15) && !__all(ln.y == tag && ln.w == tag); ++spins) {
      __builtin_amdgcn_s_sleep(2);
      asm volatile("global_load_dwordx4 %0, %1, off sc1\n\ts_waitcnt vmcnt(0)" : "=&v"(ln) : "v"(lp) : "memory");
    }
    LAS unsigned* tb = (LAS unsigned*)(lds0 + F_TAB);
    tb[lw * 64 + lane + (lw < 2 ? 0 : 128)] = ln.x;
    if (lw < 2) tb[128 + lw * 64 + lane] = ln.z;
    wait_lgkm0();
  }
  barrier();
  barrier();
  if constexpr (ID != 1) {
    barrier();
    if (lw == 1) poll_tags((char*)p.flags + FL_PAIR + ((ID == 2 ? 0 : 256) + (blockIdx.x ^ 1)) * 128, 1, tag);
    barrier();
    if constexpr (ID == 2) barrier();
  }
  asm volatile("s_waitcnt vmcnt(0)" : "+v"(touch));
}

template <int ID>
__device__ __forceinline__ void sq_compute(const FArgs& p, unsigned lds0, unsigned w, unsigned xcd, unsigned idx, unsigned tag) {
  constexpr int NT = nt_of<ID>();
  const unsigned ring = lds0 + F_RING;
  const unsigned g = w >> 2, wm = (w >> 1) & 1, wx = w & 1;
  const unsigned kh = xcd & 1, tm = (xcd >> 1) * 4 + (idx & 3), tn = idx >> 2;
  u32x4 res[2];
  unsigned rdA[4], rdB[4];
  {
    const int lane = lane_id(), fr = lane & 15, fq = lane >> 4, chunk = ((fq & 1) << 1) | (fq >> 1);
    const int row = tm * 128 + wm * 64 + g * 32 + fr, col = tn * 128 + wx * 64 + kh * 32 + chunk * 8;
    if constexpr (ID == 2) {
#pragma unroll
      for (int mb = 0; mb < 2; ++mb) res[mb] = *(const u32x4*)((const u16*)p.x + (size_t)(row + mb * 16) * D_ + col);
    } else if constexpr (ID == 4) {
      const auto rh = OUT_RSRC(p.h, S_ * D_ * 2);
#pragma unroll
      for (int mb = 0; mb < 2; ++mb)
        res[mb] = __builtin_amdgcn_raw_buffer_load_b128(rh, (((row >> 4) + mb) * (D_ / 32) + (col >> 5)) * 1024 + (chunk * 16 + fr) * 16, 0, 16);
    }
    const unsigned rotA = 2 * g, rotB = (ID == 1) ? 0u : 2 * kh, base = ring + g * F_ST;
#pragma unroll
    for (int i = 0; i < 4; ++i) {
      const unsigned kk = i >> 1, hf = (i & 1) * 2;
      if constexpr (ID == 1) {
        rdA[i] = base + (wm * 64 + (hf ^ rotA) * 16 + fr) * 128 + (((kk * 4 + fq) ^ (fr >> 1)) << 4);
        rdB[i] = base + F_A + (wx * 64 + fr) * 128 + ((((i & 1) * 4 + fq) ^ (fr >> 1)) << 4);
      } else if constexpr (ID == 2) {
        rdA[i] = base + (wm * 64 + (hf ^ rotA) * 16 + fr) * 128 + (((kk * 4 + fq) ^ (fr >> 1)) << 4);
        rdB[i] = base + F_A + wx * 8192 + (hf ^ rotB) * 2048 + lane * 16;
      } else {
        const int q = fr >> 2, pp = fr & 3, f = q | ((fq & 1) << 2);
        rdA[i] = base + wm * 8192 + (hf ^ rotA) * 2048 + lane * 16;
        rdB[i] = base + F_A + (8 * fq + q) * 256 + (((wx * 4 + (i ^ rotB)) ^ f) << 5) + pp * 8;
      }
    }
  }
  constexpr int NVA = (ID <= 2) ? 4 : 2, NVB = (ID <= 2) ? 2 : 4;
  bf16x8 fa[2][4], fb[2][4];
  f32x4 acc[4][4];
#pragma unroll
  for (int i = 0; i < 16; ++i) { acc[i >> 2][i & 3] = (f32x4){0.f, 0.f, 0.f, 0.f}; asm volatile("" : "+v"(acc[i >> 2][i & 3])); }
  auto reads = [&]() {
    SB();
#pragma unroll
    for (int s = 0; s < 2; ++s)
#pragma unroll
      for (int i = 0; i < 4; ++i) {
        if constexpr (ID == 1) {
          fb[s][i] = lds_b128(rdB[s] + i * 2048);
          fa[s][i] = lds_b128(rdA[s * 2 + (i >> 1)] + (i & 1) * 2048);
        } else if constexpr (ID == 2) {
          fb[s][i] = lds_b128(rdB[i >> 1] + (i & 1) * 2048 + s * 1024);
          fa[s][i] = lds_b128(rdA[s * 2 + (i >> 1)] + (i & 1) * 2048);
        } else {
          fb[s][i] = lds_tr(rdB[i] + s * 8192, rdB[i] + s * 8192 + 1024);
          fa[s][i] = lds_b128(rdA[i >> 1] + (i & 1) * 2048 + s * 1024);
        }
      }
    SB();
  };
  barrier();
  STAMP1_TAKE();
  if (g == 1) barrier();
  reads();
#pragma unroll 1
  for (unsigned i = 0; i < NT / 2; ++i) {
    const unsigned delta = (i & 1) ? (unsigned)(-2 * F_ST) : (unsigned)(2 * F_ST);
#pragma unroll
    for (int j = 0; j < NVA; ++j) { rdA[j] += delta; asm volatile("" : "+v"(rdA[j])); }
#pragma unroll
    for (int j = 0; j < NVB; ++j) { rdB[j] += delta; asm volatile("" : "+v"(rdB[j])); }
    wait_lgkm0();
    barrier();
    SB();
#pragma unroll
    for (int m = 0; m < 32; ++m) MFMA(fb[m >> 4][m & 3], fa[m >> 4][(m >> 2) & 3], acc[(m >> 2) & 3][m & 3]);
    SB();
    barrier();
    if (i + 1 < NT / 2) reads();
  }
  mfma_drain();
  SB();
  const int lane = lane_id(), fr = lane & 15, fq = lane >> 4, chunk = ((fq & 1) << 1) | (fq >> 1);
  {
    const unsigned wb = ring + 2 * F_ST + (w ^ 4) * 8192 + lane * 16, rb = ring + 2 * F_ST + w * 8192 + lane * 16;
#pragma unroll
    for (int i = 0; i < 8; ++i) *(LAS f32x4*)(wb + i * 1024) = acc[2 + (i >> 2)][i & 3];
    if (g == 0) barrier();
    STAMP1_PUT(ID);
    STAMP(ID, 2);
    wait_lgkm0();
    barrier();
#pragma unroll
    for (int i = 0; i < 8; ++i) acc[i >> 2][i & 3] += *(const LAS f32x4*)(rb + i * 1024);
  }
  if constexpr (ID == 1) {
    const LAS float* tb = (const LAS float*)(lds0 + F_TAB);
    const float sg = tm < 8 ? 1.f : -1.f;
    const auto ro = OUT_RSRC(p.ab, D_ * 4096 * 2);
    f32x4 mean[4], rstd[4];
#pragma unroll
    for (int j = 0; j < 4; ++j) {
      mean[j] = *(const LAS f32x4*)(tb + wx * 64 + j * 16 + fq * 4);
      rstd[j] = *(const LAS f32x4*)(tb + 128 + wx * 64 + j * 16 + fq * 4) * sg;
    }
#pragma unroll
    for (int mb = 0; mb < 2; ++mb) {
      const float wsm = tb[256 + wm * 64 + g * 32 + mb * 16 + fr];
#pragma unroll
      for (int np = 0; np < 2; ++np) {
        const f32x4 x0 = (acc[mb][2 * np] - mean[2 * np] * wsm) * rstd[2 * np], x1 = (acc[mb][2 * np + 1] - mean[2 * np + 1] * wsm) * rstd[2 * np + 1];
        auto s0 = __builtin_amdgcn_permlane16_swap(pk_bf16(x0[0], x0[1]), pk_bf16(x1[0], x1[1]), false, false);
        auto s1 = __builtin_amdgcn_permlane16_swap(pk_bf16(x0[2], x0[3]), pk_bf16(x1[2], x1[3]), false, false);
        const int rblk = (tm & 7) * 8 + wm * 4 + g * 2 + mb;
        const int kblk = (tm >> 3) * 64 + (kh * 8 + tn) * 4 + wx * 2 + np;
        STORE16(ro, (rblk * 128 + kblk) * 1024 + (chunk * 16 + fr) * 16, ((u32x4){s0[0], s1[0], s0[1], s1[1]}));
      }
    }
    return;
  }
  const auto rs = OUT_RSRC((char*)p.xch + (ID == 2 ? 0 : 256 * 16384), 256 * 16384);
#pragma unroll
  for (int mb = 0; mb < 2; ++mb) {
    auto s0 = __builtin_amdgcn_permlane16_swap(pk_bf16(acc[mb][2][0], acc[mb][2][1]), pk_bf16(acc[mb][3][0], acc[mb][3][1]), false, false);
    auto s1 = __builtin_amdgcn_permlane16_swap(pk_bf16(acc[mb][2][2], acc[mb][2][3]), pk_bf16(acc[mb][3][2], acc[mb][3][3]), false, false);
    __builtin_amdgcn_raw_buffer_store_b128((u32x4){s0[0], s1[0], s0[1], s1[1]}, rs, w * 2048 + lane * 16 + mb * 1024, blockIdx.x * 16384, 16);
  }
  f32x4 y[2][2];
#pragma unroll
  for (int mb = 0; mb < 2; ++mb)
#pragma unroll
    for (int j = 0; j < 4; ++j) {
      auto s = __builtin_amdgcn_permlane16_swap(__float_as_uint(acc[mb][0][j]), __float_as_uint(acc[mb][1][j]), false, false);
      y[mb][0][j] = __uint_as_float(s[0]) + ((j & 1) ? bf_hi(res[mb][j >> 1]) : bf_lo(res[mb][j >> 1]));
      y[mb][1][j] = __uint_as_float(s[1]) + ((j & 1) ? bf_hi(res[mb][2 + (j >> 1)]) : bf_lo(res[mb][2 + (j >> 1)]));
    }
  const int row0 = tm * 128 + wm * 64 + g * 32 + fr, col = tn * 128 + wx * 64 + kh * 32 + chunk * 8;
  unsigned optr = (ID == 2) ? ((row0 >> 4) * (D_ / 32) + (col >> 5)) * 1024 + (chunk * 16 + fr) * 16 : (row0 * D_ + col) * 2;
  const auto ro = OUT_RSRC(ID == 2 ? p.h : p.out, S_ * D_ * 2);
  asm volatile("" : "+v"(y[0][0]), "+v"(y[0][1]), "+v"(y[1][0]), "+v"(y[1][1]), "+v"(optr));
  wait_vm<0>();
  barrier();
  if (w == 0 && lane == 0) put_tag((char*)p.flags + FL_PAIR + ((ID == 2 ? 0 : 256) + blockIdx.x) * 128, tag);
  barrier();
  u32x4 in[2], v[2];
#pragma unroll
  for (int i = 0; i < 2; ++i) in[i] = __builtin_amdgcn_raw_buffer_load_b128(rs, w * 2048 + lane * 16 + i * 1024, (blockIdx.x ^ 1) * 16384, 16);
#pragma unroll
  for (int mb = 0; mb < 2; ++mb) {
    const f32x4 a = y[mb][0] + (f32x4){bf_lo(in[mb][0]), bf_hi(in[mb][0]), bf_lo(in[mb][1]), bf_hi(in[mb][1])};
    const f32x4 b = y[mb][1] + (f32x4){bf_lo(in[mb][2]), bf_hi(in[mb][2]), bf_lo(in[mb][3]), bf_hi(in[mb][3])};
    v[mb] = (u32x4){pk_bf16(a[0], a[1]), pk_bf16(a[2], a[3]), pk_bf16(b[0], b[1]), pk_bf16(b[2], b[3])};
    STORE16(ro, optr + mb * 16 * D_ * 2, v[mb]);
  }
  SB();
  if constexpr (ID == 2) {
    float m[2], q[2];
#pragma unroll
    for (int mb = 0; mb < 2; ++mb) {
      float s = 0.f;
#pragma unroll
      for (int j = 0; j < 4; ++j) s += bf_lo(v[mb][j]) + bf_hi(v[mb][j]);
      m[mb] = sum_rows(s) * (1.0f / 32.0f);
    }
#pragma unroll
    for (int mb = 0; mb < 2; ++mb) {
      float s = 0.f;
#pragma unroll
      for (int j = 0; j < 4; ++j) {
        const float a = bf_lo(v[mb][j]) - m[mb], b = bf_hi(v[mb][j]) - m[mb];
        s += a * a + b * b;
      }
      q[mb] = sum_rows(s);
    }
    const float ms = (fq & 1) ? m[1] : m[0], qs = (fq & 1) ? q[1] : q[0];
    LAS f32x2* tb = (LAS f32x2*)(lds0 + F_TAB + 3072);
    const int r = wm * 64 + g * 32 + (lane & 31);
    if (wx) tb[r] = (f32x2){ms, qs};
    wait_lgkm0();
    barrier();
    if (!wx) {
      const f32x2 o = tb[r];
      const float d = ms - o.x;
      const f32x2 st = {0.5f * (ms + o.x), qs + o.y + 16.0f * d * d};
      __hip_atomic_store((unsigned long long*)p.stats + ((size_t)tn * S_ + tm * 128 + r) * 2 + kh, __builtin_bit_cast(unsigned long long, st), __ATOMIC_RELAXED,
                         __HIP_MEMORY_SCOPE_AGENT);
    }
  }
}

__device__ __forceinline__ void glu_loader(const FArgs& p, const Ld& L, Ld& N, unsigned lds0, unsigned lw, unsigned xcd, unsigned idx) {
  constexpr int NT = 32, NEXT = 4;
  constexpr bool PRE = true;
  const unsigned tag = lds_tag(lds0);
  const unsigned ring = lds0 + F_RING;
  LAS float* cst = (LAS float*)(lds0 + F_TAB + 2048);
  unsigned long long* gcs = (unsigned long long*)((char*)p.flags + FL_CS) + (xcd * 4 + (idx >> 3)) * (8 * 224);
  if (PRE) {
    ld_a<3>(L, ring, lw, 0); ld_a<3>(L, ring, lw, 1);
  } else {
    ld_a<3>(L, ring, lw, 0); ld_b<3>(L, ring, lw, 0); ld_a<3>(L, ring, lw, 1); ld_b<3>(L, ring, lw, 1);
  }
  if constexpr (NEXT != 0) { N = ld_setup<NEXT>(p, lw, xcd, idx); ld_pin(N); }
  const int lane = lane_id();
  const unsigned u = lw * 64 + lane < 224 ? lw * 64 + lane : 223u;
  wait_vm<(PRE ? 4 : 7)>();
  barrier();
  ld_a<3>(L, ring, lw, 2);
  ld_b<3>(L, ring, lw, 2);
#pragma unroll 1
  for (unsigned t = 0; t + 3 < NT; ++t) {
    if (t == 25) {
      const unsigned hf = u >= 112, c = u - hf * 112, wn_ = c >= 56, cc = c - wn_ * 56, blk = cc >> 4;
      const unsigned src = lds0 + (wn_ * 4 + blk) * 768 + (blk == 3 ? 512 + (hf * 8 + (cc & 7)) * 4 : hf * 256 + (cc & 15) * 4);
      const float v = (*(const LAS float*)(src) + *(const LAS float*)(src + 64)) + (*(const LAS float*)(src + 128) + *(const LAS float*)(src + 192));
      asm volatile("global_store_dwordx2 %0, %1, off sc1" ::"v"(gcs + L.cls * 224 + u), "v"(((unsigned long long)tag << 32) | __float_as_uint(v)) : "memory");
    }
    ld_a<3>(L, ring, lw, t + 3);
    barrier();
    ld_b<3>(L, ring, lw, t + 3);
    wait_vm<14>();
    barrier();
  }
  unsigned long long g[8];
#pragma unroll
  for (int i = 0; i < 8; ++i) asm volatile("global_load_dwordx2 %0, %1, off sc1" : "=v"(g[i]) : "v"(gcs + i * 224 + u) : "memory");
  barrier();
  if constexpr (NEXT != 0) { ld_b<NEXT>(N, ring, lw, 0); wait_vm<19>(); } else wait_vm<15>();
  barrier();
  barrier();
  if constexpr (NEXT != 0) { ld_b<NEXT>(N, ring, lw, 1); wait_vm<16>(); } else wait_vm<8>();
  barrier();
  barrier();
  barrier();
  if constexpr (NEXT != 0) asm volatile("s_waitcnt vmcnt(8)" : "+v"(g[0]), "+v"(g[1]), "+v"(g[2]), "+v"(g[3]), "+v"(g[4]), "+v"(g[5]), "+v"(g[6]), "+v"(g[7]));
  else asm volatile("s_waitcnt vmcnt(0)" : "+v"(g[0]), "+v"(g[1]), "+v"(g[2]), "+v"(g[3]), "+v"(g[4]), "+v"(g[5]), "+v"(g[6]), "+v"(g[7]));
#pragma unroll 1
  for (unsigned spins = 0; spins < (1u << 15); ++spins) {
    unsigned tg = 0u;
#pragma unroll
    for (int i = 0; i < 8; ++i) tg |= (unsigned)(g[i] >> 32) ^ tag;
    if (__all(tg == 0u)) break;
    __builtin_amdgcn_s_sleep(2);
#pragma unroll
    for (int i = 0; i < 8; ++i) asm volatile("global_load_dwordx2 %0, %1, off sc1\n\ts_waitcnt vmcnt(0)" : "=&v"(g[i]) : "v"(gcs + i * 224 + u) : "memory");
  }
  float s = 0.f;
#pragma unroll
  for (int i = 0; i < 8; ++i) s += __uint_as_float((unsigned)g[i]);
  cst[u] = s;
  wait_lgkm0();
  unsigned touch = 0;
  if (NEXT != 0 && FT_D == 1) {
    const char* tp = (const char*)p.wd + ((size_t)blockIdx.x * 224 + (lw * 56 + (lane < 56 ? lane : 55))) * 128;
    asm volatile("global_load_dword %0, %1, off" : "+v"(touch) : "v"(tp) : "memory");
  }
  if (NEXT != 0 && FT_D == 2) {
    const char* tp = (const char*)p.wd + (size_t)((xcd & 1) * 1792 + idx * 16 + lw * 4 + (lane >> 4)) * (D_ * 2) + (lane & 15) * 128;
    asm volatile("global_load_dword %0, %1, off" : "+v"(touch) : "v"(tp) : "memory");
  }
  barrier();
  asm volatile("s_waitcnt vmcnt(0)" : "+v"(touch));
}

__device__ __forceinline__ void glu_compute(const FArgs& p, unsigned lds0, unsigned w, unsigned xcd, unsigned idx) {
  constexpr int ID = 3, BM = 256, NT = 32, MB = 4, NB = 7;
  const unsigned ring = lds0 + F_RING;
  LAS f32x2* tab = (LAS f32x2*)(lds0 + F_TAB);
  LAS float* cst = (LAS float*)(lds0 + F_TAB + 2048);
  const unsigned tm = idx & 7, tn = xcd * 4 + (idx >> 3), cls = (tm + 2 * (idx >> 3)) & 7;
  const unsigned wm = w & 3, wn = w >> 2;
  const int lane = lane_id(), fr = lane & 15, fq = lane >> 4;
  const unsigned rot = wm == 3 ? 0u : wm;
  unsigned xa = ring + wm * 4096 + lane * 16, xb[3], xb1;
  {
    const int q = fr >> 2, pp = fr & 3, r = (fq >> 1) * 16 + (fq & 1) * 4 + q;
#pragma unroll
    for (int i = 0; i < 3; ++i) xb[i] = ring + F_A + r * 224 + wn * 112 + pp * 8 + ((i + rot) % 3) * 32;
    xb1 = ring + F_A + r * 224 + wn * 112 + 96 + (pp & 1) * 8 + (pp >> 1) * G3_UP;
  }
  float cs[3] = {0.f, 0.f, 0.f};
  if (wn == 1) {
    const auto rst = OUT_RSRC(p.stats, 8 * S_ * 16);
    f32x4 sv[8];
#pragma unroll
    for (int i = 0; i < 8; ++i) sv[i] = __builtin_bit_cast(f32x4, __builtin_amdgcn_raw_buffer_load_b128(rst, (tm * BM + wm * 64 + lane) * 16, i * S_ * 16, 16));
    SB();
    barrier();
    SB();
    float m = 0.f;
#pragma unroll
    for (int i = 0; i < 8; ++i) m += sv[i][0] + sv[i][2];
    m *= (1.0f / 16.0f);
    float q = 0.f;
#pragma unroll
    for (int i = 0; i < 8; ++i) {
      const float d0 = sv[i][0] - m, d1 = sv[i][2] - m;
      q += sv[i][1] + sv[i][3] + 64.0f * (d0 * d0 + d1 * d1);
    }
    tab[wm * 64 + lane] = (f32x2){m, __builtin_amdgcn_rsqf(q * (1.0f / D_) + EPS)};
  }
  bf16x8 fa[MB], fb[NB];
  f32x4 acc[MB][NB];
#pragma unroll
  for (int mb = 0; mb < MB; ++mb)
#pragma unroll
    for (int nb = 0; nb < NB; ++nb) { acc[mb][nb] = (f32x4){0.f, 0.f, 0.f, 0.f}; asm volatile("" : "+v"(acc[mb][nb])); }
  asm volatile("" : "+v"(xa), "+v"(xb[0]), "+v"(xb[1]), "+v"(xb[2]), "+v"(xb1));
  wait_lgkm0();
  barrier();
  STAMP1_TAKE();
  const unsigned csmask = (0x010101u << cls) | (1u << ((cls + 1) & 7));
  auto reads = [&]() {
    SB();
    fa[0] = lds_b128(xa);
#pragma unroll
    for (int j = 0; j < 7; ++j) {
      const unsigned o = j < 6 ? xb[j % 3] + (j / 3) * G3_UP : xb1;
      fb[j] = lds_tr(o, o + 1792);
    }
#pragma unroll
    for (int mb = 1; mb < MB; ++mb) fa[mb] = lds_b128(xa + mb * 1024);
    SB();
  };
  reads();
#pragma unroll 1
  for (unsigned t = 0; t < NT; ++t) {
    const unsigned delta = ((t & 3) == 3) ? (unsigned)(-3 * F_ST) : (unsigned)F_ST;
    xa += delta; xb[0] += delta; xb[1] += delta; xb[2] += delta; xb1 += delta;
    asm volatile("" : "+v"(xa), "+v"(xb[0]), "+v"(xb[1]), "+v"(xb[2]), "+v"(xb1));
    if (__builtin_expect((csmask >> t) & 1u, 0)) {
#pragma unroll
      for (int j = 0; j < 4; ++j)
#pragma unroll
        for (int i = 0; i < 3; ++i)
          asm volatile("v_dot2c_f32_bf16 %0, %1, %2" : "+v"(cs[i]) : "s"(0x3f803f80u), "v"(__builtin_bit_cast(u32x4, fb[3 * i])[j]));
      asm volatile("s_mov_b32 m0, %3\n\ts_nop 2\n\tds_write_addtid_b32 %0\n\tds_write_addtid_b32 %1 offset:256\n\tds_write_addtid_b32 %2 offset:512" ::"v"(cs[0]),
                   "v"(cs[1]), "v"(cs[2]), "s"(lds0 + w * 768)
                   : "memory");
    }
    wait_lgkm0();
    barrier();
    SB();
#pragma unroll
    for (int mb = 0; mb < MB; ++mb)
#pragma unroll
      for (int nb = 0; nb < NB; ++nb) MFMA(fb[nb], fa[mb], acc[mb][nb]);
    SB();
    barrier();
    reads();
  }
  if (wn == 0) barrier();
  mfma_drain();
  SB();
  const int lane2 = lane_id();
  const int gr = lane2 & 15, gq = lane2 >> 4;
  STAMP1_PUT(ID);
  STAMP(ID, 2);
  auto act = [](float g, float u) { return g * u * __builtin_amdgcn_rcpf(1.0f + __expf(-g)); };
  const auto ro = OUT_RSRC(p.gu, S_ * F_ * 2);
  f32x2 st[MB];
#pragma unroll
  for (int mb = 0; mb < MB; ++mb) st[mb] = tab[wm * 64 + mb * 16 + gr];
  const int gch0 = tn * 14 + wn * 7;
#pragma unroll
  for (int i = 0; i < 3; ++i) {
    const unsigned ib = (i + rot) % 3;
    const f32x4 cg = *(const LAS f32x4*)(cst + wn * 56 + ib * 16 + gq * 4), cu = *(const LAS f32x4*)(cst + 112 + wn * 56 + ib * 16 + gq * 4);
    u32x2 pkd[MB];
#pragma unroll
    for (int mb = 0; mb < MB; ++mb) {
      const f32x4 g = (acc[mb][i] - cg * st[mb].x) * st[mb].y, u = (acc[mb][3 + i] - cu * st[mb].x) * st[mb].y;
      pkd[mb] = (u32x2){pk_bf16(act(g[0], u[0]), act(g[1], u[1])), pk_bf16(act(g[2], u[2]), act(g[3], u[3]))};
    }
#pragma unroll
    for (int mp = 0; mp < MB / 2; ++mp) {
      auto s0 = __builtin_amdgcn_permlane16_swap(pkd[2 * mp].x, pkd[2 * mp + 1].x, false, false);
      auto s1 = __builtin_amdgcn_permlane16_swap(pkd[2 * mp].y, pkd[2 * mp + 1].y, false, false);
      const u32x4 v = {s0[0], s1[0], s0[1], s1[1]};
      const int rblk = tm * (BM / 16) + wm * MB + 2 * mp + (gq & 1);
      const int gch = gch0 + 2 * ib + (gq >> 1);
      STORE16(ro, (rblk * (F_ / 32) + (gch >> 2)) * 1024 + ((gch & 3) * 16 + gr) * 16, v);
    }
  }
  {
    const f32x4 cm = *(const LAS f32x4*)(cst + (gq >> 1) * 112 + wn * 56 + 48 + (gq & 1) * 4);
    u32x2 pkd[MB / 2];
#pragma unroll
    for (int mp = 0; mp < MB / 2; ++mp) {
      const f32x4 v0 = (acc[2 * mp][6] - cm * st[2 * mp].x) * st[2 * mp].y, v1 = (acc[2 * mp + 1][6] - cm * st[2 * mp + 1].x) * st[2 * mp + 1].y;
      float r[4];
#pragma unroll
      for (int j = 0; j < 4; ++j) {
        auto s = __builtin_amdgcn_permlane32_swap(__float_as_uint(v0[j]), __float_as_uint(v1[j]), false, false);
        r[j] = act(__uint_as_float(s[0]), __uint_as_float(s[1]));
      }
      pkd[mp] = (u32x2){pk_bf16(r[0], r[1]), pk_bf16(r[2], r[3])};
    }
    auto s0 = __builtin_amdgcn_permlane16_swap(pkd[0].x, pkd[1].x, false, false);
    auto s1 = __builtin_amdgcn_permlane16_swap(pkd[0].y, pkd[1].y, false, false);
    const u32x4 v = {s0[0], s1[0], s0[1], s1[1]};
    const int rblk = tm * (BM / 16) + wm * MB + (gq & 1) * 2 + (gq >> 1);
    const int gch = gch0 + 6;
    STORE16(ro, (rblk * (F_ / 32) + (gch >> 2)) * 1024 + ((gch & 3) * 16 + gr) * 16, v);
  }
}

__global__ __launch_bounds__(768, 3) void k_block(FArgs p) {
  CODE_LABEL(fz_code_f);
  extern __shared__ __attribute__((aligned(16))) char shm[];
  const unsigned lds0 = (unsigned)(size_t)(LAS char*)shm;
  const unsigned w = rfl(threadIdx.x >> 6);
  const unsigned xcd = blockIdx.x & 7, idx = blockIdx.x >> 3;
  char* fl = (char*)p.flags;
  const unsigned tm = (xcd >> 1) * 4 + (idx & 3), kh = xcd & 1, lj = idx >> 2;
  if (w >= 8) {
    const unsigned lw = w - 8;
    Ld LA, LB;
    LA = ld_setup<1>(p, lw, xcd, idx);
    sq_loader<1>(p, LA, LB, lds0, lw, xcd, idx);
    const unsigned tag = lds_tag(lds0);
    barrier();
    if (lw == 1) poll_tags(fl + FL_P1 + ((kh * 8 + lj) * 4 + (xcd >> 1)) * 128, 16, tag);
    barrier();
    sq_loader<2>(p, LB, LA, lds0, lw, xcd, idx);
    barrier();
    if (lw == 1) poll_tags(fl + FL_P2 + ((idx & 7) * 8 + xcd) * 128, 32, tag);
    barrier();
    glu_loader(p, LA, LB, lds0, lw, xcd, idx);
    barrier();
    if (lw == 1) poll_tags(fl + FL_P3 + ((tm >> 1) * 2 + kh) * 128, 16, tag);
    barrier();
    sq_loader<4>(p, LB, LA, lds0, lw, xcd, idx);
    return;
  }
  STAMP(1, 0);
  unsigned csink = 0;
  if (CODE_PF && w == 0 && idx < 3) {
    unsigned long long a;
    CODE_ADDR(a, fz_code_f);
    touch_code(a + idx * 8192, lane_id(), idx == 2 ? CODE_LAST : 63, csink);
  }
  unsigned tag;
  {
    const int lane = lane_id();
    const unsigned r0 = blockIdx.x * 8 + w;
    const u16* xp = (const u16*)p.x + (size_t)r0 * D_ + lane * 8;
    const u16* wp = (const u16*)(r0 < 1024 ? p.dre : p.dim) + (size_t)(r0 & 1023) * D_ + lane * 8;
    u32x4 xv[2], wv[2];
#pragma unroll
    for (int i = 0; i < 2; ++i) xv[i] = (LN_NT & 1) ? __builtin_nontemporal_load((const u32x4*)(xp + i * 512)) : *(const u32x4*)(xp + i * 512);
#pragma unroll
    for (int i = 0; i < 2; ++i) wv[i] = (LN_NT & 2) ? __builtin_nontemporal_load((const u32x4*)(wp + i * 512)) : *(const u32x4*)(wp + i * 512);
    const unsigned tv = __hip_atomic_load((const unsigned*)(fl + FL_EPOCH), __ATOMIC_RELAXED, __HIP_MEMORY_SCOPE_AGENT) + 1u;
    SB();
    float s = 0.f, t = 0.f;
#pragma unroll
    for (int j = 0; j < 8; ++j) s += bf_lo(xv[j >> 2][j & 3]) + bf_hi(xv[j >> 2][j & 3]);
    const float m = wave_sum(s) * (1.0f / D_);
    float q = 0.f;
#pragma unroll
    for (int j = 0; j < 8; ++j) {
      const float a = bf_lo(xv[j >> 2][j & 3]) - m, b = bf_hi(xv[j >> 2][j & 3]) - m;
      q += a * a + b * b;
      t += bf_lo(wv[j >> 2][j & 3]) + bf_hi(wv[j >> 2][j & 3]);
    }
    const float rs = __builtin_amdgcn_rsqf(wave_sum(q) * (1.0f / D_) + EPS);
    t = wave_sum(t);
    const float val = (lane & 2) ? t : ((lane & 1) ? rs : m);
    if (lane < 4)
      __hip_atomic_store((unsigned long long*)(fl + FL_LN + ((lane >> 1) * 2048 + r0) * 16 + (lane & 1) * 8), ((unsigned long long)tv << 32) | __float_as_uint(val),
                         __ATOMIC_RELAXED, __HIP_MEMORY_SCOPE_AGENT);
    tag = rfl(tv);
    if (w == 0) { *(volatile LAS unsigned*)(lds0 + F_TAG) = tag; wait_lgkm0(); }
  }
  asm volatile("" ::"v"(csink));
  STAMPX(1, 5);
  sq_compute<1>(p, lds0, w, xcd, idx, tag);
  wait_vm<0>();
  barrier();
  if (w == 0) { const int l = lane_id(); if (l < 4) put_tag(fl + FL_P1 + (tm * 4 + l) * 128 + (kh * 8 + lj) * 4, tag); }
  STAMP(1, 3);
  barrier();
  STAMP(2, 0);
  sq_compute<2>(p, lds0, w, xcd, idx, tag);
  wait_vm<0>();
  barrier();
  if (w == 0) { const int l = lane_id(); if (l < 8) put_tag(fl + FL_P2 + ((tm >> 1) * 8 + l) * 128 + (((tm & 1) * 2 + kh) * 8 + lj) * 4, tag); }
  STAMP(2, 3);
  barrier();
  STAMP(3, 0);
  glu_compute(p, lds0, w, xcd, idx);
  wait_vm<0>();
  barrier();
  if (w == 0 && lane_id() == 0) put_tag(fl + FL_P3 + ((idx & 7) * 2 + (xcd >> 2)) * 128 + ((xcd & 3) * 4 + (idx >> 3)) * 4, tag);
  STAMP(3, 3);
  barrier();
  STAMP(4, 0);
  sq_compute<4>(p, lds0, w, xcd, idx, tag);
  if (blockIdx.x == 0 && w == 0 && lane_id() == 0) put_tag(fl + FL_EPOCH, tag);
  STAMP_END(4);
}
}

extern "C" void kernel_launch(void* const* d_in, const int* in_sizes, int n_in, void* d_out, int out_size, void* d_ws, size_t ws_size,
                              hipStream_t stream) {
  char* ws = (char*)d_ws;
  FArgs f = {d_in[3], d_in[4], d_in[1], d_in[2], d_in[0], d_in[6], d_in[7], d_in[8], ws + WS_AB, ws + WS_H, ws + WS_ST, ws + WS_GU, d_out, ws + WS_XC, ws + WS_FL};
  (void)hipFuncSetAttribute((const void*)k_block, hipFuncAttributeMaxDynamicSharedMemorySize, F_LDS);
  hipLaunchKernelGGL(k_block, dim3(256), dim3(768), F_LDS, stream, f);
}
```

```cpp
#include <hip/hip_runtime.h>
#include <hip/hip_bf16.h>

namespace {
#define LAS __attribute__((address_space(3)))
typedef unsigned short u16;
typedef short s16x4 __attribute__((ext_vector_type(4)));
typedef short s16x8 __attribute__((ext_vector_type(8)));
typedef __bf16 bf16x8 __attribute__((ext_vector_type(8)));
typedef __bf16 bf16x2 __attribute__((ext_vector_type(2)));
typedef float f32x4 __attribute__((ext_vector_type(4)));
typedef float f32x2 __attribute__((ext_vector_type(2)));
typedef unsigned u32x4 __attribute__((ext_vector_type(4)));
typedef unsigned u32x2 __attribute__((ext_vector_type(2)));

constexpr int S_ = 2048, D_ = 1024, F_ = 3584;
constexpr float EPS = 1e-5f;

constexpr size_t WS_AB = 0;
constexpr size_t WS_H = WS_AB + (size_t)D_ * 2 * S_ * 2;
constexpr size_t WS_GU = WS_H + (size_t)S_ * D_ * 2;
constexpr size_t WS_ST = WS_GU + (size_t)S_ * F_ * 2;
constexpr size_t WS_XC = WS_ST + (size_t)S_ * 16 * 8;
constexpr size_t WS_FL = WS_XC + (size_t)2 * 256 * 16384;
constexpr int FL_EPOCH = 0;
constexpr int FL_PAIR = 128;
constexpr int FL_P1 = FL_PAIR + 512 * 128;
constexpr int FL_P2 = FL_P1 + 64 * 128;
constexpr int FL_P3 = FL_P2 + 64 * 128;
constexpr int FL_LN = FL_P3 + 16 * 128;
constexpr int FL_CS = FL_LN + 4096 * 16;

__device__ __forceinline__ unsigned rfl(unsigned v) { return __builtin_amdgcn_readfirstlane(v); }
__device__ __forceinline__ unsigned pk_bf16(float lo, float hi) {
  bf16x2 c = {(__bf16)lo, (__bf16)hi};
  return __builtin_bit_cast(unsigned, c);
}
__device__ __forceinline__ float bf_lo(unsigned v) { return __uint_as_float(v << 16); }
__device__ __forceinline__ float bf_hi(unsigned v) { return __uint_as_float(v & 0xffff0000u); }

__device__ __forceinline__ u32x4 make_rsrc(const void* p, unsigned bytes) {
  unsigned long long a = (unsigned long long)p;
  u32x4 r;
  r.x = (unsigned)a;
  r.y = (unsigned)(a >> 32) & 0xffffu;
  r.z = bytes;
  r.w = 0x00020000u;
  return r;
}
__device__ __forceinline__ void dma16(unsigned ldsaddr, unsigned voff, u32x4 rsrc, unsigned soff) {
  asm volatile("s_mov_b32 m0, %0\n\ts_nop 4\n\tbuffer_load_dwordx4 %1, %2, %3 offen lds" ::"s"(ldsaddr), "v"(voff), "s"(rsrc), "s"(soff)
               : "memory");
}
template <int N>
__device__ __forceinline__ void wait_vm() {
  asm volatile("s_waitcnt vmcnt(%0)" ::"i"(N) : "memory");
}
__device__ __forceinline__ void wait_lgkm0() { __builtin_amdgcn_s_waitcnt(0xC07F); }
__device__ __forceinline__ void barrier() {
  asm volatile("" ::: "memory");
  __builtin_amdgcn_s_barrier();
  asm volatile("" ::: "memory");
}
__device__ __forceinline__ bf16x8 lds_b128(unsigned addr) { return *(const LAS bf16x8*)(addr); }
__device__ __forceinline__ bf16x8 lds_tr(unsigned a0, unsigned a1) {
  s16x4 lo = __builtin_amdgcn_ds_read_tr16_b64_v4i16((LAS s16x4*)(a0));
  s16x4 hi = __builtin_amdgcn_ds_read_tr16_b64_v4i16((LAS s16x4*)(a1));
  s16x8 f = __builtin_shufflevector(lo, hi, 0, 1, 2, 3, 4, 5, 6, 7);
  return __builtin_bit_cast(bf16x8, f);
}

#define ST_AUX 16
#define OUT_RSRC(ptr, bytes) __builtin_amdgcn_make_buffer_rsrc((void*)(ptr), 0, (bytes), 0x00020000)
#define STORE16(rs, byteoff, v) __builtin_amdgcn_raw_buffer_store_b128(v, rs, byteoff, 0, ST_AUX)
#define STAMP(k, i)
#define STAMPX(k, i)
#define STAMPL(k, i, first)

template <int N, bool SC1 = false>
__device__ __forceinline__ void dma_grp(unsigned ldsaddr, const unsigned (&v)[4], u32x4 rsrc, unsigned soff) {
  if constexpr (N == 4 && SC1)
    asm volatile("s_mov_b32 m0, %0\n\ts_nop 4\n\tbuffer_load_dwordx4 %1, %5, %6 offen sc1 lds\n\tbuffer_load_dwordx4 %2, %5, %6 offen offset:1024 sc1 lds\n\t"
                 "buffer_load_dwordx4 %3, %5, %6 offen offset:2048 sc1 lds\n\tbuffer_load_dwordx4 %4, %5, %6 offen offset:3072 sc1 lds" ::"s"(ldsaddr),
                 "v"(v[0]), "v"(v[1]), "v"(v[2]), "v"(v[3]), "s"(rsrc), "s"(soff)
                 : "memory");
  else if constexpr (N == 4)
    asm volatile("s_mov_b32 m0, %0\n\ts_nop 4\n\tbuffer_load_dwordx4 %1, %5, %6 offen lds\n\tbuffer_load_dwordx4 %2, %5, %6 offen offset:1024 lds\n\t"
                 "buffer_load_dwordx4 %3, %5, %6 offen offset:2048 lds\n\tbuffer_load_dwordx4 %4, %5, %6 offen offset:3072 lds" ::"s"(ldsaddr),
                 "v"(v[0]), "v"(v[1]), "v"(v[2]), "v"(v[3]), "s"(rsrc), "s"(soff)
                 : "memory");
  else
    asm volatile("s_mov_b32 m0, %0\n\ts_nop 4\n\tbuffer_load_dwordx4 %1, %4, %5 offen lds\n\tbuffer_load_dwordx4 %2, %4, %5 offen offset:1024 lds\n\t"
                 "buffer_load_dwordx4 %3, %4, %5 offen offset:2048 lds" ::"s"(ldsaddr),
                 "v"(v[0]), "v"(v[1]), "v"(v[2]), "s"(rsrc), "s"(soff)
                 : "memory");
}
__device__ __forceinline__ float sum_rows(float x) {
  auto a = __builtin_amdgcn_permlane16_swap(__float_as_uint(x), __float_as_uint(x), false, false);
  x = __uint_as_float(a[0]) + __uint_as_float(a[1]);
  auto b = __builtin_amdgcn_permlane32_swap(__float_as_uint(x), __float_as_uint(x), false, false);
  return __uint_as_float(b[0]) + __uint_as_float(b[1]);
}
__device__ __forceinline__ float sum_pair(float x) { return x + __uint_as_float(__builtin_amdgcn_mov_dpp(__float_as_uint(x), 0xB1, 0xF, 0xF, true)); }
template <int CTRL> __device__ __forceinline__ float dpp_add(float x) { return x + __uint_as_float(__builtin_amdgcn_mov_dpp(__float_as_uint(x), CTRL, 0xF, 0xF, true)); }
__device__ __forceinline__ float wave_sum(float x) { return sum_rows(dpp_add<0x140>(dpp_add<0x141>(dpp_add<0x4E>(dpp_add<0xB1>(x))))); }
__device__ __forceinline__ int lane_id() {
  int l;
  asm volatile("v_mbcnt_lo_u32_b32 %0, -1, 0\n\tv_mbcnt_hi_u32_b32 %0, -1, %0" : "=v"(l));
  return l;
}
__device__ __forceinline__ void mfma_drain() { asm volatile("s_nop 15" ::: "memory"); }
__device__ __forceinline__ void fadd(float& a, float b) { asm("v_add_f32 %0, %1, %0" : "+v"(a) : "v"(b)); }
template <int NC, int NT, int WRAP>
__device__ __forceinline__ unsigned stage_of(unsigned n, unsigned c) {
  if (NC == 1) return n;
  if (WRAP) {
    const unsigned t = ((n & (NC - 1)) == c) ? n + NC : n;
    return rfl(t >= NT ? t - NT : t);
  }
  unsigned t = (n > NT - NC + c) ? n : n - 1;
  t = ((t & (NC - 1)) == c) ? t + NC : t;
  return rfl(n == 0 ? c : t);
}
#define NC1 4
#define NC2 8
#define NC3 8
#define NC4 4
#define TOUCH_C 1
#define TOUCH_W 1
#define TOUCH_D 2
#define TOUCH_AT 6
#define ST_SLEEP 0
#define EARLY0 0
#define SNAKE 1
#define L2MASK 63
#define PD3 0
#define XCH4 (256 * 16384)
#define LN_NT 0
#define FT_W 1
#define FT_D 1
#define POLL_SLEEP 1
#define XSTR 16384
#define WRAPM 0x1e
#define SQ_PD 2
#define BAR_AT 0
#define CPRIO 0
template <int V> struct IC { static constexpr int value = V; };
#define LPRIO 0
#define SB() __builtin_amdgcn_sched_barrier(0)
#define MFMA(b, a, c) asm volatile("v_mfma_f32_16x16x32_bf16 %0, %1, %2, %0" : "+v"(c) : "v"(b), "v"(a))
#define STAMP_END(k)
#define XT(i)
#define XPUT(k)
#define STAMP1_TAKE()
#define STAMP1_PUT(k)

#define CODE_LABEL(name) asm volatile(#name ":")
#define CODE_ADDR(dst, name) do { int o_; asm volatile("s_getpc_b64 %0\n\ts_mov_b32 %1, " #name "@rel32@lo+4" : "=s"(dst), "=s"(o_)); dst += (long long)o_; } while (0)
#define CODE_LAST 30
#define CODE_PF 3
__device__ __forceinline__ void touch_code(unsigned long long base, int lane, int last, unsigned& sink) {
  const unsigned off = (lane < last ? lane : last) * 128;
  asm volatile("global_load_dword %0, %1, %2" : "+v"(sink) : "v"(off), "s"(base) : "memory");
}
struct FArgs {
  const void *dre, *dim, *cre, *cim, *x, *wg, *wu, *wd;
  void *ab, *h, *stats, *gu, *out, *xch, *flags;
};
constexpr int F_RING = 6144, F_ST = 32768, F_TAB = F_RING + 4 * F_ST, F_TAG = F_TAB + 4096, F_LDS = F_TAB + 4160, F_A = 16384, G3_UP = 7168 + 16;

__device__ __forceinline__ void poll_tags(const void* line, int n, unsigned tag) {
  const int l = lane_id();
  const unsigned* q = (const unsigned*)line + (l < n ? l : 0);
  unsigned spins = 0;
  while (!__all(__hip_atomic_load(q, __ATOMIC_RELAXED, __HIP_MEMORY_SCOPE_AGENT) == tag) && ++spins < (1u << 17)) __builtin_amdgcn_s_sleep(POLL_SLEEP);
}
__device__ __forceinline__ void put_tag(void* word, unsigned tag) { __hip_atomic_store((unsigned*)word, tag, __ATOMIC_RELAXED, __HIP_MEMORY_SCOPE_AGENT); }
__device__ __forceinline__ unsigned lds_tag(unsigned lds0) { return rfl(*(volatile LAS unsigned*)(lds0 + F_TAG)); }
template <int ID> constexpr int nt_of() { return ID == 1 ? 16 : (ID == 4 ? 28 : 32); }
template <int ID> constexpr int nc_of() { return ID == 1 ? NC1 : (ID == 2 ? NC2 : (ID == 3 ? 8 : NC4)); }
struct Ld {
  u32x4 qa, qb;
  unsigned va[4], vb[4], cls;
};
template <int ID>
__device__ __forceinline__ Ld ld_setup(const FArgs& p, unsigned lw, unsigned xcd, unsigned idx) {
  const int lane = lane_id();
  Ld L;
  if constexpr (ID == 3) {
    const unsigned tm = idx & 7, tn = xcd * 4 + (idx >> 3);
    L.qa = make_rsrc((const u16*)p.h + (size_t)tm * 256 * D_, 256 * D_ * 2);
    L.qb = make_rsrc(lw < 2 ? p.wg : p.wu, (unsigned)D_ * F_ * 2);
#pragma unroll
    for (int i = 0; i < 4; ++i) {
      L.va[i] = (lw * 4 + i) * (D_ / 32) * 1024 + lane * 16 - i * 1024;
      const int c = ((lw & 1) * 4 + i) * 64 + lane, r = c / 14, pos = c % 14;
      const int k = (r & 0x13) | ((r & 4) << 1) | ((r & 8) >> 1);
      L.vb[i] = k * F_ * 2 + tn * 224 + pos * 16 - i * 1024;
    }
    L.cls = (tm + 2 * (idx >> 3)) & 7;
  } else {
    const unsigned li = idx & 3, lj = idx >> 2, kh = xcd & 1, tm = (xcd >> 1) * 4 + li, tn = lj;
    if constexpr (ID == 1) {
      L.qa = make_rsrc((const u16*)(tm < 8 ? p.dre : p.dim) + (size_t)(tm & 7) * 128 * D_, 128 * D_ * 2);
      L.qb = make_rsrc((const u16*)p.x + (size_t)(kh * 8 + lj) * 128 * D_, 128 * D_ * 2);
    } else if constexpr (ID == 2) {
      L.qa = make_rsrc((const u16*)(kh ? p.cim : p.cre) + (size_t)tm * 128 * S_, 128 * S_ * 2);
      L.qb = make_rsrc((const u16*)p.ab + (size_t)tn * 128 * 4096 + kh * 64 * 512, 128 * 4096 * 2 - kh * 64 * 1024);
    } else {
      L.qa = make_rsrc((const u16*)p.gu + (size_t)tm * 128 * F_ + kh * 56 * 512, 128 * F_ * 2 - kh * 56 * 1024);
      L.qb = make_rsrc((const u16*)p.wd + (size_t)kh * 1792 * D_, 1792 * D_ * 2);
    }
#pragma unroll
    for (int i = 0; i < 4; ++i) {
      const int pc = lw * 4 + i;
      if constexpr (ID == 1) {
        const int row = pc * 8 + (lane >> 3);
        L.va[i] = L.vb[i] = row * D_ * 2 + (((lane & 7) ^ ((row >> 1) & 7)) << 4) - i * 1024;
      } else if constexpr (ID == 2) {
        const int row = pc * 8 + (lane >> 3);
        L.va[i] = row * S_ * 2 + (((lane & 7) ^ ((row >> 1) & 7)) << 4) - i * 1024;
        L.vb[i] = ((pc >> 1) * 128 + (pc & 1)) * 1024 + lane * 16 - i * 1024;
      } else {
        const int k = pc * 4 + (lane >> 4), f = (k & 3) | (((k >> 3) & 1) << 2);
        L.va[i] = ((pc >> 1) * (F_ / 32) + (pc & 1)) * 1024 + lane * 16 - i * 1024;
        L.vb[i] = k * D_ * 2 + tn * 256 + (((lane & 15) ^ (f << 1)) << 4) - i * 1024;
      }
    }
    L.cls = (nc_of<ID>() == 8) ? ((lj + 2 * li) & 7) : ((lj + li) & 3);
  }
  return L;
}
__device__ __forceinline__ void ld_pin(Ld& L) {
  asm volatile("" : "+v"(L.va[0]), "+v"(L.va[1]), "+v"(L.va[2]), "+v"(L.va[3]), "+v"(L.vb[0]), "+v"(L.vb[1]), "+v"(L.vb[2]), "+v"(L.vb[3]));
}
template <int ID>
__device__ __forceinline__ void ld_a(const Ld& L, unsigned ring, unsigned lw, unsigned n) {
  const unsigned st = stage_of<nc_of<ID>(), nt_of<ID>(), (WRAPM >> ID) & 1>(n, L.cls) & L2MASK;
  dma_grp<4, (ID > 2)>(ring + (n & 3) * F_ST + lw * 4096, L.va, L.qa, st * (ID <= 2 ? 128 : (ID == 3 ? 1024 : 2048)));
}
template <int ID>
__device__ __forceinline__ void ld_b(const Ld& L, unsigned ring, unsigned lw, unsigned n) {
  const unsigned st = stage_of<nc_of<ID>(), nt_of<ID>(), (WRAPM >> ID) & 1>(n, L.cls) & L2MASK;
  const unsigned dst = ring + (n & 3) * F_ST + F_A;
  if constexpr (ID == 3) {
    const unsigned d = dst + (lw >> 1) * G3_UP + (lw & 1) * 4096;
    if (lw & 1) dma_grp<3>(d, L.vb, L.qb, st * (32 * F_ * 2));
    else dma_grp<4>(d, L.vb, L.qb, st * (32 * F_ * 2));
  } else {
    dma_grp<4, ID == 2>(dst + lw * 4096, L.vb, L.qb, st * (ID == 1 ? 128 : (ID == 2 ? 2048 : 64 * D_ * 2)));
  }
}

template <int ID> __device__ __forceinline__ void ld_const(const Ld& L, unsigned ring, unsigned lw, unsigned n) { if constexpr (ID == 2) ld_a<ID>(L, ring, lw, n); else ld_b<ID>(L, ring, lw, n); }
template <int ID> __device__ __forceinline__ void ld_dep(const Ld& L, unsigned ring, unsigned lw, unsigned n) { if constexpr (ID == 2) ld_b<ID>(L, ring, lw, n); else ld_a<ID>(L, ring, lw, n); }

template <int ID>
__device__ __forceinline__ void sq_loader(const FArgs& p, const Ld& L, Ld& N, unsigned lds0, unsigned lw, unsigned xcd, unsigned idx, unsigned& touch) {
  constexpr int NT = nt_of<ID>(), NEXT = ID == 4 ? 0 : ID + 1;
  const unsigned ring = lds0 + F_RING;
  if constexpr (ID == 1) {
    ld_a<ID>(L, ring, lw, 0); ld_b<ID>(L, ring, lw, 0);
    N = ld_setup<NEXT>(p, lw, xcd, idx); ld_pin(N);
    ld_a<ID>(L, ring, lw, 1); ld_b<ID>(L, ring, lw, 1);
  } else {
    ld_dep<ID>(L, ring, lw, 0); ld_dep<ID>(L, ring, lw, 1);
  }
  constexpr unsigned KA = ID <= 2 ? 128 : 2048, KB = ID == 1 ? 128 : (ID == 2 ? 2048 : 64 * D_ * 2);
  unsigned sa, sb, ma;
  auto prep = [&](unsigned n) {
    const unsigned st = stage_of<nc_of<ID>(), NT, (WRAPM >> ID) & 1>(n, L.cls) & L2MASK;
    sa = st * KA; sb = st * KB; ma = ring + (n & 3) * F_ST + lw * 4096;
    asm volatile("" : "+s"(sa), "+s"(sb), "+s"(ma));
  };
  auto bar_a = [&]() {
#define LD4(R, S, X) "buffer_load_dwordx4 %" #R ", %" #S ", %" #X
    if constexpr (ID == 4)
      asm volatile("s_mov_b32 m0, %0\n\ts_waitcnt vmcnt(8)\n\ts_barrier\n\t" LD4(1, 5, 6) " offen sc1 lds\n\t" LD4(2, 5, 6) " offen offset:1024 sc1 lds\n\t" LD4(3, 5, 6)
                   " offen offset:2048 sc1 lds\n\t" LD4(4, 5, 6) " offen offset:3072 sc1 lds" ::"s"(ma), "v"(L.va[0]), "v"(L.va[1]), "v"(L.va[2]), "v"(L.va[3]), "s"(L.qa), "s"(sa)
                   : "memory");
    else
      asm volatile("s_mov_b32 m0, %0\n\ts_waitcnt vmcnt(8)\n\ts_barrier\n\t" LD4(1, 5, 6) " offen lds\n\t" LD4(2, 5, 6) " offen offset:1024 lds\n\t" LD4(3, 5, 6)
                   " offen offset:2048 lds\n\t" LD4(4, 5, 6) " offen offset:3072 lds" ::"s"(ma), "v"(L.va[0]), "v"(L.va[1]), "v"(L.va[2]), "v"(L.va[3]), "s"(L.qa), "s"(sa)
                   : "memory");
  };
  prep(2);
  wait_vm<(ID == 1 ? 8 : 4)>();
  bar_a();
  unsigned mb_ = ma + F_A, sb_ = sb;
  prep(3);
#pragma unroll 1
  for (unsigned n = 0; n + 3 < NT; ++n) {
    dma_grp<4, ID == 2>(mb_, L.vb, L.qb, sb_);
    bar_a();
    mb_ = ma + F_A; sb_ = sb;
    asm volatile("" : "+s"(mb_), "+s"(sb_));
    prep(n + 4);
  }
  dma_grp<4, ID == 2>(mb_, L.vb, L.qb, sb_);
  wait_vm<8>();
  barrier();
  const int lane = lane_id();
  u32x4 ln;
  const char* lp = (const char*)p.flags + FL_LN + (lw < 2 ? (((xcd & 1) * 8 + (idx >> 2)) * 128 + lw * 64 + lane) : (2048 + ((xcd >> 1) * 4 + (idx & 3)) * 128 + (lw - 2) * 64 + lane)) * 16;
  if constexpr (ID == 1) asm volatile("global_load_dwordx4 %0, %1, off sc1" : "=v"(ln) : "v"(lp) : "memory");
  if constexpr (NEXT != 0) { ld_const<NEXT>(N, ring, lw, 0); wait_vm<(ID == 1 ? 5 : 3)>(); } else wait_vm<0>();
  barrier();
  if constexpr (NEXT != 0) ld_const<NEXT>(N, ring, lw, 1);
  if constexpr (ID == 1) {
    if (TOUCH_C && lw < 2) {
      const char* tp = (const char*)((xcd & 1) ? p.cim : p.cre) + (size_t)((xcd >> 1) * 512 + idx * 16 + lw * 8 + (lane >> 3)) * (S_ * 2) + (lane & 7) * 128;
      asm volatile("global_load_dword %0, %1, off" : "+v"(touch) : "v"(tp) : "memory");
    }
  }
  if constexpr (ID == 2) {
    if (FT_W && lw < 2 * FT_W) {
      const char* tp = (const char*)((lw & 1) ? p.wu : p.wg) + (size_t)((lw >> 1) * 256 + idx * 8 + (lane >> 3)) * (F_ * 2) + xcd * 896 + ((lane & 7) < 7 ? (lane & 7) : 6) * 128;
      asm volatile("global_load_dword %0, %1, off" : "+v"(touch) : "v"(tp) : "memory");
    }
  }
  barrier();
  const unsigned tag = lds_tag(lds0);
  if constexpr (ID == 1) {
    asm volatile("s_waitcnt vmcnt(8)" : "+v"(ln));
#pragma unroll 1
    for (unsigned spins = 0; spins < (1u << 15) && !__all(ln.y == tag && ln.w == tag); ++spins) {
      __builtin_amdgcn_s_sleep(2);
      asm volatile("global_load_dwordx4 %0, %1, off sc1\n\ts_waitcnt vmcnt(0)" : "=&v"(ln) : "v"(lp) : "memory");
    }
    LAS unsigned* tb = (LAS unsigned*)(lds0 + F_TAB);
    tb[lw * 64 + lane + (lw < 2 ? 0 : 128)] = ln.x;
    if (lw < 2) tb[128 + lw * 64 + lane] = ln.z;
    wait_lgkm0();
  }
  barrier();
  barrier();
  if constexpr (ID != 1) {
    barrier();
    if (lw == 3) poll_tags((char*)p.flags + FL_PAIR + ((ID == 2 ? 0 : 256) + (blockIdx.x ^ 1)) * 128, 1, tag);
    barrier();
  }
}

template <int ID>
__device__ __forceinline__ void sq_compute(const FArgs& p, unsigned lds0, unsigned w, unsigned xcd, unsigned idx, unsigned tag) {
  constexpr int NT = nt_of<ID>();
  const unsigned ring = lds0 + F_RING;
  const unsigned g = w >> 2, wm = (w >> 1) & 1, wx = w & 1;
  const unsigned kh = xcd & 1, tm = (xcd >> 1) * 4 + (idx & 3), tn = idx >> 2;
  u32x4 res[2];
  unsigned rdA[8], rdB[4], hoff = 0;
  const unsigned rotA = (ID == 1) ? 2 * g : 2 * g + wx;
  {
    const int lane = lane_id(), fr = lane & 15, fq = lane >> 4, chunk = ((fq & 1) << 1) | (fq >> 1);
    const int row = tm * 128 + wm * 64 + rotA * 16 + fr, col = tn * 128 + kh * 32 + chunk * 8;
    if constexpr (ID == 2) {
#pragma unroll
      for (int c = 0; c < 2; ++c) res[c] = *(const u32x4*)((const u16*)p.x + (size_t)row * D_ + col + (c ^ wx) * 64);
    } else if constexpr (ID == 4) {
      hoff = ((row >> 4) * (D_ / 32) + (col >> 5)) * 1024 + (chunk * 16 + fr) * 16;
    }
    const unsigned rotB = (ID == 1) ? 0u : 2 * kh, base = ring + g * F_ST;
#pragma unroll
    for (int i = 0; i < 4; ++i) {
      const unsigned kk = i >> 1, hf = (i & 1) * 2;
      if constexpr (ID == 1) {
        rdA[i] = base + (wm * 64 + (hf ^ rotA) * 16 + fr) * 128 + (((kk * 4 + fq) ^ (fr >> 1)) << 4);
        rdB[i] = base + F_A + (wx * 64 + fr) * 128 + ((((i & 1) * 4 + fq) ^ (fr >> 1)) << 4);
      } else if constexpr (ID == 2) {
        rdA[i] = base + (wm * 64 + (i ^ rotA) * 16 + fr) * 128 + ((fq ^ (fr >> 1)) << 4);
        rdA[4 + i] = base + (wm * 64 + (i ^ rotA) * 16 + fr) * 128 + (((4 + fq) ^ (fr >> 1)) << 4);
        rdB[i] = base + F_A + wx * 8192 + (hf ^ rotB) * 2048 + lane * 16;
      } else {
        const int q = fr >> 2, pp = fr & 3, f = q | ((fq & 1) << 2);
        rdA[i] = base + wm * 8192 + (i ^ rotA) * 2048 + lane * 16;
        rdB[i] = base + F_A + (8 * fq + q) * 256 + (((wx * 4 + (i ^ rotB)) ^ f) << 5) + pp * 8;
      }
    }
  }
  constexpr int NVA = (ID == 2) ? 8 : 4, NVB = (ID <= 2) ? 2 : 4;
  bf16x8 fa[2][4], fb[2][4];
  f32x4 acc[4][4];
#pragma unroll
  for (int i = 0; i < 16; ++i) { acc[i >> 2][i & 3] = (f32x4){0.f, 0.f, 0.f, 0.f}; asm volatile("" : "+v"(acc[i >> 2][i & 3])); }
  auto reads = [&]() {
    SB();
#pragma unroll
    for (int s = 0; s < 2; ++s)
#pragma unroll
      for (int i = 0; i < 4; ++i) {
        if constexpr (ID == 1) {
          fb[s][i] = lds_b128(rdB[s] + i * 2048);
          fa[s][i] = lds_b128(rdA[s * 2 + (i >> 1)] + (i & 1) * 2048);
        } else if constexpr (ID == 2) {
          fb[s][i] = lds_b128(rdB[i >> 1] + (i & 1) * 2048 + s * 1024);
          fa[s][i] = lds_b128(rdA[s * 4 + i]);
        } else {
          fb[s][i] = lds_tr(rdB[i] + s * 8192, rdB[i] + s * 8192 + 1024);
          fa[s][i] = lds_b128(rdA[i] + s * 1024);
        }
      }
    SB();
  };
#pragma unroll
  for (int j = 0; j < NVA; ++j) asm volatile("" : "+v"(rdA[j]));
#pragma unroll
  for (int j = 0; j < NVB; ++j) asm volatile("" : "+v"(rdB[j]));
  if constexpr (ID != 1) {
    barrier();
    STAMP(ID, 0);
  }
  if constexpr (ID == 4) {
    const auto rh = OUT_RSRC(p.h, S_ * D_ * 2);
#pragma unroll
    for (int c = 0; c < 2; ++c) res[c] = __builtin_amdgcn_raw_buffer_load_b128(rh, hoff, (c ^ wx) * 2048, 16);
  }
  barrier();
  STAMP1_TAKE();
  if (g == 1) barrier();
  reads();
#pragma unroll 1
  for (unsigned i = 0; i < NT / 2; ++i) {
    const unsigned delta = (i & 1) ? (unsigned)(-2 * F_ST) : (unsigned)(2 * F_ST);
#pragma unroll
    for (int j = 0; j < NVA; ++j) { rdA[j] += delta; asm volatile("" : "+v"(rdA[j])); }
#pragma unroll
    for (int j = 0; j < NVB; ++j) { rdB[j] += delta; asm volatile("" : "+v"(rdB[j])); }
    wait_lgkm0();
    barrier();
    SB();
#pragma unroll
    for (int m = 0; m < 32; ++m) {
      const int n = (SNAKE && (m & 4)) ? 3 - (m & 3) : (m & 3);
      MFMA(fb[m >> 4][n], fa[m >> 4][(m >> 2) & 3], acc[(m >> 2) & 3][n]);
    }
    SB();
    barrier();
    if (i + 1 < NT / 2) reads();
  }
  mfma_drain();
  SB();
  const int lane = lane_id(), fr = lane & 15, fq = lane >> 4, chunk = ((fq & 1) << 1) | (fq >> 1);
  if constexpr (ID == 1) {
    const unsigned wb = ring + 2 * F_ST + (w ^ 4) * 8192 + lane * 16, rb = ring + 2 * F_ST + w * 8192 + lane * 16;
#pragma unroll
    for (int i = 0; i < 8; ++i) *(LAS f32x4*)(wb + i * 1024) = acc[2 + (i >> 2)][i & 3];
    if (g == 0) barrier();
    STAMP1_PUT(ID);
    STAMP(ID, 2);
    wait_lgkm0();
    barrier();
#pragma unroll
    for (int i = 0; i < 8; ++i) acc[i >> 2][i & 3] += *(const LAS f32x4*)(rb + i * 1024);
  }
  if constexpr (ID == 1) {
    const LAS float* tb = (const LAS float*)(lds0 + F_TAB);
    const float sg = tm < 8 ? 1.f : -1.f;
    const auto ro = OUT_RSRC(p.ab, D_ * 4096 * 2);
    f32x4 mean[4], rstd[4];
#pragma unroll
    for (int j = 0; j < 4; ++j) {
      mean[j] = *(const LAS f32x4*)(tb + wx * 64 + j * 16 + fq * 4);
      rstd[j] = *(const LAS f32x4*)(tb + 128 + wx * 64 + j * 16 + fq * 4) * sg;
    }
#pragma unroll
    for (int mb = 0; mb < 2; ++mb) {
      const float wsm = tb[256 + wm * 64 + g * 32 + mb * 16 + fr];
#pragma unroll
      for (int np = 0; np < 2; ++np) {
        const f32x4 x0 = (acc[mb][2 * np] - mean[2 * np] * wsm) * rstd[2 * np], x1 = (acc[mb][2 * np + 1] - mean[2 * np + 1] * wsm) * rstd[2 * np + 1];
        auto s0 = __builtin_amdgcn_permlane16_swap(pk_bf16(x0[0], x0[1]), pk_bf16(x1[0], x1[1]), false, false);
        auto s1 = __builtin_amdgcn_permlane16_swap(pk_bf16(x0[2], x0[3]), pk_bf16(x1[2], x1[3]), false, false);
        const int rblk = (tm & 7) * 8 + wm * 4 + g * 2 + mb;
        const int kblk = (tm >> 3) * 64 + (kh * 8 + tn) * 4 + wx * 2 + np;
        STORE16(ro, (rblk * 128 + kblk) * 1024 + (chunk * 16 + fr) * 16, ((u32x4){s0[0], s1[0], s0[1], s1[1]}));
      }
    }
    return;
  }
  const auto rs = OUT_RSRC((char*)p.xch + (ID == 2 ? 0 : XCH4), 256 * 16384);
  const unsigned sreg = ring + ((w >> 1) & 1) * F_ST + (w & 1) * 8192 + lane * 16, kreg = ring + 2 * F_ST + lane * 16;
  auto put_keep = [&]() {
#pragma unroll
    for (int j = 1; j < 4; ++j) {
      const unsigned d = kreg + ((wm * 4 + (j ^ rotA)) * 3 + (j - 1)) * 2048;
      *(LAS f32x4*)(d) = acc[j][0];
      *(LAS f32x4*)(d + 1024) = acc[j][1];
    }
  };
  if (g == 0) {
#pragma unroll
    for (int i = 0; i < 8; ++i) *(LAS f32x4*)(sreg + i * 1024) = acc[i >> 1][2 + (i & 1)];
    put_keep();
    wait_lgkm0();
    barrier();
  } else {
#pragma unroll
    for (int j = 0; j < 4; ++j) {
      const f32x4 x0 = acc[j][2] + *(const LAS f32x4*)(sreg + (j ^ 2) * 2048), x1 = acc[j][3] + *(const LAS f32x4*)(sreg + (j ^ 2) * 2048 + 1024);
      auto s0 = __builtin_amdgcn_permlane16_swap(pk_bf16(x0[0], x0[1]), pk_bf16(x1[0], x1[1]), false, false);
      auto s1 = __builtin_amdgcn_permlane16_swap(pk_bf16(x0[2], x0[3]), pk_bf16(x1[2], x1[3]), false, false);
      __builtin_amdgcn_raw_buffer_store_b128((u32x4){s0[0], s1[0], s0[1], s1[1]}, rs, lane * 16, blockIdx.x * 16384 + ((wm * 4 + (j ^ rotA)) * 2 + wx) * 1024, 16);
    }
    SB();
    put_keep();
    wait_lgkm0();
  }
  STAMP1_PUT(ID);
  STAMP(ID, 2);
  barrier();
  f32x4 y[2][2];
  {
    const unsigned kb = kreg + (wm * 4 + rotA) * 3 * 2048;
    f32x4 t[2][2];
#pragma unroll
    for (int i = 0; i < 2; ++i) {
      t[0][i] = acc[0][i] + *(const LAS f32x4*)(kb + 2048 + i * 1024);
      t[1][i] = *(const LAS f32x4*)(kb + i * 1024) + *(const LAS f32x4*)(kb + 4096 + i * 1024);
    }
#pragma unroll
    for (int c = 0; c < 2; ++c)
#pragma unroll
      for (int j = 0; j < 4; ++j) {
        auto s = __builtin_amdgcn_permlane16_swap(__float_as_uint(t[c][0][j]), __float_as_uint(t[c][1][j]), false, false);
        y[c][0][j] = __uint_as_float(s[0]) + ((j & 1) ? bf_hi(res[c][j >> 1]) : bf_lo(res[c][j >> 1]));
        y[c][1][j] = __uint_as_float(s[1]) + ((j & 1) ? bf_hi(res[c][2 + (j >> 1)]) : bf_lo(res[c][2 + (j >> 1)]));
      }
  }
  const int row0 = tm * 128 + wm * 64 + rotA * 16 + fr, col = tn * 128 + kh * 32 + chunk * 8;
  unsigned optr = (ID == 2) ? ((row0 >> 4) * (D_ / 32) + (col >> 5)) * 1024 + (chunk * 16 + fr) * 16 : (row0 * D_ + col) * 2;
  const auto ro = OUT_RSRC(ID == 2 ? p.h : p.out, S_ * D_ * 2);
  asm volatile("" : "+v"(y[0][0]), "+v"(y[0][1]), "+v"(y[1][0]), "+v"(y[1][1]), "+v"(optr));
  XT(0);
  wait_vm<0>();
  barrier();
  XT(1);
  if (w == 0 && lane == 0) put_tag((char*)p.flags + FL_PAIR + ((ID == 2 ? 0 : 256) + blockIdx.x) * 128, tag);
  barrier();
  XT(2);
  u32x4 in[2], v[2];
#pragma unroll
  for (int c = 0; c < 2; ++c) in[c] = __builtin_amdgcn_raw_buffer_load_b128(rs, lane * 16, (blockIdx.x ^ 1) * 16384 + ((wm * 4 + rotA) * 2 + (c ^ wx)) * 1024, 16);
#pragma unroll
  for (int c = 0; c < 2; ++c) {
    const f32x4 a = y[c][0] + (f32x4){bf_lo(in[c][0]), bf_hi(in[c][0]), bf_lo(in[c][1]), bf_hi(in[c][1])};
    const f32x4 b = y[c][1] + (f32x4){bf_lo(in[c][2]), bf_hi(in[c][2]), bf_lo(in[c][3]), bf_hi(in[c][3])};
    v[c] = (u32x4){pk_bf16(a[0], a[1]), pk_bf16(a[2], a[3]), pk_bf16(b[0], b[1]), pk_bf16(b[2], b[3])};
    __builtin_amdgcn_raw_buffer_store_b128(v[c], ro, optr, (c ^ wx) * (ID == 2 ? 2048 : 128), ST_AUX);
  }
  SB();
  XT(3);
  SB();
  if constexpr (ID == 2) {
    float s = 0.f;
#pragma unroll
    for (int j = 0; j < 8; ++j) s += bf_lo(v[j >> 2][j & 3]) + bf_hi(v[j >> 2][j & 3]);
    const float m = sum_rows(s) * (1.0f / 64.0f);
    float q = 0.f;
#pragma unroll
    for (int j = 0; j < 8; ++j) {
      const float a = bf_lo(v[j >> 2][j & 3]) - m, b = bf_hi(v[j >> 2][j & 3]) - m;
      q += a * a + b * b;
    }
    const f32x2 st = {m, sum_rows(q)};
    if (fq == 0)
      __hip_atomic_store((unsigned long long*)p.stats + (size_t)(tn * 2 + kh) * S_ + row0, __builtin_bit_cast(unsigned long long, st), __ATOMIC_RELAXED, __HIP_MEMORY_SCOPE_AGENT);
  }
  SB();
  XT(4);
  XPUT(ID);
}

__device__ __forceinline__ void glu_loader(const FArgs& p, const Ld& L, Ld& N, unsigned lds0, unsigned lw, unsigned xcd, unsigned idx, unsigned& touch) {
  constexpr int NT = 32, NEXT = 4;
  constexpr bool PRE = true;
  const unsigned tag = lds_tag(lds0);
  const unsigned ring = lds0 + F_RING;
  LAS float* cst = (LAS float*)(lds0 + F_TAB + 2048);
  unsigned long long* gcs = (unsigned long long*)((char*)p.flags + FL_CS) + (xcd * 4 + (idx >> 3)) * (8 * 224);
  if (PRE) {
    ld_a<3>(L, ring, lw, 0); ld_a<3>(L, ring, lw, 1);
  } else {
    ld_a<3>(L, ring, lw, 0); ld_b<3>(L, ring, lw, 0); ld_a<3>(L, ring, lw, 1); ld_b<3>(L, ring, lw, 1);
  }
  const int lane = lane_id();
  const unsigned u = lw * 64 + lane < 224 ? lw * 64 + lane : 223u;
  wait_vm<(PRE ? 4 : 7)>();
  barrier();
  ld_a<3>(L, ring, lw, 2);
  ld_b<3>(L, ring, lw, 2);
#pragma unroll 1
  for (unsigned t = 0; t + 3 < NT; ++t) {
    if (t == 25) {
      const unsigned hf = u >= 112, c = u - hf * 112, wn_ = c >= 56, cc = c - wn_ * 56, blk = cc >> 4;
      const unsigned src = lds0 + (wn_ * 4 + blk) * 768 + (blk == 3 ? 512 + (hf * 8 + (cc & 7)) * 4 : hf * 256 + (cc & 15) * 4);
      const float v = (*(const LAS float*)(src) + *(const LAS float*)(src + 64)) + (*(const LAS float*)(src + 128) + *(const LAS float*)(src + 192));
      asm volatile("global_store_dwordx2 %0, %1, off sc1" ::"v"(gcs + L.cls * 224 + u), "v"(((unsigned long long)tag << 32) | __float_as_uint(v)) : "memory");
    }
    ld_a<3>(L, ring, lw, t + 3);
    barrier();
    ld_b<3>(L, ring, lw, t + 3);
    wait_vm<14>();
    barrier();
  }
  unsigned long long g[8];
#pragma unroll
  for (int i = 0; i < 8; ++i) asm volatile("global_load_dwordx2 %0, %1, off sc1" : "=v"(g[i]) : "v"(gcs + i * 224 + u) : "memory");
  barrier();
  if constexpr (NEXT != 0) { ld_b<NEXT>(N, ring, lw, 0); wait_vm<19>(); } else wait_vm<15>();
  barrier();
  barrier();
  if constexpr (NEXT != 0) { ld_b<NEXT>(N, ring, lw, 1); wait_vm<16>(); } else wait_vm<8>();
  barrier();
  barrier();
  if (!EARLY0) barrier();
  if constexpr (NEXT != 0) asm volatile("s_waitcnt vmcnt(8)" : "+v"(g[0]), "+v"(g[1]), "+v"(g[2]), "+v"(g[3]), "+v"(g[4]), "+v"(g[5]), "+v"(g[6]), "+v"(g[7]));
  else asm volatile("s_waitcnt vmcnt(0)" : "+v"(g[0]), "+v"(g[1]), "+v"(g[2]), "+v"(g[3]), "+v"(g[4]), "+v"(g[5]), "+v"(g[6]), "+v"(g[7]));
#pragma unroll 1
  for (unsigned spins = 0; spins < (1u << 15); ++spins) {
    unsigned tg = 0u;
#pragma unroll
    for (int i = 0; i < 8; ++i) tg |= (unsigned)(g[i] >> 32) ^ tag;
    if (__all(tg == 0u)) break;
    __builtin_amdgcn_s_sleep(2);
#pragma unroll
    for (int i = 0; i < 8; ++i) asm volatile("global_load_dwordx2 %0, %1, off sc1\n\ts_waitcnt vmcnt(0)" : "=&v"(g[i]) : "v"(gcs + i * 224 + u) : "memory");
  }
  float s = 0.f;
#pragma unroll
  for (int i = 0; i < 8; ++i) s += __uint_as_float((unsigned)g[i]);
  cst[u] = s;
  wait_lgkm0();
  if (NEXT != 0 && FT_D == 1) {
    const char* tp = (const char*)p.wd + ((size_t)blockIdx.x * 224 + (lw * 56 + (lane < 56 ? lane : 55))) * 128;
    asm volatile("global_load_dword %0, %1, off" : "+v"(touch) : "v"(tp) : "memory");
  }
  if (NEXT != 0 && FT_D == 2) {
    const char* tp = (const char*)p.wd + (size_t)((xcd & 1) * 1792 + idx * 16 + lw * 4 + (lane >> 4)) * (D_ * 2) + (lane & 15) * 128;
    asm volatile("global_load_dword %0, %1, off" : "+v"(touch) : "v"(tp) : "memory");
  }
  if (EARLY0) barrier();
  barrier();
}

__device__ __forceinline__ void glu_compute(const FArgs& p, unsigned lds0, unsigned w, unsigned xcd, unsigned idx) {
  constexpr int ID = 3, BM = 256, NT = 32, MB = 4, NB = 7;
  const unsigned ring = lds0 + F_RING;
  LAS f32x2* tab = (LAS f32x2*)(lds0 + F_TAB);
  LAS float* cst = (LAS float*)(lds0 + F_TAB + 2048);
  const unsigned tm = idx & 7, tn = xcd * 4 + (idx >> 3), cls = (tm + 2 * (idx >> 3)) & 7;
  const unsigned wm = w & 3, wn = w >> 2;
  const int lane = lane_id(), fr = lane & 15, fq = lane >> 4;
  const unsigned rot = wm == 3 ? 0u : wm;
  unsigned xa = ring + wm * 4096 + lane * 16, xb[3], xb1;
  {
    const int q = fr >> 2, pp = fr & 3, r = (fq >> 1) * 16 + (fq & 1) * 4 + q;
#pragma unroll
    for (int i = 0; i < 3; ++i) xb[i] = ring + F_A + r * 224 + wn * 112 + pp * 8 + ((i + rot) % 3) * 32;
    xb1 = ring + F_A + r * 224 + wn * 112 + 96 + (pp & 1) * 8 + (pp >> 1) * G3_UP;
  }
  float cs[3] = {0.f, 0.f, 0.f};
  bf16x8 fa[MB], fb[NB];
  f32x4 acc[MB][NB];
#pragma unroll
  for (int mb = 0; mb < MB; ++mb)
#pragma unroll
    for (int nb = 0; nb < NB; ++nb) { acc[mb][nb] = (f32x4){0.f, 0.f, 0.f, 0.f}; asm volatile("" : "+v"(acc[mb][nb])); }
  asm volatile("" : "+v"(xa), "+v"(xb[0]), "+v"(xb[1]), "+v"(xb[2]), "+v"(xb1));
  barrier();
  STAMP(ID, 0);
  if (wn == 1) {
    const auto rst = OUT_RSRC(p.stats, 8 * S_ * 16);
    if (ST_SLEEP) __builtin_amdgcn_s_sleep(ST_SLEEP);
    SB();
    f32x2 sv[16];
#pragma unroll
    for (int i = 0; i < 16; ++i) sv[i] = __builtin_bit_cast(f32x2, __builtin_amdgcn_raw_buffer_load_b64(rst, (tm * BM + wm * 64 + lane) * 8, i * S_ * 8, 16));
    SB();
    barrier();
    SB();
    float m = 0.f;
#pragma unroll
    for (int i = 0; i < 16; ++i) m += sv[i][0];
    m *= (1.0f / 16.0f);
    float q = 0.f;
#pragma unroll
    for (int i = 0; i < 16; ++i) {
      const float d = sv[i][0] - m;
      q += sv[i][1] + 64.0f * d * d;
    }
    tab[wm * 64 + lane] = (f32x2){m, __builtin_amdgcn_rsqf(q * (1.0f / D_) + EPS)};
  }
  wait_lgkm0();
  barrier();
  STAMP1_TAKE();
  const unsigned csmask = (0x010101u << cls) | (1u << ((cls + 1) & 7));
  auto reads = [&]() {
    SB();
    fa[0] = lds_b128(xa);
#pragma unroll
    for (int j = 0; j < 7; ++j) {
      const unsigned o = j < 6 ? xb[j % 3] + (j / 3) * G3_UP : xb1;
      fb[j] = lds_tr(o, o + 1792);
    }
#pragma unroll
    for (int mb = 1; mb < MB; ++mb) fa[mb] = lds_b128(xa + mb * 1024);
    SB();
  };
  reads();
#pragma unroll 1
  for (unsigned t = 0; t < NT; ++t) {
    const unsigned delta = ((t & 3) == 3) ? (unsigned)(-3 * F_ST) : (unsigned)F_ST;
    xa += delta; xb[0] += delta; xb[1] += delta; xb[2] += delta; xb1 += delta;
    asm volatile("" : "+v"(xa), "+v"(xb[0]), "+v"(xb[1]), "+v"(xb[2]), "+v"(xb1));
    if (__builtin_expect((csmask >> t) & 1u, 0)) {
#pragma unroll
      for (int j = 0; j < 4; ++j)
#pragma unroll
        for (int i = 0; i < 3; ++i)
          asm volatile("v_dot2c_f32_bf16 %0, %1, %2" : "+v"(cs[i]) : "s"(0x3f803f80u), "v"(__builtin_bit_cast(u32x4, fb[3 * i])[j]));
      asm volatile("s_mov_b32 m0, %3\n\ts_nop 2\n\tds_write_addtid_b32 %0\n\tds_write_addtid_b32 %1 offset:256\n\tds_write_addtid_b32 %2 offset:512" ::"v"(cs[0]),
                   "v"(cs[1]), "v"(cs[2]), "s"(lds0 + w * 768)
                   : "memory");
    }
    wait_lgkm0();
    barrier();
    SB();
#pragma unroll
    for (int mb = 0; mb < MB; ++mb)
#pragma unroll
      for (int i = 0; i < NB; ++i) {
        const int nb = (SNAKE && (mb & 1)) ? NB - 1 - i : i;
        MFMA(fb[nb], fa[mb], acc[mb][nb]);
      }
    SB();
    barrier();
    reads();
  }
  if (!EARLY0 && wn == 0) barrier();
  mfma_drain();
  SB();
  const int lane2 = lane_id();
  const int gr = lane2 & 15, gq = lane2 >> 4;
  STAMP1_PUT(ID);
  STAMP(ID, 2);
  auto act = [](float g, float u) { return g * u * __builtin_amdgcn_rcpf(1.0f + __expf(-g)); };
  const auto ro = OUT_RSRC(p.gu, S_ * F_ * 2);
  f32x2 st[MB];
#pragma unroll
  for (int mb = 0; mb < MB; ++mb) st[mb] = tab[wm * 64 + mb * 16 + gr];
  const int gch0 = tn * 14 + wn * 7;
#pragma unroll
  for (int i = 0; i < 3; ++i) {
    if (EARLY0 && i == 1 && wn == 0) barrier();
    const unsigned ib = (i + rot) % 3;
    const f32x4 cg = *(const LAS f32x4*)(cst + wn * 56 + ib * 16 + gq * 4), cu = *(const LAS f32x4*)(cst + 112 + wn * 56 + ib * 16 + gq * 4);
    u32x2 pkd[MB];
#pragma unroll
    for (int mb = 0; mb < MB; ++mb) {
      const f32x4 g = (acc[mb][i] - cg * st[mb].x) * st[mb].y, u = (acc[mb][3 + i] - cu * st[mb].x) * st[mb].y;
      pkd[mb] = (u32x2){pk_bf16(act(g[0], u[0]), act(g[1], u[1])), pk_bf16(act(g[2], u[2]), act(g[3], u[3]))};
    }
#pragma unroll
    for (int mp = 0; mp < MB / 2; ++mp) {
      auto s0 = __builtin_amdgcn_permlane16_swap(pkd[2 * mp].x, pkd[2 * mp + 1].x, false, false);
      auto s1 = __builtin_amdgcn_permlane16_swap(pkd[2 * mp].y, pkd[2 * mp + 1].y, false, false);
      const u32x4 v = {s0[0], s1[0], s0[1], s1[1]};
      const int rblk = tm * (BM / 16) + wm * MB + 2 * mp + (gq & 1);
      const int gch = gch0 + 2 * ib + (gq >> 1);
      STORE16(ro, (rblk * (F_ / 32) + (gch >> 2)) * 1024 + ((gch & 3) * 16 + gr) * 16, v);
    }
  }
  {
    const f32x4 cm = *(const LAS f32x4*)(cst + (gq >> 1) * 112 + wn * 56 + 48 + (gq & 1) * 4);
    u32x2 pkd[MB / 2];
#pragma unroll
    for (int mp = 0; mp < MB / 2; ++mp) {
      const f32x4 v0 = (acc[2 * mp][6] - cm * st[2 * mp].x) * st[2 * mp].y, v1 = (acc[2 * mp + 1][6] - cm * st[2 * mp + 1].x) * st[2 * mp + 1].y;
      float r[4];
#pragma unroll
      for (int j = 0; j < 4; ++j) {
        auto s = __builtin_amdgcn_permlane32_swap(__float_as_uint(v0[j]), __float_as_uint(v1[j]), false, false);
        r[j] = act(__uint_as_float(s[0]), __uint_as_float(s[1]));
      }
      pkd[mp] = (u32x2){pk_bf16(r[0], r[1]), pk_bf16(r[2], r[3])};
    }
    auto s0 = __builtin_amdgcn_permlane16_swap(pkd[0].x, pkd[1].x, false, false);
    auto s1 = __builtin_amdgcn_permlane16_swap(pkd[0].y, pkd[1].y, false, false);
    const u32x4 v = {s0[0], s1[0], s0[1], s1[1]};
    const int rblk = tm * (BM / 16) + wm * MB + (gq & 1) * 2 + (gq >> 1);
    const int gch = gch0 + 6;
    STORE16(ro, (rblk * (F_ / 32) + (gch >> 2)) * 1024 + ((gch & 3) * 16 + gr) * 16, v);
  }
}

__global__ __launch_bounds__(768, 3) void k_block(FArgs p) {
  CODE_LABEL(fz_code_f);
  extern __shared__ __attribute__((aligned(16))) char shm[];
  const unsigned lds0 = (unsigned)(size_t)(LAS char*)shm;
  const unsigned w = rfl(threadIdx.x >> 6);
  const unsigned xcd = blockIdx.x & 7, idx = blockIdx.x >> 3;
  char* fl = (char*)p.flags;
  const unsigned tm = (xcd >> 1) * 4 + (idx & 3), kh = xcd & 1, lj = idx >> 2;
  if (w >= 8) {
    const unsigned lw = w - 8;
    Ld LA, LB;
    unsigned touch = 0;
    LA = ld_setup<1>(p, lw, xcd, idx);
    sq_loader<1>(p, LA, LB, lds0, lw, xcd, idx, touch);
    const unsigned tag = lds_tag(lds0);
    LA = ld_setup<3>(p, lw, xcd, idx);
    ld_pin(LA);
    barrier();
    if (lw == 3) poll_tags(fl + FL_P1 + ((kh * 8 + lj) * 4 + (xcd >> 1)) * 128, 16, tag);
    barrier();
    sq_loader<2>(p, LB, LA, lds0, lw, xcd, idx, touch);
    LB = ld_setup<4>(p, lw, xcd, idx);
    ld_pin(LB);
    barrier();
    if (lw == 3) poll_tags(fl + FL_P2 + ((idx & 7) * 8 + xcd) * 128, 32, tag);
    barrier();
    glu_loader(p, LA, LB, lds0, lw, xcd, idx, touch);
    barrier();
    if (lw == 3) poll_tags(fl + FL_P3 + ((tm >> 1) * 2 + kh) * 128, 16, tag);
    barrier();
    sq_loader<4>(p, LB, LA, lds0, lw, xcd, idx, touch);
    asm volatile("" ::"v"(touch));
    return;
  }
  STAMP(1, 0);
  unsigned csink = 0;
  if (CODE_PF && w == 0 && idx < 3) {
    unsigned long long a;
    CODE_ADDR(a, fz_code_f);
    touch_code(a + idx * 8192, lane_id(), idx == 2 ? CODE_LAST : 63, csink);
  }
  unsigned tag;
  {
    const int lane = lane_id();
    const unsigned r0 = blockIdx.x * 8 + w;
    const u16* xp = (const u16*)p.x + (size_t)r0 * D_ + lane * 8;
    const u16* wp = (const u16*)(r0 < 1024 ? p.dre : p.dim) + (size_t)(r0 & 1023) * D_ + lane * 8;
    u32x4 xv[2], wv[2];
#pragma unroll
    for (int i = 0; i < 2; ++i) xv[i] = (LN_NT & 1) ? __builtin_nontemporal_load((const u32x4*)(xp + i * 512)) : *(const u32x4*)(xp + i * 512);
#pragma unroll
    for (int i = 0; i < 2; ++i) wv[i] = (LN_NT & 2) ? __builtin_nontemporal_load((const u32x4*)(wp + i * 512)) : *(const u32x4*)(wp + i * 512);
    const unsigned tv = __hip_atomic_load((const unsigned*)(fl + FL_EPOCH), __ATOMIC_RELAXED, __HIP_MEMORY_SCOPE_AGENT) + 1u;
    SB();
    float s = 0.f, t = 0.f;
#pragma unroll
    for (int j = 0; j < 8; ++j) s += bf_lo(xv[j >> 2][j & 3]) + bf_hi(xv[j >> 2][j & 3]);
    const float m = wave_sum(s) * (1.0f / D_);
    float q = 0.f;
#pragma unroll
    for (int j = 0; j < 8; ++j) {
      const float a = bf_lo(xv[j >> 2][j & 3]) - m, b = bf_hi(xv[j >> 2][j & 3]) - m;
      q += a * a + b * b;
      t += bf_lo(wv[j >> 2][j & 3]) + bf_hi(wv[j >> 2][j & 3]);
    }
    const float rs = __builtin_amdgcn_rsqf(wave_sum(q) * (1.0f / D_) + EPS);
    t = wave_sum(t);
    const float val = (lane & 2) ? t : ((lane & 1) ? rs : m);
    if (lane < 4)
      __hip_atomic_store((unsigned long long*)(fl + FL_LN + ((lane >> 1) * 2048 + r0) * 16 + (lane & 1) * 8), ((unsigned long long)tv << 32) | __float_as_uint(val),
                         __ATOMIC_RELAXED, __HIP_MEMORY_SCOPE_AGENT);
    tag = rfl(tv);
    if (w == 0) { *(volatile LAS unsigned*)(lds0 + F_TAG) = tag; wait_lgkm0(); }
  }
  asm volatile("" ::"v"(csink));
  STAMPX(1, 5);
  sq_compute<1>(p, lds0, w, xcd, idx, tag);
  wait_vm<0>();
  barrier();
  if (w == 0) { const int l = lane_id(); if (l < 4) put_tag(fl + FL_P1 + (tm * 4 + l) * 128 + (kh * 8 + lj) * 4, tag); }
  STAMP(1, 3);
  sq_compute<2>(p, lds0, w, xcd, idx, tag);
  wait_vm<0>();
  barrier();
  if (w == 0) { const int l = lane_id(); if (l < 8) put_tag(fl + FL_P2 + ((tm >> 1) * 8 + l) * 128 + (((tm & 1) * 2 + kh) * 8 + lj) * 4, tag); }
  STAMP(2, 3);
  glu_compute(p, lds0, w, xcd, idx);
  wait_vm<0>();
  barrier();
  if (w == 0 && lane_id() == 0) put_tag(fl + FL_P3 + ((idx & 7) * 2 + (xcd >> 2)) * 128 + ((xcd & 3) * 4 + (idx >> 3)) * 4, tag);
  STAMP(3, 3);
  sq_compute<4>(p, lds0, w, xcd, idx, tag);
  if (blockIdx.x == 0 && w == 0 && lane_id() == 0) put_tag(fl + FL_EPOCH, tag);
  STAMP_END(4);
}
}

extern "C" void kernel_launch(void* const* d_in, const int* in_sizes, int n_in, void* d_out, int out_size, void* d_ws, size_t ws_size,
                              hipStream_t stream) {
  char* ws = (char*)d_ws;
  FArgs f = {d_in[3], d_in[4], d_in[1], d_in[2], d_in[0], d_in[6], d_in[7], d_in[8], ws + WS_AB, ws + WS_H, ws + WS_ST, ws + WS_GU, d_out, ws + WS_XC, ws + WS_FL};
  (void)hipFuncSetAttribute((const void*)k_block, hipFuncAttributeMaxDynamicSharedMemorySize, F_LDS);
  hipLaunchKernelGGL(k_block, dim3(256), dim3(768), F_LDS, stream, f);
}
```

```cpp
#include <hip/hip_runtime.h>
#include <hip/hip_bf16.h>

namespace {
#define LAS __attribute__((address_space(3)))
typedef unsigned short u16;
typedef short s16x4 __attribute__((ext_vector_type(4)));
typedef short s16x8 __attribute__((ext_vector_type(8)));
typedef __bf16 bf16x8 __attribute__((ext_vector_type(8)));
typedef __bf16 bf16x2 __attribute__((ext_vector_type(2)));
typedef float f32x4 __attribute__((ext_vector_type(4)));
typedef float f32x2 __attribute__((ext_vector_type(2)));
typedef unsigned u32x4 __attribute__((ext_vector_type(4)));
typedef unsigned u32x2 __attribute__((ext_vector_type(2)));

constexpr int S_ = 2048, D_ = 1024, F_ = 3584;
constexpr float EPS = 1e-5f;

constexpr size_t WS_AB = 0;
constexpr size_t WS_H = WS_AB + (size_t)D_ * 2 * S_ * 2;
constexpr size_t WS_GU = WS_H + (size_t)S_ * D_ * 2;
constexpr size_t WS_ST = WS_GU + (size_t)S_ * F_ * 2;
constexpr size_t WS_XC = WS_ST + (size_t)S_ * 16 * 8;
constexpr size_t WS_FL = WS_XC + (size_t)2 * 256 * 16384;
constexpr int FL_EPOCH = 0;
constexpr int FL_PAIR = 128;
constexpr int FL_P1 = FL_PAIR + 512 * 128;
constexpr int FL_P2 = FL_P1 + 64 * 128;
constexpr int FL_P3 = FL_P2 + 64 * 128;
constexpr int FL_LN = FL_P3 + 16 * 128;
constexpr int FL_CS = FL_LN + 4096 * 16;

__device__ __forceinline__ unsigned rfl(unsigned v) { return __builtin_amdgcn_readfirstlane(v); }
__device__ __forceinline__ unsigned pk_bf16(float lo, float hi) {
  bf16x2 c = {(__bf16)lo, (__bf16)hi};
  return __builtin_bit_cast(unsigned, c);
}
__device__ __forceinline__ float bf_lo(unsigned v) { return __uint_as_float(v << 16); }
__device__ __forceinline__ float bf_hi(unsigned v) { return __uint_as_float(v & 0xffff0000u); }

__device__ __forceinline__ u32x4 make_rsrc(const void* p, unsigned bytes) {
  unsigned long long a = (unsigned long long)p;
  u32x4 r;
  r.x = (unsigned)a;
  r.y = (unsigned)(a >> 32) & 0xffffu;
  r.z = bytes;
  r.w = 0x00020000u;
  return r;
}
template <int N>
__device__ __forceinline__ void wait_vm() {
  asm volatile("s_waitcnt vmcnt(%0)" ::"i"(N) : "memory");
}
__device__ __forceinline__ void wait_lgkm0() { __builtin_amdgcn_s_waitcnt(0xC07F); }
__device__ __forceinline__ void barrier() {
  asm volatile("" ::: "memory");
  __builtin_amdgcn_s_barrier();
  asm volatile("" ::: "memory");
}
__device__ __forceinline__ bf16x8 lds_b128(unsigned addr) { return *(const LAS bf16x8*)(addr); }
__device__ __forceinline__ bf16x8 lds_tr(unsigned a0, unsigned a1) {
  s16x4 lo = __builtin_amdgcn_ds_read_tr16_b64_v4i16((LAS s16x4*)(a0));
  s16x4 hi = __builtin_amdgcn_ds_read_tr16_b64_v4i16((LAS s16x4*)(a1));
  s16x8 f = __builtin_shufflevector(lo, hi, 0, 1, 2, 3, 4, 5, 6, 7);
  return __builtin_bit_cast(bf16x8, f);
}

#define ST_AUX 16
#define OUT_RSRC(ptr, bytes) __builtin_amdgcn_make_buffer_rsrc((void*)(ptr), 0, (bytes), 0x00020000)
#define STORE16(rs, byteoff, v) __builtin_amdgcn_raw_buffer_store_b128(v, rs, byteoff, 0, ST_AUX)

#define DMA4(M) asm volatile("s_mov_b32 m0, %0\n\ts_nop 4\n\tbuffer_load_dwordx4 %1, %5, %6 offen " M " lds\n\tbuffer_load_dwordx4 %2, %5, %6 offen offset:1024 " M " lds\n\t" \
                 "buffer_load_dwordx4 %3, %5, %6 offen offset:2048 " M " lds\n\tbuffer_load_dwordx4 %4, %5, %6 offen offset:3072 " M " lds" ::"s"(ldsaddr), \
                 "v"(v[0]), "v"(v[1]), "v"(v[2]), "v"(v[3]), "s"(rsrc), "s"(soff) : "memory")
#define DMA3(M) asm volatile("s_mov_b32 m0, %0\n\ts_nop 4\n\tbuffer_load_dwordx4 %1, %4, %5 offen " M " lds\n\tbuffer_load_dwordx4 %2, %4, %5 offen offset:1024 " M " lds\n\t" \
                 "buffer_load_dwordx4 %3, %4, %5 offen offset:2048 " M " lds" ::"s"(ldsaddr), "v"(v[0]), "v"(v[1]), "v"(v[2]), "s"(rsrc), "s"(soff) : "memory")
#define DMAB(M) asm volatile("s_mov_b32 m0, %0\n\ts_waitcnt vmcnt(8)\n\ts_barrier\n\tbuffer_load_dwordx4 %1, %5, %6 offen " M " lds\n\tbuffer_load_dwordx4 %2, %5, %6 offen offset:1024 " M " lds\n\t" \
                 "buffer_load_dwordx4 %3, %5, %6 offen offset:2048 " M " lds\n\tbuffer_load_dwordx4 %4, %5, %6 offen offset:3072 " M " lds" ::"s"(ldsaddr), \
                 "v"(v[0]), "v"(v[1]), "v"(v[2]), "v"(v[3]), "s"(rsrc), "s"(soff) : "memory")
template <int N, int MOD = 0>
__device__ __forceinline__ void dma_grp(unsigned ldsaddr, const unsigned (&v)[4], u32x4 rsrc, unsigned soff) {
  if constexpr (N == 4) { if constexpr (MOD == 1) DMA4("sc1"); else DMA4(""); }
  else if constexpr (N == 3) { if constexpr (MOD == 1) DMA3("sc1"); else DMA3(""); }
  else { if constexpr (MOD == 1) DMAB("sc1"); else DMAB(""); }
}
__device__ __forceinline__ float sum_rows(float x) {
  auto a = __builtin_amdgcn_permlane16_swap(__float_as_uint(x), __float_as_uint(x), false, false);
  x = __uint_as_float(a[0]) + __uint_as_float(a[1]);
  auto b = __builtin_amdgcn_permlane32_swap(__float_as_uint(x), __float_as_uint(x), false, false);
  return __uint_as_float(b[0]) + __uint_as_float(b[1]);
}
template <int CTRL> __device__ __forceinline__ float dpp_add(float x) { return x + __uint_as_float(__builtin_amdgcn_mov_dpp(__float_as_uint(x), CTRL, 0xF, 0xF, true)); }
__device__ __forceinline__ float wave_sum(float x) { return sum_rows(dpp_add<0x140>(dpp_add<0x141>(dpp_add<0x4E>(dpp_add<0xB1>(x))))); }
__device__ __forceinline__ int lane_id() {
  int l;
  asm volatile("v_mbcnt_lo_u32_b32 %0, -1, 0\n\tv_mbcnt_hi_u32_b32 %0, -1, %0" : "=v"(l));
  return l;
}
__device__ __forceinline__ void mfma_drain() { asm volatile("s_nop 15" ::: "memory"); }
template <int NC, int NT>
__device__ __forceinline__ unsigned stage_of(unsigned n, unsigned c) {
  const unsigned t = ((n & (NC - 1)) == c) ? n + NC : n;
  return rfl(t >= NT ? t - NT : t);
}
#define SB() __builtin_amdgcn_sched_barrier(0)
#define MFMA(b, a, c) asm volatile("v_mfma_f32_16x16x32_bf16 %0, %1, %2, %0" : "+v"(c) : "v"(b), "v"(a))

struct FArgs {
  const void *dre, *dim, *cre, *cim, *x, *wg, *wu, *wd;
  void *ab, *h, *stats, *gu, *out, *xch, *flags;
};
constexpr unsigned CODE_LINES = 156;
constexpr int F_RING = 6144, F_ST = 32768, F_TAB = F_RING + 4 * F_ST, F_TAG = F_TAB + 4096, F_LDS = F_TAB + 4160, F_A = 16384, G3_UP = 7168 + 16;

__device__ __forceinline__ void poll_tags(const void* line, int n, unsigned tag) {
  const int l = lane_id();
  const unsigned* q = (const unsigned*)line + (l < n ? l : 0);
  unsigned spins = 0;
  while (!__all(__hip_atomic_load(q, __ATOMIC_RELAXED, __HIP_MEMORY_SCOPE_AGENT) == tag) && ++spins < (1u << 17)) __builtin_amdgcn_s_sleep(1);
}
__device__ __forceinline__ void put_tag(void* word, unsigned tag) { __hip_atomic_store((unsigned*)word, tag, __ATOMIC_RELAXED, __HIP_MEMORY_SCOPE_AGENT); }
__device__ __forceinline__ unsigned lds_tag(unsigned lds0) { return rfl(*(volatile LAS unsigned*)(lds0 + F_TAG)); }
template <int ID> constexpr int nt_of() { return ID == 1 ? 16 : (ID == 4 ? 28 : 32); }
template <int ID> constexpr int nc_of() { return (ID == 1 || ID == 4) ? 4 : 8; }
template <int ID> constexpr int mod_a() { return ID > 2; }
template <int ID> constexpr int mod_b() { return ID == 2; }
struct Ld {
  u32x4 qa, qb;
  unsigned va[4], vb[4], cls;
};
template <int ID>
__device__ __forceinline__ Ld ld_setup(const FArgs& p, unsigned lw, unsigned xcd, unsigned idx) {
  const int lane = lane_id();
  Ld L;
  if constexpr (ID == 3) {
    const unsigned tm = idx & 7, tn = xcd * 4 + (idx >> 3);
    L.qa = make_rsrc((const u16*)p.h + (size_t)tm * 256 * D_, 256 * D_ * 2);
    L.qb = make_rsrc(lw < 2 ? p.wg : p.wu, (unsigned)D_ * F_ * 2);
#pragma unroll
    for (int i = 0; i < 4; ++i) {
      L.va[i] = (lw * 4 + i) * (D_ / 32) * 1024 + lane * 16 - i * 1024;
      const int c = ((lw & 1) * 4 + i) * 64 + lane, r = c / 14, pos = c % 14;
      const int k = (r & 0x13) | ((r & 4) << 1) | ((r & 8) >> 1);
      L.vb[i] = k * F_ * 2 + tn * 224 + pos * 16 - i * 1024;
    }
    L.cls = (tm + 2 * (idx >> 3)) & 7;
  } else {
    const unsigned li = idx & 3, lj = idx >> 2, kh = xcd & 1, tm = (xcd >> 1) * 4 + li, tn = lj;
    if constexpr (ID == 1) {
      L.qa = make_rsrc((const u16*)(tm < 8 ? p.dre : p.dim) + (size_t)(tm & 7) * 128 * D_, 128 * D_ * 2);
      L.qb = make_rsrc((const u16*)p.x + (size_t)(kh * 8 + lj) * 128 * D_, 128 * D_ * 2);
    } else if constexpr (ID == 2) {
      L.qa = make_rsrc((const u16*)(kh ? p.cim : p.cre) + (size_t)tm * 128 * S_, 128 * S_ * 2);
      L.qb = make_rsrc((const u16*)p.ab + (size_t)tn * 128 * 4096 + kh * 64 * 512, 128 * 4096 * 2 - kh * 64 * 1024);
    } else {
      L.qa = make_rsrc((const u16*)p.gu + (size_t)tm * 128 * F_ + kh * 56 * 512, 128 * F_ * 2 - kh * 56 * 1024);
      L.qb = make_rsrc((const u16*)p.wd + (size_t)kh * 1792 * D_, 1792 * D_ * 2);
    }
#pragma unroll
    for (int i = 0; i < 4; ++i) {
      const int pc = lw * 4 + i;
      if constexpr (ID == 1) {
        const int row = pc * 8 + (lane >> 3);
        L.va[i] = L.vb[i] = row * D_ * 2 + (((lane & 7) ^ ((row >> 1) & 7)) << 4) - i * 1024;
      } else if constexpr (ID == 2) {
        const int row = pc * 8 + (lane >> 3);
        L.va[i] = row * S_ * 2 + (((lane & 7) ^ ((row >> 1) & 7)) << 4) - i * 1024;
        L.vb[i] = ((pc >> 1) * 128 + (pc & 1)) * 1024 + lane * 16 - i * 1024;
      } else {
        const int k = pc * 4 + (lane >> 4), f = (k & 3) | (((k >> 3) & 1) << 2);
        L.va[i] = ((pc >> 1) * (F_ / 32) + (pc & 1)) * 1024 + lane * 16 - i * 1024;
        L.vb[i] = k * D_ * 2 + tn * 256 + (((lane & 15) ^ (f << 1)) << 4) - i * 1024;
      }
    }
    L.cls = (nc_of<ID>() == 8) ? ((lj + 2 * li) & 7) : ((lj + li) & 3);
  }
  return L;
}
__device__ __forceinline__ void ld_pin(Ld& L) {
  asm volatile("" : "+v"(L.va[0]), "+v"(L.va[1]), "+v"(L.va[2]), "+v"(L.va[3]), "+v"(L.vb[0]), "+v"(L.vb[1]), "+v"(L.vb[2]), "+v"(L.vb[3]));
}
template <int ID>
__device__ __forceinline__ void ld_a(const Ld& L, unsigned ring, unsigned lw, unsigned n) {
  const unsigned st = stage_of<nc_of<ID>(), nt_of<ID>()>(n, L.cls);
  dma_grp<4, mod_a<ID>()>(ring + (n & 3) * F_ST + lw * 4096, L.va, L.qa, st * (ID <= 2 ? 128 : (ID == 3 ? 1024 : 2048)));
}
template <int ID>
__device__ __forceinline__ void ld_b(const Ld& L, unsigned ring, unsigned lw, unsigned n) {
  const unsigned st = stage_of<nc_of<ID>(), nt_of<ID>()>(n, L.cls);
  const unsigned dst = ring + (n & 3) * F_ST + F_A;
  if constexpr (ID == 3) {
    const unsigned d = dst + (lw >> 1) * G3_UP + (lw & 1) * 4096;
    if (lw & 1) dma_grp<3, mod_b<ID>()>(d, L.vb, L.qb, st * (32 * F_ * 2));
    else dma_grp<4, mod_b<ID>()>(d, L.vb, L.qb, st * (32 * F_ * 2));
  } else {
    dma_grp<4, mod_b<ID>()>(dst + lw * 4096, L.vb, L.qb, st * (ID == 1 ? 128 : (ID == 2 ? 2048 : 64 * D_ * 2)));
  }
}

template <int ID> __device__ __forceinline__ void ld_const(const Ld& L, unsigned ring, unsigned lw, unsigned n) { if constexpr (ID == 2) ld_a<ID>(L, ring, lw, n); else ld_b<ID>(L, ring, lw, n); }
template <int ID> __device__ __forceinline__ void ld_dep(const Ld& L, unsigned ring, unsigned lw, unsigned n) { if constexpr (ID == 2) ld_b<ID>(L, ring, lw, n); else ld_a<ID>(L, ring, lw, n); }

template <int ID>
__device__ __forceinline__ void sq_loader(const FArgs& p, const Ld& L, Ld& N, unsigned lds0, unsigned lw, unsigned xcd, unsigned idx, unsigned& touch) {
  constexpr int NT = nt_of<ID>(), NEXT = ID == 4 ? 0 : ID + 1;
  const unsigned ring = lds0 + F_RING;
  if constexpr (ID == 1) {
    ld_a<ID>(L, ring, lw, 0); ld_b<ID>(L, ring, lw, 0);
    N = ld_setup<NEXT>(p, lw, xcd, idx); ld_pin(N);
    ld_a<ID>(L, ring, lw, 1); ld_b<ID>(L, ring, lw, 1);
  } else {
    ld_dep<ID>(L, ring, lw, 0); ld_dep<ID>(L, ring, lw, 1);
  }
  constexpr unsigned KA = ID <= 2 ? 128 : 2048, KB = ID == 1 ? 128 : (ID == 2 ? 2048 : 64 * D_ * 2);
  unsigned sa, sb, ma;
  auto prep = [&](unsigned n) {
    const unsigned st = stage_of<nc_of<ID>(), NT>(n, L.cls);
    sa = st * KA; sb = st * KB; ma = ring + (n & 3) * F_ST + lw * 4096;
    asm volatile("" : "+s"(sa), "+s"(sb), "+s"(ma));
  };
  auto bar_a = [&]() {
    dma_grp<5, mod_a<ID>()>(ma, L.va, L.qa, sa);
  };
  prep(2);
  wait_vm<(ID == 1 ? 8 : 4)>();
  bar_a();
  unsigned mb_ = ma + F_A, sb_ = sb;
  prep(3);
#pragma unroll 1
  for (unsigned n = 0; n + 3 < NT; ++n) {
    dma_grp<4, mod_b<ID>()>(mb_, L.vb, L.qb, sb_);
    bar_a();
    mb_ = ma + F_A; sb_ = sb;
    asm volatile("" : "+s"(mb_), "+s"(sb_));
    prep(n + 4);
  }
  dma_grp<4, mod_b<ID>()>(mb_, L.vb, L.qb, sb_);
  wait_vm<8>();
  barrier();
  const int lane = lane_id();
  u32x4 ln;
  const char* lp = (const char*)p.flags + FL_LN + (lw < 2 ? (((xcd & 1) * 8 + (idx >> 2)) * 128 + lw * 64 + lane) : (2048 + ((xcd >> 1) * 4 + (idx & 3)) * 128 + (lw - 2) * 64 + lane)) * 16;
  if constexpr (ID == 1) asm volatile("global_load_dwordx4 %0, %1, off sc1" : "=v"(ln) : "v"(lp) : "memory");
  if constexpr (NEXT != 0) { ld_const<NEXT>(N, ring, lw, 0); wait_vm<(ID == 1 ? 5 : 3)>(); } else wait_vm<0>();
  barrier();
  if constexpr (NEXT != 0) ld_const<NEXT>(N, ring, lw, 1);
  if constexpr (ID == 1) {
    if (lw < 2) {
      const char* tp = (const char*)((xcd & 1) ? p.cim : p.cre) + (size_t)((xcd >> 1) * 512 + idx * 16 + lw * 8 + (lane >> 3)) * (S_ * 2) + (lane & 7) * 128;
      asm volatile("global_load_dword %0, %1, off" : "+v"(touch) : "v"(tp) : "memory");
    }
  }
  if constexpr (ID == 2) {
    if (lw < 2) {
      const char* tp = (const char*)((lw & 1) ? p.wu : p.wg) + (size_t)(idx * 8 + (lane >> 3)) * (F_ * 2) + xcd * 896 + ((lane & 7) < 7 ? (lane & 7) : 6) * 128;
      asm volatile("global_load_dword %0, %1, off" : "+v"(touch) : "v"(tp) : "memory");
    }
  }
  barrier();
  const unsigned tag = lds_tag(lds0);
  if constexpr (ID == 1) {
    asm volatile("s_waitcnt vmcnt(8)" : "+v"(ln));
#pragma unroll 1
    for (unsigned spins = 0; spins < (1u << 15) && !__all(ln.y == tag && ln.w == tag); ++spins) {
      __builtin_amdgcn_s_sleep(2);
      asm volatile("global_load_dwordx4 %0, %1, off sc1\n\ts_waitcnt vmcnt(0)" : "=&v"(ln) : "v"(lp) : "memory");
    }
    LAS unsigned* tb = (LAS unsigned*)(lds0 + F_TAB);
    tb[lw * 64 + lane + (lw < 2 ? 0 : 128)] = ln.x;
    if (lw < 2) tb[128 + lw * 64 + lane] = ln.z;
    wait_lgkm0();
  }
  barrier();
  barrier();
  if constexpr (ID != 1) {
    barrier();
    if (lw == 3) poll_tags((char*)p.flags + FL_PAIR + ((ID == 2 ? 0 : 256) + (blockIdx.x ^ 1)) * 128, 1, tag);
    barrier();
  }
}

template <int ID>
__device__ __forceinline__ void sq_compute(const FArgs& p, unsigned lds0, unsigned w, unsigned xcd, unsigned idx, unsigned& tag) {
  constexpr int NT = nt_of<ID>();
  u32x4 xv[2], wv[2];
  unsigned tv = 0;
  const unsigned r0 = blockIdx.x * 8 + w;
  char* fl = (char*)p.flags;
  if constexpr (ID == 1) {
    const int lane = lane_id();
    const u16* xp = (const u16*)p.x + (size_t)r0 * D_ + lane * 8;
    const u16* wp = (const u16*)(r0 < 1024 ? p.dre : p.dim) + (size_t)(r0 & 1023) * D_ + lane * 8;
#pragma unroll
    for (int i = 0; i < 2; ++i) xv[i] = *(const u32x4*)(xp + i * 512);
#pragma unroll
    for (int i = 0; i < 2; ++i) wv[i] = *(const u32x4*)(wp + i * 512);
    tv = __hip_atomic_load((const unsigned*)(fl + FL_EPOCH), __ATOMIC_RELAXED, __HIP_MEMORY_SCOPE_AGENT);
    SB();
  }
  const unsigned ring = lds0 + F_RING;
  const unsigned g = w >> 2, wm = (w >> 1) & 1, wx = w & 1;
  const unsigned kh = xcd & 1, tm = (xcd >> 1) * 4 + (idx & 3), tn = idx >> 2;
  u32x4 res[2];
  unsigned rdA[8], rdB[4], hoff = 0;
  const unsigned rotA = (ID == 1) ? 2 * g : 2 * g + wx;
  {
    const int lane = lane_id(), fr = lane & 15, fq = lane >> 4, chunk = ((fq & 1) << 1) | (fq >> 1);
    const int row = tm * 128 + wm * 64 + rotA * 16 + fr, col = tn * 128 + kh * 32 + chunk * 8;
    if constexpr (ID == 2) {
#pragma unroll
      for (int c = 0; c < 2; ++c) res[c] = *(const u32x4*)((const u16*)p.x + (size_t)row * D_ + col + (c ^ wx) * 64);
    } else if constexpr (ID == 4) {
      hoff = ((row >> 4) * (D_ / 32) + (col >> 5)) * 1024 + (chunk * 16 + fr) * 16;
    }
    const unsigned rotB = (ID == 1) ? 0u : 2 * kh, base = ring + g * F_ST;
#pragma unroll
    for (int i = 0; i < 4; ++i) {
      const unsigned kk = i >> 1, hf = (i & 1) * 2;
      if constexpr (ID == 1) {
        rdA[i] = base + (wm * 64 + (hf ^ rotA) * 16 + fr) * 128 + (((kk * 4 + fq) ^ (fr >> 1)) << 4);
        rdB[i] = base + F_A + (wx * 64 + fr) * 128 + ((((i & 1) * 4 + fq) ^ (fr >> 1)) << 4);
      } else if constexpr (ID == 2) {
        rdA[i] = base + (wm * 64 + (i ^ rotA) * 16 + fr) * 128 + ((fq ^ (fr >> 1)) << 4);
        rdA[4 + i] = base + (wm * 64 + (i ^ rotA) * 16 + fr) * 128 + (((4 + fq) ^ (fr >> 1)) << 4);
        rdB[i] = base + F_A + wx * 8192 + (hf ^ rotB) * 2048 + lane * 16;
      } else {
        const int q = fr >> 2, pp = fr & 3, f = q | ((fq & 1) << 2);
        rdA[i] = base + wm * 8192 + (i ^ rotA) * 2048 + lane * 16;
        rdB[i] = base + F_A + (8 * fq + q) * 256 + (((wx * 4 + (i ^ rotB)) ^ f) << 5) + pp * 8;
      }
    }
  }
  constexpr int NVA = (ID == 2) ? 8 : 4, NVB = (ID <= 2) ? 2 : 4;
  bf16x8 fa[2][4], fb[2][4];
  f32x4 acc[4][4];
#pragma unroll
  for (int i = 0; i < 16; ++i) { acc[i >> 2][i & 3] = (f32x4){0.f, 0.f, 0.f, 0.f}; asm volatile("" : "+v"(acc[i >> 2][i & 3])); }
  auto reads = [&]() {
    SB();
#pragma unroll
    for (int s = 0; s < 2; ++s)
#pragma unroll
      for (int i = 0; i < 4; ++i) {
        if constexpr (ID == 1) {
          fb[s][i] = lds_b128(rdB[s] + i * 2048);
          fa[s][i] = lds_b128(rdA[s * 2 + (i >> 1)] + (i & 1) * 2048);
        } else if constexpr (ID == 2) {
          fb[s][i] = lds_b128(rdB[i >> 1] + (i & 1) * 2048 + s * 1024);
          fa[s][i] = lds_b128(rdA[s * 4 + i]);
        } else {
          fb[s][i] = lds_tr(rdB[i] + s * 8192, rdB[i] + s * 8192 + 1024);
          fa[s][i] = lds_b128(rdA[i] + s * 1024);
        }
      }
    SB();
  };
#pragma unroll
  for (int j = 0; j < NVA; ++j) asm volatile("" : "+v"(rdA[j]));
#pragma unroll
  for (int j = 0; j < NVB; ++j) asm volatile("" : "+v"(rdB[j]));
  if constexpr (ID == 1) {
    SB();
    asm volatile("" : "+v"(tv));
    tv = tv * 0x9E3779B1u + 0x7F4A7C15u;
    const int lane = lane_id();
    float s = 0.f, t = 0.f;
#pragma unroll
    for (int j = 0; j < 8; ++j) s += bf_lo(xv[j >> 2][j & 3]) + bf_hi(xv[j >> 2][j & 3]);
    const float m = wave_sum(s) * (1.0f / D_);
    float q = 0.f;
#pragma unroll
    for (int j = 0; j < 8; ++j) {
      const float a = bf_lo(xv[j >> 2][j & 3]) - m, b = bf_hi(xv[j >> 2][j & 3]) - m;
      q += a * a + b * b;
      t += bf_lo(wv[j >> 2][j & 3]) + bf_hi(wv[j >> 2][j & 3]);
    }
    const float rs = __builtin_amdgcn_rsqf(wave_sum(q) * (1.0f / D_) + EPS);
    t = wave_sum(t);
    const float val = (lane & 2) ? t : ((lane & 1) ? rs : m);
    if (lane < 4)
      __hip_atomic_store((unsigned long long*)(fl + FL_LN + ((lane >> 1) * 2048 + r0) * 16 + (lane & 1) * 8), ((unsigned long long)tv << 32) | __float_as_uint(val),
                         __ATOMIC_RELAXED, __HIP_MEMORY_SCOPE_AGENT);
    tag = rfl(tv);
    if (w == 0) { *(volatile LAS unsigned*)(lds0 + F_TAG) = tag; wait_lgkm0(); }
  }
  if constexpr (ID != 1) {
    barrier();
  }
  if constexpr (ID == 4) {
    const auto rh = OUT_RSRC(p.h, S_ * D_ * 2);
#pragma unroll
    for (int c = 0; c < 2; ++c) res[c] = __builtin_amdgcn_raw_buffer_load_b128(rh, hoff, (c ^ wx) * 2048, 16);
  }
  barrier();
  if (g == 1) barrier();
  reads();
#pragma unroll 1
  for (unsigned i = 0; i < NT / 2; ++i) {
    const unsigned delta = (i & 1) ? (unsigned)(-2 * F_ST) : (unsigned)(2 * F_ST);
#pragma unroll
    for (int j = 0; j < NVA; ++j) { rdA[j] += delta; asm volatile("" : "+v"(rdA[j])); }
#pragma unroll
    for (int j = 0; j < NVB; ++j) { rdB[j] += delta; asm volatile("" : "+v"(rdB[j])); }
    wait_lgkm0();
    barrier();
    SB();
#pragma unroll
    for (int m = 0; m < 32; ++m) {
      const int n = (m & 4) ? 3 - (m & 3) : (m & 3);
      MFMA(fb[m >> 4][n], fa[m >> 4][(m >> 2) & 3], acc[(m >> 2) & 3][n]);
    }
    SB();
    barrier();
    if (i + 1 < NT / 2) reads();
  }
  mfma_drain();
  SB();
  const int lane = lane_id(), fr = lane & 15, fq = lane >> 4, chunk = ((fq & 1) << 1) | (fq >> 1);
  if constexpr (ID == 1) {
    const unsigned wb = ring + 2 * F_ST + (w ^ 4) * 8192 + lane * 16, rb = ring + 2 * F_ST + w * 8192 + lane * 16;
#pragma unroll
    for (int i = 0; i < 8; ++i) *(LAS f32x4*)(wb + i * 1024) = acc[2 + (i >> 2)][i & 3];
    if (g == 0) barrier();
    wait_lgkm0();
    barrier();
#pragma unroll
    for (int i = 0; i < 8; ++i) acc[i >> 2][i & 3] += *(const LAS f32x4*)(rb + i * 1024);
  }
  if constexpr (ID == 1) {
    const LAS float* tb = (const LAS float*)(lds0 + F_TAB);
    const float sg = tm < 8 ? 1.f : -1.f;
    const auto ro = OUT_RSRC(p.ab, D_ * 4096 * 2);
    f32x4 mean[4], rstd[4];
#pragma unroll
    for (int j = 0; j < 4; ++j) {
      mean[j] = *(const LAS f32x4*)(tb + wx * 64 + j * 16 + fq * 4);
      rstd[j] = *(const LAS f32x4*)(tb + 128 + wx * 64 + j * 16 + fq * 4) * sg;
    }
#pragma unroll
    for (int mb = 0; mb < 2; ++mb) {
      const float wsm = tb[256 + wm * 64 + g * 32 + mb * 16 + fr];
#pragma unroll
      for (int np = 0; np < 2; ++np) {
        const f32x4 x0 = (acc[mb][2 * np] - mean[2 * np] * wsm) * rstd[2 * np], x1 = (acc[mb][2 * np + 1] - mean[2 * np + 1] * wsm) * rstd[2 * np + 1];
        auto s0 = __builtin_amdgcn_permlane16_swap(pk_bf16(x0[0], x0[1]), pk_bf16(x1[0], x1[1]), false, false);
        auto s1 = __builtin_amdgcn_permlane16_swap(pk_bf16(x0[2], x0[3]), pk_bf16(x1[2], x1[3]), false, false);
        const int rblk = (tm & 7) * 8 + wm * 4 + g * 2 + mb;
        const int kblk = (tm >> 3) * 64 + (kh * 8 + tn) * 4 + wx * 2 + np;
        STORE16(ro, (rblk * 128 + kblk) * 1024 + (chunk * 16 + fr) * 16, ((u32x4){s0[0], s1[0], s0[1], s1[1]}));
      }
    }
    return;
  }
  const auto rs = OUT_RSRC((char*)p.xch + (ID == 2 ? 0 : 256 * 16384), 256 * 16384);
  const unsigned sreg = ring + ((w >> 1) & 1) * F_ST + (w & 1) * 8192 + lane * 16, kreg = ring + 2 * F_ST + lane * 16;
  auto put_keep = [&]() {
#pragma unroll
    for (int j = 1; j < 4; ++j) {
      const unsigned d = kreg + ((wm * 4 + (j ^ rotA)) * 3 + (j - 1)) * 2048;
      *(LAS f32x4*)(d) = acc[j][0];
      *(LAS f32x4*)(d + 1024) = acc[j][1];
    }
  };
  if (g == 0) {
#pragma unroll
    for (int i = 0; i < 8; ++i) *(LAS f32x4*)(sreg + i * 1024) = acc[i >> 1][2 + (i & 1)];
    put_keep();
    wait_lgkm0();
    barrier();
  } else {
#pragma unroll
    for (int j = 0; j < 4; ++j) {
      const f32x4 x0 = acc[j][2] + *(const LAS f32x4*)(sreg + (j ^ 2) * 2048), x1 = acc[j][3] + *(const LAS f32x4*)(sreg + (j ^ 2) * 2048 + 1024);
      auto s0 = __builtin_amdgcn_permlane16_swap(pk_bf16(x0[0], x0[1]), pk_bf16(x1[0], x1[1]), false, false);
      auto s1 = __builtin_amdgcn_permlane16_swap(pk_bf16(x0[2], x0[3]), pk_bf16(x1[2], x1[3]), false, false);
      __builtin_amdgcn_raw_buffer_store_b128((u32x4){s0[0], s1[0], s0[1], s1[1]}, rs, lane * 16 + blockIdx.x * 16384 + ((wm * 4 + (j ^ rotA)) * 2 + wx) * 1024, 0, 16);
    }
    SB();
    put_keep();
    wait_lgkm0();
  }
  barrier();
  f32x4 y[2][2];
  {
    const unsigned kb = kreg + (wm * 4 + rotA) * 3 * 2048;
    f32x4 t[2][2];
#pragma unroll
    for (int i = 0; i < 2; ++i) {
      t[0][i] = acc[0][i] + *(const LAS f32x4*)(kb + 2048 + i * 1024);
      t[1][i] = *(const LAS f32x4*)(kb + i * 1024) + *(const LAS f32x4*)(kb + 4096 + i * 1024);
    }
#pragma unroll
    for (int c = 0; c < 2; ++c)
#pragma unroll
      for (int j = 0; j < 4; ++j) {
        auto s = __builtin_amdgcn_permlane16_swap(__float_as_uint(t[c][0][j]), __float_as_uint(t[c][1][j]), false, false);
        y[c][0][j] = __uint_as_float(s[0]) + ((j & 1) ? bf_hi(res[c][j >> 1]) : bf_lo(res[c][j >> 1]));
        y[c][1][j] = __uint_as_float(s[1]) + ((j & 1) ? bf_hi(res[c][2 + (j >> 1)]) : bf_lo(res[c][2 + (j >> 1)]));
      }
  }
  const int row0 = tm * 128 + wm * 64 + rotA * 16 + fr, col = tn * 128 + kh * 32 + chunk * 8;
  unsigned optr = (ID == 2) ? ((row0 >> 4) * (D_ / 32) + (col >> 5)) * 1024 + (chunk * 16 + fr) * 16 : (row0 * D_ + col) * 2;
  const auto ro = OUT_RSRC(ID == 2 ? p.h : p.out, S_ * D_ * 2);
  asm volatile("" : "+v"(y[0][0]), "+v"(y[0][1]), "+v"(y[1][0]), "+v"(y[1][1]), "+v"(optr));
  wait_vm<0>();
  barrier();
  if (w == 0 && lane == 0) put_tag((char*)p.flags + FL_PAIR + ((ID == 2 ? 0 : 256) + blockIdx.x) * 128, tag);
  barrier();
  u32x4 in[2], v[2];
#pragma unroll
  for (int c = 0; c < 2; ++c) in[c] = __builtin_amdgcn_raw_buffer_load_b128(rs, lane * 16, (blockIdx.x ^ 1) * 16384 + ((wm * 4 + rotA) * 2 + (c ^ wx)) * 1024, 16);
#pragma unroll
  for (int c = 0; c < 2; ++c) {
    const f32x4 a = y[c][0] + (f32x4){bf_lo(in[c][0]), bf_hi(in[c][0]), bf_lo(in[c][1]), bf_hi(in[c][1])};
    const f32x4 b = y[c][1] + (f32x4){bf_lo(in[c][2]), bf_hi(in[c][2]), bf_lo(in[c][3]), bf_hi(in[c][3])};
    v[c] = (u32x4){pk_bf16(a[0], a[1]), pk_bf16(a[2], a[3]), pk_bf16(b[0], b[1]), pk_bf16(b[2], b[3])};
    __builtin_amdgcn_raw_buffer_store_b128(v[c], ro, optr + (c ^ wx) * (ID == 2 ? 2048 : 128), 0, ST_AUX);
  }
  SB();
  if constexpr (ID == 2) {
    float s = 0.f;
#pragma unroll
    for (int j = 0; j < 8; ++j) s += bf_lo(v[j >> 2][j & 3]) + bf_hi(v[j >> 2][j & 3]);
    const float m = sum_rows(s) * (1.0f / 64.0f);
    float q = 0.f;
#pragma unroll
    for (int j = 0; j < 8; ++j) {
      const float a = bf_lo(v[j >> 2][j & 3]) - m, b = bf_hi(v[j >> 2][j & 3]) - m;
      q += a * a + b * b;
    }
    const f32x2 st = {m, sum_rows(q)};
    if (fq == 0)
      __hip_atomic_store((unsigned long long*)p.stats + (size_t)(tn * 2 + kh) * S_ + row0, __builtin_bit_cast(unsigned long long, st), __ATOMIC_RELAXED, __HIP_MEMORY_SCOPE_AGENT);
  }
  SB();
}

__device__ __forceinline__ void glu_loader(const FArgs& p, const Ld& L, Ld& N, unsigned lds0, unsigned lw, unsigned xcd, unsigned idx, unsigned& touch) {
  constexpr int NT = 32, NEXT = 4;
  const unsigned tag = lds_tag(lds0);
  const unsigned ring = lds0 + F_RING;
  LAS float* cst = (LAS float*)(lds0 + F_TAB + 2048);
  unsigned long long* gcs = (unsigned long long*)((char*)p.flags + FL_CS) + (xcd * 4 + (idx >> 3)) * (8 * 224);
  ld_a<3>(L, ring, lw, 0);
  ld_a<3>(L, ring, lw, 1);
  const int lane = lane_id();
  const unsigned u = lw * 64 + lane < 224 ? lw * 64 + lane : 223u;
  wait_vm<4>();
  barrier();
  ld_a<3>(L, ring, lw, 2);
  ld_b<3>(L, ring, lw, 2);
#pragma unroll 1
  for (unsigned t = 0; t + 3 < NT; ++t) {
    if (t == 25) {
      const unsigned hf = u >= 112, c = u - hf * 112, wn_ = c >= 56, cc = c - wn_ * 56, blk = cc >> 4;
      const unsigned src = lds0 + (wn_ * 4 + blk) * 768 + (blk == 3 ? 512 + (hf * 8 + (cc & 7)) * 4 : hf * 256 + (cc & 15) * 4);
      const float v = (*(const LAS float*)(src) + *(const LAS float*)(src + 64)) + (*(const LAS float*)(src + 128) + *(const LAS float*)(src + 192));
      asm volatile("global_store_dwordx2 %0, %1, off sc1" ::"v"(gcs + L.cls * 224 + u), "v"(((unsigned long long)tag << 32) | __float_as_uint(v)) : "memory");
    }
    ld_a<3>(L, ring, lw, t + 3);
    barrier();
    ld_b<3>(L, ring, lw, t + 3);
    wait_vm<14>();
    barrier();
  }
  unsigned long long g[8];
#pragma unroll
  for (int i = 0; i < 8; ++i) asm volatile("global_load_dwordx2 %0, %1, off sc1" : "=v"(g[i]) : "v"(gcs + i * 224 + u) : "memory");
  barrier();
  ld_b<NEXT>(N, ring, lw, 0);
  wait_vm<19>();
  barrier();
  barrier();
  ld_b<NEXT>(N, ring, lw, 1);
  wait_vm<16>();
  barrier();
  barrier();
  barrier();
  asm volatile("s_waitcnt vmcnt(8)" : "+v"(g[0]), "+v"(g[1]), "+v"(g[2]), "+v"(g[3]), "+v"(g[4]), "+v"(g[5]), "+v"(g[6]), "+v"(g[7]));
#pragma unroll 1
  for (unsigned spins = 0; spins < (1u << 15); ++spins) {
    unsigned tg = 0u;
#pragma unroll
    for (int i = 0; i < 8; ++i) tg |= (unsigned)(g[i] >> 32) ^ tag;
    if (__all(tg == 0u)) break;
    __builtin_amdgcn_s_sleep(2);
#pragma unroll
    for (int i = 0; i < 8; ++i) asm volatile("global_load_dwordx2 %0, %1, off sc1\n\ts_waitcnt vmcnt(0)" : "=&v"(g[i]) : "v"(gcs + i * 224 + u) : "memory");
  }
  float s = 0.f;
#pragma unroll
  for (int i = 0; i < 8; ++i) s += __uint_as_float((unsigned)g[i]);
  cst[u] = s;
  wait_lgkm0();
  {
    const char* tp = (const char*)p.wd + ((size_t)blockIdx.x * 224 + (lw * 56 + (lane < 56 ? lane : 55))) * 128;
    asm volatile("global_load_dword %0, %1, off" : "+v"(touch) : "v"(tp) : "memory");
  }
  barrier();
}

__device__ __forceinline__ void glu_compute(const FArgs& p, unsigned lds0, unsigned w, unsigned xcd, unsigned idx) {
  constexpr int ID = 3, BM = 256, NT = 32, MB = 4, NB = 7;
  const unsigned ring = lds0 + F_RING;
  LAS f32x2* tab = (LAS f32x2*)(lds0 + F_TAB);
  LAS float* cst = (LAS float*)(lds0 + F_TAB + 2048);
  const unsigned tm = idx & 7, tn = xcd * 4 + (idx >> 3), cls = (tm + 2 * (idx >> 3)) & 7;
  const unsigned wm = w & 3, wn = w >> 2;
  const int lane = lane_id(), fr = lane & 15, fq = lane >> 4;
  const unsigned rot = wm == 3 ? 0u : wm;
  unsigned xa = ring + wm * 4096 + lane * 16, xb[3], xb1;
  {
    const int q = fr >> 2, pp = fr & 3, r = (fq >> 1) * 16 + (fq & 1) * 4 + q;
#pragma unroll
    for (int i = 0; i < 3; ++i) xb[i] = ring + F_A + r * 224 + wn * 112 + pp * 8 + ((i + rot) % 3) * 32;
    xb1 = ring + F_A + r * 224 + wn * 112 + 96 + (pp & 1) * 8 + (pp >> 1) * G3_UP;
  }
  float cs[3] = {0.f, 0.f, 0.f};
  bf16x8 fa[MB], fb[NB];
  f32x4 acc[MB][NB];
#pragma unroll
  for (int mb = 0; mb < MB; ++mb)
#pragma unroll
    for (int nb = 0; nb < NB; ++nb) { acc[mb][nb] = (f32x4){0.f, 0.f, 0.f, 0.f}; asm volatile("" : "+v"(acc[mb][nb])); }
  asm volatile("" : "+v"(xa), "+v"(xb[0]), "+v"(xb[1]), "+v"(xb[2]), "+v"(xb1));
  barrier();
  if (wn == 1) {
    const auto rst = OUT_RSRC(p.stats, 8 * S_ * 16);
    f32x2 sv[16];
#pragma unroll
    for (int i = 0; i < 16; ++i) sv[i] = __builtin_bit_cast(f32x2, __builtin_amdgcn_raw_buffer_load_b64(rst, (tm * BM + wm * 64 + lane) * 8, i * S_ * 8, 16));
    SB();
    barrier();
    SB();
    float m = 0.f;
#pragma unroll
    for (int i = 0; i < 16; ++i) m += sv[i][0];
    m *= (1.0f / 16.0f);
    float q = 0.f;
#pragma unroll
    for (int i = 0; i < 16; ++i) {
      const float d = sv[i][0] - m;
      q += sv[i][1] + 64.0f * d * d;
    }
    tab[wm * 64 + lane] = (f32x2){m, q * (1.0f / D_) + EPS};
  }
  wait_lgkm0();
  barrier();
  const unsigned csmask = (0x010101u << cls) | (1u << ((cls + 1) & 7));
  auto reads = [&]() {
    SB();
    fa[0] = lds_b128(xa);
#pragma unroll
    for (int j = 0; j < 7; ++j) {
      const unsigned o = j < 6 ? xb[j % 3] + (j / 3) * G3_UP : xb1;
      fb[j] = lds_tr(o, o + 1792);
    }
#pragma unroll
    for (int mb = 1; mb < MB; ++mb) fa[mb] = lds_b128(xa + mb * 1024);
    SB();
  };
  reads();
#pragma unroll 1
  for (unsigned t = 0; t < NT; ++t) {
    const unsigned delta = ((t & 3) == 3) ? (unsigned)(-3 * F_ST) : (unsigned)F_ST;
    xa += delta; xb[0] += delta; xb[1] += delta; xb[2] += delta; xb1 += delta;
    asm volatile("" : "+v"(xa), "+v"(xb[0]), "+v"(xb[1]), "+v"(xb[2]), "+v"(xb1));
    if (__builtin_expect((csmask >> t) & 1u, 0)) {
#pragma unroll
      for (int j = 0; j < 4; ++j)
#pragma unroll
        for (int i = 0; i < 3; ++i)
          asm volatile("v_dot2c_f32_bf16 %0, %1, %2" : "+v"(cs[i]) : "s"(0x3f803f80u), "v"(__builtin_bit_cast(u32x4, fb[3 * i])[j]));
      asm volatile("s_mov_b32 m0, %3\n\ts_nop 2\n\tds_write_addtid_b32 %0\n\tds_write_addtid_b32 %1 offset:256\n\tds_write_addtid_b32 %2 offset:512" ::"v"(cs[0]),
                   "v"(cs[1]), "v"(cs[2]), "s"(lds0 + w * 768)
                   : "memory");
    }
    wait_lgkm0();
    barrier();
    SB();
#pragma unroll
    for (int mb = 0; mb < MB; ++mb)
#pragma unroll
      for (int i = 0; i < NB; ++i) {
        const int nb = (mb & 1) ? NB - 1 - i : i;
        MFMA(fb[nb], fa[mb], acc[mb][nb]);
      }
    SB();
    barrier();
    reads();
  }
  if (wn == 0) barrier();
  mfma_drain();
  SB();
  const int lane2 = lane_id();
  const int gr = lane2 & 15, gq = lane2 >> 4;
  auto act2 = [](f32x2 a, f32x2 b, float sc, float k) {
    const f32x2 t = a * sc;
    const f32x2 e = {__builtin_amdgcn_exp2f(t.x), __builtin_amdgcn_exp2f(t.y)};
    const f32x2 d = e * k + k;
    const f32x2 r = {__builtin_amdgcn_rcpf(d.x), __builtin_amdgcn_rcpf(d.y)};
    const f32x2 o = (a * b) * r;
    return pk_bf16(o.x, o.y);
  };
  const auto ro = OUT_RSRC(p.gu, S_ * F_ * 2);
  float nm[MB], kk[MB], sc[MB];
#pragma unroll
  for (int mb = 0; mb < MB; ++mb) {
    const f32x2 st = tab[wm * 64 + mb * 16 + gr];
    nm[mb] = -st.x; kk[mb] = st.y; sc[mb] = -1.44269504089f * __builtin_amdgcn_rsqf(st.y);
  }
  const int gch0 = tn * 14 + wn * 7;
  const unsigned obase = (tm * (BM / 16) + wm * MB) * (F_ / 32) * 1024 + gr * 16;
#pragma unroll
  for (int i = 0; i < 3; ++i) {
    const unsigned ib = (i + rot) % 3;
    const f32x4 cg = *(const LAS f32x4*)(cst + wn * 56 + ib * 16 + gq * 4), cu = *(const LAS f32x4*)(cst + 112 + wn * 56 + ib * 16 + gq * 4);
    const unsigned oo = obase + (gq & 1) * (F_ / 32) * 1024 + (gch0 + 2 * ib + (gq >> 1)) * 256;
#pragma unroll
    for (int mp = 0; mp < MB / 2; ++mp) {
      u32x2 pkd[2];
#pragma unroll
      for (int h = 0; h < 2; ++h) {
        const int mb = 2 * mp + h;
        const f32x4 a = cg * nm[mb] + acc[mb][i], b = cu * nm[mb] + acc[mb][3 + i];
        pkd[h] = (u32x2){act2(a.xy, b.xy, sc[mb], kk[mb]), act2(a.zw, b.zw, sc[mb], kk[mb])};
      }
      auto s0 = __builtin_amdgcn_permlane16_swap(pkd[0].x, pkd[1].x, false, false);
      auto s1 = __builtin_amdgcn_permlane16_swap(pkd[0].y, pkd[1].y, false, false);
      __builtin_amdgcn_raw_buffer_store_b128((u32x4){s0[0], s1[0], s0[1], s1[1]}, ro, oo + mp * 2 * (F_ / 32) * 1024, 0, ST_AUX);
    }
  }
  {
    const f32x4 cm = *(const LAS f32x4*)(cst + (gq >> 1) * 112 + wn * 56 + 48 + (gq & 1) * 4);
    u32x2 pkd[MB / 2];
#pragma unroll
    for (int mp = 0; mp < MB / 2; ++mp) {
      const f32x4 v0 = cm * nm[2 * mp] + acc[2 * mp][6], v1 = cm * nm[2 * mp + 1] + acc[2 * mp + 1][6];
      f32x4 a, b;
#pragma unroll
      for (int j = 0; j < 4; ++j) {
        auto s = __builtin_amdgcn_permlane32_swap(__float_as_uint(v0[j]), __float_as_uint(v1[j]), false, false);
        a[j] = __uint_as_float(s[0]); b[j] = __uint_as_float(s[1]);
      }
      const float scs = (gq & 2) ? sc[2 * mp + 1] : sc[2 * mp], ks = (gq & 2) ? kk[2 * mp + 1] : kk[2 * mp];
      pkd[mp] = (u32x2){act2(a.xy, b.xy, scs, ks), act2(a.zw, b.zw, scs, ks)};
    }
    auto s0 = __builtin_amdgcn_permlane16_swap(pkd[0].x, pkd[1].x, false, false);
    auto s1 = __builtin_amdgcn_permlane16_swap(pkd[0].y, pkd[1].y, false, false);
    STORE16(ro, obase + ((gq & 1) * 2 + (gq >> 1)) * (F_ / 32) * 1024 + (gch0 + 6) * 256, ((u32x4){s0[0], s1[0], s0[1], s1[1]}));
  }
}

__global__ __launch_bounds__(768, 3) void k_block(FArgs p) {
  extern __shared__ __attribute__((aligned(16))) char shm[];
  const unsigned lds0 = (unsigned)(size_t)(LAS char*)shm;
  const unsigned w = rfl(threadIdx.x >> 6);
  const unsigned xcd = blockIdx.x & 7, idx = blockIdx.x >> 3;
  char* fl = (char*)p.flags;
  const unsigned tm = (xcd >> 1) * 4 + (idx & 3), kh = xcd & 1, lj = idx >> 2;
  if (w >= 8) {
    const unsigned lw = w - 8;
    Ld LA, LB;
    unsigned touch = 0;
    LA = ld_setup<1>(p, lw, xcd, idx);
    sq_loader<1>(p, LA, LB, lds0, lw, xcd, idx, touch);
    const unsigned tag = lds_tag(lds0);
    LA = ld_setup<3>(p, lw, xcd, idx);
    ld_pin(LA);
    barrier();
    if (lw == 3) poll_tags(fl + FL_P1 + ((kh * 8 + lj) * 4 + (xcd >> 1)) * 128, 16, tag);
    barrier();
    sq_loader<2>(p, LB, LA, lds0, lw, xcd, idx, touch);
    LB = ld_setup<4>(p, lw, xcd, idx);
    ld_pin(LB);
    barrier();
    if (lw == 3) poll_tags(fl + FL_P2 + ((idx & 7) * 8 + xcd) * 128, 32, tag);
    barrier();
    glu_loader(p, LA, LB, lds0, lw, xcd, idx, touch);
    barrier();
    if (lw == 3) poll_tags(fl + FL_P3 + ((tm >> 1) * 2 + kh) * 128, 16, tag);
    barrier();
    sq_loader<4>(p, LB, LA, lds0, lw, xcd, idx, touch);
    asm volatile("" ::"v"(touch));
    return;
  }
  unsigned csink = 0;
  if (w == 0 && idx < 3) {
    unsigned long long pc;
    asm volatile("s_getpc_b64 %0" : "=s"(pc));
    const unsigned ln = idx * 64 + lane_id();
    asm volatile("global_load_dword %0, %1, %2" : "+v"(csink) : "v"((ln < CODE_LINES ? ln : CODE_LINES - 1) * 128), "s"(pc) : "memory");
  }
  unsigned tag = 0;
  sq_compute<1>(p, lds0, w, xcd, idx, tag);
  asm volatile("" ::"v"(csink));
  wait_vm<0>();
  barrier();
  if (w == 0) { const int l = lane_id(); if (l < 4) put_tag(fl + FL_P1 + (tm * 4 + l) * 128 + (kh * 8 + lj) * 4, tag); }
  sq_compute<2>(p, lds0, w, xcd, idx, tag);
  wait_vm<0>();
  barrier();
  if (w == 0) { const int l = lane_id(); if (l < 8) put_tag(fl + FL_P2 + ((tm >> 1) * 8 + l) * 128 + (((tm & 1) * 2 + kh) * 8 + lj) * 4, tag); }
  glu_compute(p, lds0, w, xcd, idx);
  wait_vm<0>();
  barrier();
  if (w == 0 && lane_id() == 0) put_tag(fl + FL_P3 + ((idx & 7) * 2 + (xcd >> 2)) * 128 + ((xcd & 3) * 4 + (idx >> 3)) * 4, tag);
  sq_compute<4>(p, lds0, w, xcd, idx, tag);
  if (blockIdx.x == 0 && w == 0 && lane_id() == 0) put_tag(fl + FL_EPOCH, tag);
}
}

extern "C" void kernel_launch(void* const* d_in, const int* in_sizes, int n_in, void* d_out, int out_size, void* d_ws, size_t ws_size,
                              hipStream_t stream) {
  char* ws = (char*)d_ws;
  FArgs f = {d_in[3], d_in[4], d_in[1], d_in[2], d_in[0], d_in[6], d_in[7], d_in[8], ws + WS_AB, ws + WS_H, ws + WS_ST, ws + WS_GU, d_out, ws + WS_XC, ws + WS_FL};
  (void)hipFuncSetAttribute((const void*)k_block, hipFuncAttributeMaxDynamicSharedMemorySize, F_LDS);
  hipLaunchKernelGGL(k_block, dim3(256), dim3(768), F_LDS, stream, f);
}
```

```cpp
#include <hip/hip_runtime.h>
#include <hip/hip_bf16.h>

namespace {
#define LAS __attribute__((address_space(3)))
typedef unsigned short u16;
typedef short s16x4 __attribute__((ext_vector_type(4)));
typedef short s16x8 __attribute__((ext_vector_type(8)));
typedef __bf16 bf16x8 __attribute__((ext_vector_type(8)));
typedef __bf16 bf16x2 __attribute__((ext_vector_type(2)));
typedef float f32x4 __attribute__((ext_vector_type(4)));
typedef float f32x2 __attribute__((ext_vector_type(2)));
typedef unsigned u32x4 __attribute__((ext_vector_type(4)));
typedef unsigned u32x2 __attribute__((ext_vector_type(2)));

constexpr int S_ = 2048, D_ = 1024, F_ = 3584;
constexpr float EPS = 1e-5f;

constexpr size_t WS_AB = 0;
constexpr size_t WS_H = WS_AB + (size_t)D_ * 2 * S_ * 2;
constexpr size_t WS_GU = WS_H + (size_t)S_ * D_ * 2;
constexpr size_t WS_ST = WS_GU + (size_t)S_ * F_ * 2;
constexpr size_t WS_XC = WS_ST + (size_t)S_ * 16 * 8;
constexpr size_t WS_FL = WS_XC + (size_t)2 * 256 * 16384;
constexpr int FL_EPOCH = 0;
constexpr int FL_PAIR = 128;
constexpr int FL_P1 = FL_PAIR + 512 * 128;
constexpr int FL_P2 = FL_P1 + 64 * 128;
constexpr int FL_P3 = FL_P2 + 64 * 128;
constexpr int FL_LN = FL_P3 + 16 * 128;
constexpr int FL_CS = FL_LN + 4096 * 16;

__device__ __forceinline__ unsigned rfl(unsigned v) { return __builtin_amdgcn_readfirstlane(v); }
__device__ __forceinline__ unsigned pk_bf16(float lo, float hi) {
  bf16x2 c = {(__bf16)lo, (__bf16)hi};
  return __builtin_bit_cast(unsigned, c);
}
__device__ __forceinline__ float bf_lo(unsigned v) { return __uint_as_float(v << 16); }
__device__ __forceinline__ float bf_hi(unsigned v) { return __uint_as_float(v & 0xffff0000u); }

__device__ __forceinline__ u32x4 make_rsrc(const void* p, unsigned bytes) {
  unsigned long long a = (unsigned long long)p;
  u32x4 r;
  r.x = (unsigned)a;
  r.y = (unsigned)(a >> 32) & 0xffffu;
  r.z = bytes;
  r.w = 0x00020000u;
  return r;
}
__device__ __forceinline__ void dma16(unsigned ldsaddr, unsigned voff, u32x4 rsrc, unsigned soff) {
  asm volatile("s_mov_b32 m0, %0\n\ts_nop 4\n\tbuffer_load_dwordx4 %1, %2, %3 offen lds" ::"s"(ldsaddr), "v"(voff), "s"(rsrc), "s"(soff)
               : "memory");
}
template <int N>
__device__ __forceinline__ void wait_vm() {
  asm volatile("s_waitcnt vmcnt(%0)" ::"i"(N) : "memory");
}
__device__ __forceinline__ void wait_lgkm0() { __builtin_amdgcn_s_waitcnt(0xC07F); }
__device__ __forceinline__ void barrier() {
  asm volatile("" ::: "memory");
  __builtin_amdgcn_s_barrier();
  asm volatile("" ::: "memory");
}
__device__ __forceinline__ bf16x8 lds_b128(unsigned addr) { return *(const LAS bf16x8*)(addr); }
__device__ __forceinline__ bf16x8 lds_tr(unsigned a0, unsigned a1) {
  s16x4 lo = __builtin_amdgcn_ds_read_tr16_b64_v4i16((LAS s16x4*)(a0));
  s16x4 hi = __builtin_amdgcn_ds_read_tr16_b64_v4i16((LAS s16x4*)(a1));
  s16x8 f = __builtin_shufflevector(lo, hi, 0, 1, 2, 3, 4, 5, 6, 7);
  return __builtin_bit_cast(bf16x8, f);
}

#define ST_AUX 16
#define OUT_RSRC(ptr, bytes) __builtin_amdgcn_make_buffer_rsrc((void*)(ptr), 0, (bytes), 0x00020000)
#define STORE16(rs, byteoff, v) __builtin_amdgcn_raw_buffer_store_b128(v, rs, byteoff, 0, ST_AUX)
#define STAMP(k, i)
#define STAMPX(k, i)
#define STAMPL(k, i, first)

#define DMA4(M) asm volatile("s_mov_b32 m0, %0\n\ts_nop 4\n\tbuffer_load_dwordx4 %1, %5, %6 offen " M " lds\n\tbuffer_load_dwordx4 %2, %5, %6 offen offset:1024 " M " lds\n\t" \
                 "buffer_load_dwordx4 %3, %5, %6 offen offset:2048 " M " lds\n\tbuffer_load_dwordx4 %4, %5, %6 offen offset:3072 " M " lds" ::"s"(ldsaddr), \
                 "v"(v[0]), "v"(v[1]), "v"(v[2]), "v"(v[3]), "s"(rsrc), "s"(soff) : "memory")
#define DMA3(M) asm volatile("s_mov_b32 m0, %0\n\ts_nop 4\n\tbuffer_load_dwordx4 %1, %4, %5 offen " M " lds\n\tbuffer_load_dwordx4 %2, %4, %5 offen offset:1024 " M " lds\n\t" \
                 "buffer_load_dwordx4 %3, %4, %5 offen offset:2048 " M " lds" ::"s"(ldsaddr), "v"(v[0]), "v"(v[1]), "v"(v[2]), "s"(rsrc), "s"(soff) : "memory")
#define DMAB(M) asm volatile("s_mov_b32 m0, %0\n\ts_waitcnt vmcnt(8)\n\ts_barrier\n\tbuffer_load_dwordx4 %1, %5, %6 offen " M " lds\n\tbuffer_load_dwordx4 %2, %5, %6 offen offset:1024 " M " lds\n\t" \
                 "buffer_load_dwordx4 %3, %5, %6 offen offset:2048 " M " lds\n\tbuffer_load_dwordx4 %4, %5, %6 offen offset:3072 " M " lds" ::"s"(ldsaddr), \
                 "v"(v[0]), "v"(v[1]), "v"(v[2]), "v"(v[3]), "s"(rsrc), "s"(soff) : "memory")
template <int N, int MOD = 0>
__device__ __forceinline__ void dma_grp(unsigned ldsaddr, const unsigned (&v)[4], u32x4 rsrc, unsigned soff) {
  if constexpr (N == 4) { if constexpr (MOD == 1) DMA4("sc1"); else if constexpr (MOD == 2) DMA4("nt"); else DMA4(""); }
  else if constexpr (N == 3) { if constexpr (MOD == 1) DMA3("sc1"); else if constexpr (MOD == 2) DMA3("nt"); else DMA3(""); }
  else { if constexpr (MOD == 1) DMAB("sc1"); else if constexpr (MOD == 2) DMAB("nt"); else DMAB(""); }
}
__device__ __forceinline__ float sum_rows(float x) {
  auto a = __builtin_amdgcn_permlane16_swap(__float_as_uint(x), __float_as_uint(x), false, false);
  x = __uint_as_float(a[0]) + __uint_as_float(a[1]);
  auto b = __builtin_amdgcn_permlane32_swap(__float_as_uint(x), __float_as_uint(x), false, false);
  return __uint_as_float(b[0]) + __uint_as_float(b[1]);
}
__device__ __forceinline__ float sum_pair(float x) { return x + __uint_as_float(__builtin_amdgcn_mov_dpp(__float_as_uint(x), 0xB1, 0xF, 0xF, true)); }
template <int CTRL> __device__ __forceinline__ float dpp_add(float x) { return x + __uint_as_float(__builtin_amdgcn_mov_dpp(__float_as_uint(x), CTRL, 0xF, 0xF, true)); }
__device__ __forceinline__ float wave_sum(float x) { return sum_rows(dpp_add<0x140>(dpp_add<0x141>(dpp_add<0x4E>(dpp_add<0xB1>(x))))); }
__device__ __forceinline__ int lane_id() {
  int l;
  asm volatile("v_mbcnt_lo_u32_b32 %0, -1, 0\n\tv_mbcnt_hi_u32_b32 %0, -1, %0" : "=v"(l));
  return l;
}
__device__ __forceinline__ void mfma_drain() { asm volatile("s_nop 15" ::: "memory"); }
__device__ __forceinline__ void fadd(float& a, float b) { asm("v_add_f32 %0, %1, %0" : "+v"(a) : "v"(b)); }
template <int NC, int NT, int WRAP>
__device__ __forceinline__ unsigned stage_of(unsigned n, unsigned c) {
  if (NC == 1) return n;
  if (WRAP) {
    const unsigned t = ((n & (NC - 1)) == c) ? n + NC : n;
    return rfl(t >= NT ? t - NT : t);
  }
  unsigned t = (n > NT - NC + c) ? n : n - 1;
  t = ((t & (NC - 1)) == c) ? t + NC : t;
  return rfl(n == 0 ? c : t);
}
#define NC1 4
#define NC2 8
#define NC3 8
#define NC4 4
#define TOUCH_C 1
#define TOUCH_W 1
#define TOUCH_D 2
#define TOUCH_AT 6
#define OUT_AUX 16
#define NTM 0
#define ST_SLEEP 0
#define EARLY0 0
#define SNAKE 1
#define L2MASK 63
#define PD3 0
#define XCH4 (256 * 16384)
#define LN_NT 0
#define FT_W 1
#define FT_D 1
#define POLL_SLEEP 1
#define XSTR 16384
#define WRAPM 0x1e
#define SQ_PD 2
#define BAR_AT 0
#define CPRIO 0
template <int V> struct IC { static constexpr int value = V; };
#define LPRIO 0
#define SB() __builtin_amdgcn_sched_barrier(0)
#define MFMA(b, a, c) asm volatile("v_mfma_f32_16x16x32_bf16 %0, %1, %2, %0" : "+v"(c) : "v"(b), "v"(a))
#define STAMP_END(k)
#define XT(i)
#define XPUT(k)
#define STAMP1_TAKE()
#define STAMP1_PUT(k)

#define CODE_LABEL(name) asm volatile(#name ":")
#define CODE_ADDR(dst, name) do { int o_; asm volatile("s_getpc_b64 %0\n\ts_mov_b32 %1, " #name "@rel32@lo+4" : "=s"(dst), "=s"(o_)); dst += (long long)o_; } while (0)
#define CODE_LAST 30
#define CODE_PF 3
__device__ __forceinline__ void touch_code(unsigned long long base, int lane, int last, unsigned& sink) {
  const unsigned off = (lane < last ? lane : last) * 128;
  asm volatile("global_load_dword %0, %1, %2" : "+v"(sink) : "v"(off), "s"(base) : "memory");
}
struct FArgs {
  const void *dre, *dim, *cre, *cim, *x, *wg, *wu, *wd;
  void *ab, *h, *stats, *gu, *out, *xch, *flags;
};
constexpr int F_RING = 6144, F_ST = 32768, F_TAB = F_RING + 4 * F_ST, F_TAG = F_TAB + 4096, F_LDS = F_TAB + 4160, F_A = 16384, G3_UP = 7168 + 16;

__device__ __forceinline__ void poll_tags(const void* line, int n, unsigned tag) {
  const int l = lane_id();
  const unsigned* q = (const unsigned*)line + (l < n ? l : 0);
  unsigned spins = 0;
  while (!__all(__hip_atomic_load(q, __ATOMIC_RELAXED, __HIP_MEMORY_SCOPE_AGENT) == tag) && ++spins < (1u << 17)) __builtin_amdgcn_s_sleep(POLL_SLEEP);
}
__device__ __forceinline__ void put_tag(void* word, unsigned tag) { __hip_atomic_store((unsigned*)word, tag, __ATOMIC_RELAXED, __HIP_MEMORY_SCOPE_AGENT); }
__device__ __forceinline__ unsigned lds_tag(unsigned lds0) { return rfl(*(volatile LAS unsigned*)(lds0 + F_TAG)); }
template <int ID> constexpr int nt_of() { return ID == 1 ? 16 : (ID == 4 ? 28 : 32); }
template <int ID> constexpr int nc_of() { return ID == 1 ? NC1 : (ID == 2 ? NC2 : (ID == 3 ? 8 : NC4)); }
template <int ID> constexpr int mod_a() { return ID > 2 ? 1 : (((NTM >> ID) & 1) ? 2 : 0); }
template <int ID> constexpr int mod_b() { return ID == 2 ? 1 : (((NTM >> ID) & 1) ? 2 : 0); }
struct Ld {
  u32x4 qa, qb;
  unsigned va[4], vb[4], cls;
};
template <int ID>
__device__ __forceinline__ Ld ld_setup(const FArgs& p, unsigned lw, unsigned xcd, unsigned idx) {
  const int lane = lane_id();
  Ld L;
  if constexpr (ID == 3) {
    const unsigned tm = idx & 7, tn = xcd * 4 + (idx >> 3);
    L.qa = make_rsrc((const u16*)p.h + (size_t)tm * 256 * D_, 256 * D_ * 2);
    L.qb = make_rsrc(lw < 2 ? p.wg : p.wu, (unsigned)D_ * F_ * 2);
#pragma unroll
    for (int i = 0; i < 4; ++i) {
      L.va[i] = (lw * 4 + i) * (D_ / 32) * 1024 + lane * 16 - i * 1024;
      const int c = ((lw & 1) * 4 + i) * 64 + lane, r = c / 14, pos = c % 14;
      const int k = (r & 0x13) | ((r & 4) << 1) | ((r & 8) >> 1);
      L.vb[i] = k * F_ * 2 + tn * 224 + pos * 16 - i * 1024;
    }
    L.cls = (tm + 2 * (idx >> 3)) & 7;
  } else {
    const unsigned li = idx & 3, lj = idx >> 2, kh = xcd & 1, tm = (xcd >> 1) * 4 + li, tn = lj;
    if constexpr (ID == 1) {
      L.qa = make_rsrc((const u16*)(tm < 8 ? p.dre : p.dim) + (size_t)(tm & 7) * 128 * D_, 128 * D_ * 2);
      L.qb = make_rsrc((const u16*)p.x + (size_t)(kh * 8 + lj) * 128 * D_, 128 * D_ * 2);
    } else if constexpr (ID == 2) {
      L.qa = make_rsrc((const u16*)(kh ? p.cim : p.cre) + (size_t)tm * 128 * S_, 128 * S_ * 2);
      L.qb = make_rsrc((const u16*)p.ab + (size_t)tn * 128 * 4096 + kh * 64 * 512, 128 * 4096 * 2 - kh * 64 * 1024);
    } else {
      L.qa = make_rsrc((const u16*)p.gu + (size_t)tm * 128 * F_ + kh * 56 * 512, 128 * F_ * 2 - kh * 56 * 1024);
      L.qb = make_rsrc((const u16*)p.wd + (size_t)kh * 1792 * D_, 1792 * D_ * 2);
    }
#pragma unroll
    for (int i = 0; i < 4; ++i) {
      const int pc = lw * 4 + i;
      if constexpr (ID == 1) {
        const int row = pc * 8 + (lane >> 3);
        L.va[i] = L.vb[i] = row * D_ * 2 + (((lane & 7) ^ ((row >> 1) & 7)) << 4) - i * 1024;
      } else if constexpr (ID == 2) {
        const int row = pc * 8 + (lane >> 3);
        L.va[i] = row * S_ * 2 + (((lane & 7) ^ ((row >> 1) & 7)) << 4) - i * 1024;
        L.vb[i] = ((pc >> 1) * 128 + (pc & 1)) * 1024 + lane * 16 - i * 1024;
      } else {
        const int k = pc * 4 + (lane >> 4), f = (k & 3) | (((k >> 3) & 1) << 2);
        L.va[i] = ((pc >> 1) * (F_ / 32) + (pc & 1)) * 1024 + lane * 16 - i * 1024;
        L.vb[i] = k * D_ * 2 + tn * 256 + (((lane & 15) ^ (f << 1)) << 4) - i * 1024;
      }
    }
    L.cls = (nc_of<ID>() == 8) ? ((lj + 2 * li) & 7) : ((lj + li) & 3);
  }
  return L;
}
__device__ __forceinline__ void ld_pin(Ld& L) {
  asm volatile("" : "+v"(L.va[0]), "+v"(L.va[1]), "+v"(L.va[2]), "+v"(L.va[3]), "+v"(L.vb[0]), "+v"(L.vb[1]), "+v"(L.vb[2]), "+v"(L.vb[3]));
}
template <int ID>
__device__ __forceinline__ void ld_a(const Ld& L, unsigned ring, unsigned lw, unsigned n) {
  const unsigned st = stage_of<nc_of<ID>(), nt_of<ID>(), (WRAPM >> ID) & 1>(n, L.cls) & L2MASK;
  dma_grp<4, mod_a<ID>()>(ring + (n & 3) * F_ST + lw * 4096, L.va, L.qa, st * (ID <= 2 ? 128 : (ID == 3 ? 1024 : 2048)));
}
template <int ID>
__device__ __forceinline__ void ld_b(const Ld& L, unsigned ring, unsigned lw, unsigned n) {
  const unsigned st = stage_of<nc_of<ID>(), nt_of<ID>(), (WRAPM >> ID) & 1>(n, L.cls) & L2MASK;
  const unsigned dst = ring + (n & 3) * F_ST + F_A;
  if constexpr (ID == 3) {
    const unsigned d = dst + (lw >> 1) * G3_UP + (lw & 1) * 4096;
    if (lw & 1) dma_grp<3, mod_b<ID>()>(d, L.vb, L.qb, st * (32 * F_ * 2));
    else dma_grp<4, mod_b<ID>()>(d, L.vb, L.qb, st * (32 * F_ * 2));
  } else {
    dma_grp<4, mod_b<ID>()>(dst + lw * 4096, L.vb, L.qb, st * (ID == 1 ? 128 : (ID == 2 ? 2048 : 64 * D_ * 2)));
  }
}

template <int ID> __device__ __forceinline__ void ld_const(const Ld& L, unsigned ring, unsigned lw, unsigned n) { if constexpr (ID == 2) ld_a<ID>(L, ring, lw, n); else ld_b<ID>(L, ring, lw, n); }
template <int ID> __device__ __forceinline__ void ld_dep(const Ld& L, unsigned ring, unsigned lw, unsigned n) { if constexpr (ID == 2) ld_b<ID>(L, ring, lw, n); else ld_a<ID>(L, ring, lw, n); }

template <int ID>
__device__ __forceinline__ void sq_loader(const FArgs& p, const Ld& L, Ld& N, unsigned lds0, unsigned lw, unsigned xcd, unsigned idx, unsigned& touch) {
  constexpr int NT = nt_of<ID>(), NEXT = ID == 4 ? 0 : ID + 1;
  const unsigned ring = lds0 + F_RING;
  if constexpr (ID == 1) {
    ld_a<ID>(L, ring, lw, 0); ld_b<ID>(L, ring, lw, 0);
    N = ld_setup<NEXT>(p, lw, xcd, idx); ld_pin(N);
    ld_a<ID>(L, ring, lw, 1); ld_b<ID>(L, ring, lw, 1);
  } else {
    ld_dep<ID>(L, ring, lw, 0); ld_dep<ID>(L, ring, lw, 1);
  }
  constexpr unsigned KA = ID <= 2 ? 128 : 2048, KB = ID == 1 ? 128 : (ID == 2 ? 2048 : 64 * D_ * 2);
  unsigned sa, sb, ma;
  auto prep = [&](unsigned n) {
    const unsigned st = stage_of<nc_of<ID>(), NT, (WRAPM >> ID) & 1>(n, L.cls) & L2MASK;
    sa = st * KA; sb = st * KB; ma = ring + (n & 3) * F_ST + lw * 4096;
    asm volatile("" : "+s"(sa), "+s"(sb), "+s"(ma));
  };
  auto bar_a = [&]() {
    dma_grp<5, mod_a<ID>()>(ma, L.va, L.qa, sa);
  };
  prep(2);
  wait_vm<(ID == 1 ? 8 : 4)>();
  bar_a();
  unsigned mb_ = ma + F_A, sb_ = sb;
  prep(3);
#pragma unroll 1
  for (unsigned n = 0; n + 3 < NT; ++n) {
    dma_grp<4, mod_b<ID>()>(mb_, L.vb, L.qb, sb_);
    bar_a();
    mb_ = ma + F_A; sb_ = sb;
    asm volatile("" : "+s"(mb_), "+s"(sb_));
    prep(n + 4);
  }
  dma_grp<4, mod_b<ID>()>(mb_, L.vb, L.qb, sb_);
  wait_vm<8>();
  barrier();
  const int lane = lane_id();
  u32x4 ln;
  const char* lp = (const char*)p.flags + FL_LN + (lw < 2 ? (((xcd & 1) * 8 + (idx >> 2)) * 128 + lw * 64 + lane) : (2048 + ((xcd >> 1) * 4 + (idx & 3)) * 128 + (lw - 2) * 64 + lane)) * 16;
  if constexpr (ID == 1) asm volatile("global_load_dwordx4 %0, %1, off sc1" : "=v"(ln) : "v"(lp) : "memory");
  if constexpr (NEXT != 0) { ld_const<NEXT>(N, ring, lw, 0); wait_vm<(ID == 1 ? 5 : 3)>(); } else wait_vm<0>();
  barrier();
  if constexpr (NEXT != 0) ld_const<NEXT>(N, ring, lw, 1);
  if constexpr (ID == 1) {
    if (TOUCH_C && lw < 2 * TOUCH_C) {
      const char* tp = (const char*)((xcd & 1) ? p.cim : p.cre) + (size_t)((xcd >> 1) * 512 + idx * 16 + (lw & 1) * 8 + (lane >> 3)) * (S_ * 2) + (lw >> 1) * 1024 + (lane & 7) * 128;
      asm volatile("global_load_dword %0, %1, off" : "+v"(touch) : "v"(tp) : "memory");
    }
  }
  if constexpr (ID == 2) {
    if (FT_W && lw < 2 * FT_W) {
      const char* tp = (const char*)((lw & 1) ? p.wu : p.wg) + (size_t)((lw >> 1) * 256 + idx * 8 + (lane >> 3)) * (F_ * 2) + xcd * 896 + ((lane & 7) < 7 ? (lane & 7) : 6) * 128;
      asm volatile("global_load_dword %0, %1, off" : "+v"(touch) : "v"(tp) : "memory");
    }
  }
  barrier();
  const unsigned tag = lds_tag(lds0);
  if constexpr (ID == 1) {
    asm volatile("s_waitcnt vmcnt(8)" : "+v"(ln));
#pragma unroll 1
    for (unsigned spins = 0; spins < (1u << 15) && !__all(ln.y == tag && ln.w == tag); ++spins) {
      __builtin_amdgcn_s_sleep(2);
      asm volatile("global_load_dwordx4 %0, %1, off sc1\n\ts_waitcnt vmcnt(0)" : "=&v"(ln) : "v"(lp) : "memory");
    }
    LAS unsigned* tb = (LAS unsigned*)(lds0 + F_TAB);
    tb[lw * 64 + lane + (lw < 2 ? 0 : 128)] = ln.x;
    if (lw < 2) tb[128 + lw * 64 + lane] = ln.z;
    wait_lgkm0();
  }
  barrier();
  barrier();
  if constexpr (ID != 1) {
    barrier();
    if (lw == 3) poll_tags((char*)p.flags + FL_PAIR + ((ID == 2 ? 0 : 256) + (blockIdx.x ^ 1)) * 128, 1, tag);
    barrier();
  }
}

template <int ID>
__device__ __forceinline__ void sq_compute(const FArgs& p, unsigned lds0, unsigned w, unsigned xcd, unsigned idx, unsigned tag) {
  constexpr int NT = nt_of<ID>();
  const unsigned ring = lds0 + F_RING;
  const unsigned g = w >> 2, wm = (w >> 1) & 1, wx = w & 1;
  const unsigned kh = xcd & 1, tm = (xcd >> 1) * 4 + (idx & 3), tn = idx >> 2;
  u32x4 res[2];
  unsigned rdA[8], rdB[4], hoff = 0;
  const unsigned rotA = (ID == 1) ? 2 * g : 2 * g + wx;
  {
    const int lane = lane_id(), fr = lane & 15, fq = lane >> 4, chunk = ((fq & 1) << 1) | (fq >> 1);
    const int row = tm * 128 + wm * 64 + rotA * 16 + fr, col = tn * 128 + kh * 32 + chunk * 8;
    if constexpr (ID == 2) {
#pragma unroll
      for (int c = 0; c < 2; ++c) res[c] = *(const u32x4*)((const u16*)p.x + (size_t)row * D_ + col + (c ^ wx) * 64);
    } else if constexpr (ID == 4) {
      hoff = ((row >> 4) * (D_ / 32) + (col >> 5)) * 1024 + (chunk * 16 + fr) * 16;
    }
    const unsigned rotB = (ID == 1) ? 0u : 2 * kh, base = ring + g * F_ST;
#pragma unroll
    for (int i = 0; i < 4; ++i) {
      const unsigned kk = i >> 1, hf = (i & 1) * 2;
      if constexpr (ID == 1) {
        rdA[i] = base + (wm * 64 + (hf ^ rotA) * 16 + fr) * 128 + (((kk * 4 + fq) ^ (fr >> 1)) << 4);
        rdB[i] = base + F_A + (wx * 64 + fr) * 128 + ((((i & 1) * 4 + fq) ^ (fr >> 1)) << 4);
      } else if constexpr (ID == 2) {
        rdA[i] = base + (wm * 64 + (i ^ rotA) * 16 + fr) * 128 + ((fq ^ (fr >> 1)) << 4);
        rdA[4 + i] = base + (wm * 64 + (i ^ rotA) * 16 + fr) * 128 + (((4 + fq) ^ (fr >> 1)) << 4);
        rdB[i] = base + F_A + wx * 8192 + (hf ^ rotB) * 2048 + lane * 16;
      } else {
        const int q = fr >> 2, pp = fr & 3, f = q | ((fq & 1) << 2);
        rdA[i] = base + wm * 8192 + (i ^ rotA) * 2048 + lane * 16;
        rdB[i] = base + F_A + (8 * fq + q) * 256 + (((wx * 4 + (i ^ rotB)) ^ f) << 5) + pp * 8;
      }
    }
  }
  constexpr int NVA = (ID == 2) ? 8 : 4, NVB = (ID <= 2) ? 2 : 4;
  bf16x8 fa[2][4], fb[2][4];
  f32x4 acc[4][4];
#pragma unroll
  for (int i = 0; i < 16; ++i) { acc[i >> 2][i & 3] = (f32x4){0.f, 0.f, 0.f, 0.f}; asm volatile("" : "+v"(acc[i >> 2][i & 3])); }
  auto reads = [&]() {
    SB();
#pragma unroll
    for (int s = 0; s < 2; ++s)
#pragma unroll
      for (int i = 0; i < 4; ++i) {
        if constexpr (ID == 1) {
          fb[s][i] = lds_b128(rdB[s] + i * 2048);
          fa[s][i] = lds_b128(rdA[s * 2 + (i >> 1)] + (i & 1) * 2048);
        } else if constexpr (ID == 2) {
          fb[s][i] = lds_b128(rdB[i >> 1] + (i & 1) * 2048 + s * 1024);
          fa[s][i] = lds_b128(rdA[s * 4 + i]);
        } else {
          fb[s][i] = lds_tr(rdB[i] + s * 8192, rdB[i] + s * 8192 + 1024);
          fa[s][i] = lds_b128(rdA[i] + s * 1024);
        }
      }
    SB();
  };
#pragma unroll
  for (int j = 0; j < NVA; ++j) asm volatile("" : "+v"(rdA[j]));
#pragma unroll
  for (int j = 0; j < NVB; ++j) asm volatile("" : "+v"(rdB[j]));
  if constexpr (ID != 1) {
    barrier();
    STAMP(ID, 0);
  }
  if constexpr (ID == 4) {
    const auto rh = OUT_RSRC(p.h, S_ * D_ * 2);
#pragma unroll
    for (int c = 0; c < 2; ++c) res[c] = __builtin_amdgcn_raw_buffer_load_b128(rh, hoff, (c ^ wx) * 2048, 16);
  }
  barrier();
  STAMP1_TAKE();
  if (g == 1) barrier();
  reads();
#pragma unroll 1
  for (unsigned i = 0; i < NT / 2; ++i) {
    const unsigned delta = (i & 1) ? (unsigned)(-2 * F_ST) : (unsigned)(2 * F_ST);
#pragma unroll
    for (int j = 0; j < NVA; ++j) { rdA[j] += delta; asm volatile("" : "+v"(rdA[j])); }
#pragma unroll
    for (int j = 0; j < NVB; ++j) { rdB[j] += delta; asm volatile("" : "+v"(rdB[j])); }
    wait_lgkm0();
    barrier();
    SB();
#pragma unroll
    for (int m = 0; m < 32; ++m) {
      const int n = (SNAKE && (m & 4)) ? 3 - (m & 3) : (m & 3);
      MFMA(fb[m >> 4][n], fa[m >> 4][(m >> 2) & 3], acc[(m >> 2) & 3][n]);
    }
    SB();
    barrier();
    if (i + 1 < NT / 2) reads();
  }
  mfma_drain();
  SB();
  const int lane = lane_id(), fr = lane & 15, fq = lane >> 4, chunk = ((fq & 1) << 1) | (fq >> 1);
  if constexpr (ID == 1) {
    const unsigned wb = ring + 2 * F_ST + (w ^ 4) * 8192 + lane * 16, rb = ring + 2 * F_ST + w * 8192 + lane * 16;
#pragma unroll
    for (int i = 0; i < 8; ++i) *(LAS f32x4*)(wb + i * 1024) = acc[2 + (i >> 2)][i & 3];
    if (g == 0) barrier();
    STAMP1_PUT(ID);
    STAMP(ID, 2);
    wait_lgkm0();
    barrier();
#pragma unroll
    for (int i = 0; i < 8; ++i) acc[i >> 2][i & 3] += *(const LAS f32x4*)(rb + i * 1024);
  }
  if constexpr (ID == 1) {
    const LAS float* tb = (const LAS float*)(lds0 + F_TAB);
    const float sg = tm < 8 ? 1.f : -1.f;
    const auto ro = OUT_RSRC(p.ab, D_ * 4096 * 2);
    f32x4 mean[4], rstd[4];
#pragma unroll
    for (int j = 0; j < 4; ++j) {
      mean[j] = *(const LAS f32x4*)(tb + wx * 64 + j * 16 + fq * 4);
      rstd[j] = *(const LAS f32x4*)(tb + 128 + wx * 64 + j * 16 + fq * 4) * sg;
    }
#pragma unroll
    for (int mb = 0; mb < 2; ++mb) {
      const float wsm = tb[256 + wm * 64 + g * 32 + mb * 16 + fr];
#pragma unroll
      for (int np = 0; np < 2; ++np) {
        const f32x4 x0 = (acc[mb][2 * np] - mean[2 * np] * wsm) * rstd[2 * np], x1 = (acc[mb][2 * np + 1] - mean[2 * np + 1] * wsm) * rstd[2 * np + 1];
        auto s0 = __builtin_amdgcn_permlane16_swap(pk_bf16(x0[0], x0[1]), pk_bf16(x1[0], x1[1]), false, false);
        auto s1 = __builtin_amdgcn_permlane16_swap(pk_bf16(x0[2], x0[3]), pk_bf16(x1[2], x1[3]), false, false);
        const int rblk = (tm & 7) * 8 + wm * 4 + g * 2 + mb;
        const int kblk = (tm >> 3) * 64 + (kh * 8 + tn) * 4 + wx * 2 + np;
        STORE16(ro, (rblk * 128 + kblk) * 1024 + (chunk * 16 + fr) * 16, ((u32x4){s0[0], s1[0], s0[1], s1[1]}));
      }
    }
    return;
  }
  const auto rs = OUT_RSRC((char*)p.xch + (ID == 2 ? 0 : XCH4), 256 * 16384);
  const unsigned sreg = ring + ((w >> 1) & 1) * F_ST + (w & 1) * 8192 + lane * 16, kreg = ring + 2 * F_ST + lane * 16;
  auto put_keep = [&]() {
#pragma unroll
    for (int j = 1; j < 4; ++j) {
      const unsigned d = kreg + ((wm * 4 + (j ^ rotA)) * 3 + (j - 1)) * 2048;
      *(LAS f32x4*)(d) = acc[j][0];
      *(LAS f32x4*)(d + 1024) = acc[j][1];
    }
  };
  if (g == 0) {
#pragma unroll
    for (int i = 0; i < 8; ++i) *(LAS f32x4*)(sreg + i * 1024) = acc[i >> 1][2 + (i & 1)];
    put_keep();
    wait_lgkm0();
    barrier();
  } else {
#pragma unroll
    for (int j = 0; j < 4; ++j) {
      const f32x4 x0 = acc[j][2] + *(const LAS f32x4*)(sreg + (j ^ 2) * 2048), x1 = acc[j][3] + *(const LAS f32x4*)(sreg + (j ^ 2) * 2048 + 1024);
      auto s0 = __builtin_amdgcn_permlane16_swap(pk_bf16(x0[0], x0[1]), pk_bf16(x1[0], x1[1]), false, false);
      auto s1 = __builtin_amdgcn_permlane16_swap(pk_bf16(x0[2], x0[3]), pk_bf16(x1[2], x1[3]), false, false);
      __builtin_amdgcn_raw_buffer_store_b128((u32x4){s0[0], s1[0], s0[1], s1[1]}, rs, lane * 16 + blockIdx.x * 16384 + ((wm * 4 + (j ^ rotA)) * 2 + wx) * 1024, 0, 16);
    }
    SB();
    put_keep();
    wait_lgkm0();
  }
  STAMP1_PUT(ID);
  STAMP(ID, 2);
  barrier();
  f32x4 y[2][2];
  {
    const unsigned kb = kreg + (wm * 4 + rotA) * 3 * 2048;
    f32x4 t[2][2];
#pragma unroll
    for (int i = 0; i < 2; ++i) {
      t[0][i] = acc[0][i] + *(const LAS f32x4*)(kb + 2048 + i * 1024);
      t[1][i] = *(const LAS f32x4*)(kb + i * 1024) + *(const LAS f32x4*)(kb + 4096 + i * 1024);
    }
#pragma unroll
    for (int c = 0; c < 2; ++c)
#pragma unroll
      for (int j = 0; j < 4; ++j) {
        auto s = __builtin_amdgcn_permlane16_swap(__float_as_uint(t[c][0][j]), __float_as_uint(t[c][1][j]), false, false);
        y[c][0][j] = __uint_as_float(s[0]) + ((j & 1) ? bf_hi(res[c][j >> 1]) : bf_lo(res[c][j >> 1]));
        y[c][1][j] = __uint_as_float(s[1]) + ((j & 1) ? bf_hi(res[c][2 + (j >> 1)]) : bf_lo(res[c][2 + (j >> 1)]));
      }
  }
  const int row0 = tm * 128 + wm * 64 + rotA * 16 + fr, col = tn * 128 + kh * 32 + chunk * 8;
  unsigned optr = (ID == 2) ? ((row0 >> 4) * (D_ / 32) + (col >> 5)) * 1024 + (chunk * 16 + fr) * 16 : (row0 * D_ + col) * 2;
  const auto ro = OUT_RSRC(ID == 2 ? p.h : p.out, S_ * D_ * 2);
  asm volatile("" : "+v"(y[0][0]), "+v"(y[0][1]), "+v"(y[1][0]), "+v"(y[1][1]), "+v"(optr));
  XT(0);
  wait_vm<0>();
  barrier();
  XT(1);
  if (w == 0 && lane == 0) put_tag((char*)p.flags + FL_PAIR + ((ID == 2 ? 0 : 256) + blockIdx.x) * 128, tag);
  barrier();
  XT(2);
  u32x4 in[2], v[2];
#pragma unroll
  for (int c = 0; c < 2; ++c) in[c] = __builtin_amdgcn_raw_buffer_load_b128(rs, lane * 16, (blockIdx.x ^ 1) * 16384 + ((wm * 4 + rotA) * 2 + (c ^ wx)) * 1024, 16);
#pragma unroll
  for (int c = 0; c < 2; ++c) {
    const f32x4 a = y[c][0] + (f32x4){bf_lo(in[c][0]), bf_hi(in[c][0]), bf_lo(in[c][1]), bf_hi(in[c][1])};
    const f32x4 b = y[c][1] + (f32x4){bf_lo(in[c][2]), bf_hi(in[c][2]), bf_lo(in[c][3]), bf_hi(in[c][3])};
    v[c] = (u32x4){pk_bf16(a[0], a[1]), pk_bf16(a[2], a[3]), pk_bf16(b[0], b[1]), pk_bf16(b[2], b[3])};
    __builtin_amdgcn_raw_buffer_store_b128(v[c], ro, optr + (c ^ wx) * (ID == 2 ? 2048 : 128), 0, ID == 2 ? ST_AUX : OUT_AUX);
  }
  SB();
  XT(3);
  SB();
  if constexpr (ID == 2) {
    float s = 0.f;
#pragma unroll
    for (int j = 0; j < 8; ++j) s += bf_lo(v[j >> 2][j & 3]) + bf_hi(v[j >> 2][j & 3]);
    const float m = sum_rows(s) * (1.0f / 64.0f);
    float q = 0.f;
#pragma unroll
    for (int j = 0; j < 8; ++j) {
      const float a = bf_lo(v[j >> 2][j & 3]) - m, b = bf_hi(v[j >> 2][j & 3]) - m;
      q += a * a + b * b;
    }
    const f32x2 st = {m, sum_rows(q)};
    if (fq == 0)
      __hip_atomic_store((unsigned long long*)p.stats + (size_t)(tn * 2 + kh) * S_ + row0, __builtin_bit_cast(unsigned long long, st), __ATOMIC_RELAXED, __HIP_MEMORY_SCOPE_AGENT);
  }
  SB();
  XT(4);
  XPUT(ID);
}

__device__ __forceinline__ void glu_loader(const FArgs& p, const Ld& L, Ld& N, unsigned lds0, unsigned lw, unsigned xcd, unsigned idx, unsigned& touch) {
  constexpr int NT = 32, NEXT = 4;
  constexpr bool PRE = true;
  const unsigned tag = lds_tag(lds0);
  const unsigned ring = lds0 + F_RING;
  LAS float* cst = (LAS float*)(lds0 + F_TAB + 2048);
  unsigned long long* gcs = (unsigned long long*)((char*)p.flags + FL_CS) + (xcd * 4 + (idx >> 3)) * (8 * 224);
  if (PRE) {
    ld_a<3>(L, ring, lw, 0); ld_a<3>(L, ring, lw, 1);
  } else {
    ld_a<3>(L, ring, lw, 0); ld_b<3>(L, ring, lw, 0); ld_a<3>(L, ring, lw, 1); ld_b<3>(L, ring, lw, 1);
  }
  const int lane = lane_id();
  const unsigned u = lw * 64 + lane < 224 ? lw * 64 + lane : 223u;
  wait_vm<(PRE ? 4 : 7)>();
  barrier();
  ld_a<3>(L, ring, lw, 2);
  ld_b<3>(L, ring, lw, 2);
#pragma unroll 1
  for (unsigned t = 0; t + 3 < NT; ++t) {
    if (t == 25) {
      const unsigned hf = u >= 112, c = u - hf * 112, wn_ = c >= 56, cc = c - wn_ * 56, blk = cc >> 4;
      const unsigned src = lds0 + (wn_ * 4 + blk) * 768 + (blk == 3 ? 512 + (hf * 8 + (cc & 7)) * 4 : hf * 256 + (cc & 15) * 4);
      const float v = (*(const LAS float*)(src) + *(const LAS float*)(src + 64)) + (*(const LAS float*)(src + 128) + *(const LAS float*)(src + 192));
      asm volatile("global_store_dwordx2 %0, %1, off sc1" ::"v"(gcs + L.cls * 224 + u), "v"(((unsigned long long)tag << 32) | __float_as_uint(v)) : "memory");
    }
    ld_a<3>(L, ring, lw, t + 3);
    barrier();
    ld_b<3>(L, ring, lw, t + 3);
    wait_vm<14>();
    barrier();
  }
  unsigned long long g[8];
#pragma unroll
  for (int i = 0; i < 8; ++i) asm volatile("global_load_dwordx2 %0, %1, off sc1" : "=v"(g[i]) : "v"(gcs + i * 224 + u) : "memory");
  barrier();
  if constexpr (NEXT != 0) { ld_b<NEXT>(N, ring, lw, 0); wait_vm<19>(); } else wait_vm<15>();
  barrier();
  barrier();
  if constexpr (NEXT != 0) { ld_b<NEXT>(N, ring, lw, 1); wait_vm<16>(); } else wait_vm<8>();
  barrier();
  barrier();
  if (!EARLY0) barrier();
  if constexpr (NEXT != 0) asm volatile("s_waitcnt vmcnt(8)" : "+v"(g[0]), "+v"(g[1]), "+v"(g[2]), "+v"(g[3]), "+v"(g[4]), "+v"(g[5]), "+v"(g[6]), "+v"(g[7]));
  else asm volatile("s_waitcnt vmcnt(0)" : "+v"(g[0]), "+v"(g[1]), "+v"(g[2]), "+v"(g[3]), "+v"(g[4]), "+v"(g[5]), "+v"(g[6]), "+v"(g[7]));
#pragma unroll 1
  for (unsigned spins = 0; spins < (1u << 15); ++spins) {
    unsigned tg = 0u;
#pragma unroll
    for (int i = 0; i < 8; ++i) tg |= (unsigned)(g[i] >> 32) ^ tag;
    if (__all(tg == 0u)) break;
    __builtin_amdgcn_s_sleep(2);
#pragma unroll
    for (int i = 0; i < 8; ++i) asm volatile("global_load_dwordx2 %0, %1, off sc1\n\ts_waitcnt vmcnt(0)" : "=&v"(g[i]) : "v"(gcs + i * 224 + u) : "memory");
  }
  float s = 0.f;
#pragma unroll
  for (int i = 0; i < 8; ++i) s += __uint_as_float((unsigned)g[i]);
  cst[u] = s;
  wait_lgkm0();
  if (NEXT != 0 && FT_D == 1) {
    const char* tp = (const char*)p.wd + ((size_t)blockIdx.x * 224 + (lw * 56 + (lane < 56 ? lane : 55))) * 128;
    asm volatile("global_load_dword %0, %1, off" : "+v"(touch) : "v"(tp) : "memory");
  }
  if (NEXT != 0 && FT_D == 2) {
    const char* tp = (const char*)p.wd + (size_t)((xcd & 1) * 1792 + idx * 16 + lw * 4 + (lane >> 4)) * (D_ * 2) + (lane & 15) * 128;
    asm volatile("global_load_dword %0, %1, off" : "+v"(touch) : "v"(tp) : "memory");
  }
  if (EARLY0) barrier();
  barrier();
}

__device__ __forceinline__ void glu_compute(const FArgs& p, unsigned lds0, unsigned w, unsigned xcd, unsigned idx) {
  constexpr int ID = 3, BM = 256, NT = 32, MB = 4, NB = 7;
  const unsigned ring = lds0 + F_RING;
  LAS f32x2* tab = (LAS f32x2*)(lds0 + F_TAB);
  LAS float* cst = (LAS float*)(lds0 + F_TAB + 2048);
  const unsigned tm = idx & 7, tn = xcd * 4 + (idx >> 3), cls = (tm + 2 * (idx >> 3)) & 7;
  const unsigned wm = w & 3, wn = w >> 2;
  const int lane = lane_id(), fr = lane & 15, fq = lane >> 4;
  const unsigned rot = wm == 3 ? 0u : wm;
  unsigned xa = ring + wm * 4096 + lane * 16, xb[3], xb1;
  {
    const int q = fr >> 2, pp = fr & 3, r = (fq >> 1) * 16 + (fq & 1) * 4 + q;
#pragma unroll
    for (int i = 0; i < 3; ++i) xb[i] = ring + F_A + r * 224 + wn * 112 + pp * 8 + ((i + rot) % 3) * 32;
    xb1 = ring + F_A + r * 224 + wn * 112 + 96 + (pp & 1) * 8 + (pp >> 1) * G3_UP;
  }
  float cs[3] = {0.f, 0.f, 0.f};
  bf16x8 fa[MB], fb[NB];
  f32x4 acc[MB][NB];
#pragma unroll
  for (int mb = 0; mb < MB; ++mb)
#pragma unroll
    for (int nb = 0; nb < NB; ++nb) { acc[mb][nb] = (f32x4){0.f, 0.f, 0.f, 0.f}; asm volatile("" : "+v"(acc[mb][nb])); }
  asm volatile("" : "+v"(xa), "+v"(xb[0]), "+v"(xb[1]), "+v"(xb[2]), "+v"(xb1));
  barrier();
  STAMP(ID, 0);
  if (wn == 1) {
    const auto rst = OUT_RSRC(p.stats, 8 * S_ * 16);
    if (ST_SLEEP) __builtin_amdgcn_s_sleep(ST_SLEEP);
    SB();
    f32x2 sv[16];
#pragma unroll
    for (int i = 0; i < 16; ++i) sv[i] = __builtin_bit_cast(f32x2, __builtin_amdgcn_raw_buffer_load_b64(rst, (tm * BM + wm * 64 + lane) * 8, i * S_ * 8, 16));
    SB();
    barrier();
    SB();
    float m = 0.f;
#pragma unroll
    for (int i = 0; i < 16; ++i) m += sv[i][0];
    m *= (1.0f / 16.0f);
    float q = 0.f;
#pragma unroll
    for (int i = 0; i < 16; ++i) {
      const float d = sv[i][0] - m;
      q += sv[i][1] + 64.0f * d * d;
    }
    tab[wm * 64 + lane] = (f32x2){m, q * (1.0f / D_) + EPS};
  }
  wait_lgkm0();
  barrier();
  STAMP1_TAKE();
  const unsigned csmask = (0x010101u << cls) | (1u << ((cls + 1) & 7));
  auto reads = [&]() {
    SB();
    fa[0] = lds_b128(xa);
#pragma unroll
    for (int j = 0; j < 7; ++j) {
      const unsigned o = j < 6 ? xb[j % 3] + (j / 3) * G3_UP : xb1;
      fb[j] = lds_tr(o, o + 1792);
    }
#pragma unroll
    for (int mb = 1; mb < MB; ++mb) fa[mb] = lds_b128(xa + mb * 1024);
    SB();
  };
  reads();
#pragma unroll 1
  for (unsigned t = 0; t < NT; ++t) {
    const unsigned delta = ((t & 3) == 3) ? (unsigned)(-3 * F_ST) : (unsigned)F_ST;
    xa += delta; xb[0] += delta; xb[1] += delta; xb[2] += delta; xb1 += delta;
    asm volatile("" : "+v"(xa), "+v"(xb[0]), "+v"(xb[1]), "+v"(xb[2]), "+v"(xb1));
    if (__builtin_expect((csmask >> t) & 1u, 0)) {
#pragma unroll
      for (int j = 0; j < 4; ++j)
#pragma unroll
        for (int i = 0; i < 3; ++i)
          asm volatile("v_dot2c_f32_bf16 %0, %1, %2" : "+v"(cs[i]) : "s"(0x3f803f80u), "v"(__builtin_bit_cast(u32x4, fb[3 * i])[j]));
      asm volatile("s_mov_b32 m0, %3\n\ts_nop 2\n\tds_write_addtid_b32 %0\n\tds_write_addtid_b32 %1 offset:256\n\tds_write_addtid_b32 %2 offset:512" ::"v"(cs[0]),
                   "v"(cs[1]), "v"(cs[2]), "s"(lds0 + w * 768)
                   : "memory");
    }
    wait_lgkm0();
    barrier();
    SB();
#pragma unroll
    for (int mb = 0; mb < MB; ++mb)
#pragma unroll
      for (int i = 0; i < NB; ++i) {
        const int nb = (SNAKE && (mb & 1)) ? NB - 1 - i : i;
        MFMA(fb[nb], fa[mb], acc[mb][nb]);
      }
    SB();
    barrier();
    reads();
  }
  if (!EARLY0 && wn == 0) barrier();
  mfma_drain();
  SB();
  const int lane2 = lane_id();
  const int gr = lane2 & 15, gq = lane2 >> 4;
  STAMP1_PUT(ID);
  STAMP(ID, 2);
  auto act2 = [](f32x2 a, f32x2 b, float sc, float k) {
    const f32x2 t = a * sc;
    const f32x2 e = {__builtin_amdgcn_exp2f(t.x), __builtin_amdgcn_exp2f(t.y)};
    const f32x2 d = e * k + k;
    const f32x2 r = {__builtin_amdgcn_rcpf(d.x), __builtin_amdgcn_rcpf(d.y)};
    const f32x2 o = (a * b) * r;
    return pk_bf16(o.x, o.y);
  };
  const auto ro = OUT_RSRC(p.gu, S_ * F_ * 2);
  float nm[MB], kk[MB], sc[MB];
#pragma unroll
  for (int mb = 0; mb < MB; ++mb) {
    const f32x2 st = tab[wm * 64 + mb * 16 + gr];
    nm[mb] = -st.x; kk[mb] = st.y; sc[mb] = -1.44269504089f * __builtin_amdgcn_rsqf(st.y);
  }
  const int gch0 = tn * 14 + wn * 7;
  const unsigned obase = (tm * (BM / 16) + wm * MB) * (F_ / 32) * 1024 + gr * 16;
#pragma unroll
  for (int i = 0; i < 3; ++i) {
    const unsigned ib = (i + rot) % 3;
    const f32x4 cg = *(const LAS f32x4*)(cst + wn * 56 + ib * 16 + gq * 4), cu = *(const LAS f32x4*)(cst + 112 + wn * 56 + ib * 16 + gq * 4);
    const unsigned oo = obase + (gq & 1) * (F_ / 32) * 1024 + (gch0 + 2 * ib + (gq >> 1)) * 256;
#pragma unroll
    for (int mp = 0; mp < MB / 2; ++mp) {
      u32x2 pkd[2];
#pragma unroll
      for (int h = 0; h < 2; ++h) {
        const int mb = 2 * mp + h;
        const f32x4 a = cg * nm[mb] + acc[mb][i], b = cu * nm[mb] + acc[mb][3 + i];
        pkd[h] = (u32x2){act2(a.xy, b.xy, sc[mb], kk[mb]), act2(a.zw, b.zw, sc[mb], kk[mb])};
      }
      auto s0 = __builtin_amdgcn_permlane16_swap(pkd[0].x, pkd[1].x, false, false);
      auto s1 = __builtin_amdgcn_permlane16_swap(pkd[0].y, pkd[1].y, false, false);
      __builtin_amdgcn_raw_buffer_store_b128((u32x4){s0[0], s1[0], s0[1], s1[1]}, ro, oo + mp * 2 * (F_ / 32) * 1024, 0, ST_AUX);
    }
  }
  {
    const f32x4 cm = *(const LAS f32x4*)(cst + (gq >> 1) * 112 + wn * 56 + 48 + (gq & 1) * 4);
    u32x2 pkd[MB / 2];
#pragma unroll
    for (int mp = 0; mp < MB / 2; ++mp) {
      const f32x4 v0 = cm * nm[2 * mp] + acc[2 * mp][6], v1 = cm * nm[2 * mp + 1] + acc[2 * mp + 1][6];
      f32x4 a, b;
#pragma unroll
      for (int j = 0; j < 4; ++j) {
        auto s = __builtin_amdgcn_permlane32_swap(__float_as_uint(v0[j]), __float_as_uint(v1[j]), false, false);
        a[j] = __uint_as_float(s[0]); b[j] = __uint_as_float(s[1]);
      }
      const float scs = (gq & 2) ? sc[2 * mp + 1] : sc[2 * mp], ks = (gq & 2) ? kk[2 * mp + 1] : kk[2 * mp];
      pkd[mp] = (u32x2){act2(a.xy, b.xy, scs, ks), act2(a.zw, b.zw, scs, ks)};
    }
    auto s0 = __builtin_amdgcn_permlane16_swap(pkd[0].x, pkd[1].x, false, false);
    auto s1 = __builtin_amdgcn_permlane16_swap(pkd[0].y, pkd[1].y, false, false);
    STORE16(ro, obase + ((gq & 1) * 2 + (gq >> 1)) * (F_ / 32) * 1024 + (gch0 + 6) * 256, ((u32x4){s0[0], s1[0], s0[1], s1[1]}));
  }
}

__global__ __launch_bounds__(768, 3) void k_block(FArgs p) {
  CODE_LABEL(fz_code_f);
  extern __shared__ __attribute__((aligned(16))) char shm[];
  const unsigned lds0 = (unsigned)(size_t)(LAS char*)shm;
  const unsigned w = rfl(threadIdx.x >> 6);
  const unsigned xcd = blockIdx.x & 7, idx = blockIdx.x >> 3;
  char* fl = (char*)p.flags;
  const unsigned tm = (xcd >> 1) * 4 + (idx & 3), kh = xcd & 1, lj = idx >> 2;
  if (w >= 8) {
    const unsigned lw = w - 8;
    Ld LA, LB;
    unsigned touch = 0;
    LA = ld_setup<1>(p, lw, xcd, idx);
    sq_loader<1>(p, LA, LB, lds0, lw, xcd, idx, touch);
    const unsigned tag = lds_tag(lds0);
    LA = ld_setup<3>(p, lw, xcd, idx);
    ld_pin(LA);
    barrier();
    if (lw == 3) poll_tags(fl + FL_P1 + ((kh * 8 + lj) * 4 + (xcd >> 1)) * 128, 16, tag);
    barrier();
    sq_loader<2>(p, LB, LA, lds0, lw, xcd, idx, touch);
    LB = ld_setup<4>(p, lw, xcd, idx);
    ld_pin(LB);
    barrier();
    if (lw == 3) poll_tags(fl + FL_P2 + ((idx & 7) * 8 + xcd) * 128, 32, tag);
    barrier();
    glu_loader(p, LA, LB, lds0, lw, xcd, idx, touch);
    barrier();
    if (lw == 3) poll_tags(fl + FL_P3 + ((tm >> 1) * 2 + kh) * 128, 16, tag);
    barrier();
    sq_loader<4>(p, LB, LA, lds0, lw, xcd, idx, touch);
    asm volatile("" ::"v"(touch));
    return;
  }
  STAMP(1, 0);
  unsigned csink = 0;
  if (CODE_PF && w == 0 && idx < 3) {
    unsigned long long a;
    CODE_ADDR(a, fz_code_f);
    touch_code(a + idx * 8192, lane_id(), idx == 2 ? CODE_LAST : 63, csink);
  }
  unsigned tag;
  {
    const int lane = lane_id();
    const unsigned r0 = blockIdx.x * 8 + w;
    const u16* xp = (const u16*)p.x + (size_t)r0 * D_ + lane * 8;
    const u16* wp = (const u16*)(r0 < 1024 ? p.dre : p.dim) + (size_t)(r0 & 1023) * D_ + lane * 8;
    u32x4 xv[2], wv[2];
#pragma unroll
    for (int i = 0; i < 2; ++i) xv[i] = (LN_NT & 1) ? __builtin_nontemporal_load((const u32x4*)(xp + i * 512)) : *(const u32x4*)(xp + i * 512);
#pragma unroll
    for (int i = 0; i < 2; ++i) wv[i] = (LN_NT & 2) ? __builtin_nontemporal_load((const u32x4*)(wp + i * 512)) : *(const u32x4*)(wp + i * 512);
    const unsigned tv = __hip_atomic_load((const unsigned*)(fl + FL_EPOCH), __ATOMIC_RELAXED, __HIP_MEMORY_SCOPE_AGENT) + 1u;
    SB();
    float s = 0.f, t = 0.f;
#pragma unroll
    for (int j = 0; j < 8; ++j) s += bf_lo(xv[j >> 2][j & 3]) + bf_hi(xv[j >> 2][j & 3]);
    const float m = wave_sum(s) * (1.0f / D_);
    float q = 0.f;
#pragma unroll
    for (int j = 0; j < 8; ++j) {
      const float a = bf_lo(xv[j >> 2][j & 3]) - m, b = bf_hi(xv[j >> 2][j & 3]) - m;
      q += a * a + b * b;
      t += bf_lo(wv[j >> 2][j & 3]) + bf_hi(wv[j >> 2][j & 3]);
    }
    const float rs = __builtin_amdgcn_rsqf(wave_sum(q) * (1.0f / D_) + EPS);
    t = wave_sum(t);
    const float val = (lane & 2) ? t : ((lane & 1) ? rs : m);
    if (lane < 4)
      __hip_atomic_store((unsigned long long*)(fl + FL_LN + ((lane >> 1) * 2048 + r0) * 16 + (lane & 1) * 8), ((unsigned long long)tv << 32) | __float_as_uint(val),
                         __ATOMIC_RELAXED, __HIP_MEMORY_SCOPE_AGENT);
    tag = rfl(tv);
    if (w == 0) { *(volatile LAS unsigned*)(lds0 + F_TAG) = tag; wait_lgkm0(); }
  }
  asm volatile("" ::"v"(csink));
  STAMPX(1, 5);
  sq_compute<1>(p, lds0, w, xcd, idx, tag);
  wait_vm<0>();
  barrier();
  if (w == 0) { const int l = lane_id(); if (l < 4) put_tag(fl + FL_P1 + (tm * 4 + l) * 128 + (kh * 8 + lj) * 4, tag); }
  STAMP(1, 3);
  sq_compute<2>(p, lds0, w, xcd, idx, tag);
  wait_vm<0>();
  barrier();
  if (w == 0) { const int l = lane_id(); if (l < 8) put_tag(fl + FL_P2 + ((tm >> 1) * 8 + l) * 128 + (((tm & 1) * 2 + kh) * 8 + lj) * 4, tag); }
  STAMP(2, 3);
  glu_compute(p, lds0, w, xcd, idx);
  wait_vm<0>();
  barrier();
  if (w == 0 && lane_id() == 0) put_tag(fl + FL_P3 + ((idx & 7) * 2 + (xcd >> 2)) * 128 + ((xcd & 3) * 4 + (idx >> 3)) * 4, tag);
  STAMP(3, 3);
  sq_compute<4>(p, lds0, w, xcd, idx, tag);
  if (blockIdx.x == 0 && w == 0 && lane_id() == 0) put_tag(fl + FL_EPOCH, tag);
  STAMP_END(4);
}
}

extern "C" void kernel_launch(void* const* d_in, const int* in_sizes, int n_in, void* d_out, int out_size, void* d_ws, size_t ws_size,
                              hipStream_t stream) {
  char* ws = (char*)d_ws;
  FArgs f = {d_in[3], d_in[4], d_in[1], d_in[2], d_in[0], d_in[6], d_in[7], d_in[8], ws + WS_AB, ws + WS_H, ws + WS_ST, ws + WS_GU, d_out, ws + WS_XC, ws + WS_FL};
  (void)hipFuncSetAttribute((const void*)k_block, hipFuncAttributeMaxDynamicSharedMemorySize, F_LDS);
  hipLaunchKernelGGL(k_block, dim3(256), dim3(768), F_LDS, stream, f);
}
```

```cpp
#include <hip/hip_runtime.h>
#include <hip/hip_bf16.h>

namespace {
#define LAS __attribute__((address_space(3)))
typedef unsigned short u16;
typedef short s16x4 __attribute__((ext_vector_type(4)));
typedef short s16x8 __attribute__((ext_vector_type(8)));
typedef __bf16 bf16x8 __attribute__((ext_vector_type(8)));
typedef __bf16 bf16x2 __attribute__((ext_vector_type(2)));
typedef float f32x4 __attribute__((ext_vector_type(4)));
typedef float f32x2 __attribute__((ext_vector_type(2)));
typedef unsigned u32x4 __attribute__((ext_vector_type(4)));
typedef unsigned u32x2 __attribute__((ext_vector_type(2)));

constexpr int S_ = 2048, D_ = 1024, F_ = 3584;
constexpr float EPS = 1e-5f;

constexpr size_t WS_AB = 0;
constexpr size_t WS_H = WS_AB + (size_t)D_ * 2 * S_ * 2;
constexpr size_t WS_GU = WS_H + (size_t)S_ * D_ * 2;
constexpr size_t WS_ST = WS_GU + (size_t)S_ * F_ * 2;
constexpr size_t WS_XC = WS_ST + (size_t)S_ * 16 * 8;
constexpr size_t WS_FL = WS_XC + (size_t)2 * 256 * 16384;
constexpr int FL_EPOCH = 0;
constexpr int FL_PAIR = 128;
constexpr int FL_P1 = FL_PAIR + 512 * 128;
constexpr int FL_P2 = FL_P1 + 64 * 128;
constexpr int FL_P3 = FL_P2 + 64 * 128;
constexpr int FL_LN = FL_P3 + 16 * 128;
constexpr int FL_CS = FL_LN + 4096 * 16;

__device__ __forceinline__ unsigned rfl(unsigned v) { return __builtin_amdgcn_readfirstlane(v); }
__device__ __forceinline__ unsigned pk_bf16(float lo, float hi) {
  bf16x2 c = {(__bf16)lo, (__bf16)hi};
  return __builtin_bit_cast(unsigned, c);
}
__device__ __forceinline__ float bf_lo(unsigned v) { return __uint_as_float(v << 16); }
__device__ __forceinline__ float bf_hi(unsigned v) { return __uint_as_float(v & 0xffff0000u); }

__device__ __forceinline__ u32x4 make_rsrc(const void* p, unsigned bytes) {
  unsigned long long a = (unsigned long long)p;
  u32x4 r;
  r.x = (unsigned)a;
  r.y = (unsigned)(a >> 32) & 0xffffu;
  r.z = bytes;
  r.w = 0x00020000u;
  return r;
}
template <int N>
__device__ __forceinline__ void wait_vm() {
  asm volatile("s_waitcnt vmcnt(%0)" ::"i"(N) : "memory");
}
__device__ __forceinline__ void wait_lgkm0() { __builtin_amdgcn_s_waitcnt(0xC07F); }
__device__ __forceinline__ void barrier() {
  asm volatile("" ::: "memory");
  __builtin_amdgcn_s_barrier();
  asm volatile("" ::: "memory");
}
__device__ __forceinline__ bf16x8 lds_b128(unsigned addr) { return *(const LAS bf16x8*)(addr); }
__device__ __forceinline__ bf16x8 lds_tr(unsigned a0, unsigned a1) {
  s16x4 lo = __builtin_amdgcn_ds_read_tr16_b64_v4i16((LAS s16x4*)(a0));
  s16x4 hi = __builtin_amdgcn_ds_read_tr16_b64_v4i16((LAS s16x4*)(a1));
  s16x8 f = __builtin_shufflevector(lo, hi, 0, 1, 2, 3, 4, 5, 6, 7);
  return __builtin_bit_cast(bf16x8, f);
}

#define ST_AUX 16
#define OUT_RSRC(ptr, bytes) __builtin_amdgcn_make_buffer_rsrc((void*)(ptr), 0, (bytes), 0x00020000)
#define STORE16(rs, byteoff, v) __builtin_amdgcn_raw_buffer_store_b128(v, rs, byteoff, 0, ST_AUX)

#define DMA4(M) asm volatile("s_mov_b32 m0, %0\n\ts_nop 4\n\tbuffer_load_dwordx4 %1, %5, %6 offen " M " lds\n\tbuffer_load_dwordx4 %2, %5, %6 offen offset:1024 " M " lds\n\t" \
                 "buffer_load_dwordx4 %3, %5, %6 offen offset:2048 " M " lds\n\tbuffer_load_dwordx4 %4, %5, %6 offen offset:3072 " M " lds" ::"s"(ldsaddr), \
                 "v"(v[0]), "v"(v[1]), "v"(v[2]), "v"(v[3]), "s"(rsrc), "s"(soff) : "memory")
#define DMA3(M) asm volatile("s_mov_b32 m0, %0\n\ts_nop 4\n\tbuffer_load_dwordx4 %1, %4, %5 offen " M " lds\n\tbuffer_load_dwordx4 %2, %4, %5 offen offset:1024 " M " lds\n\t" \
                 "buffer_load_dwordx4 %3, %4, %5 offen offset:2048 " M " lds" ::"s"(ldsaddr), "v"(v[0]), "v"(v[1]), "v"(v[2]), "s"(rsrc), "s"(soff) : "memory")
#define DMAB(M) asm volatile("s_mov_b32 m0, %0\n\ts_waitcnt vmcnt(8)\n\ts_barrier\n\tbuffer_load_dwordx4 %1, %5, %6 offen " M " lds\n\tbuffer_load_dwordx4 %2, %5, %6 offen offset:1024 " M " lds\n\t" \
                 "buffer_load_dwordx4 %3, %5, %6 offen offset:2048 " M " lds\n\tbuffer_load_dwordx4 %4, %5, %6 offen offset:3072 " M " lds" ::"s"(ldsaddr), \
                 "v"(v[0]), "v"(v[1]), "v"(v[2]), "v"(v[3]), "s"(rsrc), "s"(soff) : "memory")
template <int N, int MOD = 0>
__device__ __forceinline__ void dma_grp(unsigned ldsaddr, const unsigned (&v)[4], u32x4 rsrc, unsigned soff) {
  if constexpr (N == 4) { if constexpr (MOD == 1) DMA4("sc1"); else DMA4(""); }
  else if constexpr (N == 3) { if constexpr (MOD == 1) DMA3("sc1"); else DMA3(""); }
  else { if constexpr (MOD == 1) DMAB("sc1"); else DMAB(""); }
}
__device__ __forceinline__ float sum_rows(float x) {
  auto a = __builtin_amdgcn_permlane16_swap(__float_as_uint(x), __float_as_uint(x), false, false);
  x = __uint_as_float(a[0]) + __uint_as_float(a[1]);
  auto b = __builtin_amdgcn_permlane32_swap(__float_as_uint(x), __float_as_uint(x), false, false);
  return __uint_as_float(b[0]) + __uint_as_float(b[1]);
}
template <int CTRL> __device__ __forceinline__ float dpp_add(float x) { return x + __uint_as_float(__builtin_amdgcn_mov_dpp(__float_as_uint(x), CTRL, 0xF, 0xF, true)); }
__device__ __forceinline__ float wave_sum(float x) { return sum_rows(dpp_add<0x140>(dpp_add<0x141>(dpp_add<0x4E>(dpp_add<0xB1>(x))))); }
__device__ __forceinline__ int lane_id() {
  int l;
  asm volatile("v_mbcnt_lo_u32_b32 %0, -1, 0\n\tv_mbcnt_hi_u32_b32 %0, -1, %0" : "=v"(l));
  return l;
}
__device__ __forceinline__ void mfma_drain() { asm volatile("s_nop 15" ::: "memory"); }
template <int NC, int NT>
__device__ __forceinline__ unsigned stage_of(unsigned n, unsigned c) {
  const unsigned t = ((n & (NC - 1)) == c) ? n + NC : n;
  return rfl(t >= NT ? t - NT : t);
}
#define SB() __builtin_amdgcn_sched_barrier(0)
#define MFMA(b, a, c) asm volatile("v_mfma_f32_16x16x32_bf16 %0, %1, %2, %0" : "+v"(c) : "v"(b), "v"(a))

struct FArgs {
  const void *dre, *dim, *cre, *cim, *x, *wg, *wu, *wd;
  void *ab, *h, *stats, *gu, *out, *xch, *flags;
};
constexpr unsigned CODE_LINES = 156;
constexpr int F_RING = 6144, F_ST = 32768, F_TAB = F_RING + 4 * F_ST, F_TAG = F_TAB + 4096, F_LDS = F_TAB + 4160, F_A = 16384, G3_UP = 7168 + 16;

__device__ __forceinline__ void poll_tags(const void* line, int n, unsigned tag) {
  const int l = lane_id();
  const unsigned* q = (const unsigned*)line + (l < n ? l : 0);
  unsigned spins = 0;
  while (!__all(__hip_atomic_load(q, __ATOMIC_RELAXED, __HIP_MEMORY_SCOPE_AGENT) == tag) && ++spins < (1u << 17)) __builtin_amdgcn_s_sleep(1);
}
__device__ __forceinline__ void put_tag(void* word, unsigned tag) { __hip_atomic_store((unsigned*)word, tag, __ATOMIC_RELAXED, __HIP_MEMORY_SCOPE_AGENT); }
__device__ __forceinline__ unsigned lds_tag(unsigned lds0) { return rfl(*(volatile LAS unsigned*)(lds0 + F_TAG)); }
template <int ID> constexpr int nt_of() { return ID == 1 ? 16 : (ID == 4 ? 28 : 32); }
template <int ID> constexpr int nc_of() { return (ID == 1 || ID == 4) ? 4 : 8; }
template <int ID> constexpr int mod_a() { return ID > 2; }
template <int ID> constexpr int mod_b() { return ID == 2; }
struct Ld {
  u32x4 qa, qb;
  unsigned va[4], vb[4], cls;
};
template <int ID>
__device__ __forceinline__ Ld ld_setup(const FArgs& p, unsigned lw, unsigned xcd, unsigned idx) {
  const int lane = lane_id();
  Ld L;
  if constexpr (ID == 3) {
    const unsigned tm = idx & 7, tn = xcd * 4 + (idx >> 3);
    L.qa = make_rsrc((const u16*)p.h + (size_t)tm * 256 * D_, 256 * D_ * 2);
    L.qb = make_rsrc(lw < 2 ? p.wg : p.wu, (unsigned)D_ * F_ * 2);
#pragma unroll
    for (int i = 0; i < 4; ++i) {
      L.va[i] = (lw * 4 + i) * (D_ / 32) * 1024 + lane * 16 - i * 1024;
      const int c = ((lw & 1) * 4 + i) * 64 + lane, r = c / 14, pos = c % 14;
      const int k = (r & 0x13) | ((r & 4) << 1) | ((r & 8) >> 1);
      L.vb[i] = k * F_ * 2 + tn * 224 + pos * 16 - i * 1024;
    }
    L.cls = (tm + 2 * (idx >> 3)) & 7;
  } else {
    const unsigned li = idx & 3, lj = idx >> 2, kh = xcd & 1, tm = (xcd >> 1) * 4 + li, tn = lj;
    if constexpr (ID == 1) {
      L.qa = make_rsrc((const u16*)(tm < 8 ? p.dre : p.dim) + (size_t)(tm & 7) * 128 * D_, 128 * D_ * 2);
      L.qb = make_rsrc((const u16*)p.x + (size_t)(kh * 8 + lj) * 128 * D_, 128 * D_ * 2);
    } else if constexpr (ID == 2) {
      L.qa = make_rsrc((const u16*)(kh ? p.cim : p.cre) + (size_t)tm * 128 * S_, 128 * S_ * 2);
      L.qb = make_rsrc((const u16*)p.ab + (size_t)tn * 128 * 4096 + kh * 64 * 512, 128 * 4096 * 2 - kh * 64 * 1024);
    } else {
      L.qa = make_rsrc((const u16*)p.gu + (size_t)tm * 128 * F_ + kh * 56 * 512, 128 * F_ * 2 - kh * 56 * 1024);
      L.qb = make_rsrc((const u16*)p.wd + (size_t)kh * 1792 * D_, 1792 * D_ * 2);
    }
#pragma unroll
    for (int i = 0; i < 4; ++i) {
      const int pc = lw * 4 + i;
      if constexpr (ID == 1) {
        const int row = pc * 8 + (lane >> 3);
        L.va[i] = L.vb[i] = row * D_ * 2 + (((lane & 7) ^ ((row >> 1) & 7)) << 4) - i * 1024;
      } else if constexpr (ID == 2) {
        const int row = pc * 8 + (lane >> 3);
        L.va[i] = row * S_ * 2 + (((lane & 7) ^ ((row >> 1) & 7)) << 4) - i * 1024;
        L.vb[i] = ((pc >> 1) * 128 + (pc & 1)) * 1024 + lane * 16 - i * 1024;
      } else {
        const int k = pc * 4 + (lane >> 4), f = (k & 3) | (((k >> 3) & 1) << 2);
        L.va[i] = ((pc >> 1) * (F_ / 32) + (pc & 1)) * 1024 + lane * 16 - i * 1024;
        L.vb[i] = k * D_ * 2 + tn * 256 + (((lane & 15) ^ (f << 1)) << 4) - i * 1024;
      }
    }
    L.cls = (nc_of<ID>() == 8) ? ((lj + 2 * li) & 7) : ((lj + li) & 3);
  }
  return L;
}
__device__ __forceinline__ void ld_pin(Ld& L) {
  asm volatile("" : "+v"(L.va[0]), "+v"(L.va[1]), "+v"(L.va[2]), "+v"(L.va[3]), "+v"(L.vb[0]), "+v"(L.vb[1]), "+v"(L.vb[2]), "+v"(L.vb[3]));
}
template <int ID>
__device__ __forceinline__ void ld_a(const Ld& L, unsigned ring, unsigned lw, unsigned n) {
  const unsigned st = stage_of<nc_of<ID>(), nt_of<ID>()>(n, L.cls);
  dma_grp<4, mod_a<ID>()>(ring + (n & 3) * F_ST + lw * 4096, L.va, L.qa, st * (ID <= 2 ? 128 : (ID == 3 ? 1024 : 2048)));
}
template <int ID>
__device__ __forceinline__ void ld_b(const Ld& L, unsigned ring, unsigned lw, unsigned n) {
  const unsigned st = stage_of<nc_of<ID>(), nt_of<ID>()>(n, L.cls);
  const unsigned dst = ring + (n & 3) * F_ST + F_A;
  if constexpr (ID == 3) {
    const unsigned d = dst + (lw >> 1) * G3_UP + (lw & 1) * 4096;
    if (lw & 1) dma_grp<3, mod_b<ID>()>(d, L.vb, L.qb, st * (32 * F_ * 2));
    else dma_grp<4, mod_b<ID>()>(d, L.vb, L.qb, st * (32 * F_ * 2));
  } else {
    dma_grp<4, mod_b<ID>()>(dst + lw * 4096, L.vb, L.qb, st * (ID == 1 ? 128 : (ID == 2 ? 2048 : 64 * D_ * 2)));
  }
}

template <int ID> __device__ __forceinline__ void ld_const(const Ld& L, unsigned ring, unsigned lw, unsigned n) { if constexpr (ID == 2) ld_a<ID>(L, ring, lw, n); else ld_b<ID>(L, ring, lw, n); }
template <int ID> __device__ __forceinline__ void ld_dep(const Ld& L, unsigned ring, unsigned lw, unsigned n) { if constexpr (ID == 2) ld_b<ID>(L, ring, lw, n); else ld_a<ID>(L, ring, lw, n); }

template <int ID>
__device__ __forceinline__ void sq_loader(const FArgs& p, const Ld& L, Ld& N, unsigned lds0, unsigned lw, unsigned xcd, unsigned idx, unsigned& touch) {
  constexpr int NT = nt_of<ID>(), NEXT = ID == 4 ? 0 : ID + 1;
  const unsigned ring = lds0 + F_RING;
  if constexpr (ID == 1) {
    ld_a<ID>(L, ring, lw, 0); ld_b<ID>(L, ring, lw, 0);
    N = ld_setup<NEXT>(p, lw, xcd, idx); ld_pin(N);
    ld_a<ID>(L, ring, lw, 1); ld_b<ID>(L, ring, lw, 1);
  } else {
    ld_dep<ID>(L, ring, lw, 0); ld_dep<ID>(L, ring, lw, 1);
  }
  constexpr unsigned KA = ID <= 2 ? 128 : 2048, KB = ID == 1 ? 128 : (ID == 2 ? 2048 : 64 * D_ * 2);
  unsigned sa, sb, ma;
  auto prep = [&](unsigned n) {
    const unsigned st = stage_of<nc_of<ID>(), NT>(n, L.cls);
    sa = st * KA; sb = st * KB; ma = ring + (n & 3) * F_ST + lw * 4096;
    asm volatile("" : "+s"(sa), "+s"(sb), "+s"(ma));
  };
  auto bar_a = [&]() {
    dma_grp<5, mod_a<ID>()>(ma, L.va, L.qa, sa);
  };
  prep(2);
  wait_vm<(ID == 1 ? 8 : 4)>();
  bar_a();
  unsigned mb_ = ma + F_A, sb_ = sb;
  prep(3);
#pragma unroll 1
  for (unsigned n = 0; n + 3 < NT; ++n) {
    dma_grp<4, mod_b<ID>()>(mb_, L.vb, L.qb, sb_);
    bar_a();
    mb_ = ma + F_A; sb_ = sb;
    asm volatile("" : "+s"(mb_), "+s"(sb_));
    prep(n + 4);
  }
  dma_grp<4, mod_b<ID>()>(mb_, L.vb, L.qb, sb_);
  wait_vm<8>();
  barrier();
  const int lane = lane_id();
  u32x4 ln;
  const char* lp = (const char*)p.flags + FL_LN + (lw < 2 ? (((xcd & 1) * 8 + (idx >> 2)) * 128 + lw * 64 + lane) : (2048 + ((xcd >> 1) * 4 + (idx & 3)) * 128 + (lw - 2) * 64 + lane)) * 16;
  if constexpr (ID == 1) asm volatile("global_load_dwordx4 %0, %1, off sc1" : "=v"(ln) : "v"(lp) : "memory");
  if constexpr (NEXT != 0) { ld_const<NEXT>(N, ring, lw, 0); wait_vm<(ID == 1 ? 5 : 3)>(); } else wait_vm<0>();
  barrier();
  if constexpr (NEXT != 0) ld_const<NEXT>(N, ring, lw, 1);
  if constexpr (ID == 1) {
    if (lw < 2) {
      const char* tp = (const char*)((xcd & 1) ? p.cim : p.cre) + (size_t)((xcd >> 1) * 512 + idx * 16 + lw * 8 + (lane >> 3)) * (S_ * 2) + (lane & 7) * 128;
      asm volatile("global_load_dword %0, %1, off" : "+v"(touch) : "v"(tp) : "memory");
    }
  }
  if constexpr (ID == 2) {
    if (lw < 2) {
      const char* tp = (const char*)((lw & 1) ? p.wu : p.wg) + (size_t)(idx * 8 + (lane >> 3)) * (F_ * 2) + xcd * 896 + ((lane & 7) < 7 ? (lane & 7) : 6) * 128;
      asm volatile("global_load_dword %0, %1, off" : "+v"(touch) : "v"(tp) : "memory");
    }
  }
  barrier();
  const unsigned tag = lds_tag(lds0);
  if constexpr (ID == 1) {
    asm volatile("s_waitcnt vmcnt(8)" : "+v"(ln));
#pragma unroll 1
    for (unsigned spins = 0; spins < (1u << 15) && !__all(ln.y == tag && ln.w == tag); ++spins) {
      __builtin_amdgcn_s_sleep(2);
      asm volatile("global_load_dwordx4 %0, %1, off sc1\n\ts_waitcnt vmcnt(0)" : "=&v"(ln) : "v"(lp) : "memory");
    }
    LAS unsigned* tb = (LAS unsigned*)(lds0 + F_TAB);
    tb[lw * 64 + lane + (lw < 2 ? 0 : 128)] = ln.x;
    if (lw < 2) tb[128 + lw * 64 + lane] = ln.z;
    wait_lgkm0();
  }
  barrier();
  barrier();
  if constexpr (ID != 1) {
    barrier();
    if (lw == 3) poll_tags((char*)p.flags + FL_PAIR + ((ID == 2 ? 0 : 256) + (blockIdx.x ^ 1)) * 128, 1, tag);
    barrier();
  }
}

template <int ID>
__device__ __forceinline__ void sq_compute(const FArgs& p, unsigned lds0, unsigned w, unsigned xcd, unsigned idx, unsigned& tag) {
  constexpr int NT = nt_of<ID>();
  u32x4 xv[2], wv[2];
  unsigned tv = 0;
  const unsigned r0 = blockIdx.x * 8 + w;
  char* fl = (char*)p.flags;
  if constexpr (ID == 1) {
    const int lane = lane_id();
    const u16* xp = (const u16*)p.x + (size_t)r0 * D_ + lane * 8;
    const u16* wp = (const u16*)(r0 < 1024 ? p.dre : p.dim) + (size_t)(r0 & 1023) * D_ + lane * 8;
#pragma unroll
    for (int i = 0; i < 2; ++i) xv[i] = *(const u32x4*)(xp + i * 512);
#pragma unroll
    for (int i = 0; i < 2; ++i) wv[i] = *(const u32x4*)(wp + i * 512);
    tv = __hip_atomic_load((const unsigned*)(fl + FL_EPOCH), __ATOMIC_RELAXED, __HIP_MEMORY_SCOPE_AGENT);
    SB();
  }
  const unsigned ring = lds0 + F_RING;
  const unsigned g = w >> 2, wm = (w >> 1) & 1, wx = w & 1;
  const unsigned kh = xcd & 1, tm = (xcd >> 1) * 4 + (idx & 3), tn = idx >> 2;
  u32x4 res[2];
  unsigned rdA[8], rdB[4], hoff = 0;
  const unsigned rotA = (ID == 1) ? 2 * g : 2 * g + wx;
  {
    const int lane = lane_id(), fr = lane & 15, fq = lane >> 4, chunk = ((fq & 1) << 1) | (fq >> 1);
    const int row = tm * 128 + wm * 64 + rotA * 16 + fr, col = tn * 128 + kh * 32 + chunk * 8;
    if constexpr (ID == 2) {
#pragma unroll
      for (int c = 0; c < 2; ++c) res[c] = *(const u32x4*)((const u16*)p.x + (size_t)row * D_ + col + (c ^ wx) * 64);
    } else if constexpr (ID == 4) {
      hoff = ((row >> 4) * (D_ / 32) + (col >> 5)) * 1024 + (chunk * 16 + fr) * 16;
    }
    const unsigned rotB = (ID == 1) ? 0u : 2 * kh, base = ring + g * F_ST;
#pragma unroll
    for (int i = 0; i < 4; ++i) {
      const unsigned kk = i >> 1, hf = (i & 1) * 2;
      if constexpr (ID == 1) {
        rdA[i] = base + (wm * 64 + (hf ^ rotA) * 16 + fr) * 128 + (((kk * 4 + fq) ^ (fr >> 1)) << 4);
        rdB[i] = base + F_A + (wx * 64 + fr) * 128 + ((((i & 1) * 4 + fq) ^ (fr >> 1)) << 4);
      } else if constexpr (ID == 2) {
        rdA[i] = base + (wm * 64 + (i ^ rotA) * 16 + fr) * 128 + ((fq ^ (fr >> 1)) << 4);
        rdA[4 + i] = base + (wm * 64 + (i ^ rotA) * 16 + fr) * 128 + (((4 + fq) ^ (fr >> 1)) << 4);
        rdB[i] = base + F_A + wx * 8192 + (hf ^ rotB) * 2048 + lane * 16;
      } else {
        const int q = fr >> 2, pp = fr & 3, f = q | ((fq & 1) << 2);
        rdA[i] = base + wm * 8192 + (i ^ rotA) * 2048 + lane * 16;
        rdB[i] = base + F_A + (8 * fq + q) * 256 + (((wx * 4 + (i ^ rotB)) ^ f) << 5) + pp * 8;
      }
    }
  }
  constexpr int NVA = (ID == 2) ? 8 : 4, NVB = (ID <= 2) ? 2 : 4;
  bf16x8 fa[2][4], fb[2][4];
  f32x4 acc[4][4];
#pragma unroll
  for (int i = 0; i < 16; ++i) { acc[i >> 2][i & 3] = (f32x4){0.f, 0.f, 0.f, 0.f}; asm volatile("" : "+v"(acc[i >> 2][i & 3])); }
  auto reads = [&]() {
    SB();
#pragma unroll
    for (int s = 0; s < 2; ++s)
#pragma unroll
      for (int i = 0; i < 4; ++i) {
        if constexpr (ID == 1) {
          fb[s][i] = lds_b128(rdB[s] + i * 2048);
          fa[s][i] = lds_b128(rdA[s * 2 + (i >> 1)] + (i & 1) * 2048);
        } else if constexpr (ID == 2) {
          fb[s][i] = lds_b128(rdB[i >> 1] + (i & 1) * 2048 + s * 1024);
          fa[s][i] = lds_b128(rdA[s * 4 + i]);
        } else {
          fb[s][i] = lds_tr(rdB[i] + s * 8192, rdB[i] + s * 8192 + 1024);
          fa[s][i] = lds_b128(rdA[i] + s * 1024);
        }
      }
    SB();
  };
#pragma unroll
  for (int j = 0; j < NVA; ++j) asm volatile("" : "+v"(rdA[j]));
#pragma unroll
  for (int j = 0; j < NVB; ++j) asm volatile("" : "+v"(rdB[j]));
  if constexpr (ID == 1) {
    SB();
    asm volatile("" : "+v"(tv));
    tv = tv * 0x9E3779B1u + 0x7F4A7C15u;
    const int lane = lane_id();
    float s = 0.f, t = 0.f;
#pragma unroll
    for (int j = 0; j < 8; ++j) s += bf_lo(xv[j >> 2][j & 3]) + bf_hi(xv[j >> 2][j & 3]);
    const float m = wave_sum(s) * (1.0f / D_);
    float q = 0.f;
#pragma unroll
    for (int j = 0; j < 8; ++j) {
      const float a = bf_lo(xv[j >> 2][j & 3]) - m, b = bf_hi(xv[j >> 2][j & 3]) - m;
      q += a * a + b * b;
      t += bf_lo(wv[j >> 2][j & 3]) + bf_hi(wv[j >> 2][j & 3]);
    }
    const float rs = __builtin_amdgcn_rsqf(wave_sum(q) * (1.0f / D_) + EPS);
    t = wave_sum(t);
    const float val = (lane & 2) ? t : ((lane & 1) ? rs : m);
    if (lane < 4)
      __hip_atomic_store((unsigned long long*)(fl + FL_LN + ((lane >> 1) * 2048 + r0) * 16 + (lane & 1) * 8), ((unsigned long long)tv << 32) | __float_as_uint(val),
                         __ATOMIC_RELAXED, __HIP_MEMORY_SCOPE_AGENT);
    tag = rfl(tv);
    if (w == 0) { *(volatile LAS unsigned*)(lds0 + F_TAG) = tag; wait_lgkm0(); }
  }
  if constexpr (ID != 1) {
    barrier();
  }
  if constexpr (ID == 4) {
    const auto rh = OUT_RSRC(p.h, S_ * D_ * 2);
#pragma unroll
    for (int c = 0; c < 2; ++c) res[c] = __builtin_amdgcn_raw_buffer_load_b128(rh, hoff, (c ^ wx) * 2048, 16);
  }
  barrier();
  if (g == 1) barrier();
  reads();
#pragma unroll 1
  for (unsigned i = 0; i < NT / 2; ++i) {
    const unsigned delta = (i & 1) ? (unsigned)(-2 * F_ST) : (unsigned)(2 * F_ST);
#pragma unroll
    for (int j = 0; j < NVA; ++j) { rdA[j] += delta; asm volatile("" : "+v"(rdA[j])); }
#pragma unroll
    for (int j = 0; j < NVB; ++j) { rdB[j] += delta; asm volatile("" : "+v"(rdB[j])); }
    wait_lgkm0();
    barrier();
    SB();
#pragma unroll
    for (int m = 0; m < 32; ++m) {
      const int n = (m & 4) ? 3 - (m & 3) : (m & 3);
      MFMA(fb[m >> 4][n], fa[m >> 4][(m >> 2) & 3], acc[(m >> 2) & 3][n]);
    }
    SB();
    barrier();
    if (i + 1 < NT / 2) reads();
  }
  mfma_drain();
  SB();
  const int lane = lane_id(), fr = lane & 15, fq = lane >> 4, chunk = ((fq & 1) << 1) | (fq >> 1);
  if constexpr (ID == 1) {
    const unsigned wb = ring + 2 * F_ST + (w ^ 4) * 8192 + lane * 16, rb = ring + 2 * F_ST + w * 8192 + lane * 16;
#pragma unroll
    for (int i = 0; i < 8; ++i) *(LAS f32x4*)(wb + i * 1024) = acc[2 + (i >> 2)][i & 3];
    if (g == 0) barrier();
    wait_lgkm0();
    barrier();
#pragma unroll
    for (int i = 0; i < 8; ++i) acc[i >> 2][i & 3] += *(const LAS f32x4*)(rb + i * 1024);
  }
  if constexpr (ID == 1) {
    const LAS float* tb = (const LAS float*)(lds0 + F_TAB);
    const float sg = tm < 8 ? 1.f : -1.f;
    const auto ro = OUT_RSRC(p.ab, D_ * 4096 * 2);
    f32x4 mean[4], rstd[4];
#pragma unroll
    for (int j = 0; j < 4; ++j) {
      mean[j] = *(const LAS f32x4*)(tb + wx * 64 + j * 16 + fq * 4);
      rstd[j] = *(const LAS f32x4*)(tb + 128 + wx * 64 + j * 16 + fq * 4) * sg;
    }
#pragma unroll
    for (int mb = 0; mb < 2; ++mb) {
      const float wsm = tb[256 + wm * 64 + g * 32 + mb * 16 + fr];
#pragma unroll
      for (int np = 0; np < 2; ++np) {
        const f32x4 x0 = (acc[mb][2 * np] - mean[2 * np] * wsm) * rstd[2 * np], x1 = (acc[mb][2 * np + 1] - mean[2 * np + 1] * wsm) * rstd[2 * np + 1];
        auto s0 = __builtin_amdgcn_permlane16_swap(pk_bf16(x0[0], x0[1]), pk_bf16(x1[0], x1[1]), false, false);
        auto s1 = __builtin_amdgcn_permlane16_swap(pk_bf16(x0[2], x0[3]), pk_bf16(x1[2], x1[3]), false, false);
        const int rblk = (tm & 7) * 8 + wm * 4 + g * 2 + mb;
        const int kblk = (tm >> 3) * 64 + (kh * 8 + tn) * 4 + wx * 2 + np;
        STORE16(ro, (rblk * 128 + kblk) * 1024 + (chunk * 16 + fr) * 16, ((u32x4){s0[0], s1[0], s0[1], s1[1]}));
      }
    }
    return;
  }
  const auto rs = OUT_RSRC((char*)p.xch + (ID == 2 ? 0 : 256 * 16384), 256 * 16384);
  const unsigned sreg = ring + ((w >> 1) & 1) * F_ST + (w & 1) * 8192 + lane * 16, kreg = ring + 2 * F_ST + lane * 16;
  auto put_keep = [&]() {
#pragma unroll
    for (int j = 1; j < 4; ++j) {
      const unsigned d = kreg + ((wm * 4 + (j ^ rotA)) * 3 + (j - 1)) * 2048;
      *(LAS f32x4*)(d) = acc[j][0];
      *(LAS f32x4*)(d + 1024) = acc[j][1];
    }
  };
  if (g == 0) {
#pragma unroll
    for (int i = 0; i < 8; ++i) *(LAS f32x4*)(sreg + i * 1024) = acc[i >> 1][2 + (i & 1)];
    put_keep();
    wait_lgkm0();
    barrier();
  } else {
#pragma unroll
    for (int j = 0; j < 4; ++j) {
      const f32x4 x0 = acc[j][2] + *(const LAS f32x4*)(sreg + (j ^ 2) * 2048), x1 = acc[j][3] + *(const LAS f32x4*)(sreg + (j ^ 2) * 2048 + 1024);
      auto s0 = __builtin_amdgcn_permlane16_swap(pk_bf16(x0[0], x0[1]), pk_bf16(x1[0], x1[1]), false, false);
      auto s1 = __builtin_amdgcn_permlane16_swap(pk_bf16(x0[2], x0[3]), pk_bf16(x1[2], x1[3]), false, false);
      __builtin_amdgcn_raw_buffer_store_b128((u32x4){s0[0], s1[0], s0[1], s1[1]}, rs, lane * 16 + blockIdx.x * 16384 + ((wm * 4 + (j ^ rotA)) * 2 + wx) * 1024, 0, 16);
    }
    SB();
    put_keep();
    wait_lgkm0();
  }
  barrier();
  f32x4 y[2][2];
  {
    const unsigned kb = kreg + (wm * 4 + rotA) * 3 * 2048;
    f32x4 t[2][2];
#pragma unroll
    for (int i = 0; i < 2; ++i) {
      t[0][i] = acc[0][i] + *(const LAS f32x4*)(kb + 2048 + i * 1024);
      t[1][i] = *(const LAS f32x4*)(kb + i * 1024) + *(const LAS f32x4*)(kb + 4096 + i * 1024);
    }
#pragma unroll
    for (int c = 0; c < 2; ++c)
#pragma unroll
      for (int j = 0; j < 4; ++j) {
        auto s = __builtin_amdgcn_permlane16_swap(__float_as_uint(t[c][0][j]), __float_as_uint(t[c][1][j]), false, false);
        y[c][0][j] = __uint_as_float(s[0]) + ((j & 1) ? bf_hi(res[c][j >> 1]) : bf_lo(res[c][j >> 1]));
        y[c][1][j] = __uint_as_float(s[1]) + ((j & 1) ? bf_hi(res[c][2 + (j >> 1)]) : bf_lo(res[c][2 + (j >> 1)]));
      }
  }
  const int row0 = tm * 128 + wm * 64 + rotA * 16 + fr, col = tn * 128 + kh * 32 + chunk * 8;
  unsigned optr = (ID == 2) ? ((row0 >> 4) * (D_ / 32) + (col >> 5)) * 1024 + (chunk * 16 + fr) * 16 : (row0 * D_ + col) * 2;
  const auto ro = OUT_RSRC(ID == 2 ? p.h : p.out, S_ * D_ * 2);
  asm volatile("" : "+v"(y[0][0]), "+v"(y[0][1]), "+v"(y[1][0]), "+v"(y[1][1]), "+v"(optr));
  wait_vm<0>();
  barrier();
  if (w == 0 && lane == 0) put_tag((char*)p.flags + FL_PAIR + ((ID == 2 ? 0 : 256) + blockIdx.x) * 128, tag);
  barrier();
  u32x4 in[2], v[2];
#pragma unroll
  for (int c = 0; c < 2; ++c) in[c] = __builtin_amdgcn_raw_buffer_load_b128(rs, lane * 16, (blockIdx.x ^ 1) * 16384 + ((wm * 4 + rotA) * 2 + (c ^ wx)) * 1024, 16);
#pragma unroll
  for (int c = 0; c < 2; ++c) {
    const f32x4 a = y[c][0] + (f32x4){bf_lo(in[c][0]), bf_hi(in[c][0]), bf_lo(in[c][1]), bf_hi(in[c][1])};
    const f32x4 b = y[c][1] + (f32x4){bf_lo(in[c][2]), bf_hi(in[c][2]), bf_lo(in[c][3]), bf_hi(in[c][3])};
    v[c] = (u32x4){pk_bf16(a[0], a[1]), pk_bf16(a[2], a[3]), pk_bf16(b[0], b[1]), pk_bf16(b[2], b[3])};
    __builtin_amdgcn_raw_buffer_store_b128(v[c], ro, optr + (c ^ wx) * (ID == 2 ? 2048 : 128), 0, ST_AUX);
  }
  SB();
  if constexpr (ID == 2) {
    float s = 0.f;
#pragma unroll
    for (int j = 0; j < 8; ++j) s += bf_lo(v[j >> 2][j & 3]) + bf_hi(v[j >> 2][j & 3]);
    const float m = sum_rows(s) * (1.0f / 64.0f);
    float q = 0.f;
#pragma unroll
    for (int j = 0; j < 8; ++j) {
      const float a = bf_lo(v[j >> 2][j & 3]) - m, b = bf_hi(v[j >> 2][j & 3]) - m;
      q += a * a + b * b;
    }
    const f32x2 st = {m, sum_rows(q)};
    if (fq == 0)
      __hip_atomic_store((unsigned long long*)p.stats + (size_t)(tn * 2 + kh) * S_ + row0, __builtin_bit_cast(unsigned long long, st), __ATOMIC_RELAXED, __HIP_MEMORY_SCOPE_AGENT);
  }
  SB();
}

__device__ __forceinline__ void glu_loader(const FArgs& p, const Ld& L, Ld& N, unsigned lds0, unsigned lw, unsigned xcd, unsigned idx, unsigned& touch) {
  constexpr int NT = 32, NEXT = 4;
  const unsigned tag = lds_tag(lds0);
  const unsigned ring = lds0 + F_RING;
  LAS float* cst = (LAS float*)(lds0 + F_TAB + 2048);
  unsigned long long* gcs = (unsigned long long*)((char*)p.flags + FL_CS) + (xcd * 4 + (idx >> 3)) * (8 * 224);
  ld_a<3>(L, ring, lw, 0);
  ld_a<3>(L, ring, lw, 1);
  const int lane = lane_id();
  const unsigned u = lw * 64 + lane < 224 ? lw * 64 + lane : 223u;
  wait_vm<4>();
  barrier();
  ld_a<3>(L, ring, lw, 2);
  ld_b<3>(L, ring, lw, 2);
#pragma unroll 1
  for (unsigned t = 0; t + 3 < NT; ++t) {
    if (t == 25) {
      const unsigned hf = u >= 112, c = u - hf * 112, wn_ = c >= 56, cc = c - wn_ * 56, blk = cc >> 4;
      const unsigned src = lds0 + (wn_ * 4 + blk) * 768 + (blk == 3 ? 512 + (hf * 8 + (cc & 7)) * 4 : hf * 256 + (cc & 15) * 4);
      const float v = (*(const LAS float*)(src) + *(const LAS float*)(src + 64)) + (*(const LAS float*)(src + 128) + *(const LAS float*)(src + 192));
      asm volatile("global_store_dwordx2 %0, %1, off sc1" ::"v"(gcs + L.cls * 224 + u), "v"(((unsigned long long)tag << 32) | __float_as_uint(v)) : "memory");
    }
    ld_a<3>(L, ring, lw, t + 3);
    barrier();
    ld_b<3>(L, ring, lw, t + 3);
    wait_vm<14>();
    barrier();
  }
  unsigned long long g[8];
#pragma unroll
  for (int i = 0; i < 8; ++i) asm volatile("global_load_dwordx2 %0, %1, off sc1" : "=v"(g[i]) : "v"(gcs + i * 224 + u) : "memory");
  barrier();
  ld_b<NEXT>(N, ring, lw, 0);
  wait_vm<19>();
  barrier();
  barrier();
  ld_b<NEXT>(N, ring, lw, 1);
  wait_vm<16>();
  barrier();
  barrier();
  barrier();
  asm volatile("s_waitcnt vmcnt(8)" : "+v"(g[0]), "+v"(g[1]), "+v"(g[2]), "+v"(g[3]), "+v"(g[4]), "+v"(g[5]), "+v"(g[6]), "+v"(g[7]));
#pragma unroll 1
  for (unsigned spins = 0; spins < (1u << 15); ++spins) {
    unsigned tg = 0u;
#pragma unroll
    for (int i = 0; i < 8; ++i) tg |= (unsigned)(g[i] >> 32) ^ tag;
    if (__all(tg == 0u)) break;
    __builtin_amdgcn_s_sleep(2);
#pragma unroll
    for (int i = 0; i < 8; ++i) asm volatile("global_load_dwordx2 %0, %1, off sc1\n\ts_waitcnt vmcnt(0)" : "=&v"(g[i]) : "v"(gcs + i * 224 + u) : "memory");
  }
  float s = 0.f;
#pragma unroll
  for (int i = 0; i < 8; ++i) s += __uint_as_float((unsigned)g[i]);
  cst[u] = s;
  wait_lgkm0();
  {
    const char* tp = (const char*)p.wd + ((size_t)blockIdx.x * 224 + (lw * 56 + (lane < 56 ? lane : 55))) * 128;
    asm volatile("global_load_dword %0, %1, off" : "+v"(touch) : "v"(tp) : "memory");
  }
  barrier();
}

__device__ __forceinline__ void glu_compute(const FArgs& p, unsigned lds0, unsigned w, unsigned xcd, unsigned idx) {
  constexpr int ID = 3, BM = 256, NT = 32, MB = 4, NB = 7;
  const unsigned ring = lds0 + F_RING;
  LAS f32x2* tab = (LAS f32x2*)(lds0 + F_TAB);
  LAS float* cst = (LAS float*)(lds0 + F_TAB + 2048);
  const unsigned tm = idx & 7, tn = xcd * 4 + (idx >> 3), cls = (tm + 2 * (idx >> 3)) & 7;
  const unsigned wm = w & 3, wn = w >> 2;
  const int lane = lane_id(), fr = lane & 15, fq = lane >> 4;
  const unsigned rot = wm == 3 ? 0u : wm;
  unsigned xa = ring + wm * 4096 + lane * 16, xb[3], xb1;
  {
    const int q = fr >> 2, pp = fr & 3, r = (fq >> 1) * 16 + (fq & 1) * 4 + q;
#pragma unroll
    for (int i = 0; i < 3; ++i) xb[i] = ring + F_A + r * 224 + wn * 112 + pp * 8 + ((i + rot) % 3) * 32;
    xb1 = ring + F_A + r * 224 + wn * 112 + 96 + (pp & 1) * 8 + (pp >> 1) * G3_UP;
  }
  float cs[3] = {0.f, 0.f, 0.f};
  bf16x8 fa[MB], fb[NB];
  f32x4 acc[MB][NB];
#pragma unroll
  for (int mb = 0; mb < MB; ++mb)
#pragma unroll
    for (int nb = 0; nb < NB; ++nb) { acc[mb][nb] = (f32x4){0.f, 0.f, 0.f, 0.f}; asm volatile("" : "+v"(acc[mb][nb])); }
  asm volatile("" : "+v"(xa), "+v"(xb[0]), "+v"(xb[1]), "+v"(xb[2]), "+v"(xb1));
  barrier();
  if (wn == 1) {
    const auto rst = OUT_RSRC(p.stats, 8 * S_ * 16);
    f32x2 sv[16];
#pragma unroll
    for (int i = 0; i < 16; ++i) sv[i] = __builtin_bit_cast(f32x2, __builtin_amdgcn_raw_buffer_load_b64(rst, (tm * BM + wm * 64 + lane) * 8, i * S_ * 8, 16));
    SB();
    barrier();
    SB();
    float m = 0.f;
#pragma unroll
    for (int i = 0; i < 16; ++i) m += sv[i][0];
    m *= (1.0f / 16.0f);
    float q = 0.f;
#pragma unroll
    for (int i = 0; i < 16; ++i) {
      const float d = sv[i][0] - m;
      q += sv[i][1] + 64.0f * d * d;
    }
    tab[wm * 64 + lane] = (f32x2){m, q * (1.0f / D_) + EPS};
  }
  wait_lgkm0();
  barrier();
  const unsigned csmask = (0x010101u << cls) | (1u << ((cls + 1) & 7));
  auto reads = [&]() {
    SB();
    fa[0] = lds_b128(xa);
#pragma unroll
    for (int j = 0; j < 7; ++j) {
      const unsigned o = j < 6 ? xb[j % 3] + (j / 3) * G3_UP : xb1;
      fb[j] = lds_tr(o, o + 1792);
    }
#pragma unroll
    for (int mb = 1; mb < MB; ++mb) fa[mb] = lds_b128(xa + mb * 1024);
    SB();
  };
  reads();
#pragma unroll 1
  for (unsigned t = 0; t < NT; ++t) {
    const unsigned delta = ((t & 3) == 3) ? (unsigned)(-3 * F_ST) : (unsigned)F_ST;
    xa += delta; xb[0] += delta; xb[1] += delta; xb[2] += delta; xb1 += delta;
    asm volatile("" : "+v"(xa), "+v"(xb[0]), "+v"(xb[1]), "+v"(xb[2]), "+v"(xb1));
    if (__builtin_expect((csmask >> t) & 1u, 0)) {
#pragma unroll
      for (int j = 0; j < 4; ++j)
#pragma unroll
        for (int i = 0; i < 3; ++i)
          asm volatile("v_dot2c_f32_bf16 %0, %1, %2" : "+v"(cs[i]) : "s"(0x3f803f80u), "v"(__builtin_bit_cast(u32x4, fb[3 * i])[j]));
      asm volatile("s_mov_b32 m0, %3\n\ts_nop 2\n\tds_write_addtid_b32 %0\n\tds_write_addtid_b32 %1 offset:256\n\tds_write_addtid_b32 %2 offset:512" ::"v"(cs[0]),
                   "v"(cs[1]), "v"(cs[2]), "s"(lds0 + w * 768)
                   : "memory");
    }
    wait_lgkm0();
    barrier();
    SB();
#pragma unroll
    for (int mb = 0; mb < MB; ++mb)
#pragma unroll
      for (int i = 0; i < NB; ++i) {
        const int nb = (mb & 1) ? NB - 1 - i : i;
        MFMA(fb[nb], fa[mb], acc[mb][nb]);
      }
    SB();
    barrier();
    reads();
  }
  if (wn == 0) barrier();
  mfma_drain();
  SB();
  const int lane2 = lane_id();
  const int gr = lane2 & 15, gq = lane2 >> 4;
  auto act2 = [](f32x2 a, f32x2 b, float sc, float k) {
    const f32x2 t = a * sc;
    const f32x2 e = {__builtin_amdgcn_exp2f(t.x), __builtin_amdgcn_exp2f(t.y)};
    const f32x2 d = e * k + k;
    const f32x2 r = {__builtin_amdgcn_rcpf(d.x), __builtin_amdgcn_rcpf(d.y)};
    const f32x2 o = (a * b) * r;
    return pk_bf16(o.x, o.y);
  };
  const auto ro = OUT_RSRC(p.gu, S_ * F_ * 2);
  float nm[MB], kk[MB], sc[MB];
#pragma unroll
  for (int mb = 0; mb < MB; ++mb) {
    const f32x2 st = tab[wm * 64 + mb * 16 + gr];
    nm[mb] = -st.x; kk[mb] = st.y; sc[mb] = -1.44269504089f * __builtin_amdgcn_rsqf(st.y);
  }
  const int gch0 = tn * 14 + wn * 7;
  const unsigned obase = (tm * (BM / 16) + wm * MB) * (F_ / 32) * 1024 + gr * 16;
#pragma unroll
  for (int i = 0; i < 3; ++i) {
    const unsigned ib = (i + rot) % 3;
    const f32x4 cg = *(const LAS f32x4*)(cst + wn * 56 + ib * 16 + gq * 4), cu = *(const LAS f32x4*)(cst + 112 + wn * 56 + ib * 16 + gq * 4);
    const unsigned oo = obase + (gq & 1) * (F_ / 32) * 1024 + (gch0 + 2 * ib + (gq >> 1)) * 256;
#pragma unroll
    for (int mp = 0; mp < MB / 2; ++mp) {
      u32x2 pkd[2];
#pragma unroll
      for (int h = 0; h < 2; ++h) {
        const int mb = 2 * mp + h;
        const f32x4 a = cg * nm[mb] + acc[mb][i], b = cu * nm[mb] + acc[mb][3 + i];
        pkd[h] = (u32x2){act2(a.xy, b.xy, sc[mb], kk[mb]), act2(a.zw, b.zw, sc[mb], kk[mb])};
      }
      auto s0 = __builtin_amdgcn_permlane16_swap(pkd[0].x, pkd[1].x, false, false);
      auto s1 = __builtin_amdgcn_permlane16_swap(pkd[0].y, pkd[1].y, false, false);
      __builtin_amdgcn_raw_buffer_store_b128((u32x4){s0[0], s1[0], s0[1], s1[1]}, ro, oo + mp * 2 * (F_ / 32) * 1024, 0, ST_AUX);
    }
  }
  {
    const f32x4 cm = *(const LAS f32x4*)(cst + (gq >> 1) * 112 + wn * 56 + 48 + (gq & 1) * 4);
    u32x2 pkd[MB / 2];
#pragma unroll
    for (int mp = 0; mp < MB / 2; ++mp) {
      const f32x4 v0 = cm * nm[2 * mp] + acc[2 * mp][6], v1 = cm * nm[2 * mp + 1] + acc[2 * mp + 1][6];
      f32x4 a, b;
#pragma unroll
      for (int j = 0; j < 4; ++j) {
        auto s = __builtin_amdgcn_permlane32_swap(__float_as_uint(v0[j]), __float_as_uint(v1[j]), false, false);
        a[j] = __uint_as_float(s[0]); b[j] = __uint_as_float(s[1]);
      }
      const float scs = (gq & 2) ? sc[2 * mp + 1] : sc[2 * mp], ks = (gq & 2) ? kk[2 * mp + 1] : kk[2 * mp];
      pkd[mp] = (u32x2){act2(a.xy, b.xy, scs, ks), act2(a.zw, b.zw, scs, ks)};
    }
    auto s0 = __builtin_amdgcn_permlane16_swap(pkd[0].x, pkd[1].x, false, false);
    auto s1 = __builtin_amdgcn_permlane16_swap(pkd[0].y, pkd[1].y, false, false);
    STORE16(ro, obase + ((gq & 1) * 2 + (gq >> 1)) * (F_ / 32) * 1024 + (gch0 + 6) * 256, ((u32x4){s0[0], s1[0], s0[1], s1[1]}));
  }
}

__global__ __launch_bounds__(768, 3) void k_block(FArgs p) {
  extern __shared__ __attribute__((aligned(16))) char shm[];
  const unsigned lds0 = (unsigned)(size_t)(LAS char*)shm;
  const unsigned w = rfl(threadIdx.x >> 6);
  const unsigned xcd = blockIdx.x & 7, idx = blockIdx.x >> 3;
  char* fl = (char*)p.flags;
  const unsigned tm = (xcd >> 1) * 4 + (idx & 3), kh = xcd & 1, lj = idx >> 2;
  if (w >= 8) {
    const unsigned lw = w - 8;
    Ld LA, LB;
    unsigned touch = 0;
    LA = ld_setup<1>(p, lw, xcd, idx);
    sq_loader<1>(p, LA, LB, lds0, lw, xcd, idx, touch);
    const unsigned tag = lds_tag(lds0);
    LA = ld_setup<3>(p, lw, xcd, idx);
    ld_pin(LA);
    barrier();
    if (lw == 3) poll_tags(fl + FL_P1 + ((kh * 8 + lj) * 4 + (xcd >> 1)) * 128, 16, tag);
    barrier();
    sq_loader<2>(p, LB, LA, lds0, lw, xcd, idx, touch);
    LB = ld_setup<4>(p, lw, xcd, idx);
    ld_pin(LB);
    barrier();
    if (lw == 3) poll_tags(fl + FL_P2 + ((idx & 7) * 8 + xcd) * 128, 32, tag);
    barrier();
    glu_loader(p, LA, LB, lds0, lw, xcd, idx, touch);
    barrier();
    if (lw == 3) poll_tags(fl + FL_P3 + ((tm >> 1) * 2 + kh) * 128, 16, tag);
    barrier();
    sq_loader<4>(p, LB, LA, lds0, lw, xcd, idx, touch);
    asm volatile("" ::"v"(touch));
    return;
  }
  unsigned csink = 0;
  if (w == 0 && idx < 3) {
    unsigned long long pc;
    asm volatile("s_getpc_b64 %0" : "=s"(pc));
    const unsigned ln = idx * 64 + lane_id();
    asm volatile("global_load_dword %0, %1, %2" : "+v"(csink) : "v"((ln < CODE_LINES ? ln : CODE_LINES - 1) * 128), "s"(pc) : "memory");
  }
#define TLBW 1
  if (TLBW && w == 1 && idx < 20) {
    const char* base = idx < 2 ? (const char*)p.cre : idx < 4 ? (const char*)p.cim : idx < 6 ? (const char*)p.wg : idx < 8 ? (const char*)p.wu : idx < 10 ? (const char*)p.wd : idx < 11 ? (const char*)p.out : (const char*)p.ab;
    const unsigned size = idx < 4 ? S_ * S_ * 2 : idx < 10 ? D_ * F_ * 2 : idx < 11 ? S_ * D_ * 2 : (unsigned)WS_FL;
    const unsigned off = ((idx < 10 ? (idx & 1) : (idx < 11 ? 0 : idx - 11)) * 64 + lane_id()) * 65536;
    asm volatile("global_load_dword %0, %1, %2 sc1" : "+v"(csink) : "v"(off < size ? off : 0u), "s"(base) : "memory");
  }
  unsigned tag = 0;
  sq_compute<1>(p, lds0, w, xcd, idx, tag);
  asm volatile("" ::"v"(csink));
  wait_vm<0>();
  barrier();
  if (w == 0) { const int l = lane_id(); if (l < 4) put_tag(fl + FL_P1 + (tm * 4 + l) * 128 + (kh * 8 + lj) * 4, tag); }
  sq_compute<2>(p, lds0, w, xcd, idx, tag);
  wait_vm<0>();
  barrier();
  if (w == 0) { const int l = lane_id(); if (l < 8) put_tag(fl + FL_P2 + ((tm >> 1) * 8 + l) * 128 + (((tm & 1) * 2 + kh) * 8 + lj) * 4, tag); }
  glu_compute(p, lds0, w, xcd, idx);
  wait_vm<0>();
  barrier();
  if (w == 0 && lane_id() == 0) put_tag(fl + FL_P3 + ((idx & 7) * 2 + (xcd >> 2)) * 128 + ((xcd & 3) * 4 + (idx >> 3)) * 4, tag);
  sq_compute<4>(p, lds0, w, xcd, idx, tag);
  if (blockIdx.x == 0 && w == 0 && lane_id() == 0) put_tag(fl + FL_EPOCH, tag);
}
}

extern "C" void kernel_launch(void* const* d_in, const int* in_sizes, int n_in, void* d_out, int out_size, void* d_ws, size_t ws_size,
                              hipStream_t stream) {
  char* ws = (char*)d_ws;
  FArgs f = {d_in[3], d_in[4], d_in[1], d_in[2], d_in[0], d_in[6], d_in[7], d_in[8], ws + WS_AB, ws + WS_H, ws + WS_ST, ws + WS_GU, d_out, ws + WS_XC, ws + WS_FL};
  (void)hipFuncSetAttribute((const void*)k_block, hipFuncAttributeMaxDynamicSharedMemorySize, F_LDS);
  hipLaunchKernelGGL(k_block, dim3(256), dim3(768), F_LDS, stream, f);
}
```

```cpp
#include <hip/hip_runtime.h>
#include <hip/hip_bf16.h>

namespace {
#define LAS __attribute__((address_space(3)))
typedef unsigned short u16;
typedef short s16x4 __attribute__((ext_vector_type(4)));
typedef short s16x8 __attribute__((ext_vector_type(8)));
typedef __bf16 bf16x8 __attribute__((ext_vector_type(8)));
typedef __bf16 bf16x2 __attribute__((ext_vector_type(2)));
typedef float f32x4 __attribute__((ext_vector_type(4)));
typedef float f32x2 __attribute__((ext_vector_type(2)));
typedef unsigned u32x4 __attribute__((ext_vector_type(4)));
typedef unsigned u32x2 __attribute__((ext_vector_type(2)));

constexpr int S_ = 2048, D_ = 1024, F_ = 3584;
constexpr float EPS = 1e-5f;

constexpr size_t WS_AB = 0;
constexpr size_t WS_H = WS_AB + (size_t)D_ * 2 * S_ * 2;
constexpr size_t WS_GU = WS_H + (size_t)S_ * D_ * 2;
constexpr size_t WS_ST = WS_GU + (size_t)S_ * F_ * 2;
constexpr size_t WS_XC = WS_ST + (size_t)S_ * 16 * 8;
constexpr size_t WS_FL = WS_XC + (size_t)2 * 256 * 16384;
constexpr int FL_EPOCH = 0;
constexpr int FL_PAIR = 128;
constexpr int FL_P1 = FL_PAIR + 512 * 128;
constexpr int FL_P2 = FL_P1 + 64 * 128;
constexpr int FL_P3 = FL_P2 + 64 * 128;
constexpr int FL_LN = FL_P3 + 16 * 128;
constexpr int FL_CS = FL_LN + 4096 * 16;

__device__ __forceinline__ unsigned rfl(unsigned v) { return __builtin_amdgcn_readfirstlane(v); }
__device__ __forceinline__ unsigned pk_bf16(float lo, float hi) {
  bf16x2 c = {(__bf16)lo, (__bf16)hi};
  return __builtin_bit_cast(unsigned, c);
}
__device__ __forceinline__ float bf_lo(unsigned v) { return __uint_as_float(v << 16); }
__device__ __forceinline__ float bf_hi(unsigned v) { return __uint_as_float(v & 0xffff0000u); }

__device__ __forceinline__ u32x4 make_rsrc(const void* p, unsigned bytes) {
  unsigned long long a = (unsigned long long)p;
  u32x4 r;
  r.x = (unsigned)a;
  r.y = (unsigned)(a >> 32) & 0xffffu;
  r.z = bytes;
  r.w = 0x00020000u;
  return r;
}
template <int N>
__device__ __forceinline__ void wait_vm() {
  asm volatile("s_waitcnt vmcnt(%0)" ::"i"(N) : "memory");
}
__device__ __forceinline__ void wait_lgkm0() { __builtin_amdgcn_s_waitcnt(0xC07F); }
__device__ __forceinline__ void barrier() {
  asm volatile("" ::: "memory");
  __builtin_amdgcn_s_barrier();
  asm volatile("" ::: "memory");
}
__device__ __forceinline__ bf16x8 lds_b128(unsigned addr) { return *(const LAS bf16x8*)(addr); }
__device__ __forceinline__ bf16x8 lds_tr(unsigned a0, unsigned a1) {
  s16x4 lo = __builtin_amdgcn_ds_read_tr16_b64_v4i16((LAS s16x4*)(a0));
  s16x4 hi = __builtin_amdgcn_ds_read_tr16_b64_v4i16((LAS s16x4*)(a1));
  s16x8 f = __builtin_shufflevector(lo, hi, 0, 1, 2, 3, 4, 5, 6, 7);
  return __builtin_bit_cast(bf16x8, f);
}

#define ST_AUX 16
#define OUT_RSRC(ptr, bytes) __builtin_amdgcn_make_buffer_rsrc((void*)(ptr), 0, (bytes), 0x00020000)
#define STORE16(rs, byteoff, v) __builtin_amdgcn_raw_buffer_store_b128(v, rs, byteoff, 0, ST_AUX)

#define DMA4(M) asm volatile("s_mov_b32 m0, %0\n\ts_nop 4\n\tbuffer_load_dwordx4 %1, %5, %6 offen " M " lds\n\tbuffer_load_dwordx4 %2, %5, %6 offen offset:1024 " M " lds\n\t" \
                 "buffer_load_dwordx4 %3, %5, %6 offen offset:2048 " M " lds\n\tbuffer_load_dwordx4 %4, %5, %6 offen offset:3072 " M " lds" ::"s"(ldsaddr), \
                 "v"(v[0]), "v"(v[1]), "v"(v[2]), "v"(v[3]), "s"(rsrc), "s"(soff) : "memory")
#define DMA3(M) asm volatile("s_mov_b32 m0, %0\n\ts_nop 4\n\tbuffer_load_dwordx4 %1, %4, %5 offen " M " lds\n\tbuffer_load_dwordx4 %2, %4, %5 offen offset:1024 " M " lds\n\t" \
                 "buffer_load_dwordx4 %3, %4, %5 offen offset:2048 " M " lds" ::"s"(ldsaddr), "v"(v[0]), "v"(v[1]), "v"(v[2]), "s"(rsrc), "s"(soff) : "memory")
#define DMAB(M) asm volatile("s_mov_b32 m0, %0\n\ts_waitcnt vmcnt(8)\n\ts_barrier\n\tbuffer_load_dwordx4 %1, %5, %6 offen " M " lds\n\tbuffer_load_dwordx4 %2, %5, %6 offen offset:1024 " M " lds\n\t" \
                 "buffer_load_dwordx4 %3, %5, %6 offen offset:2048 " M " lds\n\tbuffer_load_dwordx4 %4, %5, %6 offen offset:3072 " M " lds" ::"s"(ldsaddr), \
                 "v"(v[0]), "v"(v[1]), "v"(v[2]), "v"(v[3]), "s"(rsrc), "s"(soff) : "memory")
template <int N, int MOD = 0>
__device__ __forceinline__ void dma_grp(unsigned ldsaddr, const unsigned (&v)[4], u32x4 rsrc, unsigned soff) {
  if constexpr (N == 4) { if constexpr (MOD == 1) DMA4("sc1"); else DMA4(""); }
  else if constexpr (N == 3) { if constexpr (MOD == 1) DMA3("sc1"); else DMA3(""); }
  else { if constexpr (MOD == 1) DMAB("sc1"); else DMAB(""); }
}
__device__ __forceinline__ float sum_rows(float x) {
  auto a = __builtin_amdgcn_permlane16_swap(__float_as_uint(x), __float_as_uint(x), false, false);
  x = __uint_as_float(a[0]) + __uint_as_float(a[1]);
  auto b = __builtin_amdgcn_permlane32_swap(__float_as_uint(x), __float_as_uint(x), false, false);
  return __uint_as_float(b[0]) + __uint_as_float(b[1]);
}
template <int CTRL> __device__ __forceinline__ float dpp_add(float x) { return x + __uint_as_float(__builtin_amdgcn_mov_dpp(__float_as_uint(x), CTRL, 0xF, 0xF, true)); }
__device__ __forceinline__ float wave_sum(float x) { return sum_rows(dpp_add<0x140>(dpp_add<0x141>(dpp_add<0x4E>(dpp_add<0xB1>(x))))); }
__device__ __forceinline__ int lane_id() {
  int l;
  asm volatile("v_mbcnt_lo_u32_b32 %0, -1, 0\n\tv_mbcnt_hi_u32_b32 %0, -1, %0" : "=v"(l));
  return l;
}
__device__ __forceinline__ void mfma_drain() { asm volatile("s_nop 15" ::: "memory"); }
template <int NC, int NT>
__device__ __forceinline__ unsigned stage_of(unsigned n, unsigned c) {
  const unsigned t = ((n & (NC - 1)) == c) ? n + NC : n;
  return rfl(t >= NT ? t - NT : t);
}
#define SB() __builtin_amdgcn_sched_barrier(0)
#define MFMA(b, a, c) asm volatile("v_mfma_f32_16x16x32_bf16 %0, %1, %2, %0" : "+v"(c) : "v"(b), "v"(a))

struct FArgs {
  const void *dre, *dim, *cre, *cim, *x, *wg, *wu, *wd;
  void *ab, *h, *stats, *gu, *out, *xch, *flags;
};
constexpr unsigned CODE_LINES = 156;
constexpr int F_RING = 6144, F_ST = 32768, F_TAB = F_RING + 4 * F_ST, F_TAG = F_TAB + 4096, F_LDS = F_TAB + 4160, F_A = 16384, G3_UP = 7168 + 16;

__device__ __forceinline__ void poll_tags(const void* line, int n, unsigned tag) {
  const int l = lane_id();
  const unsigned* q = (const unsigned*)line + (l < n ? l : 0);
  unsigned spins = 0;
  while (!__all(__hip_atomic_load(q, __ATOMIC_RELAXED, __HIP_MEMORY_SCOPE_AGENT) == tag) && ++spins < (1u << 17)) __builtin_amdgcn_s_sleep(1);
}
__device__ __forceinline__ void put_tag(void* word, unsigned tag) { __hip_atomic_store((unsigned*)word, tag, __ATOMIC_RELAXED, __HIP_MEMORY_SCOPE_AGENT); }
__device__ __forceinline__ unsigned lds_tag(unsigned lds0) { return rfl(*(volatile LAS unsigned*)(lds0 + F_TAG)); }
template <int ID> constexpr int nt_of() { return ID == 1 ? 16 : (ID == 4 ? 28 : 32); }
template <int ID> constexpr int nc_of() { return (ID == 1 || ID == 4) ? 4 : 8; }
template <int ID> constexpr int mod_a() { return ID > 2; }
template <int ID> constexpr int mod_b() { return ID == 2; }
struct Ld {
  u32x4 qa, qb;
  unsigned va[4], vb[4], cls;
};
template <int ID>
__device__ __forceinline__ Ld ld_setup(const FArgs& p, unsigned lw, unsigned xcd, unsigned idx) {
  const int lane = lane_id();
  Ld L;
  if constexpr (ID == 3) {
    const unsigned tm = idx & 7, tn = xcd * 4 + (idx >> 3);
    L.qa = make_rsrc((const u16*)p.h + (size_t)tm * 256 * D_, 256 * D_ * 2);
    L.qb = make_rsrc(lw < 2 ? p.wg : p.wu, (unsigned)D_ * F_ * 2);
#pragma unroll
    for (int i = 0; i < 4; ++i) {
      L.va[i] = (lw * 4 + i) * (D_ / 32) * 1024 + lane * 16 - i * 1024;
      const int c = ((lw & 1) * 4 + i) * 64 + lane, r = c / 14, pos = c % 14;
      const int k = (r & 0x13) | ((r & 4) << 1) | ((r & 8) >> 1);
      L.vb[i] = k * F_ * 2 + tn * 224 + pos * 16 - i * 1024;
    }
    L.cls = (tm + 2 * (idx >> 3)) & 7;
  } else {
    const unsigned li = idx & 3, lj = idx >> 2, kh = xcd & 1, tm = (xcd >> 1) * 4 + li, tn = lj;
    if constexpr (ID == 1) {
      L.qa = make_rsrc((const u16*)(tm < 8 ? p.dre : p.dim) + (size_t)(tm & 7) * 128 * D_, 128 * D_ * 2);
      L.qb = make_rsrc((const u16*)p.x + (size_t)(kh * 8 + lj) * 128 * D_, 128 * D_ * 2);
    } else if constexpr (ID == 2) {
      L.qa = make_rsrc((const u16*)(kh ? p.cim : p.cre) + (size_t)tm * 128 * S_, 128 * S_ * 2);
      L.qb = make_rsrc((const u16*)p.ab + (size_t)tn * 128 * 4096 + kh * 64 * 512, 128 * 4096 * 2 - kh * 64 * 1024);
    } else {
      L.qa = make_rsrc((const u16*)p.gu + (size_t)tm * 128 * F_ + kh * 56 * 512, 128 * F_ * 2 - kh * 56 * 1024);
      L.qb = make_rsrc((const u16*)p.wd + (size_t)kh * 1792 * D_, 1792 * D_ * 2);
    }
#pragma unroll
    for (int i = 0; i < 4; ++i) {
      const int pc = lw * 4 + i;
      if constexpr (ID == 1) {
        const int row = pc * 8 + (lane >> 3);
        L.va[i] = L.vb[i] = row * D_ * 2 + (((lane & 7) ^ ((row >> 1) & 7)) << 4) - i * 1024;
      } else if constexpr (ID == 2) {
        const int row = pc * 8 + (lane >> 3);
        L.va[i] = row * S_ * 2 + (((lane & 7) ^ ((row >> 1) & 7)) << 4) - i * 1024;
        L.vb[i] = ((pc >> 1) * 128 + (pc & 1)) * 1024 + lane * 16 - i * 1024;
      } else {
        const int k = pc * 4 + (lane >> 4), f = (k & 3) | (((k >> 3) & 1) << 2);
        L.va[i] = ((pc >> 1) * (F_ / 32) + (pc & 1)) * 1024 + lane * 16 - i * 1024;
        L.vb[i] = k * D_ * 2 + tn * 256 + (((lane & 15) ^ (f << 1)) << 4) - i * 1024;
      }
    }
    L.cls = (nc_of<ID>() == 8) ? ((lj + 2 * li) & 7) : ((lj + li) & 3);
  }
  return L;
}
__device__ __forceinline__ void ld_pin(Ld& L) {
  asm volatile("" : "+v"(L.va[0]), "+v"(L.va[1]), "+v"(L.va[2]), "+v"(L.va[3]), "+v"(L.vb[0]), "+v"(L.vb[1]), "+v"(L.vb[2]), "+v"(L.vb[3]));
}
template <int ID>
__device__ __forceinline__ void ld_a(const Ld& L, unsigned ring, unsigned lw, unsigned n) {
  const unsigned st = stage_of<nc_of<ID>(), nt_of<ID>()>(n, L.cls);
  dma_grp<4, mod_a<ID>()>(ring + (n & 3) * F_ST + lw * 4096, L.va, L.qa, st * (ID <= 2 ? 128 : (ID == 3 ? 1024 : 2048)));
}
template <int ID>
__device__ __forceinline__ void ld_b(const Ld& L, unsigned ring, unsigned lw, unsigned n) {
  const unsigned st = stage_of<nc_of<ID>(), nt_of<ID>()>(n, L.cls);
  const unsigned dst = ring + (n & 3) * F_ST + F_A;
  if constexpr (ID == 3) {
    const unsigned d = dst + (lw >> 1) * G3_UP + (lw & 1) * 4096;
    if (lw & 1) dma_grp<3, mod_b<ID>()>(d, L.vb, L.qb, st * (32 * F_ * 2));
    else dma_grp<4, mod_b<ID>()>(d, L.vb, L.qb, st * (32 * F_ * 2));
  } else {
    dma_grp<4, mod_b<ID>()>(dst + lw * 4096, L.vb, L.qb, st * (ID == 1 ? 128 : (ID == 2 ? 2048 : 64 * D_ * 2)));
  }
}

template <int ID> __device__ __forceinline__ void ld_const(const Ld& L, unsigned ring, unsigned lw, unsigned n) { if constexpr (ID == 2) ld_a<ID>(L, ring, lw, n); else ld_b<ID>(L, ring, lw, n); }
template <int ID> __device__ __forceinline__ void ld_dep(const Ld& L, unsigned ring, unsigned lw, unsigned n) { if constexpr (ID == 2) ld_b<ID>(L, ring, lw, n); else ld_a<ID>(L, ring, lw, n); }

template <int ID>
__device__ __forceinline__ void sq_loader(const FArgs& p, const Ld& L, Ld& N, unsigned lds0, unsigned lw, unsigned xcd, unsigned idx, unsigned& touch) {
  constexpr int NT = nt_of<ID>(), NEXT = ID == 4 ? 0 : ID + 1;
  const unsigned ring = lds0 + F_RING;
  if constexpr (ID == 1) {
    ld_a<ID>(L, ring, lw, 0); ld_b<ID>(L, ring, lw, 0);
    N = ld_setup<NEXT>(p, lw, xcd, idx); ld_pin(N);
    ld_a<ID>(L, ring, lw, 1); ld_b<ID>(L, ring, lw, 1);
  } else {
    ld_dep<ID>(L, ring, lw, 0); ld_dep<ID>(L, ring, lw, 1);
  }
  constexpr unsigned KA = ID <= 2 ? 128 : 2048, KB = ID == 1 ? 128 : (ID == 2 ? 2048 : 64 * D_ * 2);
  unsigned sa, sb, ma;
  auto prep = [&](unsigned n) {
    const unsigned st = stage_of<nc_of<ID>(), NT>(n, L.cls);
    sa = st * KA; sb = st * KB; ma = ring + (n & 3) * F_ST + lw * 4096;
    asm volatile("" : "+s"(sa), "+s"(sb), "+s"(ma));
  };
  auto bar_a = [&]() {
    dma_grp<5, mod_a<ID>()>(ma, L.va, L.qa, sa);
  };
  prep(2);
  wait_vm<(ID == 1 ? 8 : 4)>();
  bar_a();
  unsigned mb_ = ma + F_A, sb_ = sb;
  prep(3);
#pragma unroll 1
  for (unsigned n = 0; n + 3 < NT; ++n) {
    dma_grp<4, mod_b<ID>()>(mb_, L.vb, L.qb, sb_);
    bar_a();
    mb_ = ma + F_A; sb_ = sb;
    asm volatile("" : "+s"(mb_), "+s"(sb_));
    prep(n + 4);
  }
  dma_grp<4, mod_b<ID>()>(mb_, L.vb, L.qb, sb_);
  wait_vm<8>();
  barrier();
  const int lane = lane_id();
  u32x2 l0, l1;
  const char* lp = (const char*)p.flags + FL_LN + (lw < 2 ? (((xcd & 1) * 8 + (idx >> 2)) * 128 + lw * 64 + lane) : (2048 + ((xcd >> 1) * 4 + (idx & 3)) * 128 + (lw - 2) * 64 + lane)) * 16;
  if constexpr (ID == 1) asm volatile("global_load_dwordx2 %0, %2, off sc1\n\tglobal_load_dwordx2 %1, %2, off offset:8 sc1" : "=&v"(l0), "=&v"(l1) : "v"(lp) : "memory");
  if constexpr (NEXT != 0) ld_const<NEXT>(N, ring, lw, 0);
  if constexpr (ID == 1) wait_vm<6>();
  else if constexpr (ID == 2) { if (lw & 1) wait_vm<3>(); else wait_vm<4>(); }
  else wait_vm<0>();
  barrier();
  if constexpr (NEXT != 0) ld_const<NEXT>(N, ring, lw, 1);
  if constexpr (ID == 1) {
    if (lw < 2) {
      const char* tp = (const char*)((xcd & 1) ? p.cim : p.cre) + (size_t)((xcd >> 1) * 512 + idx * 16 + lw * 8 + (lane >> 3)) * (S_ * 2) + (lane & 7) * 128;
      asm volatile("global_load_dword %0, %1, off" : "+v"(touch) : "v"(tp) : "memory");
    }
  }
  if constexpr (ID == 2) {
    if (lw < 2) {
      const char* tp = (const char*)((lw & 1) ? p.wu : p.wg) + (size_t)(idx * 8 + (lane >> 3)) * (F_ * 2) + xcd * 896 + ((lane & 7) < 7 ? (lane & 7) : 6) * 128;
      asm volatile("global_load_dword %0, %1, off" : "+v"(touch) : "v"(tp) : "memory");
    }
  }
  barrier();
  const unsigned tag = lds_tag(lds0);
  if constexpr (ID == 1) {
    if (lw < 2) wait_vm<9>(); else wait_vm<8>();
    asm volatile("" : "+v"(l0), "+v"(l1));
#pragma unroll 1
    for (unsigned spins = 0; spins < (1u << 15) && !__all(l0.y == tag && l1.y == tag); ++spins) {
      __builtin_amdgcn_s_sleep(2);
      asm volatile("global_load_dwordx2 %0, %2, off sc1\n\tglobal_load_dwordx2 %1, %2, off offset:8 sc1\n\ts_waitcnt vmcnt(0)" : "=&v"(l0), "=&v"(l1) : "v"(lp) : "memory");
    }
    LAS unsigned* tb = (LAS unsigned*)(lds0 + F_TAB);
    tb[lw * 64 + lane + (lw < 2 ? 0 : 128)] = l0.x;
    if (lw < 2) tb[128 + lw * 64 + lane] = l1.x;
    wait_lgkm0();
  }
  barrier();
  barrier();
  if constexpr (ID != 1) {
    barrier();
    if (lw == 3) poll_tags((char*)p.flags + FL_PAIR + ((ID == 2 ? 0 : 256) + (blockIdx.x ^ 1)) * 128, 1, tag);
    barrier();
  }
}

template <int ID>
__device__ __forceinline__ void sq_compute(const FArgs& p, unsigned lds0, unsigned w, unsigned xcd, unsigned idx, unsigned& tag) {
  constexpr int NT = nt_of<ID>();
  u32x4 xv[2], wv[2];
  unsigned tv = 0;
  const unsigned r0 = blockIdx.x * 8 + w;
  char* fl = (char*)p.flags;
  if constexpr (ID == 1) {
    const int lane = lane_id();
    const u16* xp = (const u16*)p.x + (size_t)r0 * D_ + lane * 8;
    const u16* wp = (const u16*)(r0 < 1024 ? p.dre : p.dim) + (size_t)(r0 & 1023) * D_ + lane * 8;
#pragma unroll
    for (int i = 0; i < 2; ++i) xv[i] = *(const u32x4*)(xp + i * 512);
#pragma unroll
    for (int i = 0; i < 2; ++i) wv[i] = *(const u32x4*)(wp + i * 512);
    tv = __hip_atomic_load((const unsigned*)(fl + FL_EPOCH), __ATOMIC_RELAXED, __HIP_MEMORY_SCOPE_AGENT);
    SB();
  }
  const unsigned ring = lds0 + F_RING;
  const unsigned g = w >> 2, wm = (w >> 1) & 1, wx = w & 1;
  const unsigned kh = xcd & 1, tm = (xcd >> 1) * 4 + (idx & 3), tn = idx >> 2;
  u32x4 res[2];
  unsigned rdA[8], rdB[4], hoff = 0;
  const unsigned rotA = (ID == 1) ? 2 * g : 2 * g + wx;
  {
    const int lane = lane_id(), fr = lane & 15, fq = lane >> 4, chunk = ((fq & 1) << 1) | (fq >> 1);
    const int row = tm * 128 + wm * 64 + rotA * 16 + fr, col = tn * 128 + kh * 32 + chunk * 8;
    if constexpr (ID == 2) {
#pragma unroll
      for (int c = 0; c < 2; ++c) res[c] = *(const u32x4*)((const u16*)p.x + (size_t)row * D_ + col + (c ^ wx) * 64);
    } else if constexpr (ID == 4) {
      hoff = ((row >> 4) * (D_ / 32) + (col >> 5)) * 1024 + (chunk * 16 + fr) * 16;
    }
    const unsigned rotB = (ID == 1) ? 0u : 2 * kh, base = ring + g * F_ST;
#pragma unroll
    for (int i = 0; i < 4; ++i) {
      const unsigned kk = i >> 1, hf = (i & 1) * 2;
      if constexpr (ID == 1) {
        rdA[i] = base + (wm * 64 + (hf ^ rotA) * 16 + fr) * 128 + (((kk * 4 + fq) ^ (fr >> 1)) << 4);
        rdB[i] = base + F_A + (wx * 64 + fr) * 128 + ((((i & 1) * 4 + fq) ^ (fr >> 1)) << 4);
      } else if constexpr (ID == 2) {
        rdA[i] = base + (wm * 64 + (i ^ rotA) * 16 + fr) * 128 + ((fq ^ (fr >> 1)) << 4);
        rdA[4 + i] = base + (wm * 64 + (i ^ rotA) * 16 + fr) * 128 + (((4 + fq) ^ (fr >> 1)) << 4);
        rdB[i] = base + F_A + wx * 8192 + (hf ^ rotB) * 2048 + lane * 16;
      } else {
        const int q = fr >> 2, pp = fr & 3, f = q | ((fq & 1) << 2);
        rdA[i] = base + wm * 8192 + (i ^ rotA) * 2048 + lane * 16;
        rdB[i] = base + F_A + (8 * fq + q) * 256 + (((wx * 4 + (i ^ rotB)) ^ f) << 5) + pp * 8;
      }
    }
  }
  constexpr int NVA = (ID == 2) ? 8 : 4, NVB = (ID <= 2) ? 2 : 4;
  bf16x8 fa[2][4], fb[2][4];
  f32x4 acc[4][4];
#pragma unroll
  for (int i = 0; i < 16; ++i) { acc[i >> 2][i & 3] = (f32x4){0.f, 0.f, 0.f, 0.f}; asm volatile("" : "+v"(acc[i >> 2][i & 3])); }
  auto reads = [&]() {
    SB();
#pragma unroll
    for (int s = 0; s < 2; ++s)
#pragma unroll
      for (int i = 0; i < 4; ++i) {
        if constexpr (ID == 1) {
          fb[s][i] = lds_b128(rdB[s] + i * 2048);
          fa[s][i] = lds_b128(rdA[s * 2 + (i >> 1)] + (i & 1) * 2048);
        } else if constexpr (ID == 2) {
          fb[s][i] = lds_b128(rdB[i >> 1] + (i & 1) * 2048 + s * 1024);
          fa[s][i] = lds_b128(rdA[s * 4 + i]);
        } else {
          fb[s][i] = lds_tr(rdB[i] + s * 8192, rdB[i] + s * 8192 + 1024);
          fa[s][i] = lds_b128(rdA[i] + s * 1024);
        }
      }
    SB();
  };
#pragma unroll
  for (int j = 0; j < NVA; ++j) asm volatile("" : "+v"(rdA[j]));
#pragma unroll
  for (int j = 0; j < NVB; ++j) asm volatile("" : "+v"(rdB[j]));
  if constexpr (ID == 1) {
    SB();
    asm volatile("" : "+v"(tv));
    tv = tv * 0x9E3779B1u + 0x7F4A7C15u;
    const int lane = lane_id();
    float s = 0.f, t = 0.f;
#pragma unroll
    for (int j = 0; j < 8; ++j) s += bf_lo(xv[j >> 2][j & 3]) + bf_hi(xv[j >> 2][j & 3]);
    const float m = wave_sum(s) * (1.0f / D_);
    float q = 0.f;
#pragma unroll
    for (int j = 0; j < 8; ++j) {
      const float a = bf_lo(xv[j >> 2][j & 3]) - m, b = bf_hi(xv[j >> 2][j & 3]) - m;
      q += a * a + b * b;
      t += bf_lo(wv[j >> 2][j & 3]) + bf_hi(wv[j >> 2][j & 3]);
    }
    const float rs = __builtin_amdgcn_rsqf(wave_sum(q) * (1.0f / D_) + EPS);
    t = wave_sum(t);
    const float val = (lane & 2) ? t : ((lane & 1) ? rs : m);
    if (lane < 4)
      __hip_atomic_store((unsigned long long*)(fl + FL_LN + ((lane >> 1) * 2048 + r0) * 16 + (lane & 1) * 8), ((unsigned long long)tv << 32) | __float_as_uint(val),
                         __ATOMIC_RELAXED, __HIP_MEMORY_SCOPE_AGENT);
    tag = rfl(tv);
    if (w == 0) { *(volatile LAS unsigned*)(lds0 + F_TAG) = tag; wait_lgkm0(); }
  }
  if constexpr (ID != 1) {
    barrier();
  }
  if constexpr (ID == 4) {
    const auto rh = OUT_RSRC(p.h, S_ * D_ * 2);
#pragma unroll
    for (int c = 0; c < 2; ++c) res[c] = __builtin_amdgcn_raw_buffer_load_b128(rh, hoff, (c ^ wx) * 2048, 16);
  }
  barrier();
  if (g == 1) barrier();
  reads();
#pragma unroll 1
  for (unsigned i = 0; i < NT / 2; ++i) {
    const unsigned delta = (i & 1) ? (unsigned)(-2 * F_ST) : (unsigned)(2 * F_ST);
#pragma unroll
    for (int j = 0; j < NVA; ++j) { rdA[j] += delta; asm volatile("" : "+v"(rdA[j])); }
#pragma unroll
    for (int j = 0; j < NVB; ++j) { rdB[j] += delta; asm volatile("" : "+v"(rdB[j])); }
    wait_lgkm0();
    barrier();
    SB();
#pragma unroll
    for (int m = 0; m < 32; ++m) {
      const int n = (m & 4) ? 3 - (m & 3) : (m & 3);
      MFMA(fb[m >> 4][n], fa[m >> 4][(m >> 2) & 3], acc[(m >> 2) & 3][n]);
    }
    SB();
    barrier();
    if (i + 1 < NT / 2) reads();
  }
  mfma_drain();
  SB();
  const int lane = lane_id(), fr = lane & 15, fq = lane >> 4, chunk = ((fq & 1) << 1) | (fq >> 1);
  if constexpr (ID == 1) {
    const unsigned wb = ring + 2 * F_ST + (w ^ 4) * 8192 + lane * 16, rb = ring + 2 * F_ST + w * 8192 + lane * 16;
#pragma unroll
    for (int i = 0; i < 8; ++i) *(LAS f32x4*)(wb + i * 1024) = acc[2 + (i >> 2)][i & 3];
    if (g == 0) barrier();
    wait_lgkm0();
    barrier();
#pragma unroll
    for (int i = 0; i < 8; ++i) acc[i >> 2][i & 3] += *(const LAS f32x4*)(rb + i * 1024);
  }
  if constexpr (ID == 1) {
    const LAS float* tb = (const LAS float*)(lds0 + F_TAB);
    const float sg = tm < 8 ? 1.f : -1.f;
    const auto ro = OUT_RSRC(p.ab, D_ * 4096 * 2);
    f32x4 mean[4], rstd[4];
#pragma unroll
    for (int j = 0; j < 4; ++j) {
      mean[j] = *(const LAS f32x4*)(tb + wx * 64 + j * 16 + fq * 4);
      rstd[j] = *(const LAS f32x4*)(tb + 128 + wx * 64 + j * 16 + fq * 4) * sg;
    }
#pragma unroll
    for (int mb = 0; mb < 2; ++mb) {
      const float wsm = tb[256 + wm * 64 + g * 32 + mb * 16 + fr];
#pragma unroll
      for (int np = 0; np < 2; ++np) {
        const f32x4 x0 = (acc[mb][2 * np] - mean[2 * np] * wsm) * rstd[2 * np], x1 = (acc[mb][2 * np + 1] - mean[2 * np + 1] * wsm) * rstd[2 * np + 1];
        auto s0 = __builtin_amdgcn_permlane16_swap(pk_bf16(x0[0], x0[1]), pk_bf16(x1[0], x1[1]), false, false);
        auto s1 = __builtin_amdgcn_permlane16_swap(pk_bf16(x0[2], x0[3]), pk_bf16(x1[2], x1[3]), false, false);
        const int rblk = (tm & 7) * 8 + wm * 4 + g * 2 + mb;
        const int kblk = (tm >> 3) * 64 + (kh * 8 + tn) * 4 + wx * 2 + np;
        STORE16(ro, (rblk * 128 + kblk) * 1024 + (chunk * 16 + fr) * 16, ((u32x4){s0[0], s1[0], s0[1], s1[1]}));
      }
    }
    return;
  }
  const auto rs = OUT_RSRC((char*)p.xch + (ID == 2 ? 0 : 256 * 16384), 256 * 16384);
  const unsigned sreg = ring + ((w >> 1) & 1) * F_ST + (w & 1) * 8192 + lane * 16, kreg = ring + 2 * F_ST + lane * 16;
  auto put_keep = [&]() {
#pragma unroll
    for (int j = 1; j < 4; ++j) {
      const unsigned d = kreg + ((wm * 4 + (j ^ rotA)) * 3 + (j - 1)) * 2048;
      *(LAS f32x4*)(d) = acc[j][0];
      *(LAS f32x4*)(d + 1024) = acc[j][1];
    }
  };
  if (g == 0) {
#pragma unroll
    for (int i = 0; i < 8; ++i) *(LAS f32x4*)(sreg + i * 1024) = acc[i >> 1][2 + (i & 1)];
    put_keep();
    wait_lgkm0();
    barrier();
  } else {
#pragma unroll
    for (int j = 0; j < 4; ++j) {
      const f32x4 x0 = acc[j][2] + *(const LAS f32x4*)(sreg + (j ^ 2) * 2048), x1 = acc[j][3] + *(const LAS f32x4*)(sreg + (j ^ 2) * 2048 + 1024);
      auto s0 = __builtin_amdgcn_permlane16_swap(pk_bf16(x0[0], x0[1]), pk_bf16(x1[0], x1[1]), false, false);
      auto s1 = __builtin_amdgcn_permlane16_swap(pk_bf16(x0[2], x0[3]), pk_bf16(x1[2], x1[3]), false, false);
      __builtin_amdgcn_raw_buffer_store_b128((u32x4){s0[0], s1[0], s0[1], s1[1]}, rs, lane * 16 + blockIdx.x * 16384 + ((wm * 4 + (j ^ rotA)) * 2 + wx) * 1024, 0, 16);
    }
    SB();
    put_keep();
    wait_lgkm0();
  }
  barrier();
  f32x4 y[2][2];
  {
    const unsigned kb = kreg + (wm * 4 + rotA) * 3 * 2048;
    f32x4 t[2][2];
#pragma unroll
    for (int i = 0; i < 2; ++i) {
      t[0][i] = acc[0][i] + *(const LAS f32x4*)(kb + 2048 + i * 1024);
      t[1][i] = *(const LAS f32x4*)(kb + i * 1024) + *(const LAS f32x4*)(kb + 4096 + i * 1024);
    }
#pragma unroll
    for (int c = 0; c < 2; ++c)
#pragma unroll
      for (int j = 0; j < 4; ++j) {
        auto s = __builtin_amdgcn_permlane16_swap(__float_as_uint(t[c][0][j]), __float_as_uint(t[c][1][j]), false, false);
        y[c][0][j] = __uint_as_float(s[0]) + ((j & 1) ? bf_hi(res[c][j >> 1]) : bf_lo(res[c][j >> 1]));
        y[c][1][j] = __uint_as_float(s[1]) + ((j & 1) ? bf_hi(res[c][2 + (j >> 1)]) : bf_lo(res[c][2 + (j >> 1)]));
      }
  }
  const int row0 = tm * 128 + wm * 64 + rotA * 16 + fr, col = tn * 128 + kh * 32 + chunk * 8;
  unsigned optr = (ID == 2) ? ((row0 >> 4) * (D_ / 32) + (col >> 5)) * 1024 + (chunk * 16 + fr) * 16 : (row0 * D_ + col) * 2;
  const auto ro = OUT_RSRC(ID == 2 ? p.h : p.out, S_ * D_ * 2);
  asm volatile("" : "+v"(y[0][0]), "+v"(y[0][1]), "+v"(y[1][0]), "+v"(y[1][1]), "+v"(optr));
  wait_vm<0>();
  barrier();
  if (w == 0 && lane == 0) put_tag((char*)p.flags + FL_PAIR + ((ID == 2 ? 0 : 256) + blockIdx.x) * 128, tag);
  barrier();
  u32x4 in[2], v[2];
#pragma unroll
  for (int c = 0; c < 2; ++c) in[c] = __builtin_amdgcn_raw_buffer_load_b128(rs, lane * 16, (blockIdx.x ^ 1) * 16384 + ((wm * 4 + rotA) * 2 + (c ^ wx)) * 1024, 16);
#pragma unroll
  for (int c = 0; c < 2; ++c) {
    const f32x4 a = y[c][0] + (f32x4){bf_lo(in[c][0]), bf_hi(in[c][0]), bf_lo(in[c][1]), bf_hi(in[c][1])};
    const f32x4 b = y[c][1] + (f32x4){bf_lo(in[c][2]), bf_hi(in[c][2]), bf_lo(in[c][3]), bf_hi(in[c][3])};
    v[c] = (u32x4){pk_bf16(a[0], a[1]), pk_bf16(a[2], a[3]), pk_bf16(b[0], b[1]), pk_bf16(b[2], b[3])};
    __builtin_amdgcn_raw_buffer_store_b128(v[c], ro, optr + (c ^ wx) * (ID == 2 ? 2048 : 128), 0, ST_AUX);
  }
  SB();
  if constexpr (ID == 2) {
    float s = 0.f;
#pragma unroll
    for (int j = 0; j < 8; ++j) s += bf_lo(v[j >> 2][j & 3]) + bf_hi(v[j >> 2][j & 3]);
    const float m = sum_rows(s) * (1.0f / 64.0f);
    float q = 0.f;
#pragma unroll
    for (int j = 0; j < 8; ++j) {
      const float a = bf_lo(v[j >> 2][j & 3]) - m, b = bf_hi(v[j >> 2][j & 3]) - m;
      q += a * a + b * b;
    }
    const f32x2 st = {m, sum_rows(q)};
    if (fq == 0)
      __hip_atomic_store((unsigned long long*)p.stats + (size_t)(tn * 2 + kh) * S_ + row0, __builtin_bit_cast(unsigned long long, st), __ATOMIC_RELAXED, __HIP_MEMORY_SCOPE_AGENT);
  }
  SB();
}

__device__ __forceinline__ void glu_loader(const FArgs& p, const Ld& L, Ld& N, unsigned lds0, unsigned lw, unsigned xcd, unsigned idx, unsigned& touch) {
  constexpr int NT = 32, NEXT = 4;
  const unsigned tag = lds_tag(lds0);
  const unsigned ring = lds0 + F_RING;
  LAS float* cst = (LAS float*)(lds0 + F_TAB + 2048);
  unsigned long long* gcs = (unsigned long long*)((char*)p.flags + FL_CS) + (xcd * 4 + (idx >> 3)) * (8 * 224);
  ld_a<3>(L, ring, lw, 0);
  ld_a<3>(L, ring, lw, 1);
  const int lane = lane_id();
  const unsigned u = lw * 64 + lane < 224 ? lw * 64 + lane : 223u;
  wait_vm<4>();
  barrier();
  ld_a<3>(L, ring, lw, 2);
  ld_b<3>(L, ring, lw, 2);
#pragma unroll 1
  for (unsigned t = 0; t + 3 < NT; ++t) {
    if (t == 24) {
      const unsigned hf = u >= 112, c = u - hf * 112, wn_ = c >= 56, cc = c - wn_ * 56, blk = cc >> 4;
      const unsigned src = lds0 + (wn_ * 4 + blk) * 768 + (blk == 3 ? 512 + (hf * 8 + (cc & 7)) * 4 : hf * 256 + (cc & 15) * 4);
      const float v = (*(const LAS float*)(src) + *(const LAS float*)(src + 64)) + (*(const LAS float*)(src + 128) + *(const LAS float*)(src + 192));
      asm volatile("global_store_dwordx2 %0, %1, off sc1" ::"v"(gcs + L.cls * 224 + u), "v"(((unsigned long long)tag << 32) | __float_as_uint(v)) : "memory");
    }
    ld_a<3>(L, ring, lw, t + 3);
    barrier();
    ld_b<3>(L, ring, lw, t + 3);
    wait_vm<14>();
    barrier();
  }
  unsigned long long g[8];
#pragma unroll
  for (int i = 0; i < 8; ++i) asm volatile("global_load_dwordx2 %0, %1, off sc1" : "=v"(g[i]) : "v"(gcs + i * 224 + u) : "memory");
  barrier();
  ld_b<NEXT>(N, ring, lw, 0);
  wait_vm<19>();
  barrier();
  barrier();
  ld_b<NEXT>(N, ring, lw, 1);
  wait_vm<16>();
  barrier();
  barrier();
  barrier();
  asm volatile("s_waitcnt vmcnt(8)" : "+v"(g[0]), "+v"(g[1]), "+v"(g[2]), "+v"(g[3]), "+v"(g[4]), "+v"(g[5]), "+v"(g[6]), "+v"(g[7]));
#pragma unroll 1
  for (unsigned spins = 0; spins < (1u << 15); ++spins) {
    unsigned tg = 0u;
#pragma unroll
    for (int i = 0; i < 8; ++i) tg |= (unsigned)(g[i] >> 32) ^ tag;
    if (__all(tg == 0u)) break;
    __builtin_amdgcn_s_sleep(2);
#pragma unroll
    for (int i = 0; i < 8; ++i) asm volatile("global_load_dwordx2 %0, %1, off sc1" : "+v"(g[i]) : "v"(gcs + i * 224 + u) : "memory");
    asm volatile("s_waitcnt vmcnt(0)" : "+v"(g[0]), "+v"(g[1]), "+v"(g[2]), "+v"(g[3]), "+v"(g[4]), "+v"(g[5]), "+v"(g[6]), "+v"(g[7]));
  }
  float s = 0.f;
#pragma unroll
  for (int i = 0; i < 8; ++i) s += __uint_as_float((unsigned)g[i]);
  cst[u] = s;
  wait_lgkm0();
  {
    const char* tp = (const char*)p.wd + ((size_t)blockIdx.x * 224 + (lw * 56 + (lane < 56 ? lane : 55))) * 128;
    asm volatile("global_load_dword %0, %1, off" : "+v"(touch) : "v"(tp) : "memory");
  }
  barrier();
}

__device__ __forceinline__ void glu_compute(const FArgs& p, unsigned lds0, unsigned w, unsigned xcd, unsigned idx) {
  constexpr int ID = 3, BM = 256, NT = 32, MB = 4, NB = 7;
  const unsigned ring = lds0 + F_RING;
  LAS f32x2* tab = (LAS f32x2*)(lds0 + F_TAB);
  LAS float* cst = (LAS float*)(lds0 + F_TAB + 2048);
  const unsigned tm = idx & 7, tn = xcd * 4 + (idx >> 3), cls = (tm + 2 * (idx >> 3)) & 7;
  const unsigned wm = w & 3, wn = w >> 2;
  const int lane = lane_id(), fr = lane & 15, fq = lane >> 4;
  const unsigned rot = wm == 3 ? 0u : wm;
  unsigned xa = ring + wm * 4096 + lane * 16, xb[3], xb1;
  {
    const int q = fr >> 2, pp = fr & 3, r = (fq >> 1) * 16 + (fq & 1) * 4 + q;
#pragma unroll
    for (int i = 0; i < 3; ++i) xb[i] = ring + F_A + r * 224 + wn * 112 + pp * 8 + ((i + rot) % 3) * 32;
    xb1 = ring + F_A + r * 224 + wn * 112 + 96 + (pp & 1) * 8 + (pp >> 1) * G3_UP;
  }
  float cs[3] = {0.f, 0.f, 0.f};
  bf16x8 fa[MB], fb[NB];
  f32x4 acc[MB][NB];
#pragma unroll
  for (int mb = 0; mb < MB; ++mb)
#pragma unroll
    for (int nb = 0; nb < NB; ++nb) { acc[mb][nb] = (f32x4){0.f, 0.f, 0.f, 0.f}; asm volatile("" : "+v"(acc[mb][nb])); }
  asm volatile("" : "+v"(xa), "+v"(xb[0]), "+v"(xb[1]), "+v"(xb[2]), "+v"(xb1));
  barrier();
  if (wn == 1) {
    const auto rst = OUT_RSRC(p.stats, 8 * S_ * 16);
    f32x2 sv[16];
#pragma unroll
    for (int i = 0; i < 16; ++i) sv[i] = __builtin_bit_cast(f32x2, __builtin_amdgcn_raw_buffer_load_b64(rst, (tm * BM + wm * 64 + lane) * 8, i * S_ * 8, 16));
    SB();
    barrier();
    SB();
    float m = 0.f;
#pragma unroll
    for (int i = 0; i < 16; ++i) m += sv[i][0];
    m *= (1.0f / 16.0f);
    float q = 0.f;
#pragma unroll
    for (int i = 0; i < 16; ++i) {
      const float d = sv[i][0] - m;
      q += sv[i][1] + 64.0f * d * d;
    }
    tab[wm * 64 + lane] = (f32x2){m, q * (1.0f / D_) + EPS};
  }
  wait_lgkm0();
  barrier();
  const unsigned csmask = (0x010101u << cls) | (1u << ((cls + 1) & 7));
  auto reads = [&]() {
    SB();
    fa[0] = lds_b128(xa);
#pragma unroll
    for (int j = 0; j < 7; ++j) {
      const unsigned o = j < 6 ? xb[j % 3] + (j / 3) * G3_UP : xb1;
      fb[j] = lds_tr(o, o + 1792);
    }
#pragma unroll
    for (int mb = 1; mb < MB; ++mb) fa[mb] = lds_b128(xa + mb * 1024);
    SB();
  };
  reads();
#pragma unroll 1
  for (unsigned t = 0; t < NT; ++t) {
    const unsigned delta = ((t & 3) == 3) ? (unsigned)(-3 * F_ST) : (unsigned)F_ST;
    xa += delta; xb[0] += delta; xb[1] += delta; xb[2] += delta; xb1 += delta;
    asm volatile("" : "+v"(xa), "+v"(xb[0]), "+v"(xb[1]), "+v"(xb[2]), "+v"(xb1));
    if (__builtin_expect((csmask >> t) & 1u, 0)) {
#pragma unroll
      for (int j = 0; j < 4; ++j)
#pragma unroll
        for (int i = 0; i < 3; ++i)
          asm volatile("v_dot2c_f32_bf16 %0, %1, %2" : "+v"(cs[i]) : "s"(0x3f803f80u), "v"(__builtin_bit_cast(u32x4, fb[3 * i])[j]));
      asm volatile("s_mov_b32 m0, %3\n\ts_nop 2\n\tds_write_addtid_b32 %0\n\tds_write_addtid_b32 %1 offset:256\n\tds_write_addtid_b32 %2 offset:512" ::"v"(cs[0]),
                   "v"(cs[1]), "v"(cs[2]), "s"(lds0 + w * 768)
                   : "memory");
    }
    wait_lgkm0();
    barrier();
    SB();
#pragma unroll
    for (int mb = 0; mb < MB; ++mb)
#pragma unroll
      for (int i = 0; i < NB; ++i) {
        const int nb = (mb & 1) ? NB - 1 - i : i;
        MFMA(fb[nb], fa[mb], acc[mb][nb]);
      }
    SB();
    barrier();
    reads();
  }
  if (wn == 0) barrier();
  mfma_drain();
  SB();
  const int lane2 = lane_id();
  const int gr = lane2 & 15, gq = lane2 >> 4;
  auto act2 = [](f32x2 a, f32x2 b, float sc, float k) {
    const f32x2 t = a * sc;
    const f32x2 e = {__builtin_amdgcn_exp2f(t.x), __builtin_amdgcn_exp2f(t.y)};
    const f32x2 d = e * k + k;
    const f32x2 r = {__builtin_amdgcn_rcpf(d.x), __builtin_amdgcn_rcpf(d.y)};
    const f32x2 o = (a * b) * r;
    return pk_bf16(o.x, o.y);
  };
  const auto ro = OUT_RSRC(p.gu, S_ * F_ * 2);
  float nm[MB], kk[MB], sc[MB];
#pragma unroll
  for (int mb = 0; mb < MB; ++mb) {
    const f32x2 st = tab[wm * 64 + mb * 16 + gr];
    nm[mb] = -st.x; kk[mb] = st.y; sc[mb] = -1.44269504089f * __builtin_amdgcn_rsqf(st.y);
  }
  const int gch0 = tn * 14 + wn * 7;
  const unsigned obase = (tm * (BM / 16) + wm * MB) * (F_ / 32) * 1024 + gr * 16;
#pragma unroll
  for (int i = 0; i < 3; ++i) {
    const unsigned ib = (i + rot) % 3;
    const f32x4 cg = *(const LAS f32x4*)(cst + wn * 56 + ib * 16 + gq * 4), cu = *(const LAS f32x4*)(cst + 112 + wn * 56 + ib * 16 + gq * 4);
    const unsigned oo = obase + (gq & 1) * (F_ / 32) * 1024 + (gch0 + 2 * ib + (gq >> 1)) * 256;
#pragma unroll
    for (int mp = 0; mp < MB / 2; ++mp) {
      u32x2 pkd[2];
#pragma unroll
      for (int h = 0; h < 2; ++h) {
        const int mb = 2 * mp + h;
        const f32x4 a = cg * nm[mb] + acc[mb][i], b = cu * nm[mb] + acc[mb][3 + i];
        pkd[h] = (u32x2){act2(a.xy, b.xy, sc[mb], kk[mb]), act2(a.zw, b.zw, sc[mb], kk[mb])};
      }
      auto s0 = __builtin_amdgcn_permlane16_swap(pkd[0].x, pkd[1].x, false, false);
      auto s1 = __builtin_amdgcn_permlane16_swap(pkd[0].y, pkd[1].y, false, false);
      __builtin_amdgcn_raw_buffer_store_b128((u32x4){s0[0], s1[0], s0[1], s1[1]}, ro, oo + mp * 2 * (F_ / 32) * 1024, 0, ST_AUX);
    }
  }
  {
    const f32x4 cm = *(const LAS f32x4*)(cst + (gq >> 1) * 112 + wn * 56 + 48 + (gq & 1) * 4);
    u32x2 pkd[MB / 2];
#pragma unroll
    for (int mp = 0; mp < MB / 2; ++mp) {
      const f32x4 v0 = cm * nm[2 * mp] + acc[2 * mp][6], v1 = cm * nm[2 * mp + 1] + acc[2 * mp + 1][6];
      f32x4 a, b;
#pragma unroll
      for (int j = 0; j < 4; ++j) {
        auto s = __builtin_amdgcn_permlane32_swap(__float_as_uint(v0[j]), __float_as_uint(v1[j]), false, false);
        a[j] = __uint_as_float(s[0]); b[j] = __uint_as_float(s[1]);
      }
      const float scs = (gq & 2) ? sc[2 * mp + 1] : sc[2 * mp], ks = (gq & 2) ? kk[2 * mp + 1] : kk[2 * mp];
      pkd[mp] = (u32x2){act2(a.xy, b.xy, scs, ks), act2(a.zw, b.zw, scs, ks)};
    }
    auto s0 = __builtin_amdgcn_permlane16_swap(pkd[0].x, pkd[1].x, false, false);
    auto s1 = __builtin_amdgcn_permlane16_swap(pkd[0].y, pkd[1].y, false, false);
    STORE16(ro, obase + ((gq & 1) * 2 + (gq >> 1)) * (F_ / 32) * 1024 + (gch0 + 6) * 256, ((u32x4){s0[0], s1[0], s0[1], s1[1]}));
  }
}

__global__ __launch_bounds__(768, 3) void k_block(FArgs p) {
  extern __shared__ __attribute__((aligned(16))) char shm[];
  const unsigned lds0 = (unsigned)(size_t)(LAS char*)shm;
  const unsigned w = rfl(threadIdx.x >> 6);
  const unsigned xcd = blockIdx.x & 7, idx = blockIdx.x >> 3;
  char* fl = (char*)p.flags;
  const unsigned tm = (xcd >> 1) * 4 + (idx & 3), kh = xcd & 1, lj = idx >> 2;
  if (w >= 8) {
    const unsigned lw = w - 8;
    Ld LA, LB;
    unsigned touch = 0;
    LA = ld_setup<1>(p, lw, xcd, idx);
    sq_loader<1>(p, LA, LB, lds0, lw, xcd, idx, touch);
    const unsigned tag = lds_tag(lds0);
    LA = ld_setup<3>(p, lw, xcd, idx);
    ld_pin(LA);
    barrier();
    if (lw == 3) poll_tags(fl + FL_P1 + ((kh * 8 + lj) * 4 + (xcd >> 1)) * 128, 16, tag);
    barrier();
    sq_loader<2>(p, LB, LA, lds0, lw, xcd, idx, touch);
    LB = ld_setup<4>(p, lw, xcd, idx);
    ld_pin(LB);
    barrier();
    if (lw == 3) poll_tags(fl + FL_P2 + ((idx & 7) * 8 + xcd) * 128, 32, tag);
    barrier();
    glu_loader(p, LA, LB, lds0, lw, xcd, idx, touch);
    barrier();
    if (lw == 3) poll_tags(fl + FL_P3 + ((tm >> 1) * 2 + kh) * 128, 16, tag);
    barrier();
    sq_loader<4>(p, LB, LA, lds0, lw, xcd, idx, touch);
    asm volatile("" ::"v"(touch));
    return;
  }
  unsigned csink = 0;
  if (w == 0 && idx < 3) {
    unsigned long long pc;
    asm volatile("s_getpc_b64 %0" : "=s"(pc));
    const unsigned ln = idx * 64 + lane_id();
    asm volatile("global_load_dword %0, %1, %2" : "+v"(csink) : "v"((ln < CODE_LINES ? ln : CODE_LINES - 1) * 128), "s"(pc) : "memory");
  }
  if (w == 1 && idx < 20) {
    const char* base = idx < 2 ? (const char*)p.cre : idx < 4 ? (const char*)p.cim : idx < 6 ? (const char*)p.wg : idx < 8 ? (const char*)p.wu : idx < 10 ? (const char*)p.wd : idx < 11 ? (const char*)p.out : (const char*)p.ab;
    const unsigned size = idx < 4 ? S_ * S_ * 2 : idx < 10 ? D_ * F_ * 2 : idx < 11 ? S_ * D_ * 2 : (unsigned)WS_FL;
    const unsigned off = ((idx < 10 ? (idx & 1) : (idx < 11 ? 0 : idx - 11)) * 64 + lane_id()) * 65536;
    asm volatile("global_load_dword %0, %1, %2 sc1" : "+v"(csink) : "v"(off < size ? off : 0u), "s"(base) : "memory");
  }
  unsigned tag = 0;
  sq_compute<1>(p, lds0, w, xcd, idx, tag);
  asm volatile("" ::"v"(csink));
  wait_vm<0>();
  barrier();
  if (w == 0) { const int l = lane_id(); if (l < 4) put_tag(fl + FL_P1 + (tm * 4 + l) * 128 + (kh * 8 + lj) * 4, tag); }
  sq_compute<2>(p, lds0, w, xcd, idx, tag);
  wait_vm<0>();
  barrier();
  if (w == 0) { const int l = lane_id(); if (l < 8) put_tag(fl + FL_P2 + ((tm >> 1) * 8 + l) * 128 + (((tm & 1) * 2 + kh) * 8 + lj) * 4, tag); }
  glu_compute(p, lds0, w, xcd, idx);
  wait_vm<0>();
  barrier();
  if (w == 0 && lane_id() == 0) put_tag(fl + FL_P3 + ((idx & 7) * 2 + (xcd >> 2)) * 128 + ((xcd & 3) * 4 + (idx >> 3)) * 4, tag);
  sq_compute<4>(p, lds0, w, xcd, idx, tag);
  if (blockIdx.x == 0 && w == 0 && lane_id() == 0) put_tag(fl + FL_EPOCH, tag);
}
}

extern "C" void kernel_launch(void* const* d_in, const int* in_sizes, int n_in, void* d_out, int out_size, void* d_ws, size_t ws_size,
                              hipStream_t stream) {
  char* ws = (char*)d_ws;
  FArgs f = {d_in[3], d_in[4], d_in[1], d_in[2], d_in[0], d_in[6], d_in[7], d_in[8], ws + WS_AB, ws + WS_H, ws + WS_ST, ws + WS_GU, d_out, ws + WS_XC, ws + WS_FL};
  (void)hipFuncSetAttribute((const void*)k_block, hipFuncAttributeMaxDynamicSharedMemorySize, F_LDS);
  hipLaunchKernelGGL(k_block, dim3(256), dim3(768), F_LDS, stream, f);
}
```

```cpp
#include <hip/hip_runtime.h>
#include <hip/hip_bf16.h>

namespace {
#define LAS __attribute__((address_space(3)))
typedef unsigned short u16;
typedef short s16x4 __attribute__((ext_vector_type(4)));
typedef short s16x8 __attribute__((ext_vector_type(8)));
typedef __bf16 bf16x8 __attribute__((ext_vector_type(8)));
typedef __bf16 bf16x2 __attribute__((ext_vector_type(2)));
typedef float f32x4 __attribute__((ext_vector_type(4)));
typedef float f32x2 __attribute__((ext_vector_type(2)));
typedef unsigned u32x4 __attribute__((ext_vector_type(4)));
typedef unsigned u32x2 __attribute__((ext_vector_type(2)));

constexpr int S_ = 2048, D_ = 1024, F_ = 3584;
constexpr float EPS = 1e-5f;

constexpr size_t WS_AB = 0;
constexpr size_t WS_H = WS_AB + (size_t)D_ * 2 * S_ * 2;
constexpr size_t WS_GU = WS_H + (size_t)S_ * D_ * 2;
constexpr size_t WS_ST = WS_GU + (size_t)S_ * F_ * 2;
constexpr size_t WS_XC = WS_ST + (size_t)S_ * 16 * 8;
constexpr size_t WS_FL = WS_XC + (size_t)2 * 256 * 16384;
constexpr int FL_EPOCH = 0;
constexpr int FL_PAIR = 128;
constexpr int FL_P1 = FL_PAIR + 512 * 128;
constexpr int FL_P2 = FL_P1 + 256 * 128;
constexpr int FL_P3 = FL_P2 + 256 * 128;
constexpr int FL_LN = FL_P3 + 256 * 128;
constexpr int FL_CS = FL_LN + 4096 * 16;

__device__ __forceinline__ unsigned rfl(unsigned v) { return __builtin_amdgcn_readfirstlane(v); }
__device__ __forceinline__ unsigned pk_bf16(float lo, float hi) {
  bf16x2 c = {(__bf16)lo, (__bf16)hi};
  return __builtin_bit_cast(unsigned, c);
}
__device__ __forceinline__ float bf_lo(unsigned v) { return __uint_as_float(v << 16); }
__device__ __forceinline__ float bf_hi(unsigned v) { return __uint_as_float(v & 0xffff0000u); }

__device__ __forceinline__ u32x4 make_rsrc(const void* p, unsigned bytes) {
  unsigned long long a = (unsigned long long)p;
  u32x4 r;
  r.x = (unsigned)a;
  r.y = (unsigned)(a >> 32) & 0xffffu;
  r.z = bytes;
  r.w = 0x00020000u;
  return r;
}
template <int N>
__device__ __forceinline__ void wait_vm() {
  asm volatile("s_waitcnt vmcnt(%0)" ::"i"(N) : "memory");
}
__device__ __forceinline__ void wait_lgkm0() { __builtin_amdgcn_s_waitcnt(0xC07F); }
__device__ __forceinline__ void barrier() {
  asm volatile("" ::: "memory");
  __builtin_amdgcn_s_barrier();
  asm volatile("" ::: "memory");
}
__device__ __forceinline__ bf16x8 lds_b128(unsigned addr) { return *(const LAS bf16x8*)(addr); }
__device__ __forceinline__ bf16x8 lds_tr(unsigned a0, unsigned a1) {
  s16x4 lo = __builtin_amdgcn_ds_read_tr16_b64_v4i16((LAS s16x4*)(a0));
  s16x4 hi = __builtin_amdgcn_ds_read_tr16_b64_v4i16((LAS s16x4*)(a1));
  s16x8 f = __builtin_shufflevector(lo, hi, 0, 1, 2, 3, 4, 5, 6, 7);
  return __builtin_bit_cast(bf16x8, f);
}

#define ST_AUX 16
#define OUT_RSRC(ptr, bytes) __builtin_amdgcn_make_buffer_rsrc((void*)(ptr), 0, (bytes), 0x00020000)
#define STORE16(rs, byteoff, v) __builtin_amdgcn_raw_buffer_store_b128(v, rs, byteoff, 0, ST_AUX)

#define DMA4(M) asm volatile("s_mov_b32 m0, %0\n\ts_nop 4\n\tbuffer_load_dwordx4 %1, %5, %6 offen " M " lds\n\tbuffer_load_dwordx4 %2, %5, %6 offen offset:1024 " M " lds\n\t" \
                 "buffer_load_dwordx4 %3, %5, %6 offen offset:2048 " M " lds\n\tbuffer_load_dwordx4 %4, %5, %6 offen offset:3072 " M " lds" ::"s"(ldsaddr), \
                 "v"(v[0]), "v"(v[1]), "v"(v[2]), "v"(v[3]), "s"(rsrc), "s"(soff) : "memory")
#define DMA3(M) asm volatile("s_mov_b32 m0, %0\n\ts_nop 4\n\tbuffer_load_dwordx4 %1, %4, %5 offen " M " lds\n\tbuffer_load_dwordx4 %2, %4, %5 offen offset:1024 " M " lds\n\t" \
                 "buffer_load_dwordx4 %3, %4, %5 offen offset:2048 " M " lds" ::"s"(ldsaddr), "v"(v[0]), "v"(v[1]), "v"(v[2]), "s"(rsrc), "s"(soff) : "memory")
#define DMAB(M) asm volatile("s_mov_b32 m0, %0\n\ts_waitcnt vmcnt(8)\n\ts_barrier\n\tbuffer_load_dwordx4 %1, %5, %6 offen " M " lds\n\tbuffer_load_dwordx4 %2, %5, %6 offen offset:1024 " M " lds\n\t" \
                 "buffer_load_dwordx4 %3, %5, %6 offen offset:2048 " M " lds\n\tbuffer_load_dwordx4 %4, %5, %6 offen offset:3072 " M " lds" ::"s"(ldsaddr), \
                 "v"(v[0]), "v"(v[1]), "v"(v[2]), "v"(v[3]), "s"(rsrc), "s"(soff) : "memory")
template <int N, int MOD = 0>
__device__ __forceinline__ void dma_grp(unsigned ldsaddr, const unsigned (&v)[4], u32x4 rsrc, unsigned soff) {
  if constexpr (N == 4) { if constexpr (MOD == 1) DMA4("sc1"); else DMA4(""); }
  else if constexpr (N == 3) { if constexpr (MOD == 1) DMA3("sc1"); else DMA3(""); }
  else { if constexpr (MOD == 1) DMAB("sc1"); else DMAB(""); }
}
__device__ __forceinline__ float sum_rows(float x) {
  auto a = __builtin_amdgcn_permlane16_swap(__float_as_uint(x), __float_as_uint(x), false, false);
  x = __uint_as_float(a[0]) + __uint_as_float(a[1]);
  auto b = __builtin_amdgcn_permlane32_swap(__float_as_uint(x), __float_as_uint(x), false, false);
  return __uint_as_float(b[0]) + __uint_as_float(b[1]);
}
template <int CTRL> __device__ __forceinline__ float dpp_add(float x) { return x + __uint_as_float(__builtin_amdgcn_mov_dpp(__float_as_uint(x), CTRL, 0xF, 0xF, true)); }
__device__ __forceinline__ float wave_sum(float x) { return sum_rows(dpp_add<0x140>(dpp_add<0x141>(dpp_add<0x4E>(dpp_add<0xB1>(x))))); }
__device__ __forceinline__ int lane_id() {
  int l;
  asm volatile("v_mbcnt_lo_u32_b32 %0, -1, 0\n\tv_mbcnt_hi_u32_b32 %0, -1, %0" : "=v"(l));
  return l;
}
__device__ __forceinline__ void mfma_drain() { asm volatile("s_nop 15" ::: "memory"); }
template <int NC, int NT>
__device__ __forceinline__ unsigned stage_of(unsigned n, unsigned c) {
  const unsigned t = ((n & (NC - 1)) == c) ? n + NC : n;
  return rfl(t >= NT ? t - NT : t);
}
#define SB() __builtin_amdgcn_sched_barrier(0)
#define MFMA(b, a, c) asm volatile("v_mfma_f32_16x16x32_bf16 %0, %1, %2, %0" : "+v"(c) : "v"(b), "v"(a))

struct FArgs {
  const void *dre, *dim, *cre, *cim, *x, *wg, *wu, *wd;
  void *ab, *h, *stats, *gu, *out, *xch, *flags;
};
constexpr unsigned CODE_LINES = 156;
constexpr int F_RING = 6144, F_ST = 32768, F_TAB = F_RING + 4 * F_ST, F_TAG = F_TAB + 4096, F_LDS = F_TAB + 4160, F_A = 16384, G3_UP = 7168 + 16;

__device__ __forceinline__ void poll_tags(const void* line, int n, unsigned tag) {
  const int l = lane_id();
  const unsigned* q = (const unsigned*)line + (l < n ? l : 0) * 32;
  unsigned spins = 0;
  while (!__all(__hip_atomic_load(q, __ATOMIC_RELAXED, __HIP_MEMORY_SCOPE_AGENT) == tag) && ++spins < (1u << 17)) __builtin_amdgcn_s_sleep(1);
}
__device__ __forceinline__ void put_tag(void* word, unsigned tag) { __hip_atomic_store((unsigned*)word, tag, __ATOMIC_RELAXED, __HIP_MEMORY_SCOPE_AGENT); }
__device__ __forceinline__ unsigned lds_tag(unsigned lds0) { return rfl(*(volatile LAS unsigned*)(lds0 + F_TAG)); }
template <int ID> constexpr int nt_of() { return ID == 1 ? 16 : (ID == 4 ? 28 : 32); }
template <int ID> constexpr int nc_of() { return (ID == 1 || ID == 4) ? 4 : 8; }
template <int ID> constexpr int mod_a() { return ID > 2; }
template <int ID> constexpr int mod_b() { return ID == 2; }
struct Ld {
  u32x4 qa, qb;
  unsigned va[4], vb[4], cls;
};
template <int ID>
__device__ __forceinline__ Ld ld_setup(const FArgs& p, unsigned lw, unsigned xcd, unsigned idx) {
  const int lane = lane_id();
  Ld L;
  if constexpr (ID == 3) {
    const unsigned tm = idx & 7, tn = xcd * 4 + (idx >> 3);
    L.qa = make_rsrc((const u16*)p.h + (size_t)tm * 256 * D_, 256 * D_ * 2);
    L.qb = make_rsrc(lw < 2 ? p.wg : p.wu, (unsigned)D_ * F_ * 2);
#pragma unroll
    for (int i = 0; i < 4; ++i) {
      L.va[i] = (lw * 4 + i) * (D_ / 32) * 1024 + lane * 16 - i * 1024;
      const int c = ((lw & 1) * 4 + i) * 64 + lane, r = c / 14, pos = c % 14;
      const int k = (r & 0x13) | ((r & 4) << 1) | ((r & 8) >> 1);
      L.vb[i] = k * F_ * 2 + tn * 224 + pos * 16 - i * 1024;
    }
    L.cls = (tm + 2 * (idx >> 3)) & 7;
  } else {
    const unsigned li = idx & 3, lj = idx >> 2, kh = xcd & 1, tm = (xcd >> 1) * 4 + li, tn = lj;
    if constexpr (ID == 1) {
      L.qa = make_rsrc((const u16*)(tm < 8 ? p.dre : p.dim) + (size_t)(tm & 7) * 128 * D_, 128 * D_ * 2);
      L.qb = make_rsrc((const u16*)p.x + (size_t)(kh * 8 + lj) * 128 * D_, 128 * D_ * 2);
    } else if constexpr (ID == 2) {
      L.qa = make_rsrc((const u16*)(kh ? p.cim : p.cre) + (size_t)tm * 128 * S_, 128 * S_ * 2);
      L.qb = make_rsrc((const u16*)p.ab + (size_t)tn * 128 * 4096 + kh * 64 * 512, 128 * 4096 * 2 - kh * 64 * 1024);
    } else {
      L.qa = make_rsrc((const u16*)p.gu + (size_t)tm * 128 * F_ + kh * 56 * 512, 128 * F_ * 2 - kh * 56 * 1024);
      L.qb = make_rsrc((const u16*)p.wd + (size_t)kh * 1792 * D_, 1792 * D_ * 2);
    }
#pragma unroll
    for (int i = 0; i < 4; ++i) {
      const int pc = lw * 4 + i;
      if constexpr (ID == 1) {
        const int row = pc * 8 + (lane >> 3);
        L.va[i] = L.vb[i] = row * D_ * 2 + (((lane & 7) ^ ((row >> 1) & 7)) << 4) - i * 1024;
      } else if constexpr (ID == 2) {
        const int row = pc * 8 + (lane >> 3);
        L.va[i] = row * S_ * 2 + (((lane & 7) ^ ((row >> 1) & 7)) << 4) - i * 1024;
        L.vb[i] = ((pc >> 1) * 128 + (pc & 1)) * 1024 + lane * 16 - i * 1024;
      } else {
        const int k = pc * 4 + (lane >> 4), f = (k & 3) | (((k >> 3) & 1) << 2);
        L.va[i] = ((pc >> 1) * (F_ / 32) + (pc & 1)) * 1024 + lane * 16 - i * 1024;
        L.vb[i] = k * D_ * 2 + tn * 256 + (((lane & 15) ^ (f << 1)) << 4) - i * 1024;
      }
    }
    L.cls = (nc_of<ID>() == 8) ? ((lj + 2 * li) & 7) : ((lj + li) & 3);
  }
  return L;
}
__device__ __forceinline__ void ld_pin(Ld& L) {
  asm volatile("" : "+v"(L.va[0]), "+v"(L.va[1]), "+v"(L.va[2]), "+v"(L.va[3]), "+v"(L.vb[0]), "+v"(L.vb[1]), "+v"(L.vb[2]), "+v"(L.vb[3]));
}
template <int ID>
__device__ __forceinline__ void ld_a(const Ld& L, unsigned ring, unsigned lw, unsigned n) {
  const unsigned st = stage_of<nc_of<ID>(), nt_of<ID>()>(n, L.cls);
  dma_grp<4, mod_a<ID>()>(ring + (n & 3) * F_ST + lw * 4096, L.va, L.qa, st * (ID <= 2 ? 128 : (ID == 3 ? 1024 : 2048)));
}
template <int ID>
__device__ __forceinline__ void ld_b(const Ld& L, unsigned ring, unsigned lw, unsigned n) {
  const unsigned st = stage_of<nc_of<ID>(), nt_of<ID>()>(n, L.cls);
  const unsigned dst = ring + (n & 3) * F_ST + F_A;
  if constexpr (ID == 3) {
    const unsigned d = dst + (lw >> 1) * G3_UP + (lw & 1) * 4096;
    if (lw & 1) dma_grp<3, mod_b<ID>()>(d, L.vb, L.qb, st * (32 * F_ * 2));
    else dma_grp<4, mod_b<ID>()>(d, L.vb, L.qb, st * (32 * F_ * 2));
  } else {
    dma_grp<4, mod_b<ID>()>(dst + lw * 4096, L.vb, L.qb, st * (ID == 1 ? 128 : (ID == 2 ? 2048 : 64 * D_ * 2)));
  }
}

template <int ID> __device__ __forceinline__ void ld_const(const Ld& L, unsigned ring, unsigned lw, unsigned n) { if constexpr (ID == 2) ld_a<ID>(L, ring, lw, n); else ld_b<ID>(L, ring, lw, n); }
template <int ID> __device__ __forceinline__ void ld_dep(const Ld& L, unsigned ring, unsigned lw, unsigned n) { if constexpr (ID == 2) ld_b<ID>(L, ring, lw, n); else ld_a<ID>(L, ring, lw, n); }

template <int ID>
__device__ __forceinline__ void sq_loader(const FArgs& p, const Ld& L, Ld& N, unsigned lds0, unsigned lw, unsigned xcd, unsigned idx, unsigned& touch) {
  constexpr int NT = nt_of<ID>(), NEXT = ID == 4 ? 0 : ID + 1;
  const unsigned ring = lds0 + F_RING;
  if constexpr (ID == 1) {
    ld_a<ID>(L, ring, lw, 0); ld_b<ID>(L, ring, lw, 0);
    N = ld_setup<NEXT>(p, lw, xcd, idx); ld_pin(N);
    ld_a<ID>(L, ring, lw, 1); ld_b<ID>(L, ring, lw, 1);
  } else {
    ld_dep<ID>(L, ring, lw, 0); ld_dep<ID>(L, ring, lw, 1);
  }
  constexpr unsigned KA = ID <= 2 ? 128 : 2048, KB = ID == 1 ? 128 : (ID == 2 ? 2048 : 64 * D_ * 2);
  unsigned sa, sb, ma;
  auto prep = [&](unsigned n) {
    const unsigned st = stage_of<nc_of<ID>(), NT>(n, L.cls);
    sa = st * KA; sb = st * KB; ma = ring + (n & 3) * F_ST + lw * 4096;
    asm volatile("" : "+s"(sa), "+s"(sb), "+s"(ma));
  };
  auto bar_a = [&]() {
    dma_grp<5, mod_a<ID>()>(ma, L.va, L.qa, sa);
  };
  prep(2);
  wait_vm<(ID == 1 ? 8 : 4)>();
  bar_a();
  unsigned mb_ = ma + F_A, sb_ = sb;
  prep(3);
#pragma unroll 1
  for (unsigned n = 0; n + 3 < NT; ++n) {
    dma_grp<4, mod_b<ID>()>(mb_, L.vb, L.qb, sb_);
    bar_a();
    mb_ = ma + F_A; sb_ = sb;
    asm volatile("" : "+s"(mb_), "+s"(sb_));
    prep(n + 4);
  }
  dma_grp<4, mod_b<ID>()>(mb_, L.vb, L.qb, sb_);
  wait_vm<8>();
  barrier();
  const int lane = lane_id();
  u32x2 l0, l1;
  const char* lp = (const char*)p.flags + FL_LN + (lw < 2 ? (((xcd & 1) * 8 + (idx >> 2)) * 128 + lw * 64 + lane) : (2048 + ((xcd >> 1) * 4 + (idx & 3)) * 128 + (lw - 2) * 64 + lane)) * 16;
  if constexpr (ID == 1) asm volatile("global_load_dwordx2 %0, %2, off sc1\n\tglobal_load_dwordx2 %1, %2, off offset:8 sc1" : "=&v"(l0), "=&v"(l1) : "v"(lp) : "memory");
  if constexpr (NEXT != 0) ld_const<NEXT>(N, ring, lw, 0);
  if constexpr (ID == 1) wait_vm<6>();
  else if constexpr (ID == 2) { if (lw & 1) wait_vm<3>(); else wait_vm<4>(); }
  else wait_vm<0>();
  barrier();
  if constexpr (NEXT != 0) ld_const<NEXT>(N, ring, lw, 1);
  if constexpr (ID == 1) {
    if (lw < 2) {
      const char* tp = (const char*)((xcd & 1) ? p.cim : p.cre) + (size_t)((xcd >> 1) * 512 + idx * 16 + lw * 8 + (lane >> 3)) * (S_ * 2) + (lane & 7) * 128;
      asm volatile("global_load_dword %0, %1, off" : "+v"(touch) : "v"(tp) : "memory");
    }
  }
  if constexpr (ID == 2) {
    if (lw < 2) {
      const char* tp = (const char*)((lw & 1) ? p.wu : p.wg) + (size_t)(idx * 8 + (lane >> 3)) * (F_ * 2) + xcd * 896 + ((lane & 7) < 7 ? (lane & 7) : 6) * 128;
      asm volatile("global_load_dword %0, %1, off" : "+v"(touch) : "v"(tp) : "memory");
    }
  }
  barrier();
  const unsigned tag = lds_tag(lds0);
  if constexpr (ID == 1) {
    if (lw < 2) wait_vm<9>(); else wait_vm<8>();
    asm volatile("" : "+v"(l0), "+v"(l1));
#pragma unroll 1
    for (unsigned spins = 0; spins < (1u << 15) && !__all(l0.y == tag && l1.y == tag); ++spins) {
      __builtin_amdgcn_s_sleep(2);
      asm volatile("global_load_dwordx2 %0, %2, off sc1\n\tglobal_load_dwordx2 %1, %2, off offset:8 sc1\n\ts_waitcnt vmcnt(0)" : "=&v"(l0), "=&v"(l1) : "v"(lp) : "memory");
    }
    LAS unsigned* tb = (LAS unsigned*)(lds0 + F_TAB);
    tb[lw * 64 + lane + (lw < 2 ? 0 : 128)] = l0.x;
    if (lw < 2) tb[128 + lw * 64 + lane] = l1.x;
    wait_lgkm0();
  }
  barrier();
  barrier();
  if constexpr (ID != 1) {
    barrier();
    if (lw == 3) poll_tags((char*)p.flags + FL_PAIR + ((ID == 2 ? 0 : 256) + (blockIdx.x ^ 1)) * 128, 1, tag);
    barrier();
  }
}

template <int ID>
__device__ __forceinline__ void sq_compute(const FArgs& p, unsigned lds0, unsigned w, unsigned xcd, unsigned idx, unsigned& tag) {
  constexpr int NT = nt_of<ID>();
  u32x4 xv[2], wv[2];
  unsigned tv = 0;
  const unsigned r0 = blockIdx.x * 8 + w;
  char* fl = (char*)p.flags;
  if constexpr (ID == 1) {
    const int lane = lane_id();
    const u16* xp = (const u16*)p.x + (size_t)r0 * D_ + lane * 8;
    const u16* wp = (const u16*)(r0 < 1024 ? p.dre : p.dim) + (size_t)(r0 & 1023) * D_ + lane * 8;
#pragma unroll
    for (int i = 0; i < 2; ++i) xv[i] = *(const u32x4*)(xp + i * 512);
#pragma unroll
    for (int i = 0; i < 2; ++i) wv[i] = *(const u32x4*)(wp + i * 512);
    tv = __hip_atomic_load((const unsigned*)(fl + FL_EPOCH), __ATOMIC_RELAXED, __HIP_MEMORY_SCOPE_AGENT);
    SB();
  }
  const unsigned ring = lds0 + F_RING;
  const unsigned g = w >> 2, wm = (w >> 1) & 1, wx = w & 1;
  const unsigned kh = xcd & 1, tm = (xcd >> 1) * 4 + (idx & 3), tn = idx >> 2;
  u32x4 res[2];
  unsigned rdA[8], rdB[4], hoff = 0;
  const unsigned rotA = (ID == 1) ? 2 * g : 2 * g + wx;
  {
    const int lane = lane_id(), fr = lane & 15, fq = lane >> 4, chunk = ((fq & 1) << 1) | (fq >> 1);
    const int row = tm * 128 + wm * 64 + rotA * 16 + fr, col = tn * 128 + kh * 32 + chunk * 8;
    if constexpr (ID == 2) {
#pragma unroll
      for (int c = 0; c < 2; ++c) res[c] = *(const u32x4*)((const u16*)p.x + (size_t)row * D_ + col + (c ^ wx) * 64);
    } else if constexpr (ID == 4) {
      hoff = ((row >> 4) * (D_ / 32) + (col >> 5)) * 1024 + (chunk * 16 + fr) * 16;
    }
    const unsigned rotB = (ID == 1) ? 0u : 2 * kh, base = ring + g * F_ST;
#pragma unroll
    for (int i = 0; i < 4; ++i) {
      const unsigned kk = i >> 1, hf = (i & 1) * 2;
      if constexpr (ID == 1) {
        rdA[i] = base + (wm * 64 + (hf ^ rotA) * 16 + fr) * 128 + (((kk * 4 + fq) ^ (fr >> 1)) << 4);
        rdB[i] = base + F_A + (wx * 64 + fr) * 128 + ((((i & 1) * 4 + fq) ^ (fr >> 1)) << 4);
      } else if constexpr (ID == 2) {
        rdA[i] = base + (wm * 64 + (i ^ rotA) * 16 + fr) * 128 + ((fq ^ (fr >> 1)) << 4);
        rdA[4 + i] = base + (wm * 64 + (i ^ rotA) * 16 + fr) * 128 + (((4 + fq) ^ (fr >> 1)) << 4);
        rdB[i] = base + F_A + wx * 8192 + (hf ^ rotB) * 2048 + lane * 16;
      } else {
        const int q = fr >> 2, pp = fr & 3, f = q | ((fq & 1) << 2);
        rdA[i] = base + wm * 8192 + (i ^ rotA) * 2048 + lane * 16;
        rdB[i] = base + F_A + (8 * fq + q) * 256 + (((wx * 4 + (i ^ rotB)) ^ f) << 5) + pp * 8;
      }
    }
  }
  constexpr int NVA = (ID == 2) ? 8 : 4, NVB = (ID <= 2) ? 2 : 4;
  bf16x8 fa[2][4], fb[2][4];
  f32x4 acc[4][4];
#pragma unroll
  for (int i = 0; i < 16; ++i) { acc[i >> 2][i & 3] = (f32x4){0.f, 0.f, 0.f, 0.f}; asm volatile("" : "+v"(acc[i >> 2][i & 3])); }
  auto reads = [&]() {
    SB();
#pragma unroll
    for (int s = 0; s < 2; ++s)
#pragma unroll
      for (int i = 0; i < 4; ++i) {
        if constexpr (ID == 1) {
          fb[s][i] = lds_b128(rdB[s] + i * 2048);
          fa[s][i] = lds_b128(rdA[s * 2 + (i >> 1)] + (i & 1) * 2048);
        } else if constexpr (ID == 2) {
          fb[s][i] = lds_b128(rdB[i >> 1] + (i & 1) * 2048 + s * 1024);
          fa[s][i] = lds_b128(rdA[s * 4 + i]);
        } else {
          fb[s][i] = lds_tr(rdB[i] + s * 8192, rdB[i] + s * 8192 + 1024);
          fa[s][i] = lds_b128(rdA[i] + s * 1024);
        }
      }
    SB();
  };
#pragma unroll
  for (int j = 0; j < NVA; ++j) asm volatile("" : "+v"(rdA[j]));
#pragma unroll
  for (int j = 0; j < NVB; ++j) asm volatile("" : "+v"(rdB[j]));
  if constexpr (ID == 1) {
    SB();
    asm volatile("" : "+v"(tv));
    tv = tv * 0x9E3779B1u + 0x7F4A7C15u;
    const int lane = lane_id();
    float s = 0.f, t = 0.f;
#pragma unroll
    for (int j = 0; j < 8; ++j) s += bf_lo(xv[j >> 2][j & 3]) + bf_hi(xv[j >> 2][j & 3]);
    const float m = wave_sum(s) * (1.0f / D_);
    float q = 0.f;
#pragma unroll
    for (int j = 0; j < 8; ++j) {
      const float a = bf_lo(xv[j >> 2][j & 3]) - m, b = bf_hi(xv[j >> 2][j & 3]) - m;
      q += a * a + b * b;
      t += bf_lo(wv[j >> 2][j & 3]) + bf_hi(wv[j >> 2][j & 3]);
    }
    const float rs = __builtin_amdgcn_rsqf(wave_sum(q) * (1.0f / D_) + EPS);
    t = wave_sum(t);
    const float val = (lane & 2) ? t : ((lane & 1) ? rs : m);
    if (lane < 4)
      __hip_atomic_store((unsigned long long*)(fl + FL_LN + ((lane >> 1) * 2048 + r0) * 16 + (lane & 1) * 8), ((unsigned long long)tv << 32) | __float_as_uint(val),
                         __ATOMIC_RELAXED, __HIP_MEMORY_SCOPE_AGENT);
    tag = rfl(tv);
    if (w == 0) { *(volatile LAS unsigned*)(lds0 + F_TAG) = tag; wait_lgkm0(); }
  }
  if constexpr (ID != 1) {
    barrier();
  }
  if constexpr (ID == 4) {
    const auto rh = OUT_RSRC(p.h, S_ * D_ * 2);
#pragma unroll
    for (int c = 0; c < 2; ++c) res[c] = __builtin_amdgcn_raw_buffer_load_b128(rh, hoff, (c ^ wx) * 2048, 16);
  }
  barrier();
  if (g == 1) barrier();
  reads();
#pragma unroll 1
  for (unsigned i = 0; i < NT / 2; ++i) {
    const unsigned delta = (i & 1) ? (unsigned)(-2 * F_ST) : (unsigned)(2 * F_ST);
#pragma unroll
    for (int j = 0; j < NVA; ++j) { rdA[j] += delta; asm volatile("" : "+v"(rdA[j])); }
#pragma unroll
    for (int j = 0; j < NVB; ++j) { rdB[j] += delta; asm volatile("" : "+v"(rdB[j])); }
    wait_lgkm0();
    barrier();
    SB();
#pragma unroll
    for (int m = 0; m < 32; ++m) {
      const int n = (m & 4) ? 3 - (m & 3) : (m & 3);
      MFMA(fb[m >> 4][n], fa[m >> 4][(m >> 2) & 3], acc[(m >> 2) & 3][n]);
    }
    SB();
    barrier();
    if (i + 1 < NT / 2) reads();
  }
  mfma_drain();
  SB();
  const int lane = lane_id(), fr = lane & 15, fq = lane >> 4, chunk = ((fq & 1) << 1) | (fq >> 1);
  if constexpr (ID == 1) {
    const unsigned wb = ring + 2 * F_ST + (w ^ 4) * 8192 + lane * 16, rb = ring + 2 * F_ST + w * 8192 + lane * 16;
#pragma unroll
    for (int i = 0; i < 8; ++i) *(LAS f32x4*)(wb + i * 1024) = acc[2 + (i >> 2)][i & 3];
    if (g == 0) barrier();
    wait_lgkm0();
    barrier();
#pragma unroll
    for (int i = 0; i < 8; ++i) acc[i >> 2][i & 3] += *(const LAS f32x4*)(rb + i * 1024);
  }
  if constexpr (ID == 1) {
    const LAS float* tb = (const LAS float*)(lds0 + F_TAB);
    const float sg = tm < 8 ? 1.f : -1.f;
    const auto ro = OUT_RSRC(p.ab, D_ * 4096 * 2);
    f32x4 mean[4], rstd[4];
#pragma unroll
    for (int j = 0; j < 4; ++j) {
      mean[j] = *(const LAS f32x4*)(tb + wx * 64 + j * 16 + fq * 4);
      rstd[j] = *(const LAS f32x4*)(tb + 128 + wx * 64 + j * 16 + fq * 4) * sg;
    }
#pragma unroll
    for (int mb = 0; mb < 2; ++mb) {
      const float wsm = tb[256 + wm * 64 + g * 32 + mb * 16 + fr];
#pragma unroll
      for (int np = 0; np < 2; ++np) {
        const f32x4 x0 = (acc[mb][2 * np] - mean[2 * np] * wsm) * rstd[2 * np], x1 = (acc[mb][2 * np + 1] - mean[2 * np + 1] * wsm) * rstd[2 * np + 1];
        auto s0 = __builtin_amdgcn_permlane16_swap(pk_bf16(x0[0], x0[1]), pk_bf16(x1[0], x1[1]), false, false);
        auto s1 = __builtin_amdgcn_permlane16_swap(pk_bf16(x0[2], x0[3]), pk_bf16(x1[2], x1[3]), false, false);
        const int rblk = (tm & 7) * 8 + wm * 4 + g * 2 + mb;
        const int kblk = (tm >> 3) * 64 + (kh * 8 + tn) * 4 + wx * 2 + np;
        STORE16(ro, (rblk * 128 + kblk) * 1024 + (chunk * 16 + fr) * 16, ((u32x4){s0[0], s1[0], s0[1], s1[1]}));
      }
    }
    return;
  }
  const auto rs = OUT_RSRC((char*)p.xch + (ID == 2 ? 0 : 256 * 16384), 256 * 16384);
  const unsigned sreg = ring + ((w >> 1) & 1) * F_ST + (w & 1) * 8192 + lane * 16, kreg = ring + 2 * F_ST + lane * 16;
  auto put_keep = [&]() {
#pragma unroll
    for (int j = 1; j < 4; ++j) {
      const unsigned d = kreg + ((wm * 4 + (j ^ rotA)) * 3 + (j - 1)) * 2048;
      *(LAS f32x4*)(d) = acc[j][0];
      *(LAS f32x4*)(d + 1024) = acc[j][1];
    }
  };
  if (g == 0) {
#pragma unroll
    for (int i = 0; i < 8; ++i) *(LAS f32x4*)(sreg + i * 1024) = acc[i >> 1][2 + (i & 1)];
    put_keep();
    wait_lgkm0();
    barrier();
  } else {
#pragma unroll
    for (int j = 0; j < 4; ++j) {
      const f32x4 x0 = acc[j][2] + *(const LAS f32x4*)(sreg + (j ^ 2) * 2048), x1 = acc[j][3] + *(const LAS f32x4*)(sreg + (j ^ 2) * 2048 + 1024);
      auto s0 = __builtin_amdgcn_permlane16_swap(pk_bf16(x0[0], x0[1]), pk_bf16(x1[0], x1[1]), false, false);
      auto s1 = __builtin_amdgcn_permlane16_swap(pk_bf16(x0[2], x0[3]), pk_bf16(x1[2], x1[3]), false, false);
      __builtin_amdgcn_raw_buffer_store_b128((u32x4){s0[0], s1[0], s0[1], s1[1]}, rs, lane * 16 + blockIdx.x * 16384 + ((wm * 4 + (j ^ rotA)) * 2 + wx) * 1024, 0, 16);
    }
    SB();
    put_keep();
    wait_lgkm0();
  }
  barrier();
  f32x4 y[2][2];
  {
    const unsigned kb = kreg + (wm * 4 + rotA) * 3 * 2048;
    f32x4 t[2][2];
#pragma unroll
    for (int i = 0; i < 2; ++i) {
      t[0][i] = acc[0][i] + *(const LAS f32x4*)(kb + 2048 + i * 1024);
      t[1][i] = *(const LAS f32x4*)(kb + i * 1024) + *(const LAS f32x4*)(kb + 4096 + i * 1024);
    }
#pragma unroll
    for (int c = 0; c < 2; ++c)
#pragma unroll
      for (int j = 0; j < 4; ++j) {
        auto s = __builtin_amdgcn_permlane16_swap(__float_as_uint(t[c][0][j]), __float_as_uint(t[c][1][j]), false, false);
        y[c][0][j] = __uint_as_float(s[0]) + ((j & 1) ? bf_hi(res[c][j >> 1]) : bf_lo(res[c][j >> 1]));
        y[c][1][j] = __uint_as_float(s[1]) + ((j & 1) ? bf_hi(res[c][2 + (j >> 1)]) : bf_lo(res[c][2 + (j >> 1)]));
      }
  }
  const int row0 = tm * 128 + wm * 64 + rotA * 16 + fr, col = tn * 128 + kh * 32 + chunk * 8;
  unsigned optr = (ID == 2) ? ((row0 >> 4) * (D_ / 32) + (col >> 5)) * 1024 + (chunk * 16 + fr) * 16 : (row0 * D_ + col) * 2;
  const auto ro = OUT_RSRC(ID == 2 ? p.h : p.out, S_ * D_ * 2);
  asm volatile("" : "+v"(y[0][0]), "+v"(y[0][1]), "+v"(y[1][0]), "+v"(y[1][1]), "+v"(optr));
  wait_vm<0>();
  barrier();
  if (w == 0 && lane == 0) put_tag((char*)p.flags + FL_PAIR + ((ID == 2 ? 0 : 256) + blockIdx.x) * 128, tag);
  barrier();
  u32x4 in[2], v[2];
#pragma unroll
  for (int c = 0; c < 2; ++c) in[c] = __builtin_amdgcn_raw_buffer_load_b128(rs, lane * 16, (blockIdx.x ^ 1) * 16384 + ((wm * 4 + rotA) * 2 + (c ^ wx)) * 1024, 16);
#pragma unroll
  for (int c = 0; c < 2; ++c) {
    const f32x4 a = y[c][0] + (f32x4){bf_lo(in[c][0]), bf_hi(in[c][0]), bf_lo(in[c][1]), bf_hi(in[c][1])};
    const f32x4 b = y[c][1] + (f32x4){bf_lo(in[c][2]), bf_hi(in[c][2]), bf_lo(in[c][3]), bf_hi(in[c][3])};
    v[c] = (u32x4){pk_bf16(a[0], a[1]), pk_bf16(a[2], a[3]), pk_bf16(b[0], b[1]), pk_bf16(b[2], b[3])};
    __builtin_amdgcn_raw_buffer_store_b128(v[c], ro, optr + (c ^ wx) * (ID == 2 ? 2048 : 128), 0, ST_AUX);
  }
  SB();
  if constexpr (ID == 2) {
    float s = 0.f;
#pragma unroll
    for (int j = 0; j < 8; ++j) s += bf_lo(v[j >> 2][j & 3]) + bf_hi(v[j >> 2][j & 3]);
    const float m = sum_rows(s) * (1.0f / 64.0f);
    float q = 0.f;
#pragma unroll
    for (int j = 0; j < 8; ++j) {
      const float a = bf_lo(v[j >> 2][j & 3]) - m, b = bf_hi(v[j >> 2][j & 3]) - m;
      q += a * a + b * b;
    }
    const f32x2 st = {m, sum_rows(q)};
    if (fq == 0)
      __hip_atomic_store((unsigned long long*)p.stats + (size_t)(tn * 2 + kh) * S_ + row0, __builtin_bit_cast(unsigned long long, st), __ATOMIC_RELAXED, __HIP_MEMORY_SCOPE_AGENT);
  }
  SB();
}

__device__ __forceinline__ void glu_loader(const FArgs& p, const Ld& L, Ld& N, unsigned lds0, unsigned lw, unsigned xcd, unsigned idx, unsigned& touch) {
  constexpr int NT = 32, NEXT = 4;
  const unsigned tag = lds_tag(lds0);
  const unsigned ring = lds0 + F_RING;
  LAS float* cst = (LAS float*)(lds0 + F_TAB + 2048);
  unsigned long long* gcs = (unsigned long long*)((char*)p.flags + FL_CS) + (xcd * 4 + (idx >> 3)) * (8 * 224);
  ld_a<3>(L, ring, lw, 0);
  ld_a<3>(L, ring, lw, 1);
  const int lane = lane_id();
  const unsigned u = lw * 64 + lane < 224 ? lw * 64 + lane : 223u;
  wait_vm<4>();
  barrier();
  ld_a<3>(L, ring, lw, 2);
  ld_b<3>(L, ring, lw, 2);
#pragma unroll 1
  for (unsigned t = 0; t + 3 < NT; ++t) {
    if (t == 24) {
      const unsigned hf = u >= 112, c = u - hf * 112, wn_ = c >= 56, cc = c - wn_ * 56, blk = cc >> 4;
      const unsigned src = lds0 + (wn_ * 4 + blk) * 768 + (blk == 3 ? 512 + (hf * 8 + (cc & 7)) * 4 : hf * 256 + (cc & 15) * 4);
      const float v = (*(const LAS float*)(src) + *(const LAS float*)(src + 64)) + (*(const LAS float*)(src + 128) + *(const LAS float*)(src + 192));
      asm volatile("global_store_dwordx2 %0, %1, off sc1" ::"v"(gcs + L.cls * 224 + u), "v"(((unsigned long long)tag << 32) | __float_as_uint(v)) : "memory");
    }
    ld_a<3>(L, ring, lw, t + 3);
    barrier();
    ld_b<3>(L, ring, lw, t + 3);
    wait_vm<14>();
    barrier();
  }
  unsigned long long g[8];
#pragma unroll
  for (int i = 0; i < 8; ++i) asm volatile("global_load_dwordx2 %0, %1, off sc1" : "=v"(g[i]) : "v"(gcs + i * 224 + u) : "memory");
  barrier();
  ld_b<NEXT>(N, ring, lw, 0);
  wait_vm<19>();
  barrier();
  barrier();
  ld_b<NEXT>(N, ring, lw, 1);
  wait_vm<16>();
  barrier();
  barrier();
  barrier();
  asm volatile("s_waitcnt vmcnt(8)" : "+v"(g[0]), "+v"(g[1]), "+v"(g[2]), "+v"(g[3]), "+v"(g[4]), "+v"(g[5]), "+v"(g[6]), "+v"(g[7]));
#pragma unroll 1
  for (unsigned spins = 0; spins < (1u << 15); ++spins) {
    unsigned tg = 0u;
#pragma unroll
    for (int i = 0; i < 8; ++i) tg |= (unsigned)(g[i] >> 32) ^ tag;
    if (__all(tg == 0u)) break;
    __builtin_amdgcn_s_sleep(2);
#pragma unroll
    for (int i = 0; i < 8; ++i) asm volatile("global_load_dwordx2 %0, %1, off sc1" : "+v"(g[i]) : "v"(gcs + i * 224 + u) : "memory");
    asm volatile("s_waitcnt vmcnt(0)" : "+v"(g[0]), "+v"(g[1]), "+v"(g[2]), "+v"(g[3]), "+v"(g[4]), "+v"(g[5]), "+v"(g[6]), "+v"(g[7]));
  }
  float s = 0.f;
#pragma unroll
  for (int i = 0; i < 8; ++i) s += __uint_as_float((unsigned)g[i]);
  cst[u] = s;
  wait_lgkm0();
  {
    const char* tp = (const char*)p.wd + ((size_t)blockIdx.x * 224 + (lw * 56 + (lane < 56 ? lane : 55))) * 128;
    asm volatile("global_load_dword %0, %1, off" : "+v"(touch) : "v"(tp) : "memory");
  }
  barrier();
}

__device__ __forceinline__ void glu_compute(const FArgs& p, unsigned lds0, unsigned w, unsigned xcd, unsigned idx) {
  constexpr int ID = 3, BM = 256, NT = 32, MB = 4, NB = 7;
  const unsigned ring = lds0 + F_RING;
  LAS f32x2* tab = (LAS f32x2*)(lds0 + F_TAB);
  LAS float* cst = (LAS float*)(lds0 + F_TAB + 2048);
  const unsigned tm = idx & 7, tn = xcd * 4 + (idx >> 3), cls = (tm + 2 * (idx >> 3)) & 7;
  const unsigned wm = w & 3, wn = w >> 2;
  const int lane = lane_id(), fr = lane & 15, fq = lane >> 4;
  const unsigned rot = wm == 3 ? 0u : wm;
  unsigned xa = ring + wm * 4096 + lane * 16, xb[3], xb1;
  {
    const int q = fr >> 2, pp = fr & 3, r = (fq >> 1) * 16 + (fq & 1) * 4 + q;
#pragma unroll
    for (int i = 0; i < 3; ++i) xb[i] = ring + F_A + r * 224 + wn * 112 + pp * 8 + ((i + rot) % 3) * 32;
    xb1 = ring + F_A + r * 224 + wn * 112 + 96 + (pp & 1) * 8 + (pp >> 1) * G3_UP;
  }
  float cs[3] = {0.f, 0.f, 0.f};
  bf16x8 fa[MB], fb[NB];
  f32x4 acc[MB][NB];
#pragma unroll
  for (int mb = 0; mb < MB; ++mb)
#pragma unroll
    for (int nb = 0; nb < NB; ++nb) { acc[mb][nb] = (f32x4){0.f, 0.f, 0.f, 0.f}; asm volatile("" : "+v"(acc[mb][nb])); }
  asm volatile("" : "+v"(xa), "+v"(xb[0]), "+v"(xb[1]), "+v"(xb[2]), "+v"(xb1));
  barrier();
  if (wn == 1) {
    const auto rst = OUT_RSRC(p.stats, 8 * S_ * 16);
    f32x2 sv[16];
#pragma unroll
    for (int i = 0; i < 16; ++i) sv[i] = __builtin_bit_cast(f32x2, __builtin_amdgcn_raw_buffer_load_b64(rst, (tm * BM + wm * 64 + lane) * 8, i * S_ * 8, 16));
    SB();
    barrier();
    SB();
    float m = 0.f;
#pragma unroll
    for (int i = 0; i < 16; ++i) m += sv[i][0];
    m *= (1.0f / 16.0f);
    float q = 0.f;
#pragma unroll
    for (int i = 0; i < 16; ++i) {
      const float d = sv[i][0] - m;
      q += sv[i][1] + 64.0f * d * d;
    }
    tab[wm * 64 + lane] = (f32x2){m, q * (1.0f / D_) + EPS};
  }
  wait_lgkm0();
  barrier();
  const unsigned csmask = (0x010101u << cls) | (1u << ((cls + 1) & 7));
  auto reads = [&]() {
    SB();
    fa[0] = lds_b128(xa);
#pragma unroll
    for (int j = 0; j < 7; ++j) {
      const unsigned o = j < 6 ? xb[j % 3] + (j / 3) * G3_UP : xb1;
      fb[j] = lds_tr(o, o + 1792);
    }
#pragma unroll
    for (int mb = 1; mb < MB; ++mb) fa[mb] = lds_b128(xa + mb * 1024);
    SB();
  };
  reads();
#pragma unroll 1
  for (unsigned t = 0; t < NT; ++t) {
    const unsigned delta = ((t & 3) == 3) ? (unsigned)(-3 * F_ST) : (unsigned)F_ST;
    xa += delta; xb[0] += delta; xb[1] += delta; xb[2] += delta; xb1 += delta;
    asm volatile("" : "+v"(xa), "+v"(xb[0]), "+v"(xb[1]), "+v"(xb[2]), "+v"(xb1));
    if (__builtin_expect((csmask >> t) & 1u, 0)) {
#pragma unroll
      for (int j = 0; j < 4; ++j)
#pragma unroll
        for (int i = 0; i < 3; ++i)
          asm volatile("v_dot2c_f32_bf16 %0, %1, %2" : "+v"(cs[i]) : "s"(0x3f803f80u), "v"(__builtin_bit_cast(u32x4, fb[3 * i])[j]));
      asm volatile("s_mov_b32 m0, %3\n\ts_nop 2\n\tds_write_addtid_b32 %0\n\tds_write_addtid_b32 %1 offset:256\n\tds_write_addtid_b32 %2 offset:512" ::"v"(cs[0]),
                   "v"(cs[1]), "v"(cs[2]), "s"(lds0 + w * 768)
                   : "memory");
    }
    wait_lgkm0();
    barrier();
    SB();
#pragma unroll
    for (int mb = 0; mb < MB; ++mb)
#pragma unroll
      for (int i = 0; i < NB; ++i) {
        const int nb = (mb & 1) ? NB - 1 - i : i;
        MFMA(fb[nb], fa[mb], acc[mb][nb]);
      }
    SB();
    barrier();
    reads();
  }
  if (wn == 0) barrier();
  mfma_drain();
  SB();
  const int lane2 = lane_id();
  const int gr = lane2 & 15, gq = lane2 >> 4;
  auto act2 = [](f32x2 a, f32x2 b, float sc, float k) {
    const f32x2 t = a * sc;
    const f32x2 e = {__builtin_amdgcn_exp2f(t.x), __builtin_amdgcn_exp2f(t.y)};
    const f32x2 d = e * k + k;
    const f32x2 r = {__builtin_amdgcn_rcpf(d.x), __builtin_amdgcn_rcpf(d.y)};
    const f32x2 o = (a * b) * r;
    return pk_bf16(o.x, o.y);
  };
  const auto ro = OUT_RSRC(p.gu, S_ * F_ * 2);
  float nm[MB], kk[MB], sc[MB];
#pragma unroll
  for (int mb = 0; mb < MB; ++mb) {
    const f32x2 st = tab[wm * 64 + mb * 16 + gr];
    nm[mb] = -st.x; kk[mb] = st.y; sc[mb] = -1.44269504089f * __builtin_amdgcn_rsqf(st.y);
  }
  const int gch0 = tn * 14 + wn * 7;
  const unsigned obase = (tm * (BM / 16) + wm * MB) * (F_ / 32) * 1024 + gr * 16;
#pragma unroll
  for (int i = 0; i < 3; ++i) {
    const unsigned ib = (i + rot) % 3;
    const f32x4 cg = *(const LAS f32x4*)(cst + wn * 56 + ib * 16 + gq * 4), cu = *(const LAS f32x4*)(cst + 112 + wn * 56 + ib * 16 + gq * 4);
    const unsigned oo = obase + (gq & 1) * (F_ / 32) * 1024 + (gch0 + 2 * ib + (gq >> 1)) * 256;
#pragma unroll
    for (int mp = 0; mp < MB / 2; ++mp) {
      u32x2 pkd[2];
#pragma unroll
      for (int h = 0; h < 2; ++h) {
        const int mb = 2 * mp + h;
        const f32x4 a = cg * nm[mb] + acc[mb][i], b = cu * nm[mb] + acc[mb][3 + i];
        pkd[h] = (u32x2){act2(a.xy, b.xy, sc[mb], kk[mb]), act2(a.zw, b.zw, sc[mb], kk[mb])};
      }
      auto s0 = __builtin_amdgcn_permlane16_swap(pkd[0].x, pkd[1].x, false, false);
      auto s1 = __builtin_amdgcn_permlane16_swap(pkd[0].y, pkd[1].y, false, false);
      __builtin_amdgcn_raw_buffer_store_b128((u32x4){s0[0], s1[0], s0[1], s1[1]}, ro, oo + mp * 2 * (F_ / 32) * 1024, 0, ST_AUX);
    }
  }
  {
    const f32x4 cm = *(const LAS f32x4*)(cst + (gq >> 1) * 112 + wn * 56 + 48 + (gq & 1) * 4);
    u32x2 pkd[MB / 2];
#pragma unroll
    for (int mp = 0; mp < MB / 2; ++mp) {
      const f32x4 v0 = cm * nm[2 * mp] + acc[2 * mp][6], v1 = cm * nm[2 * mp + 1] + acc[2 * mp + 1][6];
      f32x4 a, b;
#pragma unroll
      for (int j = 0; j < 4; ++j) {
        auto s = __builtin_amdgcn_permlane32_swap(__float_as_uint(v0[j]), __float_as_uint(v1[j]), false, false);
        a[j] = __uint_as_float(s[0]); b[j] = __uint_as_float(s[1]);
      }
      const float scs = (gq & 2) ? sc[2 * mp + 1] : sc[2 * mp], ks = (gq & 2) ? kk[2 * mp + 1] : kk[2 * mp];
      pkd[mp] = (u32x2){act2(a.xy, b.xy, scs, ks), act2(a.zw, b.zw, scs, ks)};
    }
    auto s0 = __builtin_amdgcn_permlane16_swap(pkd[0].x, pkd[1].x, false, false);
    auto s1 = __builtin_amdgcn_permlane16_swap(pkd[0].y, pkd[1].y, false, false);
    STORE16(ro, obase + ((gq & 1) * 2 + (gq >> 1)) * (F_ / 32) * 1024 + (gch0 + 6) * 256, ((u32x4){s0[0], s1[0], s0[1], s1[1]}));
  }
}

__global__ __launch_bounds__(768, 3) void k_block(FArgs p) {
  extern __shared__ __attribute__((aligned(16))) char shm[];
  const unsigned lds0 = (unsigned)(size_t)(LAS char*)shm;
  const unsigned w = rfl(threadIdx.x >> 6);
  const unsigned xcd = blockIdx.x & 7, idx = blockIdx.x >> 3;
  char* fl = (char*)p.flags;
  const unsigned tm = (xcd >> 1) * 4 + (idx & 3), kh = xcd & 1, lj = idx >> 2;
  if (w >= 8) {
    const unsigned lw = w - 8;
    Ld LA, LB;
    unsigned touch = 0;
    LA = ld_setup<1>(p, lw, xcd, idx);
    sq_loader<1>(p, LA, LB, lds0, lw, xcd, idx, touch);
    const unsigned tag = lds_tag(lds0);
    LA = ld_setup<3>(p, lw, xcd, idx);
    ld_pin(LA);
    barrier();
    if (lw == 3) poll_tags(fl + FL_P1 + (kh * 8 + lj) * 16 * 128, 16, tag);
    barrier();
    sq_loader<2>(p, LB, LA, lds0, lw, xcd, idx, touch);
    LB = ld_setup<4>(p, lw, xcd, idx);
    ld_pin(LB);
    barrier();
    if (lw == 3) poll_tags(fl + FL_P2 + (idx & 7) * 32 * 128, 32, tag);
    barrier();
    glu_loader(p, LA, LB, lds0, lw, xcd, idx, touch);
    barrier();
    if (lw == 3) poll_tags(fl + FL_P3 + ((tm >> 1) * 2 + kh) * 16 * 128, 16, tag);
    barrier();
    sq_loader<4>(p, LB, LA, lds0, lw, xcd, idx, touch);
    asm volatile("" ::"v"(touch));
    return;
  }
  unsigned csink = 0;
  if (w == 0 && idx < 3) {
    unsigned long long pc;
    asm volatile("s_getpc_b64 %0" : "=s"(pc));
    const unsigned ln = idx * 64 + lane_id();
    asm volatile("global_load_dword %0, %1, %2" : "+v"(csink) : "v"((ln < CODE_LINES ? ln : CODE_LINES - 1) * 128), "s"(pc) : "memory");
  }
  if (w == 1 && idx < 20) {
    const char* base = idx < 2 ? (const char*)p.cre : idx < 4 ? (const char*)p.cim : idx < 6 ? (const char*)p.wg : idx < 8 ? (const char*)p.wu : idx < 10 ? (const char*)p.wd : idx < 11 ? (const char*)p.out : (const char*)p.ab;
    const unsigned size = idx < 4 ? S_ * S_ * 2 : idx < 10 ? D_ * F_ * 2 : idx < 11 ? S_ * D_ * 2 : (unsigned)WS_FL;
    const unsigned off = ((idx < 10 ? (idx & 1) : (idx < 11 ? 0 : idx - 11)) * 64 + lane_id()) * 65536;
    asm volatile("global_load_dword %0, %1, %2 sc1" : "+v"(csink) : "v"(off < size ? off : 0u), "s"(base) : "memory");
  }
  unsigned tag = 0;
  sq_compute<1>(p, lds0, w, xcd, idx, tag);
  asm volatile("" ::"v"(csink));
  wait_vm<0>();
  barrier();
  if (w == 0 && lane_id() == 0) put_tag(fl + FL_P1 + (tm * 16 + kh * 8 + lj) * 128, tag);
  sq_compute<2>(p, lds0, w, xcd, idx, tag);
  wait_vm<0>();
  barrier();
  if (w == 0 && lane_id() == 0) put_tag(fl + FL_P2 + (tm * 16 + kh * 8 + lj) * 128, tag);
  glu_compute(p, lds0, w, xcd, idx);
  wait_vm<0>();
  barrier();
  if (w == 0 && lane_id() == 0) put_tag(fl + FL_P3 + (((idx & 7) * 2 + (xcd >> 2)) * 16 + (xcd & 3) * 4 + (idx >> 3)) * 128, tag);
  sq_compute<4>(p, lds0, w, xcd, idx, tag);
  if (blockIdx.x == 0 && w == 0 && lane_id() == 0) put_tag(fl + FL_EPOCH, tag);
}
}

extern "C" void kernel_launch(void* const* d_in, const int* in_sizes, int n_in, void* d_out, int out_size, void* d_ws, size_t ws_size,
                              hipStream_t stream) {
  char* ws = (char*)d_ws;
  FArgs f = {d_in[3], d_in[4], d_in[1], d_in[2], d_in[0], d_in[6], d_in[7], d_in[8], ws + WS_AB, ws + WS_H, ws + WS_ST, ws + WS_GU, d_out, ws + WS_XC, ws + WS_FL};
  (void)hipFuncSetAttribute((const void*)k_block, hipFuncAttributeMaxDynamicSharedMemorySize, F_LDS);
  hipLaunchKernelGGL(k_block, dim3(256), dim3(768), F_LDS, stream, f);
}
```

```cpp
#include <hip/hip_runtime.h>
#include <hip/hip_bf16.h>

namespace {
#define LAS __attribute__((address_space(3)))
typedef unsigned short u16;
typedef short s16x4 __attribute__((ext_vector_type(4)));
typedef short s16x8 __attribute__((ext_vector_type(8)));
typedef __bf16 bf16x8 __attribute__((ext_vector_type(8)));
typedef __bf16 bf16x2 __attribute__((ext_vector_type(2)));
typedef float f32x4 __attribute__((ext_vector_type(4)));
typedef float f32x2 __attribute__((ext_vector_type(2)));
typedef unsigned u32x4 __attribute__((ext_vector_type(4)));
typedef unsigned u32x2 __attribute__((ext_vector_type(2)));

constexpr int S_ = 2048, D_ = 1024, F_ = 3584;
constexpr float EPS = 1e-5f;

constexpr size_t WS_AB = 0;
constexpr size_t WS_H = WS_AB + (size_t)D_ * 2 * S_ * 2;
constexpr size_t WS_GU = WS_H + (size_t)S_ * D_ * 2;
constexpr size_t WS_ST = WS_GU + (size_t)S_ * F_ * 2;
constexpr size_t WS_XC = WS_ST + (size_t)S_ * 16 * 8;
constexpr size_t WS_FL = WS_XC + (size_t)2 * 256 * 16384;
constexpr int FL_EPOCH = 0;
constexpr int FL_PAIR = 128;
constexpr int FL_P1 = FL_PAIR + 512 * 128;
constexpr int FL_P2 = FL_P1 + 256 * 128;
constexpr int FL_P3 = FL_P2 + 256 * 128;
constexpr int FL_LN = FL_P3 + 256 * 128;
constexpr int FL_CS = FL_LN + 4096 * 16;

__device__ __forceinline__ unsigned rfl(unsigned v) { return __builtin_amdgcn_readfirstlane(v); }
__device__ __forceinline__ unsigned pk_bf16(float lo, float hi) {
  bf16x2 c = {(__bf16)lo, (__bf16)hi};
  return __builtin_bit_cast(unsigned, c);
}
__device__ __forceinline__ float bf_lo(unsigned v) { return __uint_as_float(v << 16); }
__device__ __forceinline__ float bf_hi(unsigned v) { return __uint_as_float(v & 0xffff0000u); }

__device__ __forceinline__ u32x4 make_rsrc(const void* p, unsigned bytes) {
  unsigned long long a = (unsigned long long)p;
  u32x4 r;
  r.x = (unsigned)a;
  r.y = (unsigned)(a >> 32) & 0xffffu;
  r.z = bytes;
  r.w = 0x00020000u;
  return r;
}
template <int N>
__device__ __forceinline__ void wait_vm() {
  asm volatile("s_waitcnt vmcnt(%0)" ::"i"(N) : "memory");
}
__device__ __forceinline__ void wait_lgkm0() { __builtin_amdgcn_s_waitcnt(0xC07F); }
__device__ __forceinline__ void barrier() {
  asm volatile("" ::: "memory");
  __builtin_amdgcn_s_barrier();
  asm volatile("" ::: "memory");
}
__device__ __forceinline__ bf16x8 lds_b128(unsigned addr) { return *(const LAS bf16x8*)(addr); }
__device__ __forceinline__ bf16x8 lds_tr(unsigned a0, unsigned a1) {
  s16x4 lo = __builtin_amdgcn_ds_read_tr16_b64_v4i16((LAS s16x4*)(a0));
  s16x4 hi = __builtin_amdgcn_ds_read_tr16_b64_v4i16((LAS s16x4*)(a1));
  s16x8 f = __builtin_shufflevector(lo, hi, 0, 1, 2, 3, 4, 5, 6, 7);
  return __builtin_bit_cast(bf16x8, f);
}

#define ST_AUX 16
#define OUT_RSRC(ptr, bytes) __builtin_amdgcn_make_buffer_rsrc((void*)(ptr), 0, (bytes), 0x00020000)
#define STORE16(rs, byteoff, v) __builtin_amdgcn_raw_buffer_store_b128(v, rs, byteoff, 0, ST_AUX)

#define DMA4(M) asm volatile("s_mov_b32 m0, %0\n\ts_nop 4\n\tbuffer_load_dwordx4 %1, %5, %6 offen " M " lds\n\tbuffer_load_dwordx4 %2, %5, %6 offen offset:1024 " M " lds\n\t" \
                 "buffer_load_dwordx4 %3, %5, %6 offen offset:2048 " M " lds\n\tbuffer_load_dwordx4 %4, %5, %6 offen offset:3072 " M " lds" ::"s"(ldsaddr), \
                 "v"(v[0]), "v"(v[1]), "v"(v[2]), "v"(v[3]), "s"(rsrc), "s"(soff) : "memory")
#define DMA3(M) asm volatile("s_mov_b32 m0, %0\n\ts_nop 4\n\tbuffer_load_dwordx4 %1, %4, %5 offen " M " lds\n\tbuffer_load_dwordx4 %2, %4, %5 offen offset:1024 " M " lds\n\t" \
                 "buffer_load_dwordx4 %3, %4, %5 offen offset:2048 " M " lds" ::"s"(ldsaddr), "v"(v[0]), "v"(v[1]), "v"(v[2]), "s"(rsrc), "s"(soff) : "memory")
#define DMAB(M) asm volatile("s_mov_b32 m0, %0\n\ts_waitcnt vmcnt(8)\n\ts_barrier\n\tbuffer_load_dwordx4 %1, %5, %6 offen " M " lds\n\tbuffer_load_dwordx4 %2, %5, %6 offen offset:1024 " M " lds\n\t" \
                 "buffer_load_dwordx4 %3, %5, %6 offen offset:2048 " M " lds\n\tbuffer_load_dwordx4 %4, %5, %6 offen offset:3072 " M " lds" ::"s"(ldsaddr), \
                 "v"(v[0]), "v"(v[1]), "v"(v[2]), "v"(v[3]), "s"(rsrc), "s"(soff) : "memory")
template <int N, int MOD = 0>
__device__ __forceinline__ void dma_grp(unsigned ldsaddr, const unsigned (&v)[4], u32x4 rsrc, unsigned soff) {
  if constexpr (N == 4) { if constexpr (MOD == 1) DMA4("sc1"); else DMA4(""); }
  else if constexpr (N == 3) { if constexpr (MOD == 1) DMA3("sc1"); else DMA3(""); }
  else { if constexpr (MOD == 1) DMAB("sc1"); else DMAB(""); }
}
__device__ __forceinline__ float sum_rows(float x) {
  auto a = __builtin_amdgcn_permlane16_swap(__float_as_uint(x), __float_as_uint(x), false, false);
  x = __uint_as_float(a[0]) + __uint_as_float(a[1]);
  auto b = __builtin_amdgcn_permlane32_swap(__float_as_uint(x), __float_as_uint(x), false, false);
  return __uint_as_float(b[0]) + __uint_as_float(b[1]);
}
template <int CTRL> __device__ __forceinline__ float dpp_add(float x) { return x + __uint_as_float(__builtin_amdgcn_mov_dpp(__float_as_uint(x), CTRL, 0xF, 0xF, true)); }
__device__ __forceinline__ float wave_sum(float x) { return sum_rows(dpp_add<0x140>(dpp_add<0x141>(dpp_add<0x4E>(dpp_add<0xB1>(x))))); }
__device__ __forceinline__ int lane_id() {
  int l;
  asm volatile("v_mbcnt_lo_u32_b32 %0, -1, 0\n\tv_mbcnt_hi_u32_b32 %0, -1, %0" : "=v"(l));
  return l;
}
__device__ __forceinline__ void mfma_drain() { asm volatile("s_nop 15" ::: "memory"); }
template <int NC, int NT>
__device__ __forceinline__ unsigned stage_of(unsigned n, unsigned c) {
  const unsigned t = ((n & (NC - 1)) == c) ? n + NC : n;
  return rfl(t >= NT ? t - NT : t);
}
#define SB() __builtin_amdgcn_sched_barrier(0)
#define MFMA(b, a, c) asm volatile("v_mfma_f32_16x16x32_bf16 %0, %1, %2, %0" : "+v"(c) : "v"(b), "v"(a))

struct FArgs {
  const void *dre, *dim, *cre, *cim, *x, *wg, *wu, *wd;
  void *ab, *h, *stats, *gu, *out, *xch, *flags;
};
constexpr unsigned CODE_LINES = 156;
constexpr int F_RING = 6144, F_ST = 32768, F_TAB = F_RING + 4 * F_ST, F_TAG = F_TAB + 4096, F_LDS = F_TAB + 4160, F_A = 16384, G3_UP = 7168 + 16;

__device__ __forceinline__ void poll_tags(const void* line, int n, unsigned tag) {
  const int l = lane_id();
  const unsigned* q = (const unsigned*)line + (l < n ? l : 0) * 32;
  unsigned spins = 0;
  while (!__all(__hip_atomic_load(q, __ATOMIC_RELAXED, __HIP_MEMORY_SCOPE_AGENT) == tag) && ++spins < (1u << 17)) __builtin_amdgcn_s_sleep(1);
}
__device__ __forceinline__ void put_tag(void* word, unsigned tag) { __hip_atomic_store((unsigned*)word, tag, __ATOMIC_RELAXED, __HIP_MEMORY_SCOPE_AGENT); }
__device__ __forceinline__ unsigned lds_tag(unsigned lds0) { return rfl(*(volatile LAS unsigned*)(lds0 + F_TAG)); }
template <int ID> constexpr int nt_of() { return ID == 1 ? 16 : (ID == 4 ? 28 : 32); }
template <int ID> constexpr int nc_of() { return (ID == 1 || ID == 4) ? 4 : 8; }
template <int ID> constexpr int mod_a() { return ID > 2; }
template <int ID> constexpr int mod_b() { return ID == 2; }
struct Ld {
  u32x4 qa, qb;
  unsigned va[4], vb[4], cls;
};
template <int ID>
__device__ __forceinline__ Ld ld_setup(const FArgs& p, unsigned lw, unsigned xcd, unsigned idx) {
  const int lane = lane_id();
  Ld L;
  if constexpr (ID == 3) {
    const unsigned tm = idx & 7, tn = xcd * 4 + (idx >> 3);
    L.qa = make_rsrc((const u16*)p.h + (size_t)tm * 256 * D_, 256 * D_ * 2);
    L.qb = make_rsrc(lw < 2 ? p.wg : p.wu, (unsigned)D_ * F_ * 2);
#pragma unroll
    for (int i = 0; i < 4; ++i) {
      L.va[i] = (lw * 4 + i) * (D_ / 32) * 1024 + lane * 16 - i * 1024;
      const int c = ((lw & 1) * 4 + i) * 64 + lane, r = c / 14, pos = c % 14;
      const int k = (r & 0x13) | ((r & 4) << 1) | ((r & 8) >> 1);
      L.vb[i] = k * F_ * 2 + tn * 224 + pos * 16 - i * 1024;
    }
    L.cls = (tm + 2 * (idx >> 3)) & 7;
  } else {
    const unsigned li = idx & 3, lj = idx >> 2, kh = xcd & 1, tm = (xcd >> 1) * 4 + li, tn = lj;
    if constexpr (ID == 1) {
      L.qa = make_rsrc((const u16*)(tm < 8 ? p.dre : p.dim) + (size_t)(tm & 7) * 128 * D_, 128 * D_ * 2);
      L.qb = make_rsrc((const u16*)p.x + (size_t)(kh * 8 + lj) * 128 * D_, 128 * D_ * 2);
    } else if constexpr (ID == 2) {
      L.qa = make_rsrc((const u16*)(kh ? p.cim : p.cre) + (size_t)tm * 128 * S_, 128 * S_ * 2);
      L.qb = make_rsrc((const u16*)p.ab + (size_t)tn * 128 * 4096 + kh * 64 * 512, 128 * 4096 * 2 - kh * 64 * 1024);
    } else {
      L.qa = make_rsrc((const u16*)p.gu + (size_t)tm * 128 * F_ + kh * 56 * 512, 128 * F_ * 2 - kh * 56 * 1024);
      L.qb = make_rsrc((const u16*)p.wd + (size_t)kh * 1792 * D_, 1792 * D_ * 2);
    }
#pragma unroll
    for (int i = 0; i < 4; ++i) {
      const int pc = lw * 4 + i;
      if constexpr (ID == 1) {
        const int row = pc * 8 + (lane >> 3);
        L.va[i] = L.vb[i] = row * D_ * 2 + (((lane & 7) ^ ((row >> 1) & 7)) << 4) - i * 1024;
      } else if constexpr (ID == 2) {
        const int row = pc * 8 + (lane >> 3);
        L.va[i] = row * S_ * 2 + (((lane & 7) ^ ((row >> 1) & 7)) << 4) - i * 1024;
        L.vb[i] = ((pc >> 1) * 128 + (pc & 1)) * 1024 + lane * 16 - i * 1024;
      } else {
        const int k = pc * 4 + (lane >> 4), f = (k & 3) | (((k >> 3) & 1) << 2);
        L.va[i] = ((pc >> 1) * (F_ / 32) + (pc & 1)) * 1024 + lane * 16 - i * 1024;
        L.vb[i] = k * D_ * 2 + tn * 256 + (((lane & 15) ^ (f << 1)) << 4) - i * 1024;
      }
    }
    L.cls = (nc_of<ID>() == 8) ? ((lj + 2 * li) & 7) : ((lj + li) & 3);
  }
  return L;
}
__device__ __forceinline__ void ld_pin(Ld& L) {
  asm volatile("" : "+v"(L.va[0]), "+v"(L.va[1]), "+v"(L.va[2]), "+v"(L.va[3]), "+v"(L.vb[0]), "+v"(L.vb[1]), "+v"(L.vb[2]), "+v"(L.vb[3]));
}
template <int ID>
__device__ __forceinline__ void ld_a(const Ld& L, unsigned ring, unsigned lw, unsigned n) {
  const unsigned st = stage_of<nc_of<ID>(), nt_of<ID>()>(n, L.cls);
  dma_grp<4, mod_a<ID>()>(ring + (n & 3) * F_ST + lw * 4096, L.va, L.qa, st * (ID <= 2 ? 128 : (ID == 3 ? 1024 : 2048)));
}
template <int ID>
__device__ __forceinline__ void ld_b(const Ld& L, unsigned ring, unsigned lw, unsigned n) {
  const unsigned st = stage_of<nc_of<ID>(), nt_of<ID>()>(n, L.cls);
  const unsigned dst = ring + (n & 3) * F_ST + F_A;
  if constexpr (ID == 3) {
    const unsigned d = dst + (lw >> 1) * G3_UP + (lw & 1) * 4096;
    if (lw & 1) dma_grp<3, mod_b<ID>()>(d, L.vb, L.qb, st * (32 * F_ * 2));
    else dma_grp<4, mod_b<ID>()>(d, L.vb, L.qb, st * (32 * F_ * 2));
  } else {
    dma_grp<4, mod_b<ID>()>(dst + lw * 4096, L.vb, L.qb, st * (ID == 1 ? 128 : (ID == 2 ? 2048 : 64 * D_ * 2)));
  }
}

template <int ID> __device__ __forceinline__ void ld_const(const Ld& L, unsigned ring, unsigned lw, unsigned n) { if constexpr (ID == 2) ld_a<ID>(L, ring, lw, n); else ld_b<ID>(L, ring, lw, n); }
template <int ID> __device__ __forceinline__ void ld_dep(const Ld& L, unsigned ring, unsigned lw, unsigned n) { if constexpr (ID == 2) ld_b<ID>(L, ring, lw, n); else ld_a<ID>(L, ring, lw, n); }

template <int ID>
__device__ __forceinline__ void sq_loader(const FArgs& p, const Ld& L, Ld& N, unsigned lds0, unsigned lw, unsigned xcd, unsigned idx, unsigned& touch) {
  constexpr int NT = nt_of<ID>(), NEXT = ID == 4 ? 0 : ID + 1;
  const unsigned ring = lds0 + F_RING;
  if constexpr (ID == 1) {
    ld_a<ID>(L, ring, lw, 0); ld_b<ID>(L, ring, lw, 0);
    N = ld_setup<NEXT>(p, lw, xcd, idx); ld_pin(N);
    ld_a<ID>(L, ring, lw, 1); ld_b<ID>(L, ring, lw, 1);
  } else {
    ld_dep<ID>(L, ring, lw, 0); ld_dep<ID>(L, ring, lw, 1);
  }
  constexpr unsigned KA = ID <= 2 ? 128 : 2048, KB = ID == 1 ? 128 : (ID == 2 ? 2048 : 64 * D_ * 2);
  unsigned sa, sb, ma;
  auto prep = [&](unsigned n) {
    const unsigned st = stage_of<nc_of<ID>(), NT>(n, L.cls);
    sa = st * KA; sb = st * KB; ma = ring + (n & 3) * F_ST + lw * 4096;
    asm volatile("" : "+s"(sa), "+s"(sb), "+s"(ma));
  };
  auto bar_a = [&]() {
    dma_grp<5, mod_a<ID>()>(ma, L.va, L.qa, sa);
  };
  prep(2);
  wait_vm<(ID == 1 ? 8 : 4)>();
  bar_a();
  unsigned mb_ = ma + F_A, sb_ = sb;
  prep(3);
#pragma unroll 1
  for (unsigned n = 0; n + 3 < NT; ++n) {
    dma_grp<4, mod_b<ID>()>(mb_, L.vb, L.qb, sb_);
    bar_a();
    mb_ = ma + F_A; sb_ = sb;
    asm volatile("" : "+s"(mb_), "+s"(sb_));
    prep(n + 4);
  }
  dma_grp<4, mod_b<ID>()>(mb_, L.vb, L.qb, sb_);
  wait_vm<8>();
  barrier();
  const int lane = lane_id();
  u32x2 l0, l1;
  const char* lp = (const char*)p.flags + FL_LN + (lw < 2 ? (((xcd & 1) * 8 + (idx >> 2)) * 128 + lw * 64 + lane) : (2048 + ((xcd >> 1) * 4 + (idx & 3)) * 128 + (lw - 2) * 64 + lane)) * 16;
  if constexpr (ID == 1) asm volatile("global_load_dwordx2 %0, %2, off sc1\n\tglobal_load_dwordx2 %1, %2, off offset:8 sc1" : "=&v"(l0), "=&v"(l1) : "v"(lp) : "memory");
  if constexpr (NEXT != 0) ld_const<NEXT>(N, ring, lw, 0);
  if constexpr (ID == 1) wait_vm<6>();
  else if constexpr (ID == 2) { if (lw & 1) wait_vm<3>(); else wait_vm<4>(); }
  else wait_vm<0>();
  barrier();
  if constexpr (NEXT != 0) ld_const<NEXT>(N, ring, lw, 1);
  if constexpr (ID == 1) {
    if (lw < 2) {
      const char* tp = (const char*)((xcd & 1) ? p.cim : p.cre) + (size_t)((xcd >> 1) * 512 + idx * 16 + lw * 8 + (lane >> 3)) * (S_ * 2) + (lane & 7) * 128;
      asm volatile("global_load_dword %0, %1, off" : "+v"(touch) : "v"(tp) : "memory");
    }
  }
  if constexpr (ID == 2) {
    if (lw < 2) {
      const char* tp = (const char*)((lw & 1) ? p.wu : p.wg) + (size_t)(idx * 8 + (lane >> 3)) * (F_ * 2) + xcd * 896 + ((lane & 7) < 7 ? (lane & 7) : 6) * 128;
      asm volatile("global_load_dword %0, %1, off" : "+v"(touch) : "v"(tp) : "memory");
    }
  }
  barrier();
  const unsigned tag = lds_tag(lds0);
  if constexpr (ID == 1) {
    if (lw < 2) wait_vm<9>(); else wait_vm<8>();
    asm volatile("" : "+v"(l0), "+v"(l1));
#pragma unroll 1
    for (unsigned spins = 0; spins < (1u << 15) && !__all(l0.y == tag && l1.y == tag); ++spins) {
      __builtin_amdgcn_s_sleep(2);
      asm volatile("global_load_dwordx2 %0, %2, off sc1\n\tglobal_load_dwordx2 %1, %2, off offset:8 sc1\n\ts_waitcnt vmcnt(0)" : "=&v"(l0), "=&v"(l1) : "v"(lp) : "memory");
    }
    LAS unsigned* tb = (LAS unsigned*)(lds0 + F_TAB);
    tb[lw * 64 + lane + (lw < 2 ? 0 : 128)] = l0.x;
    if (lw < 2) tb[128 + lw * 64 + lane] = l1.x;
    wait_lgkm0();
  }
  barrier();
  barrier();
  if constexpr (ID != 1) {
    barrier();
    if (lw == 3) poll_tags((char*)p.flags + FL_PAIR + ((ID == 2 ? 0 : 256) + (blockIdx.x ^ 1)) * 128, 1, tag);
    barrier();
  }
}

template <int ID>
__device__ __forceinline__ void sq_compute(const FArgs& p, unsigned lds0, unsigned w, unsigned xcd, unsigned idx, unsigned& tag) {
  constexpr int NT = nt_of<ID>();
  u32x4 xv[2], wv[2];
  unsigned tv = 0;
  const unsigned r0 = blockIdx.x * 8 + w;
  char* fl = (char*)p.flags;
  if constexpr (ID == 1) {
    const int lane = lane_id();
    const u16* xp = (const u16*)p.x + (size_t)r0 * D_ + lane * 8;
    const u16* wp = (const u16*)(r0 < 1024 ? p.dre : p.dim) + (size_t)(r0 & 1023) * D_ + lane * 8;
#pragma unroll
    for (int i = 0; i < 2; ++i) xv[i] = *(const u32x4*)(xp + i * 512);
#pragma unroll
    for (int i = 0; i < 2; ++i) wv[i] = *(const u32x4*)(wp + i * 512);
    tv = __hip_atomic_load((const unsigned*)(fl + FL_EPOCH), __ATOMIC_RELAXED, __HIP_MEMORY_SCOPE_AGENT);
    SB();
  }
  const unsigned ring = lds0 + F_RING;
  const unsigned g = w >> 2, wm = (w >> 1) & 1, wx = w & 1;
  const unsigned kh = xcd & 1, tm = (xcd >> 1) * 4 + (idx & 3), tn = idx >> 2;
  u32x4 res[2];
  unsigned rdA[8], rdB[4], hoff = 0;
  const unsigned rotA = (ID == 1) ? 2 * g : 2 * g + wx;
  {
    const int lane = lane_id(), fr = lane & 15, fq = lane >> 4, chunk = ((fq & 1) << 1) | (fq >> 1);
    const int row = tm * 128 + wm * 64 + rotA * 16 + fr, col = tn * 128 + kh * 32 + chunk * 8;
    if constexpr (ID == 2) {
#pragma unroll
      for (int c = 0; c < 2; ++c) res[c] = *(const u32x4*)((const u16*)p.x + (size_t)row * D_ + col + (c ^ wx) * 64);
    } else if constexpr (ID == 4) {
      hoff = ((row >> 4) * (D_ / 32) + (col >> 5)) * 1024 + (chunk * 16 + fr) * 16;
    }
    const unsigned rotB = (ID == 1) ? 0u : 2 * kh, base = ring + g * F_ST;
#pragma unroll
    for (int i = 0; i < 4; ++i) {
      const unsigned kk = i >> 1, hf = (i & 1) * 2;
      if constexpr (ID == 1) {
        rdA[i] = base + (wm * 64 + (hf ^ rotA) * 16 + fr) * 128 + (((kk * 4 + fq) ^ (fr >> 1)) << 4);
        rdB[i] = base + F_A + (wx * 64 + fr) * 128 + ((((i & 1) * 4 + fq) ^ (fr >> 1)) << 4);
      } else if constexpr (ID == 2) {
        rdA[i] = base + (wm * 64 + (i ^ rotA) * 16 + fr) * 128 + ((fq ^ (fr >> 1)) << 4);
        rdA[4 + i] = base + (wm * 64 + (i ^ rotA) * 16 + fr) * 128 + (((4 + fq) ^ (fr >> 1)) << 4);
        rdB[i] = base + F_A + wx * 8192 + (hf ^ rotB) * 2048 + lane * 16;
      } else {
        const int q = fr >> 2, pp = fr & 3, f = q | ((fq & 1) << 2);
        rdA[i] = base + wm * 8192 + (i ^ rotA) * 2048 + lane * 16;
        rdB[i] = base + F_A + (8 * fq + q) * 256 + (((wx * 4 + (i ^ rotB)) ^ f) << 5) + pp * 8;
      }
    }
  }
  constexpr int NVA = (ID == 2) ? 8 : 4, NVB = (ID <= 2) ? 2 : 4;
  bf16x8 fa[2][4], fb[2][4];
  f32x4 acc[4][4];
#pragma unroll
  for (int i = 0; i < 16; ++i) { acc[i >> 2][i & 3] = (f32x4){0.f, 0.f, 0.f, 0.f}; asm volatile("" : "+v"(acc[i >> 2][i & 3])); }
  auto reads = [&]() {
    SB();
#pragma unroll
    for (int s = 0; s < 2; ++s)
#pragma unroll
      for (int i = 0; i < 4; ++i) {
        if constexpr (ID == 1) {
          fb[s][i] = lds_b128(rdB[s] + i * 2048);
          fa[s][i] = lds_b128(rdA[s * 2 + (i >> 1)] + (i & 1) * 2048);
        } else if constexpr (ID == 2) {
          fb[s][i] = lds_b128(rdB[i >> 1] + (i & 1) * 2048 + s * 1024);
          fa[s][i] = lds_b128(rdA[s * 4 + i]);
        } else {
          fb[s][i] = lds_tr(rdB[i] + s * 8192, rdB[i] + s * 8192 + 1024);
          fa[s][i] = lds_b128(rdA[i] + s * 1024);
        }
      }
    SB();
  };
#pragma unroll
  for (int j = 0; j < NVA; ++j) asm volatile("" : "+v"(rdA[j]));
#pragma unroll
  for (int j = 0; j < NVB; ++j) asm volatile("" : "+v"(rdB[j]));
  if constexpr (ID == 1) {
    SB();
    asm volatile("" : "+v"(tv));
    tv = tv * 0x9E3779B1u + 0x7F4A7C15u;
    const int lane = lane_id();
    float s = 0.f, t = 0.f;
#pragma unroll
    for (int j = 0; j < 8; ++j) s += bf_lo(xv[j >> 2][j & 3]) + bf_hi(xv[j >> 2][j & 3]);
    const float m = wave_sum(s) * (1.0f / D_);
    float q = 0.f;
#pragma unroll
    for (int j = 0; j < 8; ++j) {
      const float a = bf_lo(xv[j >> 2][j & 3]) - m, b = bf_hi(xv[j >> 2][j & 3]) - m;
      q += a * a + b * b;
      t += bf_lo(wv[j >> 2][j & 3]) + bf_hi(wv[j >> 2][j & 3]);
    }
    const float rs = __builtin_amdgcn_rsqf(wave_sum(q) * (1.0f / D_) + EPS);
    t = wave_sum(t);
    const float val = (lane & 2) ? t : ((lane & 1) ? rs : m);
    if (lane < 4)
      __hip_atomic_store((unsigned long long*)(fl + FL_LN + ((lane >> 1) * 2048 + r0) * 16 + (lane & 1) * 8), ((unsigned long long)tv << 32) | __float_as_uint(val),
                         __ATOMIC_RELAXED, __HIP_MEMORY_SCOPE_AGENT);
    tag = rfl(tv);
    if (w == 0) { *(volatile LAS unsigned*)(lds0 + F_TAG) = tag; wait_lgkm0(); }
  }
  if constexpr (ID != 1) {
    barrier();
  }
  if constexpr (ID == 4) {
    const auto rh = OUT_RSRC(p.h, S_ * D_ * 2);
#pragma unroll
    for (int c = 0; c < 2; ++c) res[c] = __builtin_amdgcn_raw_buffer_load_b128(rh, hoff, (c ^ wx) * 2048, 16);
  }
  barrier();
  if (g == 1) barrier();
  reads();
#pragma unroll 1
  for (unsigned i = 0; i < NT / 2; ++i) {
    const unsigned delta = (i & 1) ? (unsigned)(-2 * F_ST) : (unsigned)(2 * F_ST);
#pragma unroll
    for (int j = 0; j < NVA; ++j) { rdA[j] += delta; asm volatile("" : "+v"(rdA[j])); }
#pragma unroll
    for (int j = 0; j < NVB; ++j) { rdB[j] += delta; asm volatile("" : "+v"(rdB[j])); }
    wait_lgkm0();
    barrier();
    SB();
#pragma unroll
    for (int m = 0; m < 32; ++m) {
      const int n = (m & 4) ? 3 - (m & 3) : (m & 3);
      MFMA(fb[m >> 4][n], fa[m >> 4][(m >> 2) & 3], acc[(m >> 2) & 3][n]);
    }
    SB();
    barrier();
    if (i + 1 < NT / 2) reads();
  }
  mfma_drain();
  SB();
  const int lane = lane_id(), fr = lane & 15, fq = lane >> 4, chunk = ((fq & 1) << 1) | (fq >> 1);
  if constexpr (ID == 1) {
    const unsigned wb = ring + 2 * F_ST + (w ^ 4) * 8192 + lane * 16, rb = ring + 2 * F_ST + w * 8192 + lane * 16;
#pragma unroll
    for (int i = 0; i < 8; ++i) *(LAS f32x4*)(wb + i * 1024) = acc[2 + (i >> 2)][i & 3];
    if (g == 0) barrier();
    wait_lgkm0();
    barrier();
#pragma unroll
    for (int i = 0; i < 8; ++i) acc[i >> 2][i & 3] += *(const LAS f32x4*)(rb + i * 1024);
  }
  if constexpr (ID == 1) {
    const LAS float* tb = (const LAS float*)(lds0 + F_TAB);
    const float sg = tm < 8 ? 1.f : -1.f;
    const auto ro = OUT_RSRC(p.ab, D_ * 4096 * 2);
    f32x4 mean[4], rstd[4];
#pragma unroll
    for (int j = 0; j < 4; ++j) {
      mean[j] = *(const LAS f32x4*)(tb + wx * 64 + j * 16 + fq * 4);
      rstd[j] = *(const LAS f32x4*)(tb + 128 + wx * 64 + j * 16 + fq * 4) * sg;
    }
#pragma unroll
    for (int mb = 0; mb < 2; ++mb) {
      const float wsm = tb[256 + wm * 64 + g * 32 + mb * 16 + fr];
#pragma unroll
      for (int np = 0; np < 2; ++np) {
        const f32x4 x0 = (acc[mb][2 * np] - mean[2 * np] * wsm) * rstd[2 * np], x1 = (acc[mb][2 * np + 1] - mean[2 * np + 1] * wsm) * rstd[2 * np + 1];
        auto s0 = __builtin_amdgcn_permlane16_swap(pk_bf16(x0[0], x0[1]), pk_bf16(x1[0], x1[1]), false, false);
        auto s1 = __builtin_amdgcn_permlane16_swap(pk_bf16(x0[2], x0[3]), pk_bf16(x1[2], x1[3]), false, false);
        const int rblk = (tm & 7) * 8 + wm * 4 + g * 2 + mb;
        const int kblk = (tm >> 3) * 64 + (kh * 8 + tn) * 4 + wx * 2 + np;
        STORE16(ro, (rblk * 128 + kblk) * 1024 + (chunk * 16 + fr) * 16, ((u32x4){s0[0], s1[0], s0[1], s1[1]}));
      }
    }
    return;
  }
  const auto rs = OUT_RSRC((char*)p.xch + (ID == 2 ? 0 : 256 * 16384), 256 * 16384);
  const unsigned sreg = ring + ((w >> 1) & 1) * F_ST + (w & 1) * 8192 + lane * 16, kreg = ring + 2 * F_ST + lane * 16;
  auto put_keep = [&]() {
#pragma unroll
    for (int j = 1; j < 4; ++j) {
      const unsigned d = kreg + ((wm * 4 + (j ^ rotA)) * 3 + (j - 1)) * 2048;
      *(LAS f32x4*)(d) = acc[j][0];
      *(LAS f32x4*)(d + 1024) = acc[j][1];
    }
  };
  if (g == 0) {
#pragma unroll
    for (int i = 0; i < 8; ++i) *(LAS f32x4*)(sreg + i * 1024) = acc[i >> 1][2 + (i & 1)];
    put_keep();
    wait_lgkm0();
    barrier();
  } else {
#pragma unroll
    for (int j = 0; j < 4; ++j) {
      const f32x4 x0 = acc[j][2] + *(const LAS f32x4*)(sreg + (j ^ 2) * 2048), x1 = acc[j][3] + *(const LAS f32x4*)(sreg + (j ^ 2) * 2048 + 1024);
      auto s0 = __builtin_amdgcn_permlane16_swap(pk_bf16(x0[0], x0[1]), pk_bf16(x1[0], x1[1]), false, false);
      auto s1 = __builtin_amdgcn_permlane16_swap(pk_bf16(x0[2], x0[3]), pk_bf16(x1[2], x1[3]), false, false);
      __builtin_amdgcn_raw_buffer_store_b128((u32x4){s0[0], s1[0], s0[1], s1[1]}, rs, lane * 16 + blockIdx.x * 16384 + ((wm * 4 + (j ^ rotA)) * 2 + wx) * 1024, 0, 16);
    }
    SB();
    put_keep();
    wait_lgkm0();
  }
  barrier();
  f32x4 y[2][2];
  {
    const unsigned kb = kreg + (wm * 4 + rotA) * 3 * 2048;
    f32x4 t[2][2];
#pragma unroll
    for (int i = 0; i < 2; ++i) {
      t[0][i] = acc[0][i] + *(const LAS f32x4*)(kb + 2048 + i * 1024);
      t[1][i] = *(const LAS f32x4*)(kb + i * 1024) + *(const LAS f32x4*)(kb + 4096 + i * 1024);
    }
#pragma unroll
    for (int c = 0; c < 2; ++c)
#pragma unroll
      for (int j = 0; j < 4; ++j) {
        auto s = __builtin_amdgcn_permlane16_swap(__float_as_uint(t[c][0][j]), __float_as_uint(t[c][1][j]), false, false);
        y[c][0][j] = __uint_as_float(s[0]) + ((j & 1) ? bf_hi(res[c][j >> 1]) : bf_lo(res[c][j >> 1]));
        y[c][1][j] = __uint_as_float(s[1]) + ((j & 1) ? bf_hi(res[c][2 + (j >> 1)]) : bf_lo(res[c][2 + (j >> 1)]));
      }
  }
  const int row0 = tm * 128 + wm * 64 + rotA * 16 + fr, col = tn * 128 + kh * 32 + chunk * 8;
  unsigned optr = (ID == 2) ? ((row0 >> 4) * (D_ / 32) + (col >> 5)) * 1024 + (chunk * 16 + fr) * 16 : (row0 * D_ + col) * 2;
  const auto ro = OUT_RSRC(ID == 2 ? p.h : p.out, S_ * D_ * 2);
  asm volatile("" : "+v"(y[0][0]), "+v"(y[0][1]), "+v"(y[1][0]), "+v"(y[1][1]), "+v"(optr));
  wait_vm<0>();
  barrier();
  if (w == 0 && lane == 0) put_tag((char*)p.flags + FL_PAIR + ((ID == 2 ? 0 : 256) + blockIdx.x) * 128, tag);
  barrier();
  u32x4 in[2], v[2];
#pragma unroll
  for (int c = 0; c < 2; ++c) in[c] = __builtin_amdgcn_raw_buffer_load_b128(rs, lane * 16, (blockIdx.x ^ 1) * 16384 + ((wm * 4 + rotA) * 2 + (c ^ wx)) * 1024, 16);
#pragma unroll
  for (int c = 0; c < 2; ++c) {
    const f32x4 a = y[c][0] + (f32x4){bf_lo(in[c][0]), bf_hi(in[c][0]), bf_lo(in[c][1]), bf_hi(in[c][1])};
    const f32x4 b = y[c][1] + (f32x4){bf_lo(in[c][2]), bf_hi(in[c][2]), bf_lo(in[c][3]), bf_hi(in[c][3])};
    v[c] = (u32x4){pk_bf16(a[0], a[1]), pk_bf16(a[2], a[3]), pk_bf16(b[0], b[1]), pk_bf16(b[2], b[3])};
    __builtin_amdgcn_raw_buffer_store_b128(v[c], ro, optr + (c ^ wx) * (ID == 2 ? 2048 : 128), 0, ST_AUX);
  }
  SB();
  if constexpr (ID == 2) {
    float s = 0.f;
#pragma unroll
    for (int j = 0; j < 8; ++j) s += bf_lo(v[j >> 2][j & 3]) + bf_hi(v[j >> 2][j & 3]);
    const float m = sum_rows(s) * (1.0f / 64.0f);
    float q = 0.f;
#pragma unroll
    for (int j = 0; j < 8; ++j) {
      const float a = bf_lo(v[j >> 2][j & 3]) - m, b = bf_hi(v[j >> 2][j & 3]) - m;
      q += a * a + b * b;
    }
    const f32x2 st = {m, sum_rows(q)};
    if (fq == 0)
      __hip_atomic_store((unsigned long long*)p.stats + (size_t)(tn * 2 + kh) * S_ + row0, __builtin_bit_cast(unsigned long long, st), __ATOMIC_RELAXED, __HIP_MEMORY_SCOPE_AGENT);
  }
  SB();
}

__device__ __forceinline__ void glu_loader(const FArgs& p, const Ld& L, Ld& N, unsigned lds0, unsigned lw, unsigned xcd, unsigned idx, unsigned& touch) {
  constexpr int NT = 32, NEXT = 4;
  const unsigned tag = lds_tag(lds0);
  const unsigned ring = lds0 + F_RING;
  LAS float* cst = (LAS float*)(lds0 + F_TAB + 2048);
  unsigned long long* gcs = (unsigned long long*)((char*)p.flags + FL_CS) + (xcd * 4 + (idx >> 3)) * (8 * 224);
  ld_a<3>(L, ring, lw, 0);
  ld_a<3>(L, ring, lw, 1);
  const int lane = lane_id();
  const unsigned u = lw * 64 + lane < 224 ? lw * 64 + lane : 223u;
  wait_vm<4>();
  barrier();
  ld_a<3>(L, ring, lw, 2);
  ld_b<3>(L, ring, lw, 2);
#pragma unroll 1
  for (unsigned t = 0; t + 3 < NT; ++t) {
    if (t == 24) {
      const unsigned hf = u >= 112, c = u - hf * 112, wn_ = c >= 56, cc = c - wn_ * 56, blk = cc >> 4;
      const unsigned src = lds0 + (wn_ * 4 + blk) * 768 + (blk == 3 ? 512 + (hf * 8 + (cc & 7)) * 4 : hf * 256 + (cc & 15) * 4);
      const float v = (*(const LAS float*)(src) + *(const LAS float*)(src + 64)) + (*(const LAS float*)(src + 128) + *(const LAS float*)(src + 192));
      asm volatile("global_store_dwordx2 %0, %1, off sc1" ::"v"(gcs + L.cls * 224 + u), "v"(((unsigned long long)tag << 32) | __float_as_uint(v)) : "memory");
    }
    ld_a<3>(L, ring, lw, t + 3);
    barrier();
    ld_b<3>(L, ring, lw, t + 3);
    wait_vm<14>();
    barrier();
  }
  unsigned long long g[8];
#pragma unroll
  for (int i = 0; i < 8; ++i) asm volatile("global_load_dwordx2 %0, %1, off sc1" : "=v"(g[i]) : "v"(gcs + i * 224 + u) : "memory");
  barrier();
  ld_b<NEXT>(N, ring, lw, 0);
  wait_vm<19>();
  barrier();
  barrier();
  ld_b<NEXT>(N, ring, lw, 1);
  wait_vm<16>();
  barrier();
  barrier();
  barrier();
  asm volatile("s_waitcnt vmcnt(8)" : "+v"(g[0]), "+v"(g[1]), "+v"(g[2]), "+v"(g[3]), "+v"(g[4]), "+v"(g[5]), "+v"(g[6]), "+v"(g[7]));
#pragma unroll 1
  for (unsigned spins = 0; spins < (1u << 15); ++spins) {
    unsigned tg = 0u;
#pragma unroll
    for (int i = 0; i < 8; ++i) tg |= (unsigned)(g[i] >> 32) ^ tag;
    if (__all(tg == 0u)) break;
    __builtin_amdgcn_s_sleep(2);
#pragma unroll
    for (int i = 0; i < 8; ++i) asm volatile("global_load_dwordx2 %0, %1, off sc1" : "+v"(g[i]) : "v"(gcs + i * 224 + u) : "memory");
    asm volatile("s_waitcnt vmcnt(0)" : "+v"(g[0]), "+v"(g[1]), "+v"(g[2]), "+v"(g[3]), "+v"(g[4]), "+v"(g[5]), "+v"(g[6]), "+v"(g[7]));
  }
  float s = 0.f;
#pragma unroll
  for (int i = 0; i < 8; ++i) s += __uint_as_float((unsigned)g[i]);
  cst[u] = s;
  wait_lgkm0();
  {
    const char* tp = (const char*)p.wd + ((size_t)blockIdx.x * 224 + (lw * 56 + (lane < 56 ? lane : 55))) * 128;
    asm volatile("global_load_dword %0, %1, off" : "+v"(touch) : "v"(tp) : "memory");
  }
  barrier();
}

__device__ __forceinline__ void glu_compute(const FArgs& p, unsigned lds0, unsigned w, unsigned xcd, unsigned idx) {
  constexpr int ID = 3, BM = 256, NT = 32, MB = 4, NB = 7;
  const unsigned ring = lds0 + F_RING;
  LAS f32x2* tab = (LAS f32x2*)(lds0 + F_TAB);
  LAS float* cst = (LAS float*)(lds0 + F_TAB + 2048);
  const unsigned tm = idx & 7, tn = xcd * 4 + (idx >> 3), cls = (tm + 2 * (idx >> 3)) & 7;
  const unsigned wm = w & 3, wn = w >> 2;
  const int lane = lane_id(), fr = lane & 15, fq = lane >> 4;
  const unsigned rot = wm == 3 ? 0u : wm;
  unsigned xa = ring + wm * 4096 + lane * 16, xb[3], xb1;
  {
    const int q = fr >> 2, pp = fr & 3, r = (fq >> 1) * 16 + (fq & 1) * 4 + q;
#pragma unroll
    for (int i = 0; i < 3; ++i) xb[i] = ring + F_A + r * 224 + wn * 112 + pp * 8 + ((i + rot) % 3) * 32;
    xb1 = ring + F_A + r * 224 + wn * 112 + 96 + (pp & 1) * 8 + (pp >> 1) * G3_UP;
  }
  float cs[3] = {0.f, 0.f, 0.f};
  bf16x8 fa[MB], fb[NB];
  f32x4 acc[MB][NB];
#pragma unroll
  for (int mb = 0; mb < MB; ++mb)
#pragma unroll
    for (int nb = 0; nb < NB; ++nb) { acc[mb][nb] = (f32x4){0.f, 0.f, 0.f, 0.f}; asm volatile("" : "+v"(acc[mb][nb])); }
  asm volatile("" : "+v"(xa), "+v"(xb[0]), "+v"(xb[1]), "+v"(xb[2]), "+v"(xb1));
  barrier();
  if (wn == 1) {
    const auto rst = OUT_RSRC(p.stats, 8 * S_ * 16);
    f32x2 sv[16];
#pragma unroll
    for (int i = 0; i < 16; ++i) sv[i] = __builtin_bit_cast(f32x2, __builtin_amdgcn_raw_buffer_load_b64(rst, (tm * BM + wm * 64 + lane) * 8, i * S_ * 8, 16));
    SB();
    barrier();
    SB();
    float m = 0.f;
#pragma unroll
    for (int i = 0; i < 16; ++i) m += sv[i][0];
    m *= (1.0f / 16.0f);
    float q = 0.f;
#pragma unroll
    for (int i = 0; i < 16; ++i) {
      const float d = sv[i][0] - m;
      q += sv[i][1] + 64.0f * d * d;
    }
    tab[wm * 64 + lane] = (f32x2){m, q * (1.0f / D_) + EPS};
  }
  wait_lgkm0();
  barrier();
  const unsigned csmask = (0x010101u << cls) | (1u << ((cls + 1) & 7));
  auto reads = [&]() {
    SB();
    fa[0] = lds_b128(xa);
#pragma unroll
    for (int j = 0; j < 7; ++j) {
      const unsigned o = j < 6 ? xb[j % 3] + (j / 3) * G3_UP : xb1;
      fb[j] = lds_tr(o, o + 1792);
    }
#pragma unroll
    for (int mb = 1; mb < MB; ++mb) fa[mb] = lds_b128(xa + mb * 1024);
    SB();
  };
  reads();
#pragma unroll 1
  for (unsigned t = 0; t < NT; ++t) {
    const unsigned delta = ((t & 3) == 3) ? (unsigned)(-3 * F_ST) : (unsigned)F_ST;
    xa += delta; xb[0] += delta; xb[1] += delta; xb[2] += delta; xb1 += delta;
    asm volatile("" : "+v"(xa), "+v"(xb[0]), "+v"(xb[1]), "+v"(xb[2]), "+v"(xb1));
    if (__builtin_expect((csmask >> t) & 1u, 0)) {
#pragma unroll
      for (int j = 0; j < 4; ++j)
#pragma unroll
        for (int i = 0; i < 3; ++i)
          asm volatile("v_dot2c_f32_bf16 %0, %1, %2" : "+v"(cs[i]) : "s"(0x3f803f80u), "v"(__builtin_bit_cast(u32x4, fb[3 * i])[j]));
      asm volatile("s_mov_b32 m0, %3\n\ts_nop 2\n\tds_write_addtid_b32 %0\n\tds_write_addtid_b32 %1 offset:256\n\tds_write_addtid_b32 %2 offset:512" ::"v"(cs[0]),
                   "v"(cs[1]), "v"(cs[2]), "s"(lds0 + w * 768)
                   : "memory");
    }
    wait_lgkm0();
    barrier();
    SB();
#pragma unroll
    for (int mb = 0; mb < MB; ++mb)
#pragma unroll
      for (int i = 0; i < NB; ++i) {
        const int nb = (mb & 1) ? NB - 1 - i : i;
        MFMA(fb[nb], fa[mb], acc[mb][nb]);
      }
    SB();
    barrier();
    reads();
  }
  if (wn == 0) barrier();
  mfma_drain();
  SB();
  const int lane2 = lane_id();
  const int gr = lane2 & 15, gq = lane2 >> 4;
  auto act2 = [](f32x2 a, f32x2 b, float sc, float k) {
    const f32x2 t = a * sc;
    const f32x2 e = {__builtin_amdgcn_exp2f(t.x), __builtin_amdgcn_exp2f(t.y)};
    const f32x2 d = e * k + k;
    const f32x2 r = {__builtin_amdgcn_rcpf(d.x), __builtin_amdgcn_rcpf(d.y)};
    const f32x2 o = (a * b) * r;
    return pk_bf16(o.x, o.y);
  };
  const auto ro = OUT_RSRC(p.gu, S_ * F_ * 2);
  float nm[MB], kk[MB], sc[MB];
#pragma unroll
  for (int mb = 0; mb < MB; ++mb) {
    const f32x2 st = tab[wm * 64 + mb * 16 + gr];
    nm[mb] = -st.x; kk[mb] = st.y; sc[mb] = -1.44269504089f * __builtin_amdgcn_rsqf(st.y);
  }
  const int gch0 = tn * 14 + wn * 7;
  const unsigned obase = (tm * (BM / 16) + wm * MB) * (F_ / 32) * 1024 + gr * 16;
#pragma unroll
  for (int i = 0; i < 3; ++i) {
    const unsigned ib = (i + rot) % 3;
    const f32x4 cg = *(const LAS f32x4*)(cst + wn * 56 + ib * 16 + gq * 4), cu = *(const LAS f32x4*)(cst + 112 + wn * 56 + ib * 16 + gq * 4);
    const unsigned oo = obase + (gq & 1) * (F_ / 32) * 1024 + (gch0 + 2 * ib + (gq >> 1)) * 256;
#pragma unroll
    for (int mp = 0; mp < MB / 2; ++mp) {
      u32x2 pkd[2];
#pragma unroll
      for (int h = 0; h < 2; ++h) {
        const int mb = 2 * mp + h;
        const f32x4 a = cg * nm[mb] + acc[mb][i], b = cu * nm[mb] + acc[mb][3 + i];
        pkd[h] = (u32x2){act2(a.xy, b.xy, sc[mb], kk[mb]), act2(a.zw, b.zw, sc[mb], kk[mb])};
      }
      auto s0 = __builtin_amdgcn_permlane16_swap(pkd[0].x, pkd[1].x, false, false);
      auto s1 = __builtin_amdgcn_permlane16_swap(pkd[0].y, pkd[1].y, false, false);
      __builtin_amdgcn_raw_buffer_store_b128((u32x4){s0[0], s1[0], s0[1], s1[1]}, ro, oo + mp * 2 * (F_ / 32) * 1024, 0, ST_AUX);
    }
  }
  {
    const f32x4 cm = *(const LAS f32x4*)(cst + (gq >> 1) * 112 + wn * 56 + 48 + (gq & 1) * 4);
    u32x2 pkd[MB / 2];
#pragma unroll
    for (int mp = 0; mp < MB / 2; ++mp) {
      const f32x4 v0 = cm * nm[2 * mp] + acc[2 * mp][6], v1 = cm * nm[2 * mp + 1] + acc[2 * mp + 1][6];
      f32x4 a, b;
#pragma unroll
      for (int j = 0; j < 4; ++j) {
        auto s = __builtin_amdgcn_permlane32_swap(__float_as_uint(v0[j]), __float_as_uint(v1[j]), false, false);
        a[j] = __uint_as_float(s[0]); b[j] = __uint_as_float(s[1]);
      }
      const float scs = (gq & 2) ? sc[2 * mp + 1] : sc[2 * mp], ks = (gq & 2) ? kk[2 * mp + 1] : kk[2 * mp];
      pkd[mp] = (u32x2){act2(a.xy, b.xy, scs, ks), act2(a.zw, b.zw, scs, ks)};
    }
    auto s0 = __builtin_amdgcn_permlane16_swap(pkd[0].x, pkd[1].x, false, false);
    auto s1 = __builtin_amdgcn_permlane16_swap(pkd[0].y, pkd[1].y, false, false);
    STORE16(ro, obase + ((gq & 1) * 2 + (gq >> 1)) * (F_ / 32) * 1024 + (gch0 + 6) * 256, ((u32x4){s0[0], s1[0], s0[1], s1[1]}));
  }
}

__global__ __launch_bounds__(768, 3) void k_block(FArgs p) {
  extern __shared__ __attribute__((aligned(16))) char shm[];
  const unsigned lds0 = (unsigned)(size_t)(LAS char*)shm;
  const unsigned w = rfl(threadIdx.x >> 6);
  const unsigned xcd = blockIdx.x & 7, idx = blockIdx.x >> 3;
  char* fl = (char*)p.flags;
  const unsigned tm = (xcd >> 1) * 4 + (idx & 3), kh = xcd & 1, lj = idx >> 2;
  if (w >= 8) {
    const unsigned lw = w - 8;
    Ld LA, LB;
    unsigned touch = 0;
    LA = ld_setup<1>(p, lw, xcd, idx);
    sq_loader<1>(p, LA, LB, lds0, lw, xcd, idx, touch);
    const unsigned tag = lds_tag(lds0);
    LA = ld_setup<3>(p, lw, xcd, idx);
    ld_pin(LA);
    barrier();
    if (lw == 3) poll_tags(fl + FL_P1 + (kh * 8 + lj) * 16 * 128, 16, tag);
    barrier();
    sq_loader<2>(p, LB, LA, lds0, lw, xcd, idx, touch);
    LB = ld_setup<4>(p, lw, xcd, idx);
    ld_pin(LB);
    barrier();
    if (lw == 3) poll_tags(fl + FL_P2 + (idx & 7) * 32 * 128, 32, tag);
    barrier();
    glu_loader(p, LA, LB, lds0, lw, xcd, idx, touch);
    barrier();
    if (lw == 3) poll_tags(fl + FL_P3 + ((tm >> 1) * 2 + kh) * 16 * 128, 16, tag);
    barrier();
    sq_loader<4>(p, LB, LA, lds0, lw, xcd, idx, touch);
    asm volatile("" ::"v"(touch));
    return;
  }
  unsigned csink = 0;
  if (w == 0 && idx < 3) {
    unsigned long long pc;
    asm volatile("s_getpc_b64 %0" : "=s"(pc));
    const unsigned ln = idx * 64 + lane_id();
    asm volatile("global_load_dword %0, %1, %2" : "+v"(csink) : "v"((ln < CODE_LINES ? ln : CODE_LINES - 1) * 128), "s"(pc) : "memory");
  }
  if (w == 1 && idx < 20) {
    const char* base = idx < 2 ? (const char*)p.cre : idx < 4 ? (const char*)p.cim : idx < 6 ? (const char*)p.wg : idx < 8 ? (const char*)p.wu : idx < 10 ? (const char*)p.wd : idx < 11 ? (const char*)p.out : (const char*)p.ab;
    const unsigned size = idx < 4 ? S_ * S_ * 2 : idx < 10 ? D_ * F_ * 2 : idx < 11 ? S_ * D_ * 2 : (unsigned)WS_FL;
    const unsigned off = ((idx < 10 ? (idx & 1) : (idx < 11 ? 0 : idx - 11)) * 64 + lane_id()) * 65536;
    asm volatile("global_load_dword %0, %1, %2 sc1" : "+v"(csink) : "v"(off < size ? off : 0u), "s"(base) : "memory");
  }
  if (w == 2) {
    const unsigned l = lane_id();
    const unsigned off = l < 5 ? (unsigned)WS_FL + FL_PAIR + (l * 256 + blockIdx.x) * 128 : (l < 19 ? (unsigned)WS_FL + FL_CS + blockIdx.x * 1792 + (l - 5) * 128 : (unsigned)WS_ST + blockIdx.x * 1024 + (l & 7) * 128);
    asm volatile("global_load_dword %0, %1, %2 sc1" : "+v"(csink) : "v"(off), "s"(p.ab) : "memory");
  }
  unsigned tag = 0;
  sq_compute<1>(p, lds0, w, xcd, idx, tag);
  asm volatile("" ::"v"(csink));
  wait_vm<0>();
  barrier();
  if (w == 0 && lane_id() == 0) put_tag(fl + FL_P1 + (tm * 16 + kh * 8 + lj) * 128, tag);
  sq_compute<2>(p, lds0, w, xcd, idx, tag);
  wait_vm<0>();
  barrier();
  if (w == 0 && lane_id() == 0) put_tag(fl + FL_P2 + (tm * 16 + kh * 8 + lj) * 128, tag);
  glu_compute(p, lds0, w, xcd, idx);
  wait_vm<0>();
  barrier();
  if (w == 0 && lane_id() == 0) put_tag(fl + FL_P3 + (((idx & 7) * 2 + (xcd >> 2)) * 16 + (xcd & 3) * 4 + (idx >> 3)) * 128, tag);
  sq_compute<4>(p, lds0, w, xcd, idx, tag);
  if (blockIdx.x == 0 && w == 0 && lane_id() == 0) put_tag(fl + FL_EPOCH, tag);
}
}

extern "C" void kernel_launch(void* const* d_in, const int* in_sizes, int n_in, void* d_out, int out_size, void* d_ws, size_t ws_size,
                              hipStream_t stream) {
  char* ws = (char*)d_ws;
  FArgs f = {d_in[3], d_in[4], d_in[1], d_in[2], d_in[0], d_in[6], d_in[7], d_in[8], ws + WS_AB, ws + WS_H, ws + WS_ST, ws + WS_GU, d_out, ws + WS_XC, ws + WS_FL};
  (void)hipFuncSetAttribute((const void*)k_block, hipFuncAttributeMaxDynamicSharedMemorySize, F_LDS);
  hipLaunchKernelGGL(k_block, dim3(256), dim3(768), F_LDS, stream, f);
}
```

```cpp
#include <hip/hip_runtime.h>
#include <hip/hip_bf16.h>

namespace {
#define LAS __attribute__((address_space(3)))
typedef unsigned short u16;
typedef short s16x4 __attribute__((ext_vector_type(4)));
typedef short s16x8 __attribute__((ext_vector_type(8)));
typedef __bf16 bf16x8 __attribute__((ext_vector_type(8)));
typedef __bf16 bf16x2 __attribute__((ext_vector_type(2)));
typedef float f32x4 __attribute__((ext_vector_type(4)));
typedef float f32x2 __attribute__((ext_vector_type(2)));
typedef unsigned u32x4 __attribute__((ext_vector_type(4)));
typedef unsigned u32x2 __attribute__((ext_vector_type(2)));

constexpr int S_ = 2048, D_ = 1024, F_ = 3584;
constexpr float EPS = 1e-5f;

constexpr size_t WS_AB = 0;
constexpr size_t WS_H = WS_AB + (size_t)D_ * 2 * S_ * 2;
constexpr size_t WS_GU = WS_H + (size_t)S_ * D_ * 2;
constexpr size_t WS_ST = WS_GU + (size_t)S_ * F_ * 2;
constexpr size_t WS_XC = WS_ST + (size_t)S_ * 16 * 8;
constexpr size_t WS_FL = WS_XC + (size_t)2 * 256 * 16384;
constexpr int FL_EPOCH = 0;
constexpr int FL_PAIR = 128;
constexpr int FL_P1 = FL_PAIR + 512 * 128;
constexpr int FL_P2 = FL_P1 + 256 * 128;
constexpr int FL_P3 = FL_P2 + 256 * 128;
constexpr int FL_LN = FL_P3 + 256 * 128;
constexpr int FL_CS = FL_LN + 4096 * 16;

__device__ __forceinline__ unsigned rfl(unsigned v) { return __builtin_amdgcn_readfirstlane(v); }
__device__ __forceinline__ unsigned pk_bf16(float lo, float hi) {
  bf16x2 c = {(__bf16)lo, (__bf16)hi};
  return __builtin_bit_cast(unsigned, c);
}
__device__ __forceinline__ float bf_lo(unsigned v) { return __uint_as_float(v << 16); }
__device__ __forceinline__ float bf_hi(unsigned v) { return __uint_as_float(v & 0xffff0000u); }

__device__ __forceinline__ u32x4 make_rsrc(const void* p, unsigned bytes) {
  unsigned long long a = (unsigned long long)p;
  u32x4 r;
  r.x = (unsigned)a;
  r.y = (unsigned)(a >> 32) & 0xffffu;
  r.z = bytes;
  r.w = 0x00020000u;
  return r;
}
template <int N>
__device__ __forceinline__ void wait_vm() {
  asm volatile("s_waitcnt vmcnt(%0)" ::"i"(N) : "memory");
}
__device__ __forceinline__ void wait_lgkm0() { __builtin_amdgcn_s_waitcnt(0xC07F); }
__device__ __forceinline__ void barrier() {
  asm volatile("" ::: "memory");
  __builtin_amdgcn_s_barrier();
  asm volatile("" ::: "memory");
}
__device__ __forceinline__ bf16x8 lds_b128(unsigned addr) { return *(const LAS bf16x8*)(addr); }
__device__ __forceinline__ bf16x8 lds_tr(unsigned a0, unsigned a1) {
  s16x4 lo = __builtin_amdgcn_ds_read_tr16_b64_v4i16((LAS s16x4*)(a0));
  s16x4 hi = __builtin_amdgcn_ds_read_tr16_b64_v4i16((LAS s16x4*)(a1));
  s16x8 f = __builtin_shufflevector(lo, hi, 0, 1, 2, 3, 4, 5, 6, 7);
  return __builtin_bit_cast(bf16x8, f);
}

#define ST_AUX 16
#define OUT_RSRC(ptr, bytes) __builtin_amdgcn_make_buffer_rsrc((void*)(ptr), 0, (bytes), 0x00020000)
#define STORE16(rs, byteoff, v) __builtin_amdgcn_raw_buffer_store_b128(v, rs, byteoff, 0, ST_AUX)

#define DMA4(M) asm volatile("s_mov_b32 m0, %0\n\ts_nop 4\n\tbuffer_load_dwordx4 %1, %5, %6 offen " M " lds\n\tbuffer_load_dwordx4 %2, %5, %6 offen offset:1024 " M " lds\n\t" \
                 "buffer_load_dwordx4 %3, %5, %6 offen offset:2048 " M " lds\n\tbuffer_load_dwordx4 %4, %5, %6 offen offset:3072 " M " lds" ::"s"(ldsaddr), \
                 "v"(v[0]), "v"(v[1]), "v"(v[2]), "v"(v[3]), "s"(rsrc), "s"(soff) : "memory")
#define DMA3(M) asm volatile("s_mov_b32 m0, %0\n\ts_nop 4\n\tbuffer_load_dwordx4 %1, %4, %5 offen " M " lds\n\tbuffer_load_dwordx4 %2, %4, %5 offen offset:1024 " M " lds\n\t" \
                 "buffer_load_dwordx4 %3, %4, %5 offen offset:2048 " M " lds" ::"s"(ldsaddr), "v"(v[0]), "v"(v[1]), "v"(v[2]), "s"(rsrc), "s"(soff) : "memory")
#define DMAB(M) asm volatile("s_mov_b32 m0, %0\n\ts_waitcnt vmcnt(8)\n\ts_barrier\n\tbuffer_load_dwordx4 %1, %5, %6 offen " M " lds\n\tbuffer_load_dwordx4 %2, %5, %6 offen offset:1024 " M " lds\n\t" \
                 "buffer_load_dwordx4 %3, %5, %6 offen offset:2048 " M " lds\n\tbuffer_load_dwordx4 %4, %5, %6 offen offset:3072 " M " lds" ::"s"(ldsaddr), \
                 "v"(v[0]), "v"(v[1]), "v"(v[2]), "v"(v[3]), "s"(rsrc), "s"(soff) : "memory")
template <int N, int MOD = 0>
__device__ __forceinline__ void dma_grp(unsigned ldsaddr, const unsigned (&v)[4], u32x4 rsrc, unsigned soff) {
  if constexpr (N == 4) { if constexpr (MOD == 1) DMA4("sc1"); else DMA4(""); }
  else if constexpr (N == 3) { if constexpr (MOD == 1) DMA3("sc1"); else DMA3(""); }
  else { if constexpr (MOD == 1) DMAB("sc1"); else DMAB(""); }
}
__device__ __forceinline__ float sum_rows(float x) {
  auto a = __builtin_amdgcn_permlane16_swap(__float_as_uint(x), __float_as_uint(x), false, false);
  x = __uint_as_float(a[0]) + __uint_as_float(a[1]);
  auto b = __builtin_amdgcn_permlane32_swap(__float_as_uint(x), __float_as_uint(x), false, false);
  return __uint_as_float(b[0]) + __uint_as_float(b[1]);
}
template <int CTRL> __device__ __forceinline__ float dpp_add(float x) { return x + __uint_as_float(__builtin_amdgcn_mov_dpp(__float_as_uint(x), CTRL, 0xF, 0xF, true)); }
__device__ __forceinline__ float wave_sum(float x) { return sum_rows(dpp_add<0x140>(dpp_add<0x141>(dpp_add<0x4E>(dpp_add<0xB1>(x))))); }
__device__ __forceinline__ int lane_id() {
  int l;
  asm volatile("v_mbcnt_lo_u32_b32 %0, -1, 0\n\tv_mbcnt_hi_u32_b32 %0, -1, %0" : "=v"(l));
  return l;
}
__device__ __forceinline__ void mfma_drain() { asm volatile("s_nop 15" ::: "memory"); }
template <int NC, int NT>
__device__ __forceinline__ unsigned stage_of(unsigned n, unsigned c) {
  const unsigned t = ((n & (NC - 1)) == c) ? n + NC : n;
  return rfl(t >= NT ? t - NT : t);
}
#define SB() __builtin_amdgcn_sched_barrier(0)
#define MFMA(b, a, c) asm volatile("v_mfma_f32_16x16x32_bf16 %0, %1, %2, %0" : "+v"(c) : "v"(b), "v"(a))

struct FArgs {
  const void *dre, *dim, *cre, *cim, *x, *wg, *wu, *wd;
  void *ab, *h, *stats, *gu, *out, *xch, *flags;
};
constexpr unsigned CODE_LINES = 156;
constexpr int F_RING = 6144, F_ST = 32768, F_TAB = F_RING + 4 * F_ST, F_TAG = F_TAB + 4096, F_SINK = F_TAB + 4160, F_LDS = F_SINK + 256, F_A = 16384, G3_UP = 7168 + 16;

__device__ __forceinline__ void poll_tags(const void* line, int n, unsigned tag) {
  const int l = lane_id();
  const unsigned* q = (const unsigned*)line + (l < n ? l : 0) * 32;
  unsigned spins = 0;
  while (!__all(__hip_atomic_load(q, __ATOMIC_RELAXED, __HIP_MEMORY_SCOPE_AGENT) == tag) && ++spins < (1u << 17)) __builtin_amdgcn_s_sleep(1);
}
__device__ __forceinline__ void put_tag(void* word, unsigned tag) { __hip_atomic_store((unsigned*)word, tag, __ATOMIC_RELAXED, __HIP_MEMORY_SCOPE_AGENT); }
__device__ __forceinline__ unsigned lds_tag(unsigned lds0) { return rfl(*(volatile LAS unsigned*)(lds0 + F_TAG)); }
__device__ __forceinline__ void touch(unsigned lds0, const void* ptr) {
  asm volatile("s_mov_b32 m0, %1\n\ts_nop 0\n\tglobal_load_lds_dword %0, off sc1" ::"v"(ptr), "s"(lds0 + F_SINK) : "memory");
}
template <int ID> constexpr int nt_of() { return ID == 1 ? 16 : (ID == 4 ? 28 : 32); }
template <int ID> constexpr int nc_of() { return (ID == 1 || ID == 4) ? 4 : 8; }
template <int ID> constexpr int mod_a() { return ID > 2; }
template <int ID> constexpr int mod_b() { return ID == 2; }
struct Ld {
  u32x4 qa, qb;
  unsigned va[4], vb[4], cls;
};
template <int ID>
__device__ __forceinline__ Ld ld_setup(const FArgs& p, unsigned lw, unsigned xcd, unsigned idx) {
  const int lane = lane_id();
  Ld L;
  if constexpr (ID == 3) {
    const unsigned tm = idx & 7, tn = xcd * 4 + (idx >> 3);
    L.qa = make_rsrc((const u16*)p.h + (size_t)tm * 256 * D_, 256 * D_ * 2);
    L.qb = make_rsrc(lw < 2 ? p.wg : p.wu, (unsigned)D_ * F_ * 2);
#pragma unroll
    for (int i = 0; i < 4; ++i) {
      L.va[i] = (lw * 4 + i) * (D_ / 32) * 1024 + lane * 16 - i * 1024;
      const int c = ((lw & 1) * 4 + i) * 64 + lane, r = c / 14, pos = c % 14;
      const int k = (r & 0x13) | ((r & 4) << 1) | ((r & 8) >> 1);
      L.vb[i] = k * F_ * 2 + tn * 224 + pos * 16 - i * 1024;
    }
    L.cls = (tm + 2 * (idx >> 3)) & 7;
  } else {
    const unsigned li = idx & 3, lj = idx >> 2, kh = xcd & 1, tm = (xcd >> 1) * 4 + li, tn = lj;
    if constexpr (ID == 1) {
      L.qa = make_rsrc((const u16*)(tm < 8 ? p.dre : p.dim) + (size_t)(tm & 7) * 128 * D_, 128 * D_ * 2);
      L.qb = make_rsrc((const u16*)p.x + (size_t)(kh * 8 + lj) * 128 * D_, 128 * D_ * 2);
    } else if constexpr (ID == 2) {
      L.qa = make_rsrc((const u16*)(kh ? p.cim : p.cre) + (size_t)tm * 128 * S_, 128 * S_ * 2);
      L.qb = make_rsrc((const u16*)p.ab + (size_t)tn * 128 * 4096 + kh * 64 * 512, 128 * 4096 * 2 - kh * 64 * 1024);
    } else {
      L.qa = make_rsrc((const u16*)p.gu + (size_t)tm * 128 * F_ + kh * 56 * 512, 128 * F_ * 2 - kh * 56 * 1024);
      L.qb = make_rsrc((const u16*)p.wd + (size_t)kh * 1792 * D_, 1792 * D_ * 2);
    }
#pragma unroll
    for (int i = 0; i < 4; ++i) {
      const int pc = lw * 4 + i;
      if constexpr (ID == 1) {
        const int row = pc * 8 + (lane >> 3);
        L.va[i] = L.vb[i] = row * D_ * 2 + (((lane & 7) ^ ((row >> 1) & 7)) << 4) - i * 1024;
      } else if constexpr (ID == 2) {
        const int row = pc * 8 + (lane >> 3);
        L.va[i] = row * S_ * 2 + (((lane & 7) ^ ((row >> 1) & 7)) << 4) - i * 1024;
        L.vb[i] = ((pc >> 1) * 128 + (pc & 1)) * 1024 + lane * 16 - i * 1024;
      } else {
        const int k = pc * 4 + (lane >> 4), f = (k & 3) | (((k >> 3) & 1) << 2);
        L.va[i] = ((pc >> 1) * (F_ / 32) + (pc & 1)) * 1024 + lane * 16 - i * 1024;
        L.vb[i] = k * D_ * 2 + tn * 256 + (((lane & 15) ^ (f << 1)) << 4) - i * 1024;
      }
    }
    L.cls = (nc_of<ID>() == 8) ? ((lj + 2 * li) & 7) : ((lj + li) & 3);
  }
  return L;
}
__device__ __forceinline__ void ld_pin(Ld& L) {
  asm volatile("" : "+v"(L.va[0]), "+v"(L.va[1]), "+v"(L.va[2]), "+v"(L.va[3]), "+v"(L.vb[0]), "+v"(L.vb[1]), "+v"(L.vb[2]), "+v"(L.vb[3]));
}
template <int ID>
__device__ __forceinline__ void ld_a(const Ld& L, unsigned ring, unsigned lw, unsigned n) {
  const unsigned st = stage_of<nc_of<ID>(), nt_of<ID>()>(n, L.cls);
  dma_grp<4, mod_a<ID>()>(ring + (n & 3) * F_ST + lw * 4096, L.va, L.qa, st * (ID <= 2 ? 128 : (ID == 3 ? 1024 : 2048)));
}
template <int ID>
__device__ __forceinline__ void ld_b(const Ld& L, unsigned ring, unsigned lw, unsigned n) {
  const unsigned st = stage_of<nc_of<ID>(), nt_of<ID>()>(n, L.cls);
  const unsigned dst = ring + (n & 3) * F_ST + F_A;
  if constexpr (ID == 3) {
    const unsigned d = dst + (lw >> 1) * G3_UP + (lw & 1) * 4096;
    if (lw & 1) dma_grp<3, mod_b<ID>()>(d, L.vb, L.qb, st * (32 * F_ * 2));
    else dma_grp<4, mod_b<ID>()>(d, L.vb, L.qb, st * (32 * F_ * 2));
  } else {
    dma_grp<4, mod_b<ID>()>(dst + lw * 4096, L.vb, L.qb, st * (ID == 1 ? 128 : (ID == 2 ? 2048 : 64 * D_ * 2)));
  }
}

template <int ID> __device__ __forceinline__ void ld_const(const Ld& L, unsigned ring, unsigned lw, unsigned n) { if constexpr (ID == 2) ld_a<ID>(L, ring, lw, n); else ld_b<ID>(L, ring, lw, n); }
template <int ID> __device__ __forceinline__ void ld_dep(const Ld& L, unsigned ring, unsigned lw, unsigned n) { if constexpr (ID == 2) ld_b<ID>(L, ring, lw, n); else ld_a<ID>(L, ring, lw, n); }

template <int ID>
__device__ __forceinline__ void sq_loader(const FArgs& p, const Ld& L, Ld& N, unsigned lds0, unsigned lw, unsigned xcd, unsigned idx) {
  constexpr int NT = nt_of<ID>(), NEXT = ID == 4 ? 0 : ID + 1;
  const unsigned ring = lds0 + F_RING;
  if constexpr (ID == 1) {
    ld_a<ID>(L, ring, lw, 0); ld_b<ID>(L, ring, lw, 0);
    N = ld_setup<NEXT>(p, lw, xcd, idx); ld_pin(N);
    ld_a<ID>(L, ring, lw, 1); ld_b<ID>(L, ring, lw, 1);
  } else {
    ld_dep<ID>(L, ring, lw, 0); ld_dep<ID>(L, ring, lw, 1);
  }
  constexpr unsigned KA = ID <= 2 ? 128 : 2048, KB = ID == 1 ? 128 : (ID == 2 ? 2048 : 64 * D_ * 2);
  unsigned sa, sb, ma;
  auto prep = [&](unsigned n) {
    const unsigned st = stage_of<nc_of<ID>(), NT>(n, L.cls);
    sa = st * KA; sb = st * KB; ma = ring + (n & 3) * F_ST + lw * 4096;
    asm volatile("" : "+s"(sa), "+s"(sb), "+s"(ma));
  };
  auto bar_a = [&]() {
    dma_grp<5, mod_a<ID>()>(ma, L.va, L.qa, sa);
  };
  prep(2);
  wait_vm<(ID == 1 ? 8 : 4)>();
  bar_a();
  unsigned mb_ = ma + F_A, sb_ = sb;
  prep(3);
#pragma unroll 1
  for (unsigned n = 0; n + 3 < NT; ++n) {
    dma_grp<4, mod_b<ID>()>(mb_, L.vb, L.qb, sb_);
    bar_a();
    mb_ = ma + F_A; sb_ = sb;
    asm volatile("" : "+s"(mb_), "+s"(sb_));
    prep(n + 4);
  }
  dma_grp<4, mod_b<ID>()>(mb_, L.vb, L.qb, sb_);
  wait_vm<8>();
  barrier();
  const int lane = lane_id();
  u32x2 l0, l1;
  const char* lp = (const char*)p.flags + FL_LN + (lw < 2 ? (((xcd & 1) * 8 + (idx >> 2)) * 128 + lw * 64 + lane) : (2048 + ((xcd >> 1) * 4 + (idx & 3)) * 128 + (lw - 2) * 64 + lane)) * 16;
  if constexpr (ID == 1) asm volatile("global_load_dwordx2 %0, %2, off sc1\n\tglobal_load_dwordx2 %1, %2, off offset:8 sc1" : "=&v"(l0), "=&v"(l1) : "v"(lp) : "memory");
  if constexpr (NEXT != 0) ld_const<NEXT>(N, ring, lw, 0);
  if constexpr (ID == 1) wait_vm<6>();
  else if constexpr (ID == 2) { if (lw & 1) wait_vm<3>(); else wait_vm<4>(); }
  else wait_vm<0>();
  barrier();
  if constexpr (NEXT != 0) ld_const<NEXT>(N, ring, lw, 1);
  if constexpr (ID == 1) {
    if (lw < 2) {
      const char* tp = (const char*)((xcd & 1) ? p.cim : p.cre) + (size_t)((xcd >> 1) * 512 + idx * 16 + lw * 8 + (lane >> 3)) * (S_ * 2) + (lane & 7) * 128;
      touch(lds0, tp);
    }
  }
  if constexpr (ID == 2) {
    if (lw < 2) {
      const char* tp = (const char*)((lw & 1) ? p.wu : p.wg) + (size_t)(idx * 8 + (lane >> 3)) * (F_ * 2) + xcd * 896 + ((lane & 7) < 7 ? (lane & 7) : 6) * 128;
      touch(lds0, tp);
    }
  }
  barrier();
  const unsigned tag = lds_tag(lds0);
  if constexpr (ID == 1) {
    if (lw < 2) wait_vm<9>(); else wait_vm<8>();
    asm volatile("" : "+v"(l0), "+v"(l1));
#pragma unroll 1
    for (unsigned spins = 0; spins < (1u << 15) && !__all(l0.y == tag && l1.y == tag); ++spins) {
      __builtin_amdgcn_s_sleep(2);
      asm volatile("global_load_dwordx2 %0, %2, off sc1\n\tglobal_load_dwordx2 %1, %2, off offset:8 sc1\n\ts_waitcnt vmcnt(0)" : "=&v"(l0), "=&v"(l1) : "v"(lp) : "memory");
    }
    LAS unsigned* tb = (LAS unsigned*)(lds0 + F_TAB);
    tb[lw * 64 + lane + (lw < 2 ? 0 : 128)] = l0.x;
    if (lw < 2) tb[128 + lw * 64 + lane] = l1.x;
    wait_lgkm0();
  }
  barrier();
  barrier();
  if constexpr (ID != 1) {
    barrier();
    if (lw == 3) poll_tags((char*)p.flags + FL_PAIR + ((ID == 2 ? 0 : 256) + (blockIdx.x ^ 1)) * 128, 1, tag);
    barrier();
  }
}

template <int ID>
__device__ __forceinline__ void sq_compute(const FArgs& p, unsigned lds0, unsigned w, unsigned xcd, unsigned idx, unsigned& tag) {
  constexpr int NT = nt_of<ID>();
  u32x4 xv[2], wv[2];
  unsigned tv = 0;
  const unsigned r0 = blockIdx.x * 8 + w;
  char* fl = (char*)p.flags;
  if constexpr (ID == 1) {
    const int lane = lane_id();
    const u16* xp = (const u16*)p.x + (size_t)r0 * D_ + lane * 8;
    const u16* wp = (const u16*)(r0 < 1024 ? p.dre : p.dim) + (size_t)(r0 & 1023) * D_ + lane * 8;
#pragma unroll
    for (int i = 0; i < 2; ++i) xv[i] = *(const u32x4*)(xp + i * 512);
#pragma unroll
    for (int i = 0; i < 2; ++i) wv[i] = *(const u32x4*)(wp + i * 512);
    tv = __hip_atomic_load((const unsigned*)(fl + FL_EPOCH), __ATOMIC_RELAXED, __HIP_MEMORY_SCOPE_AGENT);
    SB();
  }
  const unsigned ring = lds0 + F_RING;
  const unsigned g = w >> 2, wm = (w >> 1) & 1, wx = w & 1;
  const unsigned kh = xcd & 1, tm = (xcd >> 1) * 4 + (idx & 3), tn = idx >> 2;
  u32x4 res[2];
  unsigned rdA[8], rdB[4], hoff = 0;
  const unsigned rotA = (ID == 1) ? 2 * g : 2 * g + wx;
  {
    const int lane = lane_id(), fr = lane & 15, fq = lane >> 4, chunk = ((fq & 1) << 1) | (fq >> 1);
    const int row = tm * 128 + wm * 64 + rotA * 16 + fr, col = tn * 128 + kh * 64 + chunk * 8;
    if constexpr (ID == 2) {
#pragma unroll
      for (int c = 0; c < 2; ++c) res[c] = *(const u32x4*)((const u16*)p.x + (size_t)row * D_ + col + (c ^ wx) * 32);
    } else if constexpr (ID == 4) {
      hoff = ((row >> 4) * (D_ / 32) + (col >> 5)) * 1024 + (chunk * 16 + fr) * 16;
    }
    const unsigned rotB = (ID == 1) ? 0u : 2 * kh, base = ring + g * F_ST;
#pragma unroll
    for (int i = 0; i < 4; ++i) {
      const unsigned kk = i >> 1, hf = (i & 1) * 2;
      if constexpr (ID == 1) {
        rdA[i] = base + (wm * 64 + (hf ^ rotA) * 16 + fr) * 128 + (((kk * 4 + fq) ^ (fr >> 1)) << 4);
        rdB[i] = base + F_A + (wx * 64 + fr) * 128 + ((((i & 1) * 4 + fq) ^ (fr >> 1)) << 4);
      } else if constexpr (ID == 2) {
        rdA[i] = base + (wm * 64 + (i ^ rotA) * 16 + fr) * 128 + ((fq ^ (fr >> 1)) << 4);
        rdA[4 + i] = base + (wm * 64 + (i ^ rotA) * 16 + fr) * 128 + (((4 + fq) ^ (fr >> 1)) << 4);
        rdB[i] = base + F_A + ((hf ^ rotB) * 2 + wx * 2) * 2048 + lane * 16;
      } else {
        const int q = fr >> 2, pp = fr & 3, f = q | ((fq & 1) << 2);
        rdA[i] = base + wm * 8192 + (i ^ rotA) * 2048 + lane * 16;
        rdB[i] = base + F_A + (8 * fq + q) * 256 + ((((((i ^ rotB) & 2) + wx) * 2 + (i & 1)) ^ f) << 5) + pp * 8;
      }
    }
  }
  constexpr int NVA = (ID == 2) ? 8 : 4, NVB = (ID <= 2) ? 2 : 4;
  bf16x8 fa[2][4], fb[2][4];
  f32x4 acc[4][4];
#pragma unroll
  for (int i = 0; i < 16; ++i) { acc[i >> 2][i & 3] = (f32x4){0.f, 0.f, 0.f, 0.f}; asm volatile("" : "+v"(acc[i >> 2][i & 3])); }
  auto reads = [&]() {
    SB();
#pragma unroll
    for (int s = 0; s < 2; ++s)
#pragma unroll
      for (int i = 0; i < 4; ++i) {
        if constexpr (ID == 1) {
          fb[s][i] = lds_b128(rdB[s] + i * 2048);
          fa[s][i] = lds_b128(rdA[s * 2 + (i >> 1)] + (i & 1) * 2048);
        } else if constexpr (ID == 2) {
          fb[s][i] = lds_b128(rdB[i >> 1] + (i & 1) * 2048 + s * 1024);
          fa[s][i] = lds_b128(rdA[s * 4 + i]);
        } else {
          fb[s][i] = lds_tr(rdB[i] + s * 8192, rdB[i] + s * 8192 + 1024);
          fa[s][i] = lds_b128(rdA[i] + s * 1024);
        }
      }
    SB();
  };
#pragma unroll
  for (int j = 0; j < NVA; ++j) asm volatile("" : "+v"(rdA[j]));
#pragma unroll
  for (int j = 0; j < NVB; ++j) asm volatile("" : "+v"(rdB[j]));
  if constexpr (ID == 1) {
    SB();
    asm volatile("" : "+v"(tv));
    tv = tv * 0x9E3779B1u + 0x7F4A7C15u;
    const int lane = lane_id();
    float s = 0.f, t = 0.f;
#pragma unroll
    for (int j = 0; j < 8; ++j) s += bf_lo(xv[j >> 2][j & 3]) + bf_hi(xv[j >> 2][j & 3]);
    const float m = wave_sum(s) * (1.0f / D_);
    float q = 0.f;
#pragma unroll
    for (int j = 0; j < 8; ++j) {
      const float a = bf_lo(xv[j >> 2][j & 3]) - m, b = bf_hi(xv[j >> 2][j & 3]) - m;
      q += a * a + b * b;
      t += bf_lo(wv[j >> 2][j & 3]) + bf_hi(wv[j >> 2][j & 3]);
    }
    const float rs = __builtin_amdgcn_rsqf(wave_sum(q) * (1.0f / D_) + EPS);
    t = wave_sum(t);
    const float val = (lane & 2) ? t : ((lane & 1) ? rs : m);
    if (lane < 4)
      __hip_atomic_store((unsigned long long*)(fl + FL_LN + ((lane >> 1) * 2048 + r0) * 16 + (lane & 1) * 8), ((unsigned long long)tv << 32) | __float_as_uint(val),
                         __ATOMIC_RELAXED, __HIP_MEMORY_SCOPE_AGENT);
    tag = rfl(tv);
    if (w == 0) { *(volatile LAS unsigned*)(lds0 + F_TAG) = tag; wait_lgkm0(); }
  }
  if constexpr (ID != 1) {
    barrier();
  }
  if constexpr (ID == 4) {
    const auto rh = OUT_RSRC(p.h, S_ * D_ * 2);
#pragma unroll
    for (int c = 0; c < 2; ++c) res[c] = __builtin_amdgcn_raw_buffer_load_b128(rh, hoff, (c ^ wx) * 1024, 16);
  }
  barrier();
  if (g == 1) barrier();
  reads();
#pragma unroll 1
  for (unsigned i = 0; i < NT / 2; ++i) {
    const unsigned delta = (i & 1) ? (unsigned)(-2 * F_ST) : (unsigned)(2 * F_ST);
#pragma unroll
    for (int j = 0; j < NVA; ++j) { rdA[j] += delta; asm volatile("" : "+v"(rdA[j])); }
#pragma unroll
    for (int j = 0; j < NVB; ++j) { rdB[j] += delta; asm volatile("" : "+v"(rdB[j])); }
    wait_lgkm0();
    barrier();
    SB();
#pragma unroll
    for (int m = 0; m < 32; ++m) {
      const int n = (m & 4) ? 3 - (m & 3) : (m & 3);
      MFMA(fb[m >> 4][n], fa[m >> 4][(m >> 2) & 3], acc[(m >> 2) & 3][n]);
    }
    SB();
    barrier();
    if (i + 1 < NT / 2) reads();
  }
  mfma_drain();
  SB();
  const int lane = lane_id(), fr = lane & 15, fq = lane >> 4, chunk = ((fq & 1) << 1) | (fq >> 1);
  if constexpr (ID == 1) {
    const unsigned wb = ring + 2 * F_ST + (w ^ 4) * 8192 + lane * 16, rb = ring + 2 * F_ST + w * 8192 + lane * 16;
#pragma unroll
    for (int i = 0; i < 8; ++i) *(LAS f32x4*)(wb + i * 1024) = acc[2 + (i >> 2)][i & 3];
    if (g == 0) barrier();
    wait_lgkm0();
    barrier();
#pragma unroll
    for (int i = 0; i < 8; ++i) acc[i >> 2][i & 3] += *(const LAS f32x4*)(rb + i * 1024);
  }
  if constexpr (ID == 1) {
    const LAS float* tb = (const LAS float*)(lds0 + F_TAB);
    const float sg = tm < 8 ? 1.f : -1.f;
    const auto ro = OUT_RSRC(p.ab, D_ * 4096 * 2);
    f32x4 mean[4], rstd[4];
#pragma unroll
    for (int j = 0; j < 4; ++j) {
      mean[j] = *(const LAS f32x4*)(tb + wx * 64 + j * 16 + fq * 4);
      rstd[j] = *(const LAS f32x4*)(tb + 128 + wx * 64 + j * 16 + fq * 4) * sg;
    }
#pragma unroll
    for (int mb = 0; mb < 2; ++mb) {
      const float wsm = tb[256 + wm * 64 + g * 32 + mb * 16 + fr];
#pragma unroll
      for (int np = 0; np < 2; ++np) {
        const f32x4 x0 = (acc[mb][2 * np] - mean[2 * np] * wsm) * rstd[2 * np], x1 = (acc[mb][2 * np + 1] - mean[2 * np + 1] * wsm) * rstd[2 * np + 1];
        auto s0 = __builtin_amdgcn_permlane16_swap(pk_bf16(x0[0], x0[1]), pk_bf16(x1[0], x1[1]), false, false);
        auto s1 = __builtin_amdgcn_permlane16_swap(pk_bf16(x0[2], x0[3]), pk_bf16(x1[2], x1[3]), false, false);
        const int rblk = (tm & 7) * 8 + wm * 4 + g * 2 + mb;
        const int kblk = (tm >> 3) * 64 + (kh * 8 + tn) * 4 + wx * 2 + np;
        STORE16(ro, (rblk * 128 + kblk) * 1024 + (chunk * 16 + fr) * 16, ((u32x4){s0[0], s1[0], s0[1], s1[1]}));
      }
    }
    return;
  }
  const auto rs = OUT_RSRC((char*)p.xch + (ID == 2 ? 0 : 256 * 16384), 256 * 16384);
  const unsigned sreg = ring + ((w >> 1) & 1) * F_ST + (w & 1) * 8192 + lane * 16, kreg = ring + 2 * F_ST + lane * 16;
  auto put_keep = [&]() {
#pragma unroll
    for (int j = 1; j < 4; ++j) {
      const unsigned d = kreg + ((wm * 4 + (j ^ rotA)) * 3 + (j - 1)) * 2048;
      *(LAS f32x4*)(d) = acc[j][0];
      *(LAS f32x4*)(d + 1024) = acc[j][1];
    }
  };
  if (g == 0) {
#pragma unroll
    for (int i = 0; i < 8; ++i) *(LAS f32x4*)(sreg + i * 1024) = acc[i >> 1][2 + (i & 1)];
    put_keep();
    wait_lgkm0();
    barrier();
  } else {
#pragma unroll
    for (int j = 0; j < 4; ++j) {
      const f32x4 x0 = acc[j][2] + *(const LAS f32x4*)(sreg + (j ^ 2) * 2048), x1 = acc[j][3] + *(const LAS f32x4*)(sreg + (j ^ 2) * 2048 + 1024);
      auto s0 = __builtin_amdgcn_permlane16_swap(pk_bf16(x0[0], x0[1]), pk_bf16(x1[0], x1[1]), false, false);
      auto s1 = __builtin_amdgcn_permlane16_swap(pk_bf16(x0[2], x0[3]), pk_bf16(x1[2], x1[3]), false, false);
      __builtin_amdgcn_raw_buffer_store_b128((u32x4){s0[0], s1[0], s0[1], s1[1]}, rs, lane * 16 + blockIdx.x * 16384 + ((wm * 4 + (j ^ rotA)) * 2 + wx) * 1024, 0, 16);
    }
    SB();
    put_keep();
    wait_lgkm0();
  }
  barrier();
  f32x4 y[2][2];
  {
    const unsigned kb = kreg + (wm * 4 + rotA) * 3 * 2048;
    f32x4 t[2][2];
#pragma unroll
    for (int i = 0; i < 2; ++i) {
      t[0][i] = acc[0][i] + *(const LAS f32x4*)(kb + 2048 + i * 1024);
      t[1][i] = *(const LAS f32x4*)(kb + i * 1024) + *(const LAS f32x4*)(kb + 4096 + i * 1024);
    }
#pragma unroll
    for (int c = 0; c < 2; ++c)
#pragma unroll
      for (int j = 0; j < 4; ++j) {
        auto s = __builtin_amdgcn_permlane16_swap(__float_as_uint(t[c][0][j]), __float_as_uint(t[c][1][j]), false, false);
        y[c][0][j] = __uint_as_float(s[0]) + ((j & 1) ? bf_hi(res[c][j >> 1]) : bf_lo(res[c][j >> 1]));
        y[c][1][j] = __uint_as_float(s[1]) + ((j & 1) ? bf_hi(res[c][2 + (j >> 1)]) : bf_lo(res[c][2 + (j >> 1)]));
      }
  }
  const int row0 = tm * 128 + wm * 64 + rotA * 16 + fr, col = tn * 128 + kh * 64 + chunk * 8;
  unsigned optr = (ID == 2) ? ((row0 >> 4) * (D_ / 32) + (col >> 5)) * 1024 + (chunk * 16 + fr) * 16 : ((row0 & ~8) * D_ + col + ((fr >> 3) ^ wx) * 32) * 2;
  const auto ro = OUT_RSRC(ID == 2 ? p.h : p.out, S_ * D_ * 2);
  asm volatile("" : "+v"(y[0][0]), "+v"(y[0][1]), "+v"(y[1][0]), "+v"(y[1][1]), "+v"(optr));
  wait_vm<0>();
  barrier();
  if (w == 0 && lane == 0) put_tag((char*)p.flags + FL_PAIR + ((ID == 2 ? 0 : 256) + blockIdx.x) * 128, tag);
  barrier();
  u32x4 in[2], v[2];
#pragma unroll
  for (int c = 0; c < 2; ++c) in[c] = __builtin_amdgcn_raw_buffer_load_b128(rs, lane * 16, (blockIdx.x ^ 1) * 16384 + ((wm * 4 + rotA) * 2 + (c ^ wx)) * 1024, 16);
#pragma unroll
  for (int c = 0; c < 2; ++c) {
    const f32x4 a = y[c][0] + (f32x4){bf_lo(in[c][0]), bf_hi(in[c][0]), bf_lo(in[c][1]), bf_hi(in[c][1])};
    const f32x4 b = y[c][1] + (f32x4){bf_lo(in[c][2]), bf_hi(in[c][2]), bf_lo(in[c][3]), bf_hi(in[c][3])};
    v[c] = (u32x4){pk_bf16(a[0], a[1]), pk_bf16(a[2], a[3]), pk_bf16(b[0], b[1]), pk_bf16(b[2], b[3])};
    if constexpr (ID == 2) STORE16(ro, optr + (c ^ wx) * 1024, v[c]);
  }
  if constexpr (ID == 4) {
    u32x4 sa, sb;
#pragma unroll
    for (int j = 0; j < 4; ++j) {
      sa[j] = __builtin_amdgcn_update_dpp(v[0][j], v[1][j], 0x128, 0xF, 0xC, false);
      sb[j] = __builtin_amdgcn_update_dpp(v[1][j], v[0][j], 0x128, 0xF, 0x3, false);
    }
    STORE16(ro, optr, sa);
    STORE16(ro, optr + 8 * D_ * 2, sb);
  }
  SB();
  if constexpr (ID == 2) {
    float s = 0.f;
#pragma unroll
    for (int j = 0; j < 8; ++j) s += bf_lo(v[j >> 2][j & 3]) + bf_hi(v[j >> 2][j & 3]);
    const float m = sum_rows(s) * (1.0f / 64.0f);
    float q = 0.f;
#pragma unroll
    for (int j = 0; j < 8; ++j) {
      const float a = bf_lo(v[j >> 2][j & 3]) - m, b = bf_hi(v[j >> 2][j & 3]) - m;
      q += a * a + b * b;
    }
    const f32x2 st = {m, sum_rows(q)};
    if (fq == 0)
      __hip_atomic_store((unsigned long long*)p.stats + (size_t)(tn * 2 + kh) * S_ + row0, __builtin_bit_cast(unsigned long long, st), __ATOMIC_RELAXED, __HIP_MEMORY_SCOPE_AGENT);
  }
  SB();
}

__device__ __forceinline__ void glu_loader(const FArgs& p, const Ld& L, Ld& N, unsigned lds0, unsigned lw, unsigned xcd, unsigned idx) {
  constexpr int NT = 32, NEXT = 4;
  const unsigned tag = lds_tag(lds0);
  const unsigned ring = lds0 + F_RING;
  LAS float* cst = (LAS float*)(lds0 + F_TAB + 2048);
  unsigned long long* gcs = (unsigned long long*)((char*)p.flags + FL_CS) + (xcd * 4 + (idx >> 3)) * (8 * 224);
  ld_a<3>(L, ring, lw, 0);
  ld_a<3>(L, ring, lw, 1);
  const int lane = lane_id();
  const unsigned u = lw * 64 + lane < 224 ? lw * 64 + lane : 223u;
  wait_vm<4>();
  barrier();
  ld_a<3>(L, ring, lw, 2);
  ld_b<3>(L, ring, lw, 2);
#pragma unroll 1
  for (unsigned t = 0; t + 3 < NT; ++t) {
    if (t == 24) {
      const unsigned hf = u >= 112, c = u - hf * 112, wn_ = c >= 56, cc = c - wn_ * 56, blk = cc >> 4;
      const unsigned src = lds0 + (wn_ * 4 + blk) * 768 + (blk == 3 ? 512 + (hf * 8 + (cc & 7)) * 4 : hf * 256 + (cc & 15) * 4);
      const float v = (*(const LAS float*)(src) + *(const LAS float*)(src + 64)) + (*(const LAS float*)(src + 128) + *(const LAS float*)(src + 192));
      asm volatile("global_store_dwordx2 %0, %1, off sc1" ::"v"(gcs + L.cls * 224 + u), "v"(((unsigned long long)tag << 32) | __float_as_uint(v)) : "memory");
    }
    ld_a<3>(L, ring, lw, t + 3);
    barrier();
    ld_b<3>(L, ring, lw, t + 3);
    wait_vm<14>();
    barrier();
  }
  unsigned long long g[8];
#pragma unroll
  for (int i = 0; i < 8; ++i) asm volatile("global_load_dwordx2 %0, %1, off sc1" : "=v"(g[i]) : "v"(gcs + i * 224 + u) : "memory");
  barrier();
  ld_b<NEXT>(N, ring, lw, 0);
  wait_vm<19>();
  barrier();
  barrier();
  ld_b<NEXT>(N, ring, lw, 1);
  wait_vm<16>();
  barrier();
  barrier();
  barrier();
  asm volatile("s_waitcnt vmcnt(8)" : "+v"(g[0]), "+v"(g[1]), "+v"(g[2]), "+v"(g[3]), "+v"(g[4]), "+v"(g[5]), "+v"(g[6]), "+v"(g[7]));
#pragma unroll 1
  for (unsigned spins = 0; spins < (1u << 15); ++spins) {
    unsigned tg = 0u;
#pragma unroll
    for (int i = 0; i < 8; ++i) tg |= (unsigned)(g[i] >> 32) ^ tag;
    if (__all(tg == 0u)) break;
    __builtin_amdgcn_s_sleep(2);
#pragma unroll
    for (int i = 0; i < 8; ++i) asm volatile("global_load_dwordx2 %0, %1, off sc1" : "+v"(g[i]) : "v"(gcs + i * 224 + u) : "memory");
    asm volatile("s_waitcnt vmcnt(0)" : "+v"(g[0]), "+v"(g[1]), "+v"(g[2]), "+v"(g[3]), "+v"(g[4]), "+v"(g[5]), "+v"(g[6]), "+v"(g[7]));
  }
  float s = 0.f;
#pragma unroll
  for (int i = 0; i < 8; ++i) s += __uint_as_float((unsigned)g[i]);
  cst[u] = s;
  wait_lgkm0();
  {
    const char* tp = (const char*)p.wd + ((size_t)blockIdx.x * 224 + (lw * 56 + (lane < 56 ? lane : 55))) * 128;
    touch(lds0, tp);
  }
  barrier();
}

__device__ __forceinline__ void glu_compute(const FArgs& p, unsigned lds0, unsigned w, unsigned xcd, unsigned idx) {
  constexpr int ID = 3, BM = 256, NT = 32, MB = 4, NB = 7;
  const unsigned ring = lds0 + F_RING;
  LAS f32x2* tab = (LAS f32x2*)(lds0 + F_TAB);
  LAS float* cst = (LAS float*)(lds0 + F_TAB + 2048);
  const unsigned tm = idx & 7, tn = xcd * 4 + (idx >> 3), cls = (tm + 2 * (idx >> 3)) & 7;
  const unsigned wm = w & 3, wn = w >> 2;
  const int lane = lane_id(), fr = lane & 15, fq = lane >> 4;
  const unsigned rot = wm == 3 ? 0u : wm;
  unsigned xa = ring + wm * 4096 + lane * 16, xb[3], xb1;
  {
    const int q = fr >> 2, pp = fr & 3, r = (fq >> 1) * 16 + (fq & 1) * 4 + q;
#pragma unroll
    for (int i = 0; i < 3; ++i) xb[i] = ring + F_A + r * 224 + wn * 112 + pp * 8 + ((i + rot) % 3) * 32;
    xb1 = ring + F_A + r * 224 + wn * 112 + 96 + (pp & 1) * 8 + (pp >> 1) * G3_UP;
  }
  float cs[3] = {0.f, 0.f, 0.f};
  bf16x8 fa[MB], fb[NB];
  f32x4 acc[MB][NB];
#pragma unroll
  for (int mb = 0; mb < MB; ++mb)
#pragma unroll
    for (int nb = 0; nb < NB; ++nb) { acc[mb][nb] = (f32x4){0.f, 0.f, 0.f, 0.f}; asm volatile("" : "+v"(acc[mb][nb])); }
  asm volatile("" : "+v"(xa), "+v"(xb[0]), "+v"(xb[1]), "+v"(xb[2]), "+v"(xb1));
  barrier();
  if (wn == 1) {
    const auto rst = OUT_RSRC(p.stats, 8 * S_ * 16);
    f32x2 sv[16];
#pragma unroll
    for (int i = 0; i < 16; ++i) sv[i] = __builtin_bit_cast(f32x2, __builtin_amdgcn_raw_buffer_load_b64(rst, (tm * BM + wm * 64 + lane) * 8, i * S_ * 8, 16));
    SB();
    barrier();
    SB();
    float m = 0.f;
#pragma unroll
    for (int i = 0; i < 16; ++i) m += sv[i][0];
    m *= (1.0f / 16.0f);
    float q = 0.f;
#pragma unroll
    for (int i = 0; i < 16; ++i) {
      const float d = sv[i][0] - m;
      q += sv[i][1] + 64.0f * d * d;
    }
    tab[wm * 64 + lane] = (f32x2){m, q * (1.0f / D_) + EPS};
  }
  wait_lgkm0();
  barrier();
  const unsigned csmask = (0x010101u << cls) | (1u << ((cls + 1) & 7));
  auto reads = [&]() {
    SB();
    fa[0] = lds_b128(xa);
#pragma unroll
    for (int j = 0; j < 7; ++j) {
      const unsigned o = j < 6 ? xb[j % 3] + (j / 3) * G3_UP : xb1;
      fb[j] = lds_tr(o, o + 1792);
    }
#pragma unroll
    for (int mb = 1; mb < MB; ++mb) fa[mb] = lds_b128(xa + mb * 1024);
    SB();
  };
  reads();
#pragma unroll 1
  for (unsigned t = 0; t < NT; ++t) {
    const unsigned delta = ((t & 3) == 3) ? (unsigned)(-3 * F_ST) : (unsigned)F_ST;
    xa += delta; xb[0] += delta; xb[1] += delta; xb[2] += delta; xb1 += delta;
    asm volatile("" : "+v"(xa), "+v"(xb[0]), "+v"(xb[1]), "+v"(xb[2]), "+v"(xb1));
    if (__builtin_expect((csmask >> t) & 1u, 0)) {
#pragma unroll
      for (int j = 0; j < 4; ++j)
#pragma unroll
        for (int i = 0; i < 3; ++i)
          asm volatile("v_dot2c_f32_bf16 %0, %1, %2" : "+v"(cs[i]) : "s"(0x3f803f80u), "v"(__builtin_bit_cast(u32x4, fb[3 * i])[j]));
      asm volatile("s_mov_b32 m0, %3\n\ts_nop 2\n\tds_write_addtid_b32 %0\n\tds_write_addtid_b32 %1 offset:256\n\tds_write_addtid_b32 %2 offset:512" ::"v"(cs[0]),
                   "v"(cs[1]), "v"(cs[2]), "s"(lds0 + w * 768)
                   : "memory");
    }
    wait_lgkm0();
    barrier();
    SB();
#pragma unroll
    for (int mb = 0; mb < MB; ++mb)
#pragma unroll
      for (int i = 0; i < NB; ++i) {
        const int nb = (mb & 1) ? NB - 1 - i : i;
        MFMA(fb[nb], fa[mb], acc[mb][nb]);
      }
    SB();
    barrier();
    reads();
  }
  if (wn == 0) barrier();
  mfma_drain();
  SB();
  const int lane2 = lane_id();
  const int gr = lane2 & 15, gq = lane2 >> 4;
  auto act2 = [](f32x2 a, f32x2 b, float sc, float k) {
    const f32x2 t = a * sc;
    const f32x2 e = {__builtin_amdgcn_exp2f(t.x), __builtin_amdgcn_exp2f(t.y)};
    const f32x2 d = e * k + k;
    const f32x2 r = {__builtin_amdgcn_rcpf(d.x), __builtin_amdgcn_rcpf(d.y)};
    const f32x2 o = (a * b) * r;
    return pk_bf16(o.x, o.y);
  };
  const auto ro = OUT_RSRC(p.gu, S_ * F_ * 2);
  float nm[MB], kk[MB], sc[MB];
#pragma unroll
  for (int mb = 0; mb < MB; ++mb) {
    const f32x2 st = tab[wm * 64 + mb * 16 + gr];
    nm[mb] = -st.x; kk[mb] = st.y; sc[mb] = -1.44269504089f * __builtin_amdgcn_rsqf(st.y);
  }
  const int gch0 = tn * 14 + wn * 7;
  const unsigned obase = (tm * (BM / 16) + wm * MB) * (F_ / 32) * 1024 + gr * 16;
#pragma unroll
  for (int i = 0; i < 3; ++i) {
    const unsigned ib = (i + rot) % 3;
    const f32x4 cg = *(const LAS f32x4*)(cst + wn * 56 + ib * 16 + gq * 4), cu = *(const LAS f32x4*)(cst + 112 + wn * 56 + ib * 16 + gq * 4);
    const unsigned oo = obase + (gq & 1) * (F_ / 32) * 1024 + (gch0 + 2 * ib + (gq >> 1)) * 256;
#pragma unroll
    for (int mp = 0; mp < MB / 2; ++mp) {
      u32x2 pkd[2];
#pragma unroll
      for (int h = 0; h < 2; ++h) {
        const int mb = 2 * mp + h;
        const f32x4 a = cg * nm[mb] + acc[mb][i], b = cu * nm[mb] + acc[mb][3 + i];
        pkd[h] = (u32x2){act2(a.xy, b.xy, sc[mb], kk[mb]), act2(a.zw, b.zw, sc[mb], kk[mb])};
      }
      auto s0 = __builtin_amdgcn_permlane16_swap(pkd[0].x, pkd[1].x, false, false);
      auto s1 = __builtin_amdgcn_permlane16_swap(pkd[0].y, pkd[1].y, false, false);
      __builtin_amdgcn_raw_buffer_store_b128((u32x4){s0[0], s1[0], s0[1], s1[1]}, ro, oo + mp * 2 * (F_ / 32) * 1024, 0, ST_AUX);
    }
  }
  {
    const f32x4 cm = *(const LAS f32x4*)(cst + (gq >> 1) * 112 + wn * 56 + 48 + (gq & 1) * 4);
    u32x2 pkd[MB / 2];
#pragma unroll
    for (int mp = 0; mp < MB / 2; ++mp) {
      const f32x4 v0 = cm * nm[2 * mp] + acc[2 * mp][6], v1 = cm * nm[2 * mp + 1] + acc[2 * mp + 1][6];
      f32x4 a, b;
#pragma unroll
      for (int j = 0; j < 4; ++j) {
        auto s = __builtin_amdgcn_permlane32_swap(__float_as_uint(v0[j]), __float_as_uint(v1[j]), false, false);
        a[j] = __uint_as_float(s[0]); b[j] = __uint_as_float(s[1]);
      }
      const float scs = (gq & 2) ? sc[2 * mp + 1] : sc[2 * mp], ks = (gq & 2) ? kk[2 * mp + 1] : kk[2 * mp];
      pkd[mp] = (u32x2){act2(a.xy, b.xy, scs, ks), act2(a.zw, b.zw, scs, ks)};
    }
    auto s0 = __builtin_amdgcn_permlane16_swap(pkd[0].x, pkd[1].x, false, false);
    auto s1 = __builtin_amdgcn_permlane16_swap(pkd[0].y, pkd[1].y, false, false);
    STORE16(ro, obase + ((gq & 1) * 2 + (gq >> 1)) * (F_ / 32) * 1024 + (gch0 + 6) * 256, ((u32x4){s0[0], s1[0], s0[1], s1[1]}));
  }
}

__global__ __launch_bounds__(768, 3) void k_block(FArgs p) {
  extern __shared__ __attribute__((aligned(16))) char shm[];
  const unsigned lds0 = (unsigned)(size_t)(LAS char*)shm;
  const unsigned w = rfl(threadIdx.x >> 6);
  const unsigned xcd = blockIdx.x & 7, idx = blockIdx.x >> 3;
  char* fl = (char*)p.flags;
  const unsigned tm = (xcd >> 1) * 4 + (idx & 3), kh = xcd & 1, lj = idx >> 2;
  if (w >= 8) {
    const unsigned lw = w - 8;
    Ld LA, LB;
    LA = ld_setup<1>(p, lw, xcd, idx);
    sq_loader<1>(p, LA, LB, lds0, lw, xcd, idx);
    const unsigned tag = lds_tag(lds0);
    LA = ld_setup<3>(p, lw, xcd, idx);
    ld_pin(LA);
    barrier();
    if (lw == 3) poll_tags(fl + FL_P1 + (kh * 8 + lj) * 16 * 128, 16, tag);
    barrier();
    sq_loader<2>(p, LB, LA, lds0, lw, xcd, idx);
    LB = ld_setup<4>(p, lw, xcd, idx);
    ld_pin(LB);
    barrier();
    if (lw == 3) poll_tags(fl + FL_P2 + (idx & 7) * 32 * 128, 32, tag);
    barrier();
    glu_loader(p, LA, LB, lds0, lw, xcd, idx);
    barrier();
    if (lw == 3) poll_tags(fl + FL_P3 + ((tm >> 1) * 2 + kh) * 16 * 128, 16, tag);
    barrier();
    sq_loader<4>(p, LB, LA, lds0, lw, xcd, idx);
    return;
  }
  if (w == 0 && idx < 3) {
    unsigned long long pc;
    asm volatile("s_getpc_b64 %0" : "=s"(pc));
    const unsigned ln = idx * 64 + lane_id();
    touch(lds0, (const char*)pc + (ln < CODE_LINES ? ln : CODE_LINES - 1) * 128);
  }
  if (w == 1 && idx < 20) {
    const char* base = idx < 2 ? (const char*)p.cre : idx < 4 ? (const char*)p.cim : idx < 6 ? (const char*)p.wg : idx < 8 ? (const char*)p.wu : idx < 10 ? (const char*)p.wd : idx < 11 ? (const char*)p.out : (const char*)p.ab;
    const unsigned size = idx < 4 ? S_ * S_ * 2 : idx < 10 ? D_ * F_ * 2 : idx < 11 ? S_ * D_ * 2 : (unsigned)WS_FL;
    const unsigned off = ((idx < 10 ? (idx & 1) : (idx < 11 ? 0 : idx - 11)) * 64 + lane_id()) * 65536;
    touch(lds0, base + (off < size ? off : 0u));
  }
  if (w == 2) {
    const unsigned l = lane_id();
    const unsigned off = l < 5 ? (unsigned)WS_FL + FL_PAIR + (l * 256 + blockIdx.x) * 128 : (l < 19 ? (unsigned)WS_FL + FL_CS + blockIdx.x * 1792 + (l - 5) * 128 : (unsigned)WS_ST + blockIdx.x * 1024 + (l & 7) * 128);
    touch(lds0, (const char*)p.ab + off);
  }
  unsigned tag = 0;
  sq_compute<1>(p, lds0, w, xcd, idx, tag);
  wait_vm<0>();
  barrier();
  if (w == 0 && lane_id() == 0) put_tag(fl + FL_P1 + (tm * 16 + kh * 8 + lj) * 128, tag);
  sq_compute<2>(p, lds0, w, xcd, idx, tag);
  wait_vm<0>();
  barrier();
  if (w == 0 && lane_id() == 0) put_tag(fl + FL_P2 + (tm * 16 + kh * 8 + lj) * 128, tag);
  glu_compute(p, lds0, w, xcd, idx);
  wait_vm<0>();
  barrier();
  if (w == 0 && lane_id() == 0) put_tag(fl + FL_P3 + (((idx & 7) * 2 + (xcd >> 2)) * 16 + (xcd & 3) * 4 + (idx >> 3)) * 128, tag);
  sq_compute<4>(p, lds0, w, xcd, idx, tag);
  if (blockIdx.x == 0 && w == 0 && lane_id() == 0) put_tag(fl + FL_EPOCH, tag);
}
}

extern "C" void kernel_launch(void* const* d_in, const int* in_sizes, int n_in, void* d_out, int out_size, void* d_ws, size_t ws_size,
                              hipStream_t stream) {
  char* ws = (char*)d_ws;
  FArgs f = {d_in[3], d_in[4], d_in[1], d_in[2], d_in[0], d_in[6], d_in[7], d_in[8], ws + WS_AB, ws + WS_H, ws + WS_ST, ws + WS_GU, d_out, ws + WS_XC, ws + WS_FL};
  (void)hipFuncSetAttribute((const void*)k_block, hipFuncAttributeMaxDynamicSharedMemorySize, F_LDS);
  hipLaunchKernelGGL(k_block, dim3(256), dim3(768), F_LDS, stream, f);
}
```

```cpp
#include <hip/hip_runtime.h>
#include <hip/hip_bf16.h>

namespace {
#define LAS __attribute__((address_space(3)))
typedef unsigned short u16;
typedef short s16x4 __attribute__((ext_vector_type(4)));
typedef short s16x8 __attribute__((ext_vector_type(8)));
typedef __bf16 bf16x8 __attribute__((ext_vector_type(8)));
typedef __bf16 bf16x2 __attribute__((ext_vector_type(2)));
typedef float f32x4 __attribute__((ext_vector_type(4)));
typedef float f32x2 __attribute__((ext_vector_type(2)));
typedef unsigned u32x4 __attribute__((ext_vector_type(4)));
typedef unsigned u32x2 __attribute__((ext_vector_type(2)));

constexpr int S_ = 2048, D_ = 1024, F_ = 3584;
constexpr float EPS = 1e-5f;

constexpr size_t WS_AB = 0;
constexpr size_t WS_H = WS_AB + (size_t)D_ * 2 * S_ * 2;
constexpr size_t WS_GU = WS_H + (size_t)S_ * D_ * 2;
constexpr size_t WS_ST = WS_GU + (size_t)S_ * F_ * 2;
constexpr size_t WS_XC = WS_ST + (size_t)S_ * 16 * 8;
constexpr size_t WS_FL = WS_XC + (size_t)2 * 256 * 16384;
constexpr int FL_EPOCH = 0;
constexpr int FL_PAIR = 128;
constexpr int FL_P1 = FL_PAIR + 512 * 128;
constexpr int FL_P2 = FL_P1 + 256 * 128;
constexpr int FL_P3 = FL_P2 + 256 * 128;
constexpr int FL_LN = FL_P3 + 256 * 128;
constexpr int FL_CS = FL_LN + 4096 * 16;

__device__ __forceinline__ unsigned rfl(unsigned v) { return __builtin_amdgcn_readfirstlane(v); }
__device__ __forceinline__ unsigned pk_bf16(float lo, float hi) {
  bf16x2 c = {(__bf16)lo, (__bf16)hi};
  return __builtin_bit_cast(unsigned, c);
}
__device__ __forceinline__ float bf_lo(unsigned v) { return __uint_as_float(v << 16); }
__device__ __forceinline__ float bf_hi(unsigned v) { return __uint_as_float(v & 0xffff0000u); }

__device__ __forceinline__ u32x4 make_rsrc(const void* p, unsigned bytes) {
  unsigned long long a = (unsigned long long)p;
  u32x4 r;
  r.x = (unsigned)a;
  r.y = (unsigned)(a >> 32) & 0xffffu;
  r.z = bytes;
  r.w = 0x00020000u;
  return r;
}
template <int N>
__device__ __forceinline__ void wait_vm() {
  asm volatile("s_waitcnt vmcnt(%0)" ::"i"(N) : "memory");
}
__device__ __forceinline__ void wait_lgkm0() { __builtin_amdgcn_s_waitcnt(0xC07F); }
__device__ __forceinline__ void barrier() {
  asm volatile("" ::: "memory");
  __builtin_amdgcn_s_barrier();
  asm volatile("" ::: "memory");
}
__device__ __forceinline__ bf16x8 lds_b128(unsigned addr) { return *(const LAS bf16x8*)(addr); }
__device__ __forceinline__ bf16x8 lds_tr(unsigned a0, unsigned a1) {
  s16x4 lo = __builtin_amdgcn_ds_read_tr16_b64_v4i16((LAS s16x4*)(a0));
  s16x4 hi = __builtin_amdgcn_ds_read_tr16_b64_v4i16((LAS s16x4*)(a1));
  s16x8 f = __builtin_shufflevector(lo, hi, 0, 1, 2, 3, 4, 5, 6, 7);
  return __builtin_bit_cast(bf16x8, f);
}

#define ST_AUX 16
#define OUT_RSRC(ptr, bytes) __builtin_amdgcn_make_buffer_rsrc((void*)(ptr), 0, (bytes), 0x00020000)
#define STORE16(rs, byteoff, v) __builtin_amdgcn_raw_buffer_store_b128(v, rs, byteoff, 0, ST_AUX)

#define DMA4(M) asm volatile("s_mov_b32 m0, %0\n\ts_nop 4\n\tbuffer_load_dwordx4 %1, %5, %6 offen " M " lds\n\tbuffer_load_dwordx4 %2, %5, %6 offen offset:1024 " M " lds\n\t" \
                 "buffer_load_dwordx4 %3, %5, %6 offen offset:2048 " M " lds\n\tbuffer_load_dwordx4 %4, %5, %6 offen offset:3072 " M " lds" ::"s"(ldsaddr), \
                 "v"(v[0]), "v"(v[1]), "v"(v[2]), "v"(v[3]), "s"(rsrc), "s"(soff) : "memory")
#define DMA3(M) asm volatile("s_mov_b32 m0, %0\n\ts_nop 4\n\tbuffer_load_dwordx4 %1, %4, %5 offen " M " lds\n\tbuffer_load_dwordx4 %2, %4, %5 offen offset:1024 " M " lds\n\t" \
                 "buffer_load_dwordx4 %3, %4, %5 offen offset:2048 " M " lds" ::"s"(ldsaddr), "v"(v[0]), "v"(v[1]), "v"(v[2]), "s"(rsrc), "s"(soff) : "memory")
#define DMAB(M) asm volatile("s_mov_b32 m0, %0\n\ts_waitcnt vmcnt(8)\n\ts_barrier\n\tbuffer_load_dwordx4 %1, %5, %6 offen " M " lds\n\tbuffer_load_dwordx4 %2, %5, %6 offen offset:1024 " M " lds\n\t" \
                 "buffer_load_dwordx4 %3, %5, %6 offen offset:2048 " M " lds\n\tbuffer_load_dwordx4 %4, %5, %6 offen offset:3072 " M " lds" ::"s"(ldsaddr), \
                 "v"(v[0]), "v"(v[1]), "v"(v[2]), "v"(v[3]), "s"(rsrc), "s"(soff) : "memory")
template <int N, int MOD = 0>
__device__ __forceinline__ void dma_grp(unsigned ldsaddr, const unsigned (&v)[4], u32x4 rsrc, unsigned soff) {
  if constexpr (N == 4) { if constexpr (MOD == 1) DMA4("sc1"); else DMA4(""); }
  else if constexpr (N == 3) { if constexpr (MOD == 1) DMA3("sc1"); else DMA3(""); }
  else { if constexpr (MOD == 1) DMAB("sc1"); else DMAB(""); }
}
__device__ __forceinline__ float sum_rows(float x) {
  auto a = __builtin_amdgcn_permlane16_swap(__float_as_uint(x), __float_as_uint(x), false, false);
  x = __uint_as_float(a[0]) + __uint_as_float(a[1]);
  auto b = __builtin_amdgcn_permlane32_swap(__float_as_uint(x), __float_as_uint(x), false, false);
  return __uint_as_float(b[0]) + __uint_as_float(b[1]);
}
template <int CTRL> __device__ __forceinline__ float dpp_add(float x) { return x + __uint_as_float(__builtin_amdgcn_mov_dpp(__float_as_uint(x), CTRL, 0xF, 0xF, true)); }
__device__ __forceinline__ float wave_sum(float x) { return sum_rows(dpp_add<0x140>(dpp_add<0x141>(dpp_add<0x4E>(dpp_add<0xB1>(x))))); }
__device__ __forceinline__ int lane_id() {
  int l;
  asm volatile("v_mbcnt_lo_u32_b32 %0, -1, 0\n\tv_mbcnt_hi_u32_b32 %0, -1, %0" : "=v"(l));
  return l;
}
__device__ __forceinline__ void mfma_drain() { asm volatile("s_nop 15" ::: "memory"); }
template <int NC, int NT>
__device__ __forceinline__ unsigned stage_of(unsigned n, unsigned c) {
  const unsigned t = ((n & (NC - 1)) == c) ? n + NC : n;
  return rfl(t >= NT ? t - NT : t);
}
#define SB() __builtin_amdgcn_sched_barrier(0)
#define MFMA(b, a, c) asm volatile("v_mfma_f32_16x16x32_bf16 %0, %1, %2, %0" : "+v"(c) : "v"(b), "v"(a))

struct FArgs {
  const void *dre, *dim, *cre, *cim, *x, *wg, *wu, *wd;
  void *ab, *h, *stats, *gu, *out, *xch, *flags;
};
constexpr unsigned CODE_LINES = 156;
constexpr int F_RING = 6144, F_ST = 32768, F_TAB = F_RING + 4 * F_ST, F_TAG = F_TAB + 4096, F_SINK = F_TAB + 4160, F_LDS = F_SINK + 256, F_A = 16384, G3_UP = 7168 + 16;

__device__ __forceinline__ void poll_tags(const void* line, int n, unsigned tag) {
  const int l = lane_id();
  const unsigned* q = (const unsigned*)line + (l < n ? l : 0) * 32;
  unsigned spins = 0;
  while (!__all(__hip_atomic_load(q, __ATOMIC_RELAXED, __HIP_MEMORY_SCOPE_AGENT) == tag) && ++spins < (1u << 17)) __builtin_amdgcn_s_sleep(1);
}
__device__ __forceinline__ void put_tag(void* word, unsigned tag) { __hip_atomic_store((unsigned*)word, tag, __ATOMIC_RELAXED, __HIP_MEMORY_SCOPE_AGENT); }
__device__ __forceinline__ unsigned lds_tag(unsigned lds0) { return rfl(*(volatile LAS unsigned*)(lds0 + F_TAG)); }
__device__ __forceinline__ void touch(unsigned lds0, const void* ptr) {
  asm volatile("s_mov_b32 m0, %1\n\ts_nop 0\n\tglobal_load_lds_dword %0, off sc1" ::"v"(ptr), "s"(lds0 + F_SINK) : "memory");
}
template <int ID> constexpr int nt_of() { return ID == 1 ? 16 : (ID == 4 ? 28 : 32); }
template <int ID> constexpr int nc_of() { return (ID == 1 || ID == 4) ? 4 : 8; }
template <int ID> constexpr int mod_a() { return ID > 2; }
template <int ID> constexpr int mod_b() { return ID == 2; }
struct Ld {
  u32x4 qa, qb;
  unsigned va[4], vb[4], cls;
};
template <int ID>
__device__ __forceinline__ Ld ld_setup(const FArgs& p, unsigned lw, unsigned xcd, unsigned idx) {
  const int lane = lane_id();
  Ld L;
  if constexpr (ID == 3) {
    const unsigned tm = idx & 7, tn = xcd * 4 + (idx >> 3);
    L.qa = make_rsrc((const u16*)p.h + (size_t)tm * 256 * D_, 256 * D_ * 2);
    L.qb = make_rsrc(lw < 2 ? p.wg : p.wu, (unsigned)D_ * F_ * 2);
#pragma unroll
    for (int i = 0; i < 4; ++i) {
      L.va[i] = (lw * 4 + i) * (D_ / 32) * 1024 + lane * 16 - i * 1024;
      const int c = ((lw & 1) * 4 + i) * 64 + lane, r = c / 14, pos = c % 14;
      const int k = (r & 0x13) | ((r & 4) << 1) | ((r & 8) >> 1);
      L.vb[i] = k * F_ * 2 + tn * 224 + pos * 16 - i * 1024;
    }
    L.cls = (tm + 2 * (idx >> 3)) & 7;
  } else {
    const unsigned li = idx & 3, lj = idx >> 2, kh = xcd & 1, tm = (xcd >> 1) * 4 + li, tn = lj;
    if constexpr (ID == 1) {
      L.qa = make_rsrc((const u16*)(tm < 8 ? p.dre : p.dim) + (size_t)(tm & 7) * 128 * D_, 128 * D_ * 2);
      L.qb = make_rsrc((const u16*)p.x + (size_t)(kh * 8 + lj) * 128 * D_, 128 * D_ * 2);
    } else if constexpr (ID == 2) {
      L.qa = make_rsrc((const u16*)(kh ? p.cim : p.cre) + (size_t)tm * 128 * S_, 128 * S_ * 2);
      L.qb = make_rsrc((const u16*)p.ab + (size_t)tn * 128 * 4096 + kh * 64 * 512, 128 * 4096 * 2 - kh * 64 * 1024);
    } else {
      L.qa = make_rsrc((const u16*)p.gu + (size_t)tm * 128 * F_ + kh * 56 * 512, 128 * F_ * 2 - kh * 56 * 1024);
      L.qb = make_rsrc((const u16*)p.wd + (size_t)kh * 1792 * D_, 1792 * D_ * 2);
    }
#pragma unroll
    for (int i = 0; i < 4; ++i) {
      const int pc = lw * 4 + i;
      if constexpr (ID == 1) {
        const int row = pc * 8 + (lane >> 3);
        L.va[i] = L.vb[i] = row * D_ * 2 + (((lane & 7) ^ ((row >> 1) & 7)) << 4) - i * 1024;
      } else if constexpr (ID == 2) {
        const int row = pc * 8 + (lane >> 3);
        L.va[i] = row * S_ * 2 + (((lane & 7) ^ ((row >> 1) & 7)) << 4) - i * 1024;
        L.vb[i] = ((pc >> 1) * 128 + (pc & 1)) * 1024 + lane * 16 - i * 1024;
      } else {
        const int k = pc * 4 + (lane >> 4), f = (k & 3) | (((k >> 3) & 1) << 2);
        L.va[i] = ((pc >> 1) * (F_ / 32) + (pc & 1)) * 1024 + lane * 16 - i * 1024;
        L.vb[i] = k * D_ * 2 + tn * 256 + (((lane & 15) ^ (f << 1)) << 4) - i * 1024;
      }
    }
    L.cls = (nc_of<ID>() == 8) ? ((lj + 2 * li) & 7) : ((lj + li) & 3);
  }
  return L;
}
__device__ __forceinline__ void ld_pin(Ld& L) {
  asm volatile("" : "+v"(L.va[0]), "+v"(L.va[1]), "+v"(L.va[2]), "+v"(L.va[3]), "+v"(L.vb[0]), "+v"(L.vb[1]), "+v"(L.vb[2]), "+v"(L.vb[3]));
}
template <int ID>
__device__ __forceinline__ void ld_a(const Ld& L, unsigned ring, unsigned lw, unsigned n) {
  const unsigned st = stage_of<nc_of<ID>(), nt_of<ID>()>(n, L.cls);
  dma_grp<4, mod_a<ID>()>(ring + (n & 3) * F_ST + lw * 4096, L.va, L.qa, st * (ID <= 2 ? 128 : (ID == 3 ? 1024 : 2048)));
}
template <int ID>
__device__ __forceinline__ void ld_b(const Ld& L, unsigned ring, unsigned lw, unsigned n) {
  const unsigned st = stage_of<nc_of<ID>(), nt_of<ID>()>(n, L.cls);
  const unsigned dst = ring + (n & 3) * F_ST + F_A;
  if constexpr (ID == 3) {
    const unsigned d = dst + (lw >> 1) * G3_UP + (lw & 1) * 4096;
    if (lw & 1) dma_grp<3, mod_b<ID>()>(d, L.vb, L.qb, st * (32 * F_ * 2));
    else dma_grp<4, mod_b<ID>()>(d, L.vb, L.qb, st * (32 * F_ * 2));
  } else {
    dma_grp<4, mod_b<ID>()>(dst + lw * 4096, L.vb, L.qb, st * (ID == 1 ? 128 : (ID == 2 ? 2048 : 64 * D_ * 2)));
  }
}

template <int ID> __device__ __forceinline__ void ld_const(const Ld& L, unsigned ring, unsigned lw, unsigned n) { if constexpr (ID == 2) ld_a<ID>(L, ring, lw, n); else ld_b<ID>(L, ring, lw, n); }
template <int ID> __device__ __forceinline__ void ld_dep(const Ld& L, unsigned ring, unsigned lw, unsigned n) { if constexpr (ID == 2) ld_b<ID>(L, ring, lw, n); else ld_a<ID>(L, ring, lw, n); }

template <int ID>
__device__ __forceinline__ void sq_loader(const FArgs& p, const Ld& L, Ld& N, unsigned lds0, unsigned lw, unsigned xcd, unsigned idx) {
  constexpr int NT = nt_of<ID>(), NEXT = ID == 4 ? 0 : ID + 1;
  const unsigned ring = lds0 + F_RING;
  if constexpr (ID == 1) {
    ld_a<ID>(L, ring, lw, 0); ld_b<ID>(L, ring, lw, 0);
    N = ld_setup<NEXT>(p, lw, xcd, idx); ld_pin(N);
    ld_a<ID>(L, ring, lw, 1); ld_b<ID>(L, ring, lw, 1);
  } else {
    ld_dep<ID>(L, ring, lw, 0); ld_dep<ID>(L, ring, lw, 1);
  }
  constexpr unsigned KA = ID <= 2 ? 128 : 2048, KB = ID == 1 ? 128 : (ID == 2 ? 2048 : 64 * D_ * 2);
  unsigned sa, sb, ma;
  auto prep = [&](unsigned n) {
    const unsigned st = stage_of<nc_of<ID>(), NT>(n, L.cls);
    sa = st * KA; sb = st * KB; ma = ring + (n & 3) * F_ST + lw * 4096;
    asm volatile("" : "+s"(sa), "+s"(sb), "+s"(ma));
  };
  auto bar_a = [&]() {
    dma_grp<5, mod_a<ID>()>(ma, L.va, L.qa, sa);
  };
  prep(2);
  wait_vm<(ID == 1 ? 8 : 4)>();
  bar_a();
  unsigned mb_ = ma + F_A, sb_ = sb;
  prep(3);
#pragma unroll 1
  for (unsigned n = 0; n + 3 < NT; ++n) {
    dma_grp<4, mod_b<ID>()>(mb_, L.vb, L.qb, sb_);
    bar_a();
    mb_ = ma + F_A; sb_ = sb;
    asm volatile("" : "+s"(mb_), "+s"(sb_));
    prep(n + 4);
  }
  dma_grp<4, mod_b<ID>()>(mb_, L.vb, L.qb, sb_);
  wait_vm<8>();
  barrier();
  const int lane = lane_id();
  u32x2 l0, l1;
  const char* lp = (const char*)p.flags + FL_LN + (lw < 2 ? (((xcd & 1) * 8 + (idx >> 2)) * 128 + lw * 64 + lane) : (2048 + ((xcd >> 1) * 4 + (idx & 3)) * 128 + (lw - 2) * 64 + lane)) * 16;
  if constexpr (ID == 1) asm volatile("global_load_dwordx2 %0, %2, off sc1\n\tglobal_load_dwordx2 %1, %2, off offset:8 sc1" : "=&v"(l0), "=&v"(l1) : "v"(lp) : "memory");
  if constexpr (NEXT != 0) ld_const<NEXT>(N, ring, lw, 0);
  if constexpr (ID == 1) wait_vm<6>();
  else if constexpr (ID == 2) { if (lw & 1) wait_vm<3>(); else wait_vm<4>(); }
  else wait_vm<0>();
  barrier();
  if constexpr (NEXT != 0) ld_const<NEXT>(N, ring, lw, 1);
  if constexpr (ID == 1) {
    if (lw < 2) {
      const char* tp = (const char*)((xcd & 1) ? p.cim : p.cre) + (size_t)((xcd >> 1) * 512 + idx * 16 + lw * 8 + (lane >> 3)) * (S_ * 2) + (lane & 7) * 128;
      touch(lds0, tp);
    }
  }
  if constexpr (ID == 2) {
    if (lw < 2) {
      const char* tp = (const char*)((lw & 1) ? p.wu : p.wg) + (size_t)(idx * 8 + (lane >> 3)) * (F_ * 2) + xcd * 896 + ((lane & 7) < 7 ? (lane & 7) : 6) * 128;
      touch(lds0, tp);
    }
  }
  barrier();
  const unsigned tag = lds_tag(lds0);
  if constexpr (ID == 1) {
    if (lw < 2) wait_vm<9>(); else wait_vm<8>();
    asm volatile("" : "+v"(l0), "+v"(l1));
#pragma unroll 1
    for (unsigned spins = 0; spins < (1u << 15) && !__all(l0.y == tag && l1.y == tag); ++spins) {
      __builtin_amdgcn_s_sleep(2);
      asm volatile("global_load_dwordx2 %0, %2, off sc1\n\tglobal_load_dwordx2 %1, %2, off offset:8 sc1\n\ts_waitcnt vmcnt(0)" : "=&v"(l0), "=&v"(l1) : "v"(lp) : "memory");
    }
    LAS unsigned* tb = (LAS unsigned*)(lds0 + F_TAB);
    tb[lw * 64 + lane + (lw < 2 ? 0 : 128)] = l0.x;
    if (lw < 2) tb[128 + lw * 64 + lane] = l1.x;
    wait_lgkm0();
  }
  barrier();
  barrier();
  if constexpr (ID != 1) {
    barrier();
    if (lw == 3) poll_tags((char*)p.flags + FL_PAIR + ((ID == 2 ? 0 : 256) + (blockIdx.x ^ 1)) * 128, 1, tag);
    barrier();
  }
}

template <int ID>
__device__ __forceinline__ void sq_compute(const FArgs& p, unsigned lds0, unsigned w, unsigned xcd, unsigned idx, unsigned& tag) {
  constexpr int NT = nt_of<ID>();
  u32x4 xv[2], wv[2];
  unsigned tv = 0;
  const unsigned r0 = blockIdx.x * 8 + w;
  char* fl = (char*)p.flags;
  if constexpr (ID == 1) {
    const int lane = lane_id();
    const u16* xp = (const u16*)p.x + (size_t)r0 * D_ + lane * 8;
    const u16* wp = (const u16*)(r0 < 1024 ? p.dre : p.dim) + (size_t)(r0 & 1023) * D_ + lane * 8;
#pragma unroll
    for (int i = 0; i < 2; ++i) xv[i] = *(const u32x4*)(xp + i * 512);
#pragma unroll
    for (int i = 0; i < 2; ++i) wv[i] = *(const u32x4*)(wp + i * 512);
    tv = __hip_atomic_load((const unsigned*)(fl + FL_EPOCH), __ATOMIC_RELAXED, __HIP_MEMORY_SCOPE_AGENT);
    SB();
  }
  const unsigned ring = lds0 + F_RING;
  const unsigned g = w >> 2, wm = (w >> 1) & 1, wx = w & 1;
  const unsigned kh = xcd & 1, tm = (xcd >> 1) * 4 + (idx & 3), tn = idx >> 2;
  u32x4 res[2];
  unsigned rdA[8], rdB[4], hoff = 0;
  const unsigned rotA = (ID == 1) ? 2 * g : 2 * g + wx;
  {
    const int lane = lane_id(), fr = lane & 15, fq = lane >> 4, chunk = ((fq & 1) << 1) | (fq >> 1);
    const int row = tm * 128 + wm * 64 + rotA * 16 + fr, col = tn * 128 + kh * 32 + chunk * 8;
    if constexpr (ID == 2) {
#pragma unroll
      for (int c = 0; c < 2; ++c) res[c] = *(const u32x4*)((const u16*)p.x + (size_t)row * D_ + col + (c ^ wx) * 64);
    } else if constexpr (ID == 4) {
      hoff = ((row >> 4) * (D_ / 32) + (col >> 5)) * 1024 + (chunk * 16 + fr) * 16;
    }
    const unsigned rotB = (ID == 1) ? 0u : 2 * kh, base = ring + g * F_ST;
#pragma unroll
    for (int i = 0; i < 4; ++i) {
      const unsigned kk = i >> 1, hf = (i & 1) * 2;
      if constexpr (ID == 1) {
        rdA[i] = base + (wm * 64 + (hf ^ rotA) * 16 + fr) * 128 + (((kk * 4 + fq) ^ (fr >> 1)) << 4);
        rdB[i] = base + F_A + (wx * 64 + fr) * 128 + ((((i & 1) * 4 + fq) ^ (fr >> 1)) << 4);
      } else if constexpr (ID == 2) {
        rdA[i] = base + (wm * 64 + (i ^ rotA) * 16 + fr) * 128 + ((fq ^ (fr >> 1)) << 4);
        rdA[4 + i] = base + (wm * 64 + (i ^ rotA) * 16 + fr) * 128 + (((4 + fq) ^ (fr >> 1)) << 4);
        rdB[i] = base + F_A + wx * 8192 + (hf ^ rotB) * 2048 + lane * 16;
      } else {
        const int q = fr >> 2, pp = fr & 3, f = q | ((fq & 1) << 2);
        rdA[i] = base + wm * 8192 + (i ^ rotA) * 2048 + lane * 16;
        rdB[i] = base + F_A + (8 * fq + q) * 256 + (((wx * 4 + (i ^ rotB)) ^ f) << 5) + pp * 8;
      }
    }
  }
  constexpr int NVA = (ID == 2) ? 8 : 4, NVB = (ID <= 2) ? 2 : 4;
  bf16x8 fa[2][4], fb[2][4];
  f32x4 acc[4][4];
#pragma unroll
  for (int i = 0; i < 16; ++i) { acc[i >> 2][i & 3] = (f32x4){0.f, 0.f, 0.f, 0.f}; asm volatile("" : "+v"(acc[i >> 2][i & 3])); }
  auto reads = [&]() {
    SB();
#pragma unroll
    for (int s = 0; s < 2; ++s)
#pragma unroll
      for (int i = 0; i < 4; ++i) {
        if constexpr (ID == 1) {
          fb[s][i] = lds_b128(rdB[s] + i * 2048);
          fa[s][i] = lds_b128(rdA[s * 2 + (i >> 1)] + (i & 1) * 2048);
        } else if constexpr (ID == 2) {
          fb[s][i] = lds_b128(rdB[i >> 1] + (i & 1) * 2048 + s * 1024);
          fa[s][i] = lds_b128(rdA[s * 4 + i]);
        } else {
          fb[s][i] = lds_tr(rdB[i] + s * 8192, rdB[i] + s * 8192 + 1024);
          fa[s][i] = lds_b128(rdA[i] + s * 1024);
        }
      }
    SB();
  };
#pragma unroll
  for (int j = 0; j < NVA; ++j) asm volatile("" : "+v"(rdA[j]));
#pragma unroll
  for (int j = 0; j < NVB; ++j) asm volatile("" : "+v"(rdB[j]));
  if constexpr (ID == 1) {
    SB();
    asm volatile("" : "+v"(tv));
    tv = tv * 0x9E3779B1u + 0x7F4A7C15u;
    const int lane = lane_id();
    float s = 0.f, t = 0.f;
#pragma unroll
    for (int j = 0; j < 8; ++j) s += bf_lo(xv[j >> 2][j & 3]) + bf_hi(xv[j >> 2][j & 3]);
    const float m = wave_sum(s) * (1.0f / D_);
    float q = 0.f;
#pragma unroll
    for (int j = 0; j < 8; ++j) {
      const float a = bf_lo(xv[j >> 2][j & 3]) - m, b = bf_hi(xv[j >> 2][j & 3]) - m;
      q += a * a + b * b;
      t += bf_lo(wv[j >> 2][j & 3]) + bf_hi(wv[j >> 2][j & 3]);
    }
    const float rs = __builtin_amdgcn_rsqf(wave_sum(q) * (1.0f / D_) + EPS);
    t = wave_sum(t);
    const float val = (lane & 2) ? t : ((lane & 1) ? rs : m);
    if (lane < 4)
      __hip_atomic_store((unsigned long long*)(fl + FL_LN + ((lane >> 1) * 2048 + r0) * 16 + (lane & 1) * 8), ((unsigned long long)tv << 32) | __float_as_uint(val),
                         __ATOMIC_RELAXED, __HIP_MEMORY_SCOPE_AGENT);
    tag = rfl(tv);
    if (w == 0) { *(volatile LAS unsigned*)(lds0 + F_TAG) = tag; wait_lgkm0(); }
  }
  if constexpr (ID != 1) {
    barrier();
  }
  if constexpr (ID == 4) {
    const auto rh = OUT_RSRC(p.h, S_ * D_ * 2);
#pragma unroll
    for (int c = 0; c < 2; ++c) res[c] = __builtin_amdgcn_raw_buffer_load_b128(rh, hoff, (c ^ wx) * 2048, 16);
  }
  barrier();
  if (g == 1) barrier();
  reads();
#pragma unroll 1
  for (unsigned i = 0; i < NT / 2; ++i) {
    const unsigned delta = (i & 1) ? (unsigned)(-2 * F_ST) : (unsigned)(2 * F_ST);
#pragma unroll
    for (int j = 0; j < NVA; ++j) { rdA[j] += delta; asm volatile("" : "+v"(rdA[j])); }
#pragma unroll
    for (int j = 0; j < NVB; ++j) { rdB[j] += delta; asm volatile("" : "+v"(rdB[j])); }
    wait_lgkm0();
    barrier();
    SB();
#pragma unroll
    for (int m = 0; m < 32; ++m) {
      const int n = (m & 4) ? 3 - (m & 3) : (m & 3);
      MFMA(fb[m >> 4][n], fa[m >> 4][(m >> 2) & 3], acc[(m >> 2) & 3][n]);
    }
    SB();
    barrier();
    if (i + 1 < NT / 2) reads();
  }
  mfma_drain();
  SB();
  const int lane = lane_id(), fr = lane & 15, fq = lane >> 4, chunk = ((fq & 1) << 1) | (fq >> 1);
  if constexpr (ID == 1) {
    const unsigned wb = ring + 2 * F_ST + (w ^ 4) * 8192 + lane * 16, rb = ring + 2 * F_ST + w * 8192 + lane * 16;
#pragma unroll
    for (int i = 0; i < 8; ++i) *(LAS f32x4*)(wb + i * 1024) = acc[2 + (i >> 2)][i & 3];
    if (g == 0) barrier();
    wait_lgkm0();
    barrier();
#pragma unroll
    for (int i = 0; i < 8; ++i) acc[i >> 2][i & 3] += *(const LAS f32x4*)(rb + i * 1024);
  }
  if constexpr (ID == 1) {
    const LAS float* tb = (const LAS float*)(lds0 + F_TAB);
    const float sg = tm < 8 ? 1.f : -1.f;
    const auto ro = OUT_RSRC(p.ab, D_ * 4096 * 2);
    f32x4 mean[4], rstd[4];
#pragma unroll
    for (int j = 0; j < 4; ++j) {
      mean[j] = *(const LAS f32x4*)(tb + wx * 64 + j * 16 + fq * 4);
      rstd[j] = *(const LAS f32x4*)(tb + 128 + wx * 64 + j * 16 + fq * 4) * sg;
    }
#pragma unroll
    for (int mb = 0; mb < 2; ++mb) {
      const float wsm = tb[256 + wm * 64 + g * 32 + mb * 16 + fr];
#pragma unroll
      for (int np = 0; np < 2; ++np) {
        const f32x4 x0 = (acc[mb][2 * np] - mean[2 * np] * wsm) * rstd[2 * np], x1 = (acc[mb][2 * np + 1] - mean[2 * np + 1] * wsm) * rstd[2 * np + 1];
        auto s0 = __builtin_amdgcn_permlane16_swap(pk_bf16(x0[0], x0[1]), pk_bf16(x1[0], x1[1]), false, false);
        auto s1 = __builtin_amdgcn_permlane16_swap(pk_bf16(x0[2], x0[3]), pk_bf16(x1[2], x1[3]), false, false);
        const int rblk = (tm & 7) * 8 + wm * 4 + g * 2 + mb;
        const int kblk = (tm >> 3) * 64 + (kh * 8 + tn) * 4 + wx * 2 + np;
        STORE16(ro, (rblk * 128 + kblk) * 1024 + (chunk * 16 + fr) * 16, ((u32x4){s0[0], s1[0], s0[1], s1[1]}));
      }
    }
    return;
  }
  const auto rs = OUT_RSRC((char*)p.xch + (ID == 2 ? 0 : 256 * 16384), 256 * 16384);
  const unsigned sreg = ring + ((w >> 1) & 1) * F_ST + (w & 1) * 8192 + lane * 16, kreg = ring + 2 * F_ST + lane * 16;
  auto put_keep = [&]() {
#pragma unroll
    for (int j = 1; j < 4; ++j) {
      const unsigned d = kreg + ((wm * 4 + (j ^ rotA)) * 3 + (j - 1)) * 2048;
      *(LAS f32x4*)(d) = acc[j][0];
      *(LAS f32x4*)(d + 1024) = acc[j][1];
    }
  };
  if (g == 0) {
#pragma unroll
    for (int i = 0; i < 8; ++i) *(LAS f32x4*)(sreg + i * 1024) = acc[i >> 1][2 + (i & 1)];
    put_keep();
    wait_lgkm0();
    barrier();
  } else {
#pragma unroll
    for (int j = 0; j < 4; ++j) {
      const f32x4 x0 = acc[j][2] + *(const LAS f32x4*)(sreg + (j ^ 2) * 2048), x1 = acc[j][3] + *(const LAS f32x4*)(sreg + (j ^ 2) * 2048 + 1024);
      auto s0 = __builtin_amdgcn_permlane16_swap(pk_bf16(x0[0], x0[1]), pk_bf16(x1[0], x1[1]), false, false);
      auto s1 = __builtin_amdgcn_permlane16_swap(pk_bf16(x0[2], x0[3]), pk_bf16(x1[2], x1[3]), false, false);
      __builtin_amdgcn_raw_buffer_store_b128((u32x4){s0[0], s1[0], s0[1], s1[1]}, rs, lane * 16 + blockIdx.x * 16384 + ((wm * 4 + (j ^ rotA)) * 2 + wx) * 1024, 0, 16);
    }
    SB();
    put_keep();
    wait_lgkm0();
  }
  barrier();
  f32x4 y[2][2];
  {
    const unsigned kb = kreg + (wm * 4 + rotA) * 3 * 2048;
    f32x4 t[2][2];
#pragma unroll
    for (int i = 0; i < 2; ++i) {
      t[0][i] = acc[0][i] + *(const LAS f32x4*)(kb + 2048 + i * 1024);
      t[1][i] = *(const LAS f32x4*)(kb + i * 1024) + *(const LAS f32x4*)(kb + 4096 + i * 1024);
    }
#pragma unroll
    for (int c = 0; c < 2; ++c)
#pragma unroll
      for (int j = 0; j < 4; ++j) {
        auto s = __builtin_amdgcn_permlane16_swap(__float_as_uint(t[c][0][j]), __float_as_uint(t[c][1][j]), false, false);
        y[c][0][j] = __uint_as_float(s[0]) + ((j & 1) ? bf_hi(res[c][j >> 1]) : bf_lo(res[c][j >> 1]));
        y[c][1][j] = __uint_as_float(s[1]) + ((j & 1) ? bf_hi(res[c][2 + (j >> 1)]) : bf_lo(res[c][2 + (j >> 1)]));
      }
  }
  const int row0 = tm * 128 + wm * 64 + rotA * 16 + fr, col = tn * 128 + kh * 32 + chunk * 8;
  unsigned optr = (ID == 2) ? ((row0 >> 4) * (D_ / 32) + (col >> 5)) * 1024 + (chunk * 16 + fr) * 16 : (row0 * D_ + col) * 2;
  const auto ro = OUT_RSRC(ID == 2 ? p.h : p.out, S_ * D_ * 2);
  asm volatile("" : "+v"(y[0][0]), "+v"(y[0][1]), "+v"(y[1][0]), "+v"(y[1][1]), "+v"(optr));
  wait_vm<0>();
  barrier();
  if (w == 0 && lane == 0) put_tag((char*)p.flags + FL_PAIR + ((ID == 2 ? 0 : 256) + blockIdx.x) * 128, tag);
  barrier();
  u32x4 in[2], v[2];
#pragma unroll
  for (int c = 0; c < 2; ++c) in[c] = __builtin_amdgcn_raw_buffer_load_b128(rs, lane * 16, (blockIdx.x ^ 1) * 16384 + ((wm * 4 + rotA) * 2 + (c ^ wx)) * 1024, 16);
#pragma unroll
  for (int c = 0; c < 2; ++c) {
    const f32x4 a = y[c][0] + (f32x4){bf_lo(in[c][0]), bf_hi(in[c][0]), bf_lo(in[c][1]), bf_hi(in[c][1])};
    const f32x4 b = y[c][1] + (f32x4){bf_lo(in[c][2]), bf_hi(in[c][2]), bf_lo(in[c][3]), bf_hi(in[c][3])};
    v[c] = (u32x4){pk_bf16(a[0], a[1]), pk_bf16(a[2], a[3]), pk_bf16(b[0], b[1]), pk_bf16(b[2], b[3])};
    __builtin_amdgcn_raw_buffer_store_b128(v[c], ro, optr + (c ^ wx) * (ID == 2 ? 2048 : 128), 0, ST_AUX);
  }
  SB();
  if constexpr (ID == 2) {
    float s = 0.f;
#pragma unroll
    for (int j = 0; j < 8; ++j) s += bf_lo(v[j >> 2][j & 3]) + bf_hi(v[j >> 2][j & 3]);
    const float m = sum_rows(s) * (1.0f / 64.0f);
    float q = 0.f;
#pragma unroll
    for (int j = 0; j < 8; ++j) {
      const float a = bf_lo(v[j >> 2][j & 3]) - m, b = bf_hi(v[j >> 2][j & 3]) - m;
      q += a * a + b * b;
    }
    const f32x2 st = {m, sum_rows(q)};
    if (fq == 0)
      __hip_atomic_store((unsigned long long*)p.stats + (size_t)(tn * 2 + kh) * S_ + row0, __builtin_bit_cast(unsigned long long, st), __ATOMIC_RELAXED, __HIP_MEMORY_SCOPE_AGENT);
  }
  SB();
}

__device__ __forceinline__ void glu_loader(const FArgs& p, const Ld& L, Ld& N, unsigned lds0, unsigned lw, unsigned xcd, unsigned idx) {
  constexpr int NT = 32, NEXT = 4;
  const unsigned tag = lds_tag(lds0);
  const unsigned ring = lds0 + F_RING;
  LAS float* cst = (LAS float*)(lds0 + F_TAB + 2048);
  unsigned long long* gcs = (unsigned long long*)((char*)p.flags + FL_CS) + (xcd * 4 + (idx >> 3)) * (8 * 224);
  ld_a<3>(L, ring, lw, 0);
  ld_a<3>(L, ring, lw, 1);
  const int lane = lane_id();
  const unsigned u = lw * 64 + lane < 224 ? lw * 64 + lane : 223u;
  wait_vm<4>();
  barrier();
  ld_a<3>(L, ring, lw, 2);
  ld_b<3>(L, ring, lw, 2);
#pragma unroll 1
  for (unsigned t = 0; t + 3 < NT; ++t) {
    if (t == 24) {
      const unsigned hf = u >= 112, c = u - hf * 112, wn_ = c >= 56, cc = c - wn_ * 56, blk = cc >> 4;
      const unsigned src = lds0 + (wn_ * 4 + blk) * 768 + (blk == 3 ? 512 + (hf * 8 + (cc & 7)) * 4 : hf * 256 + (cc & 15) * 4);
      const float v = (*(const LAS float*)(src) + *(const LAS float*)(src + 64)) + (*(const LAS float*)(src + 128) + *(const LAS float*)(src + 192));
      asm volatile("global_store_dwordx2 %0, %1, off sc1" ::"v"(gcs + L.cls * 224 + u), "v"(((unsigned long long)tag << 32) | __float_as_uint(v)) : "memory");
    }
    ld_a<3>(L, ring, lw, t + 3);
    barrier();
    ld_b<3>(L, ring, lw, t + 3);
    wait_vm<14>();
    barrier();
  }
  unsigned long long g[8];
#pragma unroll
  for (int i = 0; i < 8; ++i) asm volatile("global_load_dwordx2 %0, %1, off sc1" : "=v"(g[i]) : "v"(gcs + i * 224 + u) : "memory");
  barrier();
  ld_b<NEXT>(N, ring, lw, 0);
  wait_vm<19>();
  barrier();
  barrier();
  ld_b<NEXT>(N, ring, lw, 1);
  wait_vm<16>();
  barrier();
  barrier();
  barrier();
  asm volatile("s_waitcnt vmcnt(8)" : "+v"(g[0]), "+v"(g[1]), "+v"(g[2]), "+v"(g[3]), "+v"(g[4]), "+v"(g[5]), "+v"(g[6]), "+v"(g[7]));
#pragma unroll 1
  for (unsigned spins = 0; spins < (1u << 15); ++spins) {
    unsigned tg = 0u;
#pragma unroll
    for (int i = 0; i < 8; ++i) tg |= (unsigned)(g[i] >> 32) ^ tag;
    if (__all(tg == 0u)) break;
    __builtin_amdgcn_s_sleep(2);
#pragma unroll
    for (int i = 0; i < 8; ++i) asm volatile("global_load_dwordx2 %0, %1, off sc1" : "+v"(g[i]) : "v"(gcs + i * 224 + u) : "memory");
    asm volatile("s_waitcnt vmcnt(0)" : "+v"(g[0]), "+v"(g[1]), "+v"(g[2]), "+v"(g[3]), "+v"(g[4]), "+v"(g[5]), "+v"(g[6]), "+v"(g[7]));
  }
  float s = 0.f;
#pragma unroll
  for (int i = 0; i < 8; ++i) s += __uint_as_float((unsigned)g[i]);
  cst[u] = s;
  wait_lgkm0();
  {
    const char* tp = (const char*)p.wd + ((size_t)blockIdx.x * 224 + (lw * 56 + (lane < 56 ? lane : 55))) * 128;
    touch(lds0, tp);
  }
  barrier();
}

__device__ __forceinline__ void glu_compute(const FArgs& p, unsigned lds0, unsigned w, unsigned xcd, unsigned idx) {
  constexpr int ID = 3, BM = 256, NT = 32, MB = 4, NB = 7;
  const unsigned ring = lds0 + F_RING;
  LAS f32x2* tab = (LAS f32x2*)(lds0 + F_TAB);
  LAS float* cst = (LAS float*)(lds0 + F_TAB + 2048);
  const unsigned tm = idx & 7, tn = xcd * 4 + (idx >> 3), cls = (tm + 2 * (idx >> 3)) & 7;
  const unsigned wm = w & 3, wn = w >> 2;
  const int lane = lane_id(), fr = lane & 15, fq = lane >> 4;
  const unsigned rot = wm == 3 ? 0u : wm;
  unsigned xa = ring + wm * 4096 + lane * 16, xb[3], xb1;
  {
    const int q = fr >> 2, pp = fr & 3, r = (fq >> 1) * 16 + (fq & 1) * 4 + q;
#pragma unroll
    for (int i = 0; i < 3; ++i) xb[i] = ring + F_A + r * 224 + wn * 112 + pp * 8 + ((i + rot) % 3) * 32;
    xb1 = ring + F_A + r * 224 + wn * 112 + 96 + (pp & 1) * 8 + (pp >> 1) * G3_UP;
  }
  float cs[3] = {0.f, 0.f, 0.f};
  bf16x8 fa[MB], fb[NB];
  f32x4 acc[MB][NB];
#pragma unroll
  for (int mb = 0; mb < MB; ++mb)
#pragma unroll
    for (int nb = 0; nb < NB; ++nb) { acc[mb][nb] = (f32x4){0.f, 0.f, 0.f, 0.f}; asm volatile("" : "+v"(acc[mb][nb])); }
  asm volatile("" : "+v"(xa), "+v"(xb[0]), "+v"(xb[1]), "+v"(xb[2]), "+v"(xb1));
  barrier();
  if (wn == 1) {
    const auto rst = OUT_RSRC(p.stats, 8 * S_ * 16);
    f32x2 sv[16];
#pragma unroll
    for (int i = 0; i < 16; ++i) sv[i] = __builtin_bit_cast(f32x2, __builtin_amdgcn_raw_buffer_load_b64(rst, (tm * BM + wm * 64 + lane) * 8, i * S_ * 8, 16));
    SB();
    barrier();
    SB();
    float m = 0.f;
#pragma unroll
    for (int i = 0; i < 16; ++i) m += sv[i][0];
    m *= (1.0f / 16.0f);
    float q = 0.f;
#pragma unroll
    for (int i = 0; i < 16; ++i) {
      const float d = sv[i][0] - m;
      q += sv[i][1] + 64.0f * d * d;
    }
    tab[wm * 64 + lane] = (f32x2){m, q * (1.0f / D_) + EPS};
  }
  wait_lgkm0();
  barrier();
  const unsigned csmask = (0x010101u << cls) | (1u << ((cls + 1) & 7));
  auto reads = [&]() {
    SB();
    fa[0] = lds_b128(xa);
#pragma unroll
    for (int j = 0; j < 7; ++j) {
      const unsigned o = j < 6 ? xb[j % 3] + (j / 3) * G3_UP : xb1;
      fb[j] = lds_tr(o, o + 1792);
    }
#pragma unroll
    for (int mb = 1; mb < MB; ++mb) fa[mb] = lds_b128(xa + mb * 1024);
    SB();
  };
  reads();
#pragma unroll 1
  for (unsigned t = 0; t < NT; ++t) {
    const unsigned delta = ((t & 3) == 3) ? (unsigned)(-3 * F_ST) : (unsigned)F_ST;
    xa += delta; xb[0] += delta; xb[1] += delta; xb[2] += delta; xb1 += delta;
    asm volatile("" : "+v"(xa), "+v"(xb[0]), "+v"(xb[1]), "+v"(xb[2]), "+v"(xb1));
    if (__builtin_expect((csmask >> t) & 1u, 0)) {
#pragma unroll
      for (int j = 0; j < 4; ++j)
#pragma unroll
        for (int i = 0; i < 3; ++i)
          asm volatile("v_dot2c_f32_bf16 %0, %1, %2" : "+v"(cs[i]) : "s"(0x3f803f80u), "v"(__builtin_bit_cast(u32x4, fb[3 * i])[j]));
      asm volatile("s_mov_b32 m0, %3\n\ts_nop 2\n\tds_write_addtid_b32 %0\n\tds_write_addtid_b32 %1 offset:256\n\tds_write_addtid_b32 %2 offset:512" ::"v"(cs[0]),
                   "v"(cs[1]), "v"(cs[2]), "s"(lds0 + w * 768)
                   : "memory");
    }
    wait_lgkm0();
    barrier();
    SB();
#pragma unroll
    for (int mb = 0; mb < MB; ++mb)
#pragma unroll
      for (int i = 0; i < NB; ++i) {
        const int nb = (mb & 1) ? NB - 1 - i : i;
        MFMA(fb[nb], fa[mb], acc[mb][nb]);
      }
    SB();
    barrier();
    reads();
  }
  if (wn == 0) barrier();
  mfma_drain();
  SB();
  const int lane2 = lane_id();
  const int gr = lane2 & 15, gq = lane2 >> 4;
  auto act2 = [](f32x2 a, f32x2 b, float sc, float k) {
    const f32x2 t = a * sc;
    const f32x2 e = {__builtin_amdgcn_exp2f(t.x), __builtin_amdgcn_exp2f(t.y)};
    const f32x2 d = e * k + k;
    const f32x2 r = {__builtin_amdgcn_rcpf(d.x), __builtin_amdgcn_rcpf(d.y)};
    const f32x2 o = (a * b) * r;
    return pk_bf16(o.x, o.y);
  };
  const auto ro = OUT_RSRC(p.gu, S_ * F_ * 2);
  float nm[MB], kk[MB], sc[MB];
#pragma unroll
  for (int mb = 0; mb < MB; ++mb) {
    const f32x2 st = tab[wm * 64 + mb * 16 + gr];
    nm[mb] = -st.x; kk[mb] = st.y; sc[mb] = -1.44269504089f * __builtin_amdgcn_rsqf(st.y);
  }
  const int gch0 = tn * 14 + wn * 7;
  const unsigned obase = (tm * (BM / 16) + wm * MB) * (F_ / 32) * 1024 + gr * 16;
#pragma unroll
  for (int i = 0; i < 3; ++i) {
    const unsigned ib = (i + rot) % 3;
    const f32x4 cg = *(const LAS f32x4*)(cst + wn * 56 + ib * 16 + gq * 4), cu = *(const LAS f32x4*)(cst + 112 + wn * 56 + ib * 16 + gq * 4);
    const unsigned oo = obase + (gq & 1) * (F_ / 32) * 1024 + (gch0 + 2 * ib + (gq >> 1)) * 256;
#pragma unroll
    for (int mp = 0; mp < MB / 2; ++mp) {
      u32x2 pkd[2];
#pragma unroll
      for (int h = 0; h < 2; ++h) {
        const int mb = 2 * mp + h;
        const f32x4 a = cg * nm[mb] + acc[mb][i], b = cu * nm[mb] + acc[mb][3 + i];
        pkd[h] = (u32x2){act2(a.xy, b.xy, sc[mb], kk[mb]), act2(a.zw, b.zw, sc[mb], kk[mb])};
      }
      auto s0 = __builtin_amdgcn_permlane16_swap(pkd[0].x, pkd[1].x, false, false);
      auto s1 = __builtin_amdgcn_permlane16_swap(pkd[0].y, pkd[1].y, false, false);
      __builtin_amdgcn_raw_buffer_store_b128((u32x4){s0[0], s1[0], s0[1], s1[1]}, ro, oo + mp * 2 * (F_ / 32) * 1024, 0, ST_AUX);
    }
  }
  {
    const f32x4 cm = *(const LAS f32x4*)(cst + (gq >> 1) * 112 + wn * 56 + 48 + (gq & 1) * 4);
    u32x2 pkd[MB / 2];
#pragma unroll
    for (int mp = 0; mp < MB / 2; ++mp) {
      const f32x4 v0 = cm * nm[2 * mp] + acc[2 * mp][6], v1 = cm * nm[2 * mp + 1] + acc[2 * mp + 1][6];
      f32x4 a, b;
#pragma unroll
      for (int j = 0; j < 4; ++j) {
        auto s = __builtin_amdgcn_permlane32_swap(__float_as_uint(v0[j]), __float_as_uint(v1[j]), false, false);
        a[j] = __uint_as_float(s[0]); b[j] = __uint_as_float(s[1]);
      }
      const float scs = (gq & 2) ? sc[2 * mp + 1] : sc[2 * mp], ks = (gq & 2) ? kk[2 * mp + 1] : kk[2 * mp];
      pkd[mp] = (u32x2){act2(a.xy, b.xy, scs, ks), act2(a.zw, b.zw, scs, ks)};
    }
    auto s0 = __builtin_amdgcn_permlane16_swap(pkd[0].x, pkd[1].x, false, false);
    auto s1 = __builtin_amdgcn_permlane16_swap(pkd[0].y, pkd[1].y, false, false);
    STORE16(ro, obase + ((gq & 1) * 2 + (gq >> 1)) * (F_ / 32) * 1024 + (gch0 + 6) * 256, ((u32x4){s0[0], s1[0], s0[1], s1[1]}));
  }
}

__global__ __launch_bounds__(768, 3) void k_block(FArgs p) {
  extern __shared__ __attribute__((aligned(16))) char shm[];
  const unsigned lds0 = (unsigned)(size_t)(LAS char*)shm;
  const unsigned w = rfl(threadIdx.x >> 6);
  const unsigned xcd = blockIdx.x & 7, idx = blockIdx.x >> 3;
  char* fl = (char*)p.flags;
  const unsigned tm = (xcd >> 1) * 4 + (idx & 3), kh = xcd & 1, lj = idx >> 2;
  if (w >= 8) {
    const unsigned lw = w - 8;
    Ld LA, LB;
    LA = ld_setup<1>(p, lw, xcd, idx);
    sq_loader<1>(p, LA, LB, lds0, lw, xcd, idx);
    const unsigned tag = lds_tag(lds0);
    LA = ld_setup<3>(p, lw, xcd, idx);
    ld_pin(LA);
    barrier();
    if (lw == 3) poll_tags(fl + FL_P1 + (kh * 8 + lj) * 16 * 128, 16, tag);
    barrier();
    sq_loader<2>(p, LB, LA, lds0, lw, xcd, idx);
    LB = ld_setup<4>(p, lw, xcd, idx);
    ld_pin(LB);
    barrier();
    if (lw == 3) poll_tags(fl + FL_P2 + (idx & 7) * 32 * 128, 32, tag);
    barrier();
    glu_loader(p, LA, LB, lds0, lw, xcd, idx);
    barrier();
    if (lw == 3) poll_tags(fl + FL_P3 + ((tm >> 1) * 2 + kh) * 16 * 128, 16, tag);
    barrier();
    sq_loader<4>(p, LB, LA, lds0, lw, xcd, idx);
    return;
  }
  if (w == 0 && idx < 3) {
    unsigned long long pc;
    asm volatile("s_getpc_b64 %0" : "=s"(pc));
    const unsigned ln = idx * 64 + lane_id();
    touch(lds0, (const char*)pc + (ln < CODE_LINES ? ln : CODE_LINES - 1) * 128);
  }
  if (w == 1 && idx < 20) {
    const char* base = idx < 2 ? (const char*)p.cre : idx < 4 ? (const char*)p.cim : idx < 6 ? (const char*)p.wg : idx < 8 ? (const char*)p.wu : idx < 10 ? (const char*)p.wd : idx < 11 ? (const char*)p.out : (const char*)p.ab;
    const unsigned size = idx < 4 ? S_ * S_ * 2 : idx < 10 ? D_ * F_ * 2 : idx < 11 ? S_ * D_ * 2 : (unsigned)WS_FL;
    const unsigned off = ((idx < 10 ? (idx & 1) : (idx < 11 ? 0 : idx - 11)) * 64 + lane_id()) * 65536;
    touch(lds0, base + (off < size ? off : 0u));
  }
  if (w == 2) {
    const unsigned l = lane_id();
    const unsigned off = l < 5 ? (unsigned)WS_FL + FL_PAIR + (l * 256 + blockIdx.x) * 128 : (l < 19 ? (unsigned)WS_FL + FL_CS + blockIdx.x * 1792 + (l - 5) * 128 : (unsigned)WS_ST + blockIdx.x * 1024 + (l & 7) * 128);
    touch(lds0, (const char*)p.ab + off);
  }
  unsigned tag = 0;
  sq_compute<1>(p, lds0, w, xcd, idx, tag);
  wait_vm<0>();
  barrier();
  if (w == 0 && lane_id() == 0) put_tag(fl + FL_P1 + (tm * 16 + kh * 8 + lj) * 128, tag);
  sq_compute<2>(p, lds0, w, xcd, idx, tag);
  wait_vm<0>();
  barrier();
  if (w == 0 && lane_id() == 0) put_tag(fl + FL_P2 + (tm * 16 + kh * 8 + lj) * 128, tag);
  glu_compute(p, lds0, w, xcd, idx);
  wait_vm<0>();
  barrier();
  if (w == 0 && lane_id() == 0) put_tag(fl + FL_P3 + (((idx & 7) * 2 + (xcd >> 2)) * 16 + (xcd & 3) * 4 + (idx >> 3)) * 128, tag);
  sq_compute<4>(p, lds0, w, xcd, idx, tag);
  if (blockIdx.x == 0 && w == 0 && lane_id() == 0) put_tag(fl + FL_EPOCH, tag);
}
}

extern "C" void kernel_launch(void* const* d_in, const int* in_sizes, int n_in, void* d_out, int out_size, void* d_ws, size_t ws_size,
                              hipStream_t stream) {
  char* ws = (char*)d_ws;
  FArgs f = {d_in[3], d_in[4], d_in[1], d_in[2], d_in[0], d_in[6], d_in[7], d_in[8], ws + WS_AB, ws + WS_H, ws + WS_ST, ws + WS_GU, d_out, ws + WS_XC, ws + WS_FL};
  (void)hipFuncSetAttribute((const void*)k_block, hipFuncAttributeMaxDynamicSharedMemorySize, F_LDS);
  hipLaunchKernelGGL(k_block, dim3(256), dim3(768), F_LDS, stream, f);
}
```

```cpp
#include <hip/hip_runtime.h>
#include <hip/hip_bf16.h>

namespace {
#define LAS __attribute__((address_space(3)))
typedef unsigned short u16;
typedef short s16x4 __attribute__((ext_vector_type(4)));
typedef short s16x8 __attribute__((ext_vector_type(8)));
typedef __bf16 bf16x8 __attribute__((ext_vector_type(8)));
typedef __bf16 bf16x2 __attribute__((ext_vector_type(2)));
typedef float f32x4 __attribute__((ext_vector_type(4)));
typedef float f32x2 __attribute__((ext_vector_type(2)));
typedef unsigned u32x4 __attribute__((ext_vector_type(4)));
typedef unsigned u32x2 __attribute__((ext_vector_type(2)));

constexpr int S_ = 2048, D_ = 1024, F_ = 3584;
constexpr float EPS = 1e-5f;

constexpr size_t WS_AB = 0;
constexpr size_t WS_H = WS_AB + (size_t)D_ * 2 * S_ * 2;
constexpr size_t WS_GU = WS_H + (size_t)S_ * D_ * 2;
constexpr size_t WS_ST = WS_GU + (size_t)S_ * F_ * 2;
constexpr size_t WS_XC = WS_ST + (size_t)S_ * 16 * 8;
constexpr size_t WS_FL = WS_XC + (size_t)2 * 256 * 16384;
constexpr int FL_EPOCH = 0;
constexpr int FL_PAIR = 128;
constexpr int FL_P1 = FL_PAIR + 512 * 128;
constexpr int FL_P2 = FL_P1 + 256 * 128;
constexpr int FL_P3 = FL_P2 + 256 * 128;
constexpr int FL_LN = FL_P3 + 256 * 128;
constexpr int FL_CS = FL_LN + 4096 * 16;

__device__ __forceinline__ unsigned rfl(unsigned v) { return __builtin_amdgcn_readfirstlane(v); }
__device__ __forceinline__ unsigned pk_bf16(float lo, float hi) {
  bf16x2 c = {(__bf16)lo, (__bf16)hi};
  return __builtin_bit_cast(unsigned, c);
}
__device__ __forceinline__ float bf_lo(unsigned v) { return __uint_as_float(v << 16); }
__device__ __forceinline__ float bf_hi(unsigned v) { return __uint_as_float(v & 0xffff0000u); }

__device__ __forceinline__ u32x4 make_rsrc(const void* p, unsigned bytes) {
  unsigned long long a = (unsigned long long)p;
  u32x4 r;
  r.x = (unsigned)a;
  r.y = (unsigned)(a >> 32) & 0xffffu;
  r.z = bytes;
  r.w = 0x00020000u;
  return r;
}
template <int N>
__device__ __forceinline__ void wait_vm() {
  asm volatile("s_waitcnt vmcnt(%0)" ::"i"(N) : "memory");
}
__device__ __forceinline__ void wait_lgkm0() { __builtin_amdgcn_s_waitcnt(0xC07F); }
__device__ __forceinline__ void barrier() {
  asm volatile("" ::: "memory");
  __builtin_amdgcn_s_barrier();
  asm volatile("" ::: "memory");
}
__device__ __forceinline__ bf16x8 lds_b128(unsigned addr) { return *(const LAS bf16x8*)(addr); }
__device__ __forceinline__ bf16x8 lds_tr(unsigned a0, unsigned a1) {
  s16x4 lo = __builtin_amdgcn_ds_read_tr16_b64_v4i16((LAS s16x4*)(a0));
  s16x4 hi = __builtin_amdgcn_ds_read_tr16_b64_v4i16((LAS s16x4*)(a1));
  s16x8 f = __builtin_shufflevector(lo, hi, 0, 1, 2, 3, 4, 5, 6, 7);
  return __builtin_bit_cast(bf16x8, f);
}

#define ST_AUX 16
#define OUT_RSRC(ptr, bytes) __builtin_amdgcn_make_buffer_rsrc((void*)(ptr), 0, (bytes), 0x00020000)
#define STORE16(rs, byteoff, v) __builtin_amdgcn_raw_buffer_store_b128(v, rs, byteoff, 0, ST_AUX)

#define DMA4(M) asm volatile("s_mov_b32 m0, %0\n\ts_nop 4\n\tbuffer_load_dwordx4 %1, %5, %6 offen " M " lds\n\tbuffer_load_dwordx4 %2, %5, %6 offen offset:1024 " M " lds\n\t" \
                 "buffer_load_dwordx4 %3, %5, %6 offen offset:2048 " M " lds\n\tbuffer_load_dwordx4 %4, %5, %6 offen offset:3072 " M " lds" ::"s"(ldsaddr), \
                 "v"(v[0]), "v"(v[1]), "v"(v[2]), "v"(v[3]), "s"(rsrc), "s"(soff) : "memory")
#define DMA3(M) asm volatile("s_mov_b32 m0, %0\n\ts_nop 4\n\tbuffer_load_dwordx4 %1, %4, %5 offen " M " lds\n\tbuffer_load_dwordx4 %2, %4, %5 offen offset:1024 " M " lds\n\t" \
                 "buffer_load_dwordx4 %3, %4, %5 offen offset:2048 " M " lds" ::"s"(ldsaddr), "v"(v[0]), "v"(v[1]), "v"(v[2]), "s"(rsrc), "s"(soff) : "memory")
#define DMAB(M) asm volatile("s_mov_b32 m0, %0\n\ts_waitcnt vmcnt(8)\n\ts_barrier\n\tbuffer_load_dwordx4 %1, %5, %6 offen " M " lds\n\tbuffer_load_dwordx4 %2, %5, %6 offen offset:1024 " M " lds\n\t" \
                 "buffer_load_dwordx4 %3, %5, %6 offen offset:2048 " M " lds\n\tbuffer_load_dwordx4 %4, %5, %6 offen offset:3072 " M " lds" ::"s"(ldsaddr), \
                 "v"(v[0]), "v"(v[1]), "v"(v[2]), "v"(v[3]), "s"(rsrc), "s"(soff) : "memory")
template <int N, int MOD = 0>
__device__ __forceinline__ void dma_grp(unsigned ldsaddr, const unsigned (&v)[4], u32x4 rsrc, unsigned soff) {
  if constexpr (N == 4) { if constexpr (MOD == 1) DMA4("sc1"); else DMA4(""); }
  else if constexpr (N == 3) { if constexpr (MOD == 1) DMA3("sc1"); else DMA3(""); }
  else { if constexpr (MOD == 1) DMAB("sc1"); else DMAB(""); }
}
__device__ __forceinline__ float sum_rows(float x) {
  auto a = __builtin_amdgcn_permlane16_swap(__float_as_uint(x), __float_as_uint(x), false, false);
  x = __uint_as_float(a[0]) + __uint_as_float(a[1]);
  auto b = __builtin_amdgcn_permlane32_swap(__float_as_uint(x), __float_as_uint(x), false, false);
  return __uint_as_float(b[0]) + __uint_as_float(b[1]);
}
template <int CTRL> __device__ __forceinline__ float dpp_add(float x) { return x + __uint_as_float(__builtin_amdgcn_mov_dpp(__float_as_uint(x), CTRL, 0xF, 0xF, true)); }
__device__ __forceinline__ float wave_sum(float x) { return sum_rows(dpp_add<0x140>(dpp_add<0x141>(dpp_add<0x4E>(dpp_add<0xB1>(x))))); }
__device__ __forceinline__ int lane_id() {
  int l;
  asm volatile("v_mbcnt_lo_u32_b32 %0, -1, 0\n\tv_mbcnt_hi_u32_b32 %0, -1, %0" : "=v"(l));
  return l;
}
__device__ __forceinline__ void mfma_drain() { asm volatile("s_nop 15" ::: "memory"); }
template <int NC, int NT>
__device__ __forceinline__ unsigned stage_of(unsigned n, unsigned c) {
  const unsigned t = ((n & (NC - 1)) == c) ? n + NC : n;
  return rfl(t >= NT ? t - NT : t);
}
#define SB() __builtin_amdgcn_sched_barrier(0)
#define MFMA(b, a, c) asm volatile("v_mfma_f32_16x16x32_bf16 %0, %1, %2, %0" : "+v"(c) : "v"(b), "v"(a))

struct FArgs {
  const void *dre, *dim, *cre, *cim, *x, *wg, *wu, *wd;
  void *ab, *h, *stats, *gu, *out, *xch, *flags;
};
constexpr unsigned CODE_LINES = 156;
constexpr int F_RING = 6144, F_ST = 32768, F_TAB = F_RING + 4 * F_ST, F_TAG = F_TAB + 4096, F_SINK = F_TAB + 4160, F_LDS = F_SINK + 256, F_A = 16384, G3_UP = 7168 + 16;

__device__ __forceinline__ void poll_tags(const void* line, int n, unsigned tag) {
  const int l = lane_id();
  const unsigned* q = (const unsigned*)line + (l < n ? l : 0) * 32;
  unsigned spins = 0;
  while (!__all(__hip_atomic_load(q, __ATOMIC_RELAXED, __HIP_MEMORY_SCOPE_AGENT) == tag) && ++spins < (1u << 17)) __builtin_amdgcn_s_sleep(1);
}
__device__ __forceinline__ void put_tag(void* word, unsigned tag) { __hip_atomic_store((unsigned*)word, tag, __ATOMIC_RELAXED, __HIP_MEMORY_SCOPE_AGENT); }
__device__ __forceinline__ unsigned lds_tag(unsigned lds0) { return rfl(*(volatile LAS unsigned*)(lds0 + F_TAG)); }
__device__ __forceinline__ void touch(unsigned lds0, const void* ptr) {
  asm volatile("s_mov_b32 m0, %1\n\ts_nop 0\n\tglobal_load_lds_dword %0, off sc1" ::"v"(ptr), "s"(lds0 + F_SINK) : "memory");
}
template <int ID> constexpr int nt_of() { return ID == 1 ? 16 : (ID == 4 ? 28 : 32); }
template <int ID> constexpr int nc_of() { return (ID == 1 || ID == 4) ? 4 : 8; }
template <int ID> constexpr int mod_a() { return ID > 2; }
template <int ID> constexpr int mod_b() { return ID == 2; }
struct Ld {
  u32x4 qa, qb;
  unsigned va[4], vb[4], cls;
};
template <int ID>
__device__ __forceinline__ Ld ld_setup(const FArgs& p, unsigned lw, unsigned xcd, unsigned idx) {
  const int lane = lane_id();
  Ld L;
  if constexpr (ID == 3) {
    const unsigned tm = idx & 7, tn = xcd * 4 + (idx >> 3);
    L.qa = make_rsrc((const u16*)p.h + (size_t)tm * 256 * D_, 256 * D_ * 2);
    L.qb = make_rsrc(lw < 2 ? p.wg : p.wu, (unsigned)D_ * F_ * 2);
#pragma unroll
    for (int i = 0; i < 4; ++i) {
      L.va[i] = (lw * 4 + i) * (D_ / 32) * 1024 + lane * 16 - i * 1024;
      const int c = ((lw & 1) * 4 + i) * 64 + lane, r = c / 14, pos = c % 14;
      const int k = (r & 0x13) | ((r & 4) << 1) | ((r & 8) >> 1);
      L.vb[i] = k * F_ * 2 + tn * 224 + pos * 16 - i * 1024;
    }
    L.cls = (tm + 2 * (idx >> 3)) & 7;
  } else {
    const unsigned li = idx & 3, lj = idx >> 2, kh = xcd & 1, tm = (xcd >> 1) * 4 + li, tn = lj;
    if constexpr (ID == 1) {
      L.qa = make_rsrc((const u16*)(tm < 8 ? p.dre : p.dim) + (size_t)(tm & 7) * 128 * D_, 128 * D_ * 2);
      L.qb = make_rsrc((const u16*)p.x + (size_t)(kh * 8 + lj) * 128 * D_, 128 * D_ * 2);
    } else if constexpr (ID == 2) {
      L.qa = make_rsrc((const u16*)(kh ? p.cim : p.cre) + (size_t)tm * 128 * S_, 128 * S_ * 2);
      L.qb = make_rsrc((const u16*)p.ab + (size_t)tn * 128 * 4096 + kh * 64 * 512, 128 * 4096 * 2 - kh * 64 * 1024);
    } else {
      L.qa = make_rsrc((const u16*)p.gu + (size_t)tm * 128 * F_ + kh * 56 * 512, 128 * F_ * 2 - kh * 56 * 1024);
      L.qb = make_rsrc((const u16*)p.wd + (size_t)kh * 1792 * D_, 1792 * D_ * 2);
    }
#pragma unroll
    for (int i = 0; i < 4; ++i) {
      const int pc = lw * 4 + i;
      if constexpr (ID == 1) {
        const int row = pc * 8 + (lane >> 3);
        L.va[i] = L.vb[i] = row * D_ * 2 + (((lane & 7) ^ ((row >> 1) & 7)) << 4) - i * 1024;
      } else if constexpr (ID == 2) {
        const int row = pc * 8 + (lane >> 3);
        L.va[i] = row * S_ * 2 + (((lane & 7) ^ ((row >> 1) & 7)) << 4) - i * 1024;
        L.vb[i] = ((pc >> 1) * 128 + (pc & 1)) * 1024 + lane * 16 - i * 1024;
      } else {
        const int k = pc * 4 + (lane >> 4), f = (k & 3) | (((k >> 3) & 1) << 2);
        L.va[i] = ((pc >> 1) * (F_ / 32) + (pc & 1)) * 1024 + lane * 16 - i * 1024;
        L.vb[i] = k * D_ * 2 + tn * 256 + (((lane & 15) ^ (f << 1)) << 4) - i * 1024;
      }
    }
    L.cls = (nc_of<ID>() == 8) ? ((lj + 2 * li) & 7) : ((lj + li) & 3);
  }
  return L;
}
__device__ __forceinline__ void ld_pin(Ld& L) {
  asm volatile("" : "+v"(L.va[0]), "+v"(L.va[1]), "+v"(L.va[2]), "+v"(L.va[3]), "+v"(L.vb[0]), "+v"(L.vb[1]), "+v"(L.vb[2]), "+v"(L.vb[3]));
}
template <int ID>
__device__ __forceinline__ void ld_a(const Ld& L, unsigned ring, unsigned lw, unsigned n) {
  const unsigned st = stage_of<nc_of<ID>(), nt_of<ID>()>(n, L.cls);
  dma_grp<4, mod_a<ID>()>(ring + (n & 3) * F_ST + lw * 4096, L.va, L.qa, st * (ID <= 2 ? 128 : (ID == 3 ? 1024 : 2048)));
}
template <int ID>
__device__ __forceinline__ void ld_b(const Ld& L, unsigned ring, unsigned lw, unsigned n) {
  const unsigned st = stage_of<nc_of<ID>(), nt_of<ID>()>(n, L.cls);
  const unsigned dst = ring + (n & 3) * F_ST + F_A;
  if constexpr (ID == 3) {
    const unsigned d = dst + (lw >> 1) * G3_UP + (lw & 1) * 4096;
    if (lw & 1) dma_grp<3, mod_b<ID>()>(d, L.vb, L.qb, st * (32 * F_ * 2));
    else dma_grp<4, mod_b<ID>()>(d, L.vb, L.qb, st * (32 * F_ * 2));
  } else {
    dma_grp<4, mod_b<ID>()>(dst + lw * 4096, L.vb, L.qb, st * (ID == 1 ? 128 : (ID == 2 ? 2048 : 64 * D_ * 2)));
  }
}

template <int ID> __device__ __forceinline__ void ld_const(const Ld& L, unsigned ring, unsigned lw, unsigned n) { if constexpr (ID == 2) ld_a<ID>(L, ring, lw, n); else ld_b<ID>(L, ring, lw, n); }
template <int ID> __device__ __forceinline__ void ld_dep(const Ld& L, unsigned ring, unsigned lw, unsigned n) { if constexpr (ID == 2) ld_b<ID>(L, ring, lw, n); else ld_a<ID>(L, ring, lw, n); }

template <int ID>
__device__ __forceinline__ void sq_loader(const FArgs& p, const Ld& L, Ld& N, unsigned lds0, unsigned lw, unsigned xcd, unsigned idx) {
  constexpr int NT = nt_of<ID>(), NEXT = ID == 4 ? 0 : ID + 1;
  const unsigned ring = lds0 + F_RING;
  if constexpr (ID == 1) {
    ld_a<ID>(L, ring, lw, 0); ld_b<ID>(L, ring, lw, 0);
    N = ld_setup<NEXT>(p, lw, xcd, idx); ld_pin(N);
    ld_a<ID>(L, ring, lw, 1); ld_b<ID>(L, ring, lw, 1);
  } else {
    ld_dep<ID>(L, ring, lw, 0); ld_dep<ID>(L, ring, lw, 1);
  }
  constexpr unsigned KA = ID <= 2 ? 128 : 2048, KB = ID == 1 ? 128 : (ID == 2 ? 2048 : 64 * D_ * 2);
  unsigned sa, sb, ma;
  auto prep = [&](unsigned n) {
    const unsigned st = stage_of<nc_of<ID>(), NT>(n, L.cls);
    sa = st * KA; sb = st * KB; ma = ring + (n & 3) * F_ST + lw * 4096;
    asm volatile("" : "+s"(sa), "+s"(sb), "+s"(ma));
  };
  auto bar_a = [&]() {
    dma_grp<5, mod_a<ID>()>(ma, L.va, L.qa, sa);
  };
  prep(2);
  wait_vm<(ID == 1 ? 8 : 4)>();
  bar_a();
  unsigned mb_ = ma + F_A, sb_ = sb;
  prep(3);
#pragma unroll 1
  for (unsigned n = 0; n + 3 < NT; ++n) {
    dma_grp<4, mod_b<ID>()>(mb_, L.vb, L.qb, sb_);
    bar_a();
    mb_ = ma + F_A; sb_ = sb;
    asm volatile("" : "+s"(mb_), "+s"(sb_));
    prep(n + 4);
  }
  dma_grp<4, mod_b<ID>()>(mb_, L.vb, L.qb, sb_);
  wait_vm<8>();
  barrier();
  const int lane = lane_id();
  u32x2 l0, l1;
  const char* lp = (const char*)p.flags + FL_LN + (lw < 2 ? (((xcd & 1) * 8 + (idx >> 2)) * 128 + lw * 64 + lane) : (2048 + ((xcd >> 1) * 4 + (idx & 3)) * 128 + (lw - 2) * 64 + lane)) * 16;
  if constexpr (ID == 1) asm volatile("global_load_dwordx2 %0, %2, off sc1\n\tglobal_load_dwordx2 %1, %2, off offset:8 sc1" : "=&v"(l0), "=&v"(l1) : "v"(lp) : "memory");
  if constexpr (NEXT != 0) ld_const<NEXT>(N, ring, lw, 0);
  if constexpr (ID == 1) wait_vm<6>();
  else if constexpr (ID == 2) { if (lw & 1) wait_vm<3>(); else wait_vm<4>(); }
  else wait_vm<0>();
  barrier();
  if constexpr (NEXT != 0) ld_const<NEXT>(N, ring, lw, 1);
  if constexpr (ID == 1) {
    if (lw < 2) {
      const char* tp = (const char*)((xcd & 1) ? p.cim : p.cre) + (size_t)((xcd >> 1) * 512 + idx * 16 + lw * 8 + (lane >> 3)) * (S_ * 2) + (lane & 7) * 128;
      touch(lds0, tp);
    }
  }
  if constexpr (ID == 2) {
    if (lw < 2) {
      const char* tp = (const char*)((lw & 1) ? p.wu : p.wg) + (size_t)(idx * 8 + (lane >> 3)) * (F_ * 2) + xcd * 896 + ((lane & 7) < 7 ? (lane & 7) : 6) * 128;
      touch(lds0, tp);
    }
  }
  barrier();
  const unsigned tag = lds_tag(lds0);
  if constexpr (ID == 1) {
    if (lw < 2) wait_vm<9>(); else wait_vm<8>();
    asm volatile("" : "+v"(l0), "+v"(l1));
#pragma unroll 1
    for (unsigned spins = 0; spins < (1u << 15) && !__all(l0.y == tag && l1.y == tag); ++spins) {
      __builtin_amdgcn_s_sleep(2);
      asm volatile("global_load_dwordx2 %0, %2, off sc1\n\tglobal_load_dwordx2 %1, %2, off offset:8 sc1\n\ts_waitcnt vmcnt(0)" : "=&v"(l0), "=&v"(l1) : "v"(lp) : "memory");
    }
    LAS unsigned* tb = (LAS unsigned*)(lds0 + F_TAB);
    tb[lw * 64 + lane + (lw < 2 ? 0 : 128)] = l0.x;
    if (lw < 2) tb[128 + lw * 64 + lane] = l1.x;
    wait_lgkm0();
  }
  barrier();
  barrier();
  if constexpr (ID != 1) {
    barrier();
    if (lw == 3) poll_tags((char*)p.flags + FL_PAIR + ((ID == 2 ? 0 : 256) + (blockIdx.x ^ 1)) * 128, 1, tag);
    barrier();
  }
}

template <int ID>
__device__ __forceinline__ void sq_compute(const FArgs& p, unsigned lds0, unsigned w, unsigned xcd, unsigned idx, unsigned& tag) {
  constexpr int NT = nt_of<ID>();
  u32x4 xv[2], wv[2];
  unsigned tv = 0;
  const unsigned r0 = blockIdx.x * 8 + w;
  char* fl = (char*)p.flags;
  if constexpr (ID == 1) {
    const int lane = lane_id();
    const u16* xp = (const u16*)p.x + (size_t)r0 * D_ + lane * 8;
    const u16* wp = (const u16*)(r0 < 1024 ? p.dre : p.dim) + (size_t)(r0 & 1023) * D_ + lane * 8;
#pragma unroll
    for (int i = 0; i < 2; ++i) xv[i] = *(const u32x4*)(xp + i * 512);
#pragma unroll
    for (int i = 0; i < 2; ++i) wv[i] = *(const u32x4*)(wp + i * 512);
    tv = __hip_atomic_load((const unsigned*)(fl + FL_EPOCH), __ATOMIC_RELAXED, __HIP_MEMORY_SCOPE_AGENT);
    SB();
  }
  const unsigned ring = lds0 + F_RING;
  const unsigned g = w >> 2, wm = (w >> 1) & 1, wx = w & 1;
  const unsigned kh = xcd & 1, tm = (xcd >> 1) * 4 + (idx & 3), tn = idx >> 2;
  u32x4 res[2];
  unsigned rdA[8], rdB[4], hoff = 0;
  const unsigned rotA = (ID == 1) ? 2 * g : 2 * g + wx;
  {
    const int lane = lane_id(), fr = lane & 15, fq = lane >> 4, chunk = ((fq & 1) << 1) | (fq >> 1);
    const int row = tm * 128 + wm * 64 + rotA * 16 + fr, col = tn * 128 + kh * 64 + chunk * 8;
    if constexpr (ID == 2) {
#pragma unroll
      for (int c = 0; c < 2; ++c) res[c] = *(const u32x4*)((const u16*)p.x + (size_t)row * D_ + col + (c ^ wx) * 32);
    } else if constexpr (ID == 4) {
      hoff = ((row >> 4) * (D_ / 32) + (col >> 5)) * 1024 + (chunk * 16 + fr) * 16;
    }
    const unsigned rotB = (ID == 1) ? 0u : 2 * kh, base = ring + g * F_ST;
#pragma unroll
    for (int i = 0; i < 4; ++i) {
      const unsigned kk = i >> 1, hf = (i & 1) * 2;
      if constexpr (ID == 1) {
        rdA[i] = base + (wm * 64 + (hf ^ rotA) * 16 + fr) * 128 + (((kk * 4 + fq) ^ (fr >> 1)) << 4);
        rdB[i] = base + F_A + (wx * 64 + fr) * 128 + ((((i & 1) * 4 + fq) ^ (fr >> 1)) << 4);
      } else if constexpr (ID == 2) {
        rdA[i] = base + (wm * 64 + (i ^ rotA) * 16 + fr) * 128 + ((fq ^ (fr >> 1)) << 4);
        rdA[4 + i] = base + (wm * 64 + (i ^ rotA) * 16 + fr) * 128 + (((4 + fq) ^ (fr >> 1)) << 4);
        rdB[i] = base + F_A + ((hf ^ rotB) * 2 + wx * 2) * 2048 + lane * 16;
      } else {
        const int q = fr >> 2, pp = fr & 3, f = q | ((fq & 1) << 2);
        rdA[i] = base + wm * 8192 + (i ^ rotA) * 2048 + lane * 16;
        rdB[i] = base + F_A + (8 * fq + q) * 256 + ((((((i ^ rotB) & 2) + wx) * 2 + (i & 1)) ^ f) << 5) + pp * 8;
      }
    }
  }
  constexpr int NVA = (ID == 2) ? 8 : 4, NVB = (ID <= 2) ? 2 : 4;
  bf16x8 fa[2][4], fb[2][4];
  f32x4 acc[4][4];
#pragma unroll
  for (int i = 0; i < 16; ++i) { acc[i >> 2][i & 3] = (f32x4){0.f, 0.f, 0.f, 0.f}; asm volatile("" : "+v"(acc[i >> 2][i & 3])); }
  auto reads = [&]() {
    SB();
#pragma unroll
    for (int s = 0; s < 2; ++s)
#pragma unroll
      for (int i = 0; i < 4; ++i) {
        if constexpr (ID == 1) {
          fb[s][i] = lds_b128(rdB[s] + i * 2048);
          fa[s][i] = lds_b128(rdA[s * 2 + (i >> 1)] + (i & 1) * 2048);
        } else if constexpr (ID == 2) {
          fb[s][i] = lds_b128(rdB[i >> 1] + (i & 1) * 2048 + s * 1024);
          fa[s][i] = lds_b128(rdA[s * 4 + i]);
        } else {
          fb[s][i] = lds_tr(rdB[i] + s * 8192, rdB[i] + s * 8192 + 1024);
          fa[s][i] = lds_b128(rdA[i] + s * 1024);
        }
      }
    SB();
  };
#pragma unroll
  for (int j = 0; j < NVA; ++j) asm volatile("" : "+v"(rdA[j]));
#pragma unroll
  for (int j = 0; j < NVB; ++j) asm volatile("" : "+v"(rdB[j]));
  if constexpr (ID == 1) {
    SB();
    asm volatile("" : "+v"(tv));
    tv = tv * 0x9E3779B1u + 0x7F4A7C15u;
    const int lane = lane_id();
    float s = 0.f, t = 0.f;
#pragma unroll
    for (int j = 0; j < 8; ++j) s += bf_lo(xv[j >> 2][j & 3]) + bf_hi(xv[j >> 2][j & 3]);
    const float m = wave_sum(s) * (1.0f / D_);
    float q = 0.f;
#pragma unroll
    for (int j = 0; j < 8; ++j) {
      const float a = bf_lo(xv[j >> 2][j & 3]) - m, b = bf_hi(xv[j >> 2][j & 3]) - m;
      q += a * a + b * b;
      t += bf_lo(wv[j >> 2][j & 3]) + bf_hi(wv[j >> 2][j & 3]);
    }
    const float rs = __builtin_amdgcn_rsqf(wave_sum(q) * (1.0f / D_) + EPS);
    t = wave_sum(t);
    const float val = (lane & 2) ? t : ((lane & 1) ? rs : m);
    if (lane < 4)
      __hip_atomic_store((unsigned long long*)(fl + FL_LN + ((lane >> 1) * 2048 + r0) * 16 + (lane & 1) * 8), ((unsigned long long)tv << 32) | __float_as_uint(val),
                         __ATOMIC_RELAXED, __HIP_MEMORY_SCOPE_AGENT);
    tag = rfl(tv);
    if (w == 0) { *(volatile LAS unsigned*)(lds0 + F_TAG) = tag; wait_lgkm0(); }
  }
  if constexpr (ID != 1) {
    barrier();
  }
  if constexpr (ID == 4) {
    const auto rh = OUT_RSRC(p.h, S_ * D_ * 2);
#pragma unroll
    for (int c = 0; c < 2; ++c) res[c] = __builtin_amdgcn_raw_buffer_load_b128(rh, hoff, (c ^ wx) * 1024, 16);
  }
  barrier();
  if (g == 1) barrier();
  reads();
#pragma unroll 1
  for (unsigned i = 0; i < NT / 2; ++i) {
    const unsigned delta = (i & 1) ? (unsigned)(-2 * F_ST) : (unsigned)(2 * F_ST);
#pragma unroll
    for (int j = 0; j < NVA; ++j) { rdA[j] += delta; asm volatile("" : "+v"(rdA[j])); }
#pragma unroll
    for (int j = 0; j < NVB; ++j) { rdB[j] += delta; asm volatile("" : "+v"(rdB[j])); }
    wait_lgkm0();
    barrier();
    SB();
#pragma unroll
    for (int m = 0; m < 32; ++m) {
      const int n = (m & 4) ? 3 - (m & 3) : (m & 3);
      MFMA(fb[m >> 4][n], fa[m >> 4][(m >> 2) & 3], acc[(m >> 2) & 3][n]);
    }
    SB();
    barrier();
    if (i + 1 < NT / 2) reads();
  }
  mfma_drain();
  SB();
  const int lane = lane_id(), fr = lane & 15, fq = lane >> 4, chunk = ((fq & 1) << 1) | (fq >> 1);
  if constexpr (ID == 1) {
    const unsigned wb = ring + 2 * F_ST + (w ^ 4) * 8192 + lane * 16, rb = ring + 2 * F_ST + w * 8192 + lane * 16;
#pragma unroll
    for (int i = 0; i < 8; ++i) *(LAS f32x4*)(wb + i * 1024) = acc[2 + (i >> 2)][i & 3];
    if (g == 0) barrier();
    wait_lgkm0();
    barrier();
#pragma unroll
    for (int i = 0; i < 8; ++i) acc[i >> 2][i & 3] += *(const LAS f32x4*)(rb + i * 1024);
  }
  if constexpr (ID == 1) {
    const LAS float* tb = (const LAS float*)(lds0 + F_TAB);
    const float sg = tm < 8 ? 1.f : -1.f;
    const auto ro = OUT_RSRC(p.ab, D_ * 4096 * 2);
    f32x4 mean[4], rstd[4];
#pragma unroll
    for (int j = 0; j < 4; ++j) {
      mean[j] = *(const LAS f32x4*)(tb + wx * 64 + j * 16 + fq * 4);
      rstd[j] = *(const LAS f32x4*)(tb + 128 + wx * 64 + j * 16 + fq * 4) * sg;
    }
#pragma unroll
    for (int mb = 0; mb < 2; ++mb) {
      const float wsm = tb[256 + wm * 64 + g * 32 + mb * 16 + fr];
#pragma unroll
      for (int np = 0; np < 2; ++np) {
        const f32x4 x0 = (acc[mb][2 * np] - mean[2 * np] * wsm) * rstd[2 * np], x1 = (acc[mb][2 * np + 1] - mean[2 * np + 1] * wsm) * rstd[2 * np + 1];
        auto s0 = __builtin_amdgcn_permlane16_swap(pk_bf16(x0[0], x0[1]), pk_bf16(x1[0], x1[1]), false, false);
        auto s1 = __builtin_amdgcn_permlane16_swap(pk_bf16(x0[2], x0[3]), pk_bf16(x1[2], x1[3]), false, false);
        const int rblk = (tm & 7) * 8 + wm * 4 + g * 2 + mb;
        const int kblk = (tm >> 3) * 64 + (kh * 8 + tn) * 4 + wx * 2 + np;
        STORE16(ro, (rblk * 128 + kblk) * 1024 + (chunk * 16 + fr) * 16, ((u32x4){s0[0], s1[0], s0[1], s1[1]}));
      }
    }
    return;
  }
  const auto rs = OUT_RSRC((char*)p.xch + (ID == 2 ? 0 : 256 * 16384), 256 * 16384);
  const unsigned sreg = ring + ((w >> 1) & 1) * F_ST + (w & 1) * 8192 + lane * 16, kreg = ring + 2 * F_ST + lane * 16;
  auto put_keep = [&]() {
#pragma unroll
    for (int j = 1; j < 4; ++j) {
      const unsigned d = kreg + ((wm * 4 + (j ^ rotA)) * 3 + (j - 1)) * 2048;
      *(LAS f32x4*)(d) = acc[j][0];
      *(LAS f32x4*)(d + 1024) = acc[j][1];
    }
  };
  if (g == 0) {
#pragma unroll
    for (int i = 0; i < 8; ++i) *(LAS f32x4*)(sreg + i * 1024) = acc[i >> 1][2 + (i & 1)];
    put_keep();
    wait_lgkm0();
    barrier();
  } else {
#pragma unroll
    for (int j = 0; j < 4; ++j) {
      const f32x4 x0 = acc[j][2] + *(const LAS f32x4*)(sreg + (j ^ 2) * 2048), x1 = acc[j][3] + *(const LAS f32x4*)(sreg + (j ^ 2) * 2048 + 1024);
      auto s0 = __builtin_amdgcn_permlane16_swap(pk_bf16(x0[0], x0[1]), pk_bf16(x1[0], x1[1]), false, false);
      auto s1 = __builtin_amdgcn_permlane16_swap(pk_bf16(x0[2], x0[3]), pk_bf16(x1[2], x1[3]), false, false);
      __builtin_amdgcn_raw_buffer_store_b128((u32x4){s0[0], s1[0], s0[1], s1[1]}, rs, lane * 16 + blockIdx.x * 16384 + ((wm * 4 + (j ^ rotA)) * 2 + wx) * 1024, 0, 16);
    }
    SB();
    put_keep();
    wait_lgkm0();
  }
  barrier();
  f32x4 y[2][2];
  {
    const unsigned kb = kreg + (wm * 4 + rotA) * 3 * 2048;
    f32x4 t[2][2];
#pragma unroll
    for (int i = 0; i < 2; ++i) {
      t[0][i] = acc[0][i] + *(const LAS f32x4*)(kb + 2048 + i * 1024);
      t[1][i] = *(const LAS f32x4*)(kb + i * 1024) + *(const LAS f32x4*)(kb + 4096 + i * 1024);
    }
#pragma unroll
    for (int c = 0; c < 2; ++c)
#pragma unroll
      for (int j = 0; j < 4; ++j) {
        auto s = __builtin_amdgcn_permlane16_swap(__float_as_uint(t[c][0][j]), __float_as_uint(t[c][1][j]), false, false);
        y[c][0][j] = __uint_as_float(s[0]) + ((j & 1) ? bf_hi(res[c][j >> 1]) : bf_lo(res[c][j >> 1]));
        y[c][1][j] = __uint_as_float(s[1]) + ((j & 1) ? bf_hi(res[c][2 + (j >> 1)]) : bf_lo(res[c][2 + (j >> 1)]));
      }
  }
  const int row0 = tm * 128 + wm * 64 + rotA * 16 + fr, col = tn * 128 + kh * 64 + chunk * 8;
  unsigned optr = (ID == 2) ? ((row0 >> 4) * (D_ / 32) + (col >> 5)) * 1024 + (chunk * 16 + fr) * 16 : ((row0 & ~8) * D_ + col + ((fr >> 3) ^ wx) * 32) * 2;
  const auto ro = OUT_RSRC(ID == 2 ? p.h : p.out, S_ * D_ * 2);
  asm volatile("" : "+v"(y[0][0]), "+v"(y[0][1]), "+v"(y[1][0]), "+v"(y[1][1]), "+v"(optr));
  wait_vm<0>();
  barrier();
  if (w == 0 && lane == 0) put_tag((char*)p.flags + FL_PAIR + ((ID == 2 ? 0 : 256) + blockIdx.x) * 128, tag);
  barrier();
  u32x4 in[2], v[2];
#pragma unroll
  for (int c = 0; c < 2; ++c) in[c] = __builtin_amdgcn_raw_buffer_load_b128(rs, lane * 16, (blockIdx.x ^ 1) * 16384 + ((wm * 4 + rotA) * 2 + (c ^ wx)) * 1024, 16);
#pragma unroll
  for (int c = 0; c < 2; ++c) {
    const f32x4 a = y[c][0] + (f32x4){bf_lo(in[c][0]), bf_hi(in[c][0]), bf_lo(in[c][1]), bf_hi(in[c][1])};
    const f32x4 b = y[c][1] + (f32x4){bf_lo(in[c][2]), bf_hi(in[c][2]), bf_lo(in[c][3]), bf_hi(in[c][3])};
    v[c] = (u32x4){pk_bf16(a[0], a[1]), pk_bf16(a[2], a[3]), pk_bf16(b[0], b[1]), pk_bf16(b[2], b[3])};
    if constexpr (ID == 2) STORE16(ro, optr + (c ^ wx) * 1024, v[c]);
  }
  if constexpr (ID == 4) {
    u32x4 sa, sb;
#pragma unroll
    for (int j = 0; j < 4; ++j) {
      sa[j] = __builtin_amdgcn_update_dpp(v[0][j], v[1][j], 0x128, 0xF, 0xC, false);
      sb[j] = __builtin_amdgcn_update_dpp(v[1][j], v[0][j], 0x128, 0xF, 0x3, false);
    }
    STORE16(ro, optr, sa);
    STORE16(ro, optr + 8 * D_ * 2, sb);
  }
  SB();
  if constexpr (ID == 2) {
    float s = 0.f;
#pragma unroll
    for (int j = 0; j < 8; ++j) s += bf_lo(v[j >> 2][j & 3]) + bf_hi(v[j >> 2][j & 3]);
    const float m = sum_rows(s) * (1.0f / 64.0f);
    float q = 0.f;
#pragma unroll
    for (int j = 0; j < 8; ++j) {
      const float a = bf_lo(v[j >> 2][j & 3]) - m, b = bf_hi(v[j >> 2][j & 3]) - m;
      q += a * a + b * b;
    }
    const f32x2 st = {m, sum_rows(q)};
    if (fq == 0)
      __hip_atomic_store((unsigned long long*)p.stats + (size_t)(tn * 2 + kh) * S_ + row0, __builtin_bit_cast(unsigned long long, st), __ATOMIC_RELAXED, __HIP_MEMORY_SCOPE_AGENT);
  }
  SB();
}

__device__ __forceinline__ void glu_loader(const FArgs& p, const Ld& L, Ld& N, unsigned lds0, unsigned lw, unsigned xcd, unsigned idx) {
  constexpr int NT = 32, NEXT = 4;
  const unsigned tag = lds_tag(lds0);
  const unsigned ring = lds0 + F_RING;
  LAS float* cst = (LAS float*)(lds0 + F_TAB + 2048);
  unsigned long long* gcs = (unsigned long long*)((char*)p.flags + FL_CS) + (xcd * 4 + (idx >> 3)) * (8 * 224);
  ld_a<3>(L, ring, lw, 0);
  ld_a<3>(L, ring, lw, 1);
  const int lane = lane_id();
  const unsigned u = lw * 64 + lane < 224 ? lw * 64 + lane : 223u;
  wait_vm<4>();
  barrier();
  ld_a<3>(L, ring, lw, 2);
  ld_b<3>(L, ring, lw, 2);
#pragma unroll 1
  for (unsigned t = 0; t + 3 < NT; ++t) {
    if (t == 24) {
      const unsigned hf = u >= 112, c = u - hf * 112, wn_ = c >= 56, cc = c - wn_ * 56, blk = cc >> 4;
      const unsigned src = lds0 + (wn_ * 4 + blk) * 768 + (blk == 3 ? 512 + (hf * 8 + (cc & 7)) * 4 : hf * 256 + (cc & 15) * 4);
      const float v = (*(const LAS float*)(src) + *(const LAS float*)(src + 64)) + (*(const LAS float*)(src + 128) + *(const LAS float*)(src + 192));
      asm volatile("global_store_dwordx2 %0, %1, off sc1" ::"v"(gcs + L.cls * 224 + u), "v"(((unsigned long long)tag << 32) | __float_as_uint(v)) : "memory");
    }
    ld_a<3>(L, ring, lw, t + 3);
    barrier();
    ld_b<3>(L, ring, lw, t + 3);
    wait_vm<14>();
    barrier();
  }
  unsigned long long g[8];
#pragma unroll
  for (int i = 0; i < 8; ++i) asm volatile("global_load_dwordx2 %0, %1, off sc1" : "=v"(g[i]) : "v"(gcs + i * 224 + u) : "memory");
  barrier();
  ld_b<NEXT>(N, ring, lw, 0);
  wait_vm<19>();
  barrier();
  ld_b<NEXT>(N, ring, lw, 1);
  {
    const char* tp = (const char*)p.wd + ((size_t)blockIdx.x * 224 + (lw * 56 + (lane < 56 ? lane : 55))) * 128;
    touch(lds0, tp);
  }
  barrier();
  wait_vm<17>();
  barrier();
  barrier();
  barrier();
  asm volatile("s_waitcnt vmcnt(9)" : "+v"(g[0]), "+v"(g[1]), "+v"(g[2]), "+v"(g[3]), "+v"(g[4]), "+v"(g[5]), "+v"(g[6]), "+v"(g[7]));
#pragma unroll 1
  for (unsigned spins = 0; spins < (1u << 15); ++spins) {
    unsigned tg = 0u;
#pragma unroll
    for (int i = 0; i < 8; ++i) tg |= (unsigned)(g[i] >> 32) ^ tag;
    if (__all(tg == 0u)) break;
    __builtin_amdgcn_s_sleep(2);
#pragma unroll
    for (int i = 0; i < 8; ++i) asm volatile("global_load_dwordx2 %0, %1, off sc1" : "+v"(g[i]) : "v"(gcs + i * 224 + u) : "memory");
    asm volatile("s_waitcnt vmcnt(0)" : "+v"(g[0]), "+v"(g[1]), "+v"(g[2]), "+v"(g[3]), "+v"(g[4]), "+v"(g[5]), "+v"(g[6]), "+v"(g[7]));
  }
  float s = 0.f;
#pragma unroll
  for (int i = 0; i < 8; ++i) s += __uint_as_float((unsigned)g[i]);
  cst[u] = s;
  wait_lgkm0();
  barrier();
}

__device__ __forceinline__ void glu_compute(const FArgs& p, unsigned lds0, unsigned w, unsigned xcd, unsigned idx) {
  constexpr int BM = 256, NT = 32, MB = 4, NB = 7;
  const unsigned ring = lds0 + F_RING;
  LAS f32x2* tab = (LAS f32x2*)(lds0 + F_TAB);
  LAS float* cst = (LAS float*)(lds0 + F_TAB + 2048);
  const unsigned tm = idx & 7, tn = xcd * 4 + (idx >> 3), cls = (tm + 2 * (idx >> 3)) & 7;
  const unsigned wm = w & 3, wn = w >> 2;
  const int lane = lane_id(), fr = lane & 15, fq = lane >> 4;
  const unsigned rot = wm == 3 ? 0u : wm;
  unsigned xa = ring + wm * 4096 + lane * 16, xb[3], xb1;
  {
    const int q = fr >> 2, pp = fr & 3, r = (fq >> 1) * 16 + (fq & 1) * 4 + q;
#pragma unroll
    for (int i = 0; i < 3; ++i) xb[i] = ring + F_A + r * 224 + wn * 112 + pp * 8 + ((i + rot) % 3) * 32;
    xb1 = ring + F_A + r * 224 + wn * 112 + 96 + (pp & 1) * 8 + (pp >> 1) * G3_UP;
  }
  float cs[3] = {0.f, 0.f, 0.f};
  bf16x8 fa[MB], fb[NB];
  f32x4 acc[MB][NB];
#pragma unroll
  for (int mb = 0; mb < MB; ++mb)
#pragma unroll
    for (int nb = 0; nb < NB; ++nb) { acc[mb][nb] = (f32x4){0.f, 0.f, 0.f, 0.f}; asm volatile("" : "+v"(acc[mb][nb])); }
  asm volatile("" : "+v"(xa), "+v"(xb[0]), "+v"(xb[1]), "+v"(xb[2]), "+v"(xb1));
  barrier();
  if (wn == 1) {
    const auto rst = OUT_RSRC(p.stats, 8 * S_ * 16);
    f32x2 sv[16];
#pragma unroll
    for (int i = 0; i < 16; ++i) sv[i] = __builtin_bit_cast(f32x2, __builtin_amdgcn_raw_buffer_load_b64(rst, (tm * BM + wm * 64 + lane) * 8, i * S_ * 8, 16));
    SB();
    barrier();
    SB();
    float m = 0.f;
#pragma unroll
    for (int i = 0; i < 16; ++i) m += sv[i][0];
    m *= (1.0f / 16.0f);
    float q = 0.f;
#pragma unroll
    for (int i = 0; i < 16; ++i) {
      const float d = sv[i][0] - m;
      q += sv[i][1] + 64.0f * d * d;
    }
    tab[wm * 64 + lane] = (f32x2){m, q * (1.0f / D_) + EPS};
  }
  wait_lgkm0();
  barrier();
  const unsigned csmask = (0x010101u << cls) | (1u << ((cls + 1) & 7));
  auto reads = [&]() {
    SB();
    fa[0] = lds_b128(xa);
#pragma unroll
    for (int j = 0; j < 7; ++j) {
      const unsigned o = j < 6 ? xb[j % 3] + (j / 3) * G3_UP : xb1;
      fb[j] = lds_tr(o, o + 1792);
    }
#pragma unroll
    for (int mb = 1; mb < MB; ++mb) fa[mb] = lds_b128(xa + mb * 1024);
    SB();
  };
  reads();
#pragma unroll 1
  for (unsigned t = 0; t < NT; ++t) {
    const unsigned delta = ((t & 3) == 3) ? (unsigned)(-3 * F_ST) : (unsigned)F_ST;
    xa += delta; xb[0] += delta; xb[1] += delta; xb[2] += delta; xb1 += delta;
    asm volatile("" : "+v"(xa), "+v"(xb[0]), "+v"(xb[1]), "+v"(xb[2]), "+v"(xb1));
    if (__builtin_expect((csmask >> t) & 1u, 0)) {
#pragma unroll
      for (int j = 0; j < 4; ++j)
#pragma unroll
        for (int i = 0; i < 3; ++i)
          asm volatile("v_dot2c_f32_bf16 %0, %1, %2" : "+v"(cs[i]) : "s"(0x3f803f80u), "v"(__builtin_bit_cast(u32x4, fb[3 * i])[j]));
      asm volatile("s_mov_b32 m0, %3\n\ts_nop 2\n\tds_write_addtid_b32 %0\n\tds_write_addtid_b32 %1 offset:256\n\tds_write_addtid_b32 %2 offset:512" ::"v"(cs[0]),
                   "v"(cs[1]), "v"(cs[2]), "s"(lds0 + w * 768)
                   : "memory");
    }
    wait_lgkm0();
    barrier();
    SB();
#pragma unroll
    for (int mb = 0; mb < MB; ++mb)
#pragma unroll
      for (int i = 0; i < NB; ++i) {
        const int nb = (mb & 1) ? NB - 1 - i : i;
        MFMA(fb[nb], fa[mb], acc[mb][nb]);
      }
    SB();
    barrier();
    reads();
  }
  if (wn == 0) barrier();
  mfma_drain();
  SB();
  const int lane2 = lane_id();
  const int gr = lane2 & 15, gq = lane2 >> 4;
  auto act2 = [](f32x2 a, f32x2 b, float sc, float k) {
    const f32x2 t = a * sc;
    const f32x2 e = {__builtin_amdgcn_exp2f(t.x), __builtin_amdgcn_exp2f(t.y)};
    const f32x2 d = e * k + k;
    const f32x2 r = {__builtin_amdgcn_rcpf(d.x), __builtin_amdgcn_rcpf(d.y)};
    const f32x2 o = (a * r) * b;
    return pk_bf16(o.x, o.y);
  };
  const auto ro = OUT_RSRC(p.gu, S_ * F_ * 2);
  float nm[MB], kk[MB], sc[MB];
#pragma unroll
  for (int mb = 0; mb < MB; ++mb) {
    const f32x2 st = tab[wm * 64 + mb * 16 + gr];
    nm[mb] = -st.x; kk[mb] = st.y; sc[mb] = -1.44269504089f * __builtin_amdgcn_rsqf(st.y);
  }
  const int gch0 = tn * 14 + wn * 7;
  const unsigned obase = (tm * (BM / 16) + wm * MB) * (F_ / 32) * 1024 + gr * 16;
#pragma unroll
  for (int i = 0; i < 3; ++i) {
    const unsigned ib = (i + rot) % 3;
    const f32x4 cg = *(const LAS f32x4*)(cst + wn * 56 + ib * 16 + gq * 4), cu = *(const LAS f32x4*)(cst + 112 + wn * 56 + ib * 16 + gq * 4);
    const unsigned oo = obase + (gq & 1) * (F_ / 32) * 1024 + (gch0 + 2 * ib + (gq >> 1)) * 256;
#pragma unroll
    for (int mp = 0; mp < MB / 2; ++mp) {
      u32x2 pkd[2];
#pragma unroll
      for (int h = 0; h < 2; ++h) {
        const int mb = 2 * mp + h;
        const f32x4 a = cg * nm[mb] + acc[mb][i], b = cu * nm[mb] + acc[mb][3 + i];
        pkd[h] = (u32x2){act2(a.xy, b.xy, sc[mb], kk[mb]), act2(a.zw, b.zw, sc[mb], kk[mb])};
      }
      auto s0 = __builtin_amdgcn_permlane16_swap(pkd[0].x, pkd[1].x, false, false);
      auto s1 = __builtin_amdgcn_permlane16_swap(pkd[0].y, pkd[1].y, false, false);
      __builtin_amdgcn_raw_buffer_store_b128((u32x4){s0[0], s1[0], s0[1], s1[1]}, ro, oo + mp * 2 * (F_ / 32) * 1024, 0, ST_AUX);
    }
  }
  {
    const f32x4 cm = *(const LAS f32x4*)(cst + (gq >> 1) * 112 + wn * 56 + 48 + (gq & 1) * 4);
    u32x2 pkd[MB / 2];
#pragma unroll
    for (int mp = 0; mp < MB / 2; ++mp) {
      const f32x4 v0 = cm * nm[2 * mp] + acc[2 * mp][6], v1 = cm * nm[2 * mp + 1] + acc[2 * mp + 1][6];
      f32x4 a, b;
#pragma unroll
      for (int j = 0; j < 4; ++j) {
        auto s = __builtin_amdgcn_permlane32_swap(__float_as_uint(v0[j]), __float_as_uint(v1[j]), false, false);
        a[j] = __uint_as_float(s[0]); b[j] = __uint_as_float(s[1]);
      }
      const float scs = (gq & 2) ? sc[2 * mp + 1] : sc[2 * mp], ks = (gq & 2) ? kk[2 * mp + 1] : kk[2 * mp];
      pkd[mp] = (u32x2){act2(a.xy, b.xy, scs, ks), act2(a.zw, b.zw, scs, ks)};
    }
    auto s0 = __builtin_amdgcn_permlane16_swap(pkd[0].x, pkd[1].x, false, false);
    auto s1 = __builtin_amdgcn_permlane16_swap(pkd[0].y, pkd[1].y, false, false);
    STORE16(ro, obase + ((gq & 1) * 2 + (gq >> 1)) * (F_ / 32) * 1024 + (gch0 + 6) * 256, ((u32x4){s0[0], s1[0], s0[1], s1[1]}));
  }
}

__global__ __launch_bounds__(768, 3) void k_block(FArgs p) {
  extern __shared__ __attribute__((aligned(16))) char shm[];
  const unsigned lds0 = (unsigned)(size_t)(LAS char*)shm;
  const unsigned w = rfl(threadIdx.x >> 6);
  const unsigned xcd = blockIdx.x & 7, idx = blockIdx.x >> 3;
  char* fl = (char*)p.flags;
  const unsigned tm = (xcd >> 1) * 4 + (idx & 3), kh = xcd & 1, lj = idx >> 2;
  if (w >= 8) {
    const unsigned lw = w - 8;
    Ld LA, LB;
    LA = ld_setup<1>(p, lw, xcd, idx);
    sq_loader<1>(p, LA, LB, lds0, lw, xcd, idx);
    const unsigned tag = lds_tag(lds0);
    LA = ld_setup<3>(p, lw, xcd, idx);
    ld_pin(LA);
    barrier();
    if (lw == 3) poll_tags(fl + FL_P1 + (kh * 8 + lj) * 16 * 128, 16, tag);
    barrier();
    sq_loader<2>(p, LB, LA, lds0, lw, xcd, idx);
    LB = ld_setup<4>(p, lw, xcd, idx);
    ld_pin(LB);
    barrier();
    if (lw == 3) poll_tags(fl + FL_P2 + (idx & 7) * 32 * 128, 32, tag);
    barrier();
    glu_loader(p, LA, LB, lds0, lw, xcd, idx);
    barrier();
    if (lw == 3) poll_tags(fl + FL_P3 + ((tm >> 1) * 2 + kh) * 16 * 128, 16, tag);
    barrier();
    sq_loader<4>(p, LB, LA, lds0, lw, xcd, idx);
    return;
  }
  if (w == 0 && idx < 3) {
    unsigned long long pc;
    asm volatile("s_getpc_b64 %0" : "=s"(pc));
    const unsigned ln = idx * 64 + lane_id();
    touch(lds0, (const char*)pc + (ln < CODE_LINES ? ln : CODE_LINES - 1) * 128);
  }
  if (w == 1 && idx < 20) {
    const char* base = idx < 2 ? (const char*)p.cre : idx < 4 ? (const char*)p.cim : idx < 6 ? (const char*)p.wg : idx < 8 ? (const char*)p.wu : idx < 10 ? (const char*)p.wd : idx < 11 ? (const char*)p.out : (const char*)p.ab;
    const unsigned size = idx < 4 ? S_ * S_ * 2 : idx < 10 ? D_ * F_ * 2 : idx < 11 ? S_ * D_ * 2 : (unsigned)WS_FL;
    const unsigned off = ((idx < 10 ? (idx & 1) : (idx < 11 ? 0 : idx - 11)) * 64 + lane_id()) * 65536;
    touch(lds0, base + (off < size ? off : 0u));
  }
  if (w == 2) {
    const unsigned l = lane_id();
    const unsigned off = l < 5 ? (unsigned)WS_FL + FL_PAIR + (l * 256 + blockIdx.x) * 128 : (l < 19 ? (unsigned)WS_FL + FL_CS + blockIdx.x * 1792 + (l - 5) * 128 : (unsigned)WS_ST + blockIdx.x * 1024 + (l & 7) * 128);
    touch(lds0, (const char*)p.ab + off);
  }
  unsigned tag = 0;
  sq_compute<1>(p, lds0, w, xcd, idx, tag);
  wait_vm<0>();
  barrier();
  if (w == 0 && lane_id() == 0) put_tag(fl + FL_P1 + (tm * 16 + kh * 8 + lj) * 128, tag);
  sq_compute<2>(p, lds0, w, xcd, idx, tag);
  wait_vm<0>();
  barrier();
  if (w == 0 && lane_id() == 0) put_tag(fl + FL_P2 + (tm * 16 + kh * 8 + lj) * 128, tag);
  glu_compute(p, lds0, w, xcd, idx);
  wait_vm<0>();
  barrier();
  if (w == 0 && lane_id() == 0) put_tag(fl + FL_P3 + (((idx & 7) * 2 + (xcd >> 2)) * 16 + (xcd & 3) * 4 + (idx >> 3)) * 128, tag);
  sq_compute<4>(p, lds0, w, xcd, idx, tag);
  if (blockIdx.x == 0 && w == 0 && lane_id() == 0) put_tag(fl + FL_EPOCH, tag);
}
}

extern "C" void kernel_launch(void* const* d_in, const int* in_sizes, int n_in, void* d_out, int out_size, void* d_ws, size_t ws_size,
                              hipStream_t stream) {
  char* ws = (char*)d_ws;
  FArgs f = {d_in[3], d_in[4], d_in[1], d_in[2], d_in[0], d_in[6], d_in[7], d_in[8], ws + WS_AB, ws + WS_H, ws + WS_ST, ws + WS_GU, d_out, ws + WS_XC, ws + WS_FL};
  (void)hipFuncSetAttribute((const void*)k_block, hipFuncAttributeMaxDynamicSharedMemorySize, F_LDS);
  hipLaunchKernelGGL(k_block, dim3(256), dim3(768), F_LDS, stream, f);
}
```
